```python
import torch

def _softmax(x, axis=-1):
    return torch.softmax(x, dim=axis)
def _gelu(x):
    return torch.nn.functional.gelu(x, approximate="tanh")
def _silu(x):
    return torch.nn.functional.silu(x)
def _sigmoid(x):
    return torch.sigmoid(x)
def _layernorm(x, axis=-1, eps=1e-5):
    m = x.mean(dim=axis, keepdim=True)
    v = x.var(dim=axis, keepdim=True, unbiased=False)
    return (x - m) / torch.sqrt(v + eps)
def _scatter_add(tgt, idx, src, axis=0):
    return tgt.clone().index_add_(axis, idx, src)
def _segment_sum(src, seg, n):
    out = torch.zeros((n,) + src.shape[1:], dtype=src.dtype)
    return out.index_add_(0, seg, src)
def _pad(x, pw):
    flat = tuple(v for pair in reversed(pw) for v in pair)
    return torch.nn.functional.pad(x, flat)

kernel_name = 'recurrent_scan__gqa_block_cached__glu_gated_geglu__prompt'

def setup_inputs():

    _w = (lambda a: (lambda t: t.to(torch.bfloat16) if t.is_floating_point() else t)(torch.as_tensor(a)))
    def _A_setup():
        B, Sq, D = 1, 2048, 448
        Hq, Hkv, Sk, Dh = 16, 8, 2048, 256
        assert Hq % Hkv == 0, (Hq, Hkv)
        return {
            "x": _w((lambda *s: torch.randn(*s, dtype=torch.bfloat16) * 0.05)(B, Sq, D)),
            "k_cache": _w((lambda *s: torch.randn(*s, dtype=torch.bfloat16) * 3.2)(B, Hkv, Sk, Dh)),
            "v_cache": _w((lambda *s: torch.randn(*s, dtype=torch.bfloat16) * 3.2)(B, Hkv, Sk, Dh)),
            "wq": _w((lambda *s: torch.randn(*s, dtype=torch.bfloat16) * 3.2)(D, Hq * Dh) * (D ** -0.5)),
            "wo": _w((lambda *s: torch.randn(*s, dtype=torch.bfloat16) * 3.2)(Hq * Dh, D) * ((Hq * Dh) ** -0.5)),
        }
    _A_in = _A_setup()

    def _B_setup():
        B, S, Dm, Df = 1, 2048, 448, 1792
        return {
            "x": _w((lambda *s: torch.randn(*s, dtype=torch.bfloat16) * 0.05)(B, S, Dm)),
            "w_gate": _w((lambda *s: torch.randn(*s, dtype=torch.bfloat16) * 0.4)(Dm, Df) * (Dm ** -0.5)),
            "w_up": _w((lambda *s: torch.randn(*s, dtype=torch.bfloat16) * 0.4)(Dm, Df) * (Dm ** -0.5)),
            "w_down": _w((lambda *s: torch.randn(*s, dtype=torch.bfloat16) * 0.4)(Df, Dm) * (Df ** -0.5)),
        }
    _B_in = _B_setup()

    return {
        "A_x": _A_in["x"],
        "A_k_cache": _A_in["k_cache"],
        "A_v_cache": _A_in["v_cache"],
        "A_wq": _A_in["wq"],
        "A_wo": _A_in["wo"],
        "B_x": _B_in["x"],
        "B_w_gate": _B_in["w_gate"],
        "B_w_up": _B_in["w_up"],
        "B_w_down": _B_in["w_down"],
    }

def reference(A_x, A_k_cache, A_v_cache, A_wq, A_wo, B_x, B_w_gate, B_w_up, B_w_down):
    def _A_ref(x, k_cache, v_cache, wq, wo):
        B, Sq, D = x.shape
        Hq, Dh = 16, 256
        Hkv = k_cache.shape[1]
        rep = Hq // Hkv
        q = (lambda x, *d: x.permute(*d))((x @ wq).reshape(B, Sq, Hq, Dh), 0, 2, 1, 3)
        k = (lambda x, n, axis: torch.repeat_interleave(x, n, dim=axis))(k_cache, rep, axis=1)
        v = (lambda x, n, axis: torch.repeat_interleave(x, n, dim=axis))(v_cache, rep, axis=1)
        scores = torch.einsum("bhqd,bhkd->bhqk", q, k) * (Dh ** -0.5)
        p = _softmax(scores, axis=-1)
        o = torch.einsum("bhqk,bhkd->bhqd", p, v)
        return (lambda x, *d: x.permute(*d))(o, 0, 2, 1, 3).reshape(B, Sq, Hq * Dh) @ wo

    def _B_ref(x, w_gate, w_up, w_down):
        g = torch.einsum("bsd,df->bsf", x, w_gate)
        u = torch.einsum("bsd,df->bsf", x, w_up)
        h = _gelu(g) * u
        return torch.einsum("bsf,fd->bsd", h, w_down)

    x = A_x
    xn = _layernorm(x, axis=-1)
    s = _A_ref(xn, A_k_cache, A_v_cache, A_wq, A_wo)
    g = _silu(_B_ref(xn, B_w_gate, B_w_up, B_w_down))
    return x + s * g
```

```cpp
#include <hip/hip_runtime.h>
#include <hip/hip_bf16.h>
typedef unsigned short u16;
__device__ __forceinline__ float bf2f(u16 x){ return __uint_as_float(((unsigned)x)<<16); }
__device__ __forceinline__ u16 f2bf(float f){ unsigned u=__float_as_uint(f); u += 0x7fffu + ((u>>16)&1u); return (u16)(u>>16); }
__device__ __forceinline__ float rbf(float f){ return bf2f(f2bf(f)); }

constexpr int S=2048, D=448, HQ=16, HKV=8, DH=256, DF=1792;

__global__ void ln_k(const u16* x, u16* xn){
  int row=blockIdx.x; int t=threadIdx.x;
  float v[7]; float s=0;
  for(int i=0;i<7;i++){ v[i]=bf2f(x[row*D+t+64*i]); s+=v[i]; }
  for(int o=32;o;o>>=1) s+=__shfl_xor(s,o);
  float m=s/D; float q=0;
  for(int i=0;i<7;i++){ float d=v[i]-m; q+=d*d; }
  for(int o=32;o;o>>=1) q+=__shfl_xor(q,o);
  float var=q/D;
  float mb=rbf(m), vb=rbf(var);
  float den=rbf(sqrtf(rbf(vb+1e-5f)));
  for(int i=0;i<7;i++) xn[row*D+t+64*i]=f2bf(rbf(v[i]-mb)/den);
}
__global__ void gemm_k(const u16* A, const u16* B, float* C, int M,int N,int K){
  __shared__ float As[16][17], Bs[16][17];
  int tx=threadIdx.x, ty=threadIdx.y; int row=blockIdx.y*16+ty, col=blockIdx.x*16+tx;
  float acc=0;
  for(int k0=0;k0<K;k0+=16){
    As[ty][tx]=bf2f(A[(long)row*K+k0+tx]); Bs[ty][tx]=bf2f(B[(long)(k0+ty)*N+col]);
    __syncthreads();
    for(int k=0;k<16;k++) acc+=As[ty][k]*Bs[k][tx];
    __syncthreads();
  }
  C[(long)row*N+col]=acc;
}
__global__ void cvt_k(const float* a, u16* b, long n){ long i=blockIdx.x*256L+threadIdx.x; if(i<n) b[i]=f2bf(a[i]); }
__global__ void geglu_k(const float* g,const float* u,u16* h,long n){ long i=blockIdx.x*256L+threadIdx.x; if(i<n){ float x=rbf(g[i]); float t=tanhf(0.7978845608028654f*(x+0.044715f*x*x*x)); float ge=rbf(0.5f*x*(1+t)); h[i]=f2bf(ge*rbf(u[i])); } }
__global__ void attn_k(const u16* q, const u16* k, const u16* v, u16* o){
  __shared__ float qs[8][DH]; __shared__ float sc[8][S]; __shared__ float red[8];
  int h=blockIdx.y, r0=blockIdx.x*8, t=threadIdx.x; int kh=h/2;
  for(int r=0;r<8;r++) qs[r][t]=bf2f(q[(long)(r0+r)*(HQ*DH)+h*DH+t]);
  __syncthreads();
  for(int key=t;key<S;key+=256){
    float a[8]={0,0,0,0,0,0,0,0};
    const u16* kp=k+((long)kh*S+key)*DH;
    for(int d=0;d<DH;d++){ float kv=bf2f(kp[d]); for(int r=0;r<8;r++) a[r]+=qs[r][d]*kv; }
    for(int r=0;r<8;r++) sc[r][key]=a[r]*0.0625f;
  }
  __syncthreads();
  int w=t>>6, l=t&63;
  for(int r=w;r<8;r+=4){
    float m=-1e30f; for(int i=l;i<S;i+=64) m=fmaxf(m,sc[r][i]);
    for(int of=32;of;of>>=1) m=fmaxf(m,__shfl_xor(m,of));
    float s=0; for(int i=l;i<S;i+=64){ float p=__expf(sc[r][i]-m); sc[r][i]=p; s+=p; }
    for(int of=32;of;of>>=1) s+=__shfl_xor(s,of);
    if(l==0) red[r]=1.f/s;
  }
  __syncthreads();
  float a[8]={0,0,0,0,0,0,0,0};
  for(int key=0;key<S;key++){ float vv=bf2f(v[((long)kh*S+key)*DH+t]); for(int r=0;r<8;r++) a[r]+=sc[r][key]*vv; }
  for(int r=0;r<8;r++) o[(long)(r0+r)*(HQ*DH)+h*DH+t]=f2bf(a[r]*red[r]);
}
__global__ void fin_k(const u16* x,const float* s,const float* y,u16* out,long n){ long i=blockIdx.x*256L+threadIdx.x; if(i<n){ float yy=rbf(y[i]); float g=rbf(yy/(1+__expf(-yy))); out[i]=f2bf(bf2f(x[i])+rbf(rbf(s[i])*g)); } }

extern "C" void kernel_launch(void* const* d_in, const int* in_sizes, int n_in, void* d_out, int out_size, void* d_ws, size_t ws_size, hipStream_t stream){
  const u16* x=(const u16*)d_in[0]; const u16* k=(const u16*)d_in[1]; const u16* v=(const u16*)d_in[2];
  const u16* wq=(const u16*)d_in[3]; const u16* wo=(const u16*)d_in[4];
  const u16* wg=(const u16*)d_in[6]; const u16* wu=(const u16*)d_in[7]; const u16* wd=(const u16*)d_in[8];
  char* ws=(char*)d_ws;
  u16* xn=(u16*)ws; ws+=(size_t)S*D*2;
  float* qf=(float*)ws; ws+=(size_t)S*4096*4;
  u16* qb=(u16*)ws; ws+=(size_t)S*4096*2;
  u16* ob=(u16*)ws; ws+=(size_t)S*4096*2;
  float* gf=(float*)ws; ws+=(size_t)S*DF*4;
  float* uf=(float*)ws; ws+=(size_t)S*DF*4;
  u16* hb=(u16*)ws; ws+=(size_t)S*DF*2;
  float* sf=(float*)ws; ws+=(size_t)S*D*4;
  float* yf=(float*)ws; ws+=(size_t)S*D*4;
  ln_k<<<S,64,0,stream>>>(x,xn);
  dim3 b(16,16);
  gemm_k<<<dim3(4096/16,S/16),b,0,stream>>>(xn,wq,qf,S,4096,D);
  cvt_k<<<(S*4096)/256,256,0,stream>>>(qf,qb,(long)S*4096);
  attn_k<<<dim3(S/8,HQ),256,0,stream>>>(qb,k,v,ob);
  gemm_k<<<dim3(D/16,S/16),b,0,stream>>>(ob,wo,sf,S,D,4096);
  gemm_k<<<dim3(DF/16,S/16),b,0,stream>>>(xn,wg,gf,S,DF,D);
  gemm_k<<<dim3(DF/16,S/16),b,0,stream>>>(xn,wu,uf,S,DF,D);
  geglu_k<<<(S*DF)/256,256,0,stream>>>(gf,uf,hb,(long)S*DF);
  gemm_k<<<dim3(D/16,S/16),b,0,stream>>>(hb,wd,yf,S,D,DF);
  fin_k<<<(S*D)/256,256,0,stream>>>(x,sf,yf,(u16*)d_out,(long)S*D);
}
```

```cpp
#include <hip/hip_runtime.h>
#include <hip/hip_bf16.h>

__global__ __launch_bounds__(256, 1) void k1_attn(const char* __restrict__ kc, const char* __restrict__ vc, const char* __restrict__ wq,
                                                  const char* __restrict__ wo, const char* __restrict__ wg, const char* __restrict__ wu,
                                                  const char* __restrict__ wd, const char* __restrict__ x, char* __restrict__ ws, char* __restrict__ out) {
  __shared__ __attribute__((aligned(1024))) char lds[163840];
  const unsigned b = blockIdx.x;
  const unsigned kvh = b & 7, idx = b >> 3;
  const unsigned w = __builtin_amdgcn_readfirstlane(threadIdx.x >> 6);
  const unsigned h = 2 * kvh + (w & 1);
  const unsigned tb = 4 * idx + (w & 2) + (unsigned)(size_t)lds;
  const char *pk_ = kc + ((size_t)kvh << 20), *pv_ = vc + ((size_t)kvh << 20), *pq_ = wq + kvh * 1024;
  const char *pg_ = wg + (kvh >> 1) * 896, *pu_ = wu + (kvh >> 1) * 896, *px_ = x;
#define OPERANDS \
    : "+{s[24:25]}"(pk_), "+{s[40:41]}"(pv_), "+{s[56:57]}"(pg_), "+{s[60:61]}"(pu_), "+{s[72:73]}"(px_), "+{s[76:77]}"(pq_), "+{s[88:89]}"(ws) \
    : [wo] "s"(wo + kvh * (512 * 896)), [wd] "s"(wd + (kvh >> 1) * (448 * 896)), [w] "s"(w), [h] "s"(h), [tb] "s"(tb), [x] "s"(x), [out] "s"(out), [ka] "s"(__builtin_amdgcn_kernarg_segment_ptr()) \
    : "v0","v1","v2","v3","v4","v5","v6","v7","v8","v9","v10","v11","v12","v13","v14","v15","v16","v17","v18","v19","v20","v21","v22","v23","v24","v25","v26","v27","v28","v29","v30","v31","v32","v33","v34","v35","v36","v37","v38","v39","v40","v41","v42","v43","v44","v45","v46","v47","v48","v49","v50","v51","v52","v53","v54","v55","v56","v57","v58","v59","v60","v61","v62","v63","v64","v65","v66","v67","v68","v69","v70","v71","v72","v73","v74","v75","v76","v77","v78","v79","v80","v81","v82","v83","v84","v85","v86","v87","v88","v89","v90","v91","v92","v93","v94","v95","v96","v97","v98","v99","v100","v101","v102","v103","v104","v105","v106","v107","v108","v109","v110","v111","v112","v113","v114","v115","v116","v117","v118","v119","v120","v121","v122","v123","v124","v125","v126","v127","v128","v129","v130","v131","v132","v133","v134","v135","v136","v137","v138","v139","v140","v141","v142","v143","v144","v145","v146","v147","v148","v149","v150","v151","v152","v153","v154","v155","v156","v157","v158","v159","v160","v161","v162","v163","v164","v165","v166","v167","v168","v169","v170","v171","v172","v173","v174","v175","v176","v177","v178","v179","v180","v181","v182","v183","v184","v185","v186","v187","v188","v189","v190","v191","v192","v193","v194","v195","v196","v197","v198","v199","v200","v201","v202","v203","v204","v205","v206","v207","v208","v209","v210","v211","v212","v213","v214","v215","v216","v217","v218","v219","v220","v221","v222","v223","v224","v225","v226","v227","v228","v229","v230","v231","v232","v233","v234","v235","v236","v237","v238","v239","v240","v241","v242","v243","v244","v245","v246","v247","v248","v249","v250","v251","v252","v253","v254","v255","a0","a1","a2","a3","a4","a5","a6","a7","a8","a9","a10","a11","a12","a13","a14","a15","a16","a17","a18","a19","a20","a21","a22","a23","a24","a25","a26","a27","a28","a29","a30","a31","a32","a33","a34","a35","a36","a37","a38","a39","a40","a41","a42","a43","a44","a45","a46","a47","a48","a49","a50","a51","a52","a53","a54","a55","a56","a57","a58","a59","a60","a61","a62","a63","a64","a65","a66","a67","a68","a69","a70","a71","a72","a73","a74","a75","a76","a77","a78","a79","a80","a81","a82","a83","a84","a85","a86","a87","a88","a89","a90","a91","a92","a93","a94","a95","a96","a97","a98","a99","a100","a101","a102","a103","a104","a105","a106","a107","a108","a109","a110","a111","a112","a113","a114","a115","a116","a117","a118","a119","a120","a121","a122","a123","a124","a125","a126","a127","a128","a129","a130","a131","a132","a133","a134","a135","a136","a137","a138","a139","a140","a141","a142","a143","a144","a145","a146","a147","a148","a149","a150","a151","a152","a153","a154","a155","a156","a157","a158","a159","a160","a161","a162","a163","a164","a165","a166","a167","a168","a169","a170","a171","a172","a173","a174","a175","a176","a177","a178","a179","a180","a181","a182","a183","a184","a185","a186","a187","a188","a189","a190","a191","a192","a193","a194","a195","a196","a197","a198","a199","a200","a201","a202","a203","a204","a205","a206","a207","a208","a209","a210","a211","a212","a213","a214","a215","a216","a217","a218","a219","a220","a221","a222","a223","a224","a225","a226","a227","a228","a229","a230","a231","a232","a233","a234","a235","a236","a237","a238","a239","a240","a241","a242","a243","a244","a245","a246","a247","a248","a249","a250","a251","a252","a253","a254","a255","s26","s27","s28","s29","s30","s31","s32","s33","s34","s35","s36","s37","s38","s39","s42","s43","s44","s45","s46","s47","s48","s49","s50","s51","s52","s53","s54","s55","s58","s59","s62","s63","s64","s65","s66","s67","s68","s69","s70","s71","s74","s75","s78","s79","s80","s81","s82","s83","s84","s85","s86","s87","s90","s91","s92","s93","s94","s95","s96","s97","s98","s99","s100","s101","vcc","scc","memory"
  asm volatile(
R"(s_getreg_b32 s100,hwreg(HW_REG_XCC_ID)
v_mov_b32 v63,0
Lre_%=:
s_getpc_b64 s[80:81]
Lpc0_%=:
s_mov_b32 s82,Lendb_%=-Lpc0_%=
s_mov_b32 s83,0x20000
s_and_b32 s96,%[tb],124
s_add_u32 s96,s96,%[w]
s_lshl_b32 s96,s96,10
s_lshl_b32 s96,s96,1
v_mbcnt_lo_u32_b32 v254,-1,0
v_mbcnt_hi_u32_b32 v254,-1,v254
v_and_b32 v254,15,v254
v_lshlrev_b32 v254,7,v254
v_add_u32 v254,s96,v254
buffer_load_dword v181,v254,s[80:83],0 offen
s_mov_b32 s90,0x2b40000
s_mov_b32 s91,0x20000
s_mov_b32 s74,0x1c0000
s_mov_b32 s75,0x20000
v_mbcnt_lo_u32_b32 v254,-1,0
v_mbcnt_hi_u32_b32 v254,-1,v254
v_lshrrev_b32 v255,3,v254
v_mul_u32_u24 v144,896,v255
v_lshrrev_b32 v255,4,v254
v_and_b32 v254,7,v254
v_xor_b32 v254,v254,v255
v_lshl_add_u32 v144,v254,4,v144
v_xor_b32 v145,64,v144
s_and_b32 s95,%[w],1
s_add_u32 s95,s95,%[tb]
s_mul_i32 s95,s95,14336
v_add_u32 v145,7168,v145
buffer_load_dwordx4 v[0:3],v144,s[72:75],s95 offen
buffer_load_dwordx4 v[4:7],v145,s[72:75],s95 offen
buffer_load_dwordx4 v[8:11],v144,s[72:75],s95 offen offset:128
buffer_load_dwordx4 v[12:15],v145,s[72:75],s95 offen offset:128
buffer_load_dwordx4 v[16:19],v144,s[72:75],s95 offen offset:256
buffer_load_dwordx4 v[20:23],v145,s[72:75],s95 offen offset:256
buffer_load_dwordx4 v[24:27],v144,s[72:75],s95 offen offset:384
buffer_load_dwordx4 v[28:31],v145,s[72:75],s95 offen offset:384
buffer_load_dwordx4 v[32:35],v144,s[72:75],s95 offen offset:512
buffer_load_dwordx4 v[36:39],v145,s[72:75],s95 offen offset:512
buffer_load_dwordx4 v[40:43],v144,s[72:75],s95 offen offset:640
buffer_load_dwordx4 v[44:47],v145,s[72:75],s95 offen offset:640
buffer_load_dwordx4 v[48:51],v144,s[72:75],s95 offen offset:768
buffer_load_dwordx4 v[52:55],v145,s[72:75],s95 offen offset:768
s_lshl_b32 s92,%[w],12

)"
"s_bitcmp0_b32 s100, 0\n\ts_cbranch_scc1 Lodd_%=\n"
R"(s_mov_b64 s[68:69],s[60:61]
s_add_u32 s68,s68,0x6400
s_addc_u32 s69,s69,0
s_mov_b32 s70,0x181180
s_mov_b32 s71,0x20000
s_mov_b64 s[64:65],s[56:57]
s_add_u32 s64,s64,0x6800
s_addc_u32 s65,s65,0
s_mov_b32 s66,0x180d80
s_mov_b32 s67,0x20000
s_add_u32 s60,s60,0xfffffc00
s_addc_u32 s61,s61,0xffffffff
s_mov_b32 s62,0x187980
s_mov_b32 s63,0x20000
s_mov_b32 s58,0x187580
s_mov_b32 s59,0x20000
v_mbcnt_lo_u32_b32 v237,-1,0
v_mbcnt_hi_u32_b32 v237,-1,v237
s_lshl_b32 s99,%[w],4
v_bfe_u32 v238,v237,2,3
v_add_u32 v238,s99,v238
v_mul_u32_u24 v230,0xe00,v238
v_lshrrev_b32 v238,5,v237
v_lshl_add_u32 v230,v238,6,v230
v_and_b32 v238,3,v237
v_lshl_add_u32 v230,v238,4,v230
v_xor_b32 v231,32,v230
s_and_b32 s97,%[h],2
s_mul_i32 s97,s97,256
v_add_u32 v230,s97,v230
v_add_u32 v231,s97,v231
v_accvgpr_write_b32 a255,v63
v_mbcnt_lo_u32_b32 v254,-1,0
v_mbcnt_hi_u32_b32 v254,-1,v254
v_lshlrev_b32 v254,4,v254
s_and_b32 s99,%[tb],124
s_add_u32 s99,s99,%[w]
s_lshl_b32 s99,s99,10
s_mul_i32 s99,s99,7
s_lshr_b32 s99,s99,11
v_mbcnt_lo_u32_b32 v237,-1,0
v_mbcnt_hi_u32_b32 v237,-1,v237
v_bfe_u32 v238,v237,3,2
v_add_u32 v238,s99,v238
v_and_b32 v237,7,v237
v_mul_u32_u24 v176,3584,v238
v_min_u32 v175,2,v237
v_lshl_add_u32 v176,v175,7,v176
v_add_u32 v176,s97,v176
v_lshlrev_b32 v177,13,v238
v_lshl_add_u32 v177,v237,7,v177
s_mov_b32 s26,0x100000
s_mov_b32 s27,0x20000
s_mov_b64 s[28:29],s[24:25]
s_add_u32 s28,s28,0xfffffc80
s_addc_u32 s29,s29,0xffffffff
s_mov_b32 s30,0x100380
s_mov_b32 s31,0x20000
s_mov_b64 s[32:33],s[24:25]
s_add_u32 s32,s32,0xfffff900
s_addc_u32 s33,s33,0xffffffff
s_mov_b32 s34,0x100700
s_mov_b32 s35,0x20000
s_mov_b64 s[36:37],s[24:25]
s_add_u32 s36,s36,0xfffff580
s_addc_u32 s37,s37,0xffffffff
s_mov_b32 s38,0x100a80
s_mov_b32 s39,0x20000
s_mov_b32 s42,0x100000
s_mov_b32 s43,0x20000
s_mov_b64 s[44:45],s[40:41]
s_add_u32 s44,s44,0xfffffc80
s_addc_u32 s45,s45,0xffffffff
s_mov_b32 s46,0x100380
s_mov_b32 s47,0x20000
s_mov_b64 s[48:49],s[40:41]
s_add_u32 s48,s48,0xfffff900
s_addc_u32 s49,s49,0xffffffff
s_mov_b32 s50,0x100700
s_mov_b32 s51,0x20000
s_mov_b64 s[52:53],s[40:41]
s_add_u32 s52,s52,0xfffff580
s_addc_u32 s53,s53,0xffffffff
s_mov_b32 s54,0x100a80
s_mov_b32 s55,0x20000
v_mbcnt_lo_u32_b32 v237,-1,0
v_mbcnt_hi_u32_b32 v237,-1,v237
s_lshl_b32 s99,%[w],4
v_bfe_u32 v238,v237,2,3
v_add_u32 v238,s99,v238
v_mul_u32_u24 v232,0x2000,v238
v_lshrrev_b32 v238,5,v237
v_lshl_add_u32 v232,v238,6,v232
v_and_b32 v238,3,v237
v_lshl_add_u32 v232,v238,4,v232
v_xor_b32 v233,32,v232
v_mbcnt_lo_u32_b32 v237,-1,0
v_mbcnt_hi_u32_b32 v237,-1,v237
s_mul_i32 s99,%[w],8
v_bfe_u32 v238,v237,2,3
v_add_u32 v238,s99,v238
v_mul_u32_u24 v253,0x200,v238
v_lshrrev_b32 v238,5,v237
v_lshl_add_u32 v253,v238,6,v253
v_lshrrev_b32 v238,3,v237
v_and_b32 v238,2,v238
v_and_b32 v237,3,v237
v_xor_b32 v238,v238,v237
v_lshl_add_u32 v253,v238,4,v253
v_mbcnt_lo_u32_b32 v237,-1,0
v_mbcnt_hi_u32_b32 v237,-1,v237
v_bfe_u32 v238,v237,2,2
v_lshlrev_b32 v226,6,v238
v_and_b32 v238,3,v237
v_lshl_add_u32 v226,v238,3,v226
v_bfe_u32 v238,v237,4,1
v_lshl_add_u32 v226,v238,5,v226
v_lshrrev_b32 v238,4,v237
v_lshl_add_u32 v226,v238,11,v226
s_lshr_b32 s99,%[w],1
s_lshl_b32 s101,s99,9
s_and_b32 s98,%[w],1
s_lshl_b32 s100,s98,5
s_lshl_b32 s98,s98,14
v_add_u32 v226,s101,v226
v_add_u32 v224,s98,v226
v_xor_b32 v225,32,v224
v_xor_b32 v226,s100,v226
s_mov_b32 s93,0
s_mov_b32 s94,0
v_add_u32 v227,65536,v224
v_add_u32 v228,65536,v225
v_add_u32 v229,65536,v226
v_mbcnt_lo_u32_b32 v237,-1,0
v_mbcnt_hi_u32_b32 v237,-1,v237
v_bfe_u32 v238,v237,2,3
v_lshlrev_b32 v250,6,v238
v_and_b32 v238,3,v237
v_lshl_add_u32 v250,v238,3,v250
v_bfe_u32 v238,v237,4,1
v_lshl_add_u32 v250,v238,5,v250
v_lshrrev_b32 v238,5,v237
v_lshl_add_u32 v250,v238,12,v250
s_lshl_b32 s101,s99,14
s_add_u32 s98,s101,65536
v_add_u32 v250,s98,v250
v_xor_b32 v251,32,v250
v_mbcnt_lo_u32_b32 v237,-1,0
v_mbcnt_hi_u32_b32 v237,-1,v237
v_lshrrev_b32 v238,4,v237
v_bfe_u32 v239,v237,1,3
v_xor_b32 v238,v238,v239
v_and_b32 v248,4,v238
v_and_b32 v239,1,v238
v_lshl_add_u32 v248,v239,1,v248
v_bfe_u32 v239,v238,1,1
v_add_u32 v248,v248,v239
s_lshl_b32 s98,s99,2
v_xor_b32 v248,s98,v248
v_lshlrev_b32 v248,4,v248
v_and_b32 v238,7,v237
v_lshl_add_u32 v248,v238,7,v248
v_bfe_u32 v238,v237,3,1
v_lshl_add_u32 v248,v238,12,v248
v_add_u32 v248,s101,v248
v_xor_b32 v249,64,v248
v_lshrrev_b32 v238,4,v237
v_and_b32 v252,4,v238
v_and_b32 v239,1,v238
v_lshl_add_u32 v252,v239,1,v252
v_bfe_u32 v239,v238,1,1
v_add_u32 v252,v252,v239
s_and_b32 s98,%[w],1
s_lshl_b32 s98,s98,2
v_or_b32 v252,s98,v252
v_and_b32 v238,7,v237
v_xor_b32 v252,v252,v238
v_lshlrev_b32 v252,4,v252
v_lshrrev_b32 v238,3,v237
v_lshl_add_u32 v252,v238,9,v252
v_add_u32 v252,s92,v252
s_lshl_b32 s98,%[w],13
s_add_u32 s98,s98,98304
v_add_u32 v235,s98,v254
v_xor_b32 v236,16384,v235
s_mul_i32 s96,%[tb],57344
s_xor_b32 s97,%[tb],2
s_mul_i32 s97,s97,57344
s_add_u32 s96,s96,0x700000
s_add_u32 s97,s97,0x700000
s_mov_b32 s100,0xbdd2d3e8
v_mov_b32 v234,0xc0135761
v_mbcnt_lo_u32_b32 v237,-1,0
v_mbcnt_hi_u32_b32 v237,-1,v237
v_lshrrev_b32 v238,4,v237
v_bfe_u32 v239,v237,1,3
v_xor_b32 v238,v238,v239
v_lshlrev_b32 v146,4,v238
v_and_b32 v238,7,v237
v_lshl_add_u32 v146,v238,7,v146
)"
R"(v_bfe_u32 v238,v237,3,1
v_lshl_add_u32 v146,v238,10,v146
s_lshr_b32 s98,%[w],1
s_mul_i32 s98,s98,32768
s_sub_u32 s101,131072,s98
s_add_u32 s98,s98,98304
v_add_u32 v148,s101,v146
v_add_u32 v146,s98,v146
v_xor_b32 v147,64,v146
v_xor_b32 v149,64,v148
s_and_b32 s101,%[w],1
s_lshl_b32 s101,s101,14
s_add_u32 s98,s98,s101
v_add_u32 v150,s98,v254
s_mov_b32 s99,0x3f803f80
s_mov_b32 s98,0x00003f80
s_mov_b32 s101,0x3f800000
s_add_u32 m0,s92,0x10000
s_mov_b32 s95,0x100
buffer_load_dwordx4 v230,s[56:59],s95 offen lds
buffer_load_dwordx4 v230,s[60:63],s95 offen offset:1024 lds
buffer_load_dwordx4 v231,s[64:67],s95 offen offset:2048 lds
buffer_load_dwordx4 v231,s[68:71],s95 offen offset:3072 lds
s_add_u32 m0,s92,0x14000
s_mov_b32 s95,0x38100
buffer_load_dwordx4 v230,s[56:59],s95 offen lds
buffer_load_dwordx4 v230,s[60:63],s95 offen offset:1024 lds
buffer_load_dwordx4 v231,s[64:67],s95 offen offset:2048 lds
buffer_load_dwordx4 v231,s[68:71],s95 offen offset:3072 lds
v_mov_b32 v128,0
v_mov_b32 v129,0
v_mov_b32 v130,0
v_mov_b32 v131,0
s_waitcnt vmcnt(20)
v_dot2_f32_bf16 v129,v0,s99,v129
v_dot2_f32_bf16 v128,v0,v0,v128
v_dot2_f32_bf16 v131,v4,s99,v131
v_dot2_f32_bf16 v130,v4,v4,v130
v_dot2_f32_bf16 v129,v1,s99,v129
v_dot2_f32_bf16 v128,v1,v1,v128
v_dot2_f32_bf16 v131,v5,s99,v131
v_dot2_f32_bf16 v130,v5,v5,v130
v_dot2_f32_bf16 v129,v2,s99,v129
v_dot2_f32_bf16 v128,v2,v2,v128
v_dot2_f32_bf16 v131,v6,s99,v131
v_dot2_f32_bf16 v130,v6,v6,v130
v_dot2_f32_bf16 v129,v3,s99,v129
v_dot2_f32_bf16 v128,v3,v3,v128
v_dot2_f32_bf16 v131,v7,s99,v131
v_dot2_f32_bf16 v130,v7,v7,v130
s_waitcnt vmcnt(18)
v_dot2_f32_bf16 v129,v8,s99,v129
v_dot2_f32_bf16 v128,v8,v8,v128
v_dot2_f32_bf16 v131,v12,s99,v131
v_dot2_f32_bf16 v130,v12,v12,v130
v_dot2_f32_bf16 v129,v9,s99,v129
v_dot2_f32_bf16 v128,v9,v9,v128
v_dot2_f32_bf16 v131,v13,s99,v131
v_dot2_f32_bf16 v130,v13,v13,v130
v_dot2_f32_bf16 v129,v10,s99,v129
v_dot2_f32_bf16 v128,v10,v10,v128
v_dot2_f32_bf16 v131,v14,s99,v131
v_dot2_f32_bf16 v130,v14,v14,v130
v_dot2_f32_bf16 v129,v11,s99,v129
v_dot2_f32_bf16 v128,v11,v11,v128
v_dot2_f32_bf16 v131,v15,s99,v131
v_dot2_f32_bf16 v130,v15,v15,v130
s_waitcnt vmcnt(16)
v_dot2_f32_bf16 v129,v16,s99,v129
v_dot2_f32_bf16 v128,v16,v16,v128
v_dot2_f32_bf16 v131,v20,s99,v131
v_dot2_f32_bf16 v130,v20,v20,v130
v_dot2_f32_bf16 v129,v17,s99,v129
v_dot2_f32_bf16 v128,v17,v17,v128
v_dot2_f32_bf16 v131,v21,s99,v131
v_dot2_f32_bf16 v130,v21,v21,v130
v_dot2_f32_bf16 v129,v18,s99,v129
v_dot2_f32_bf16 v128,v18,v18,v128
v_dot2_f32_bf16 v131,v22,s99,v131
v_dot2_f32_bf16 v130,v22,v22,v130
v_dot2_f32_bf16 v129,v19,s99,v129
v_dot2_f32_bf16 v128,v19,v19,v128
v_dot2_f32_bf16 v131,v23,s99,v131
v_dot2_f32_bf16 v130,v23,v23,v130
s_waitcnt vmcnt(14)
v_dot2_f32_bf16 v129,v24,s99,v129
v_dot2_f32_bf16 v128,v24,v24,v128
v_dot2_f32_bf16 v131,v28,s99,v131
v_dot2_f32_bf16 v130,v28,v28,v130
v_dot2_f32_bf16 v129,v25,s99,v129
v_dot2_f32_bf16 v128,v25,v25,v128
v_dot2_f32_bf16 v131,v29,s99,v131
v_dot2_f32_bf16 v130,v29,v29,v130
v_dot2_f32_bf16 v129,v26,s99,v129
v_dot2_f32_bf16 v128,v26,v26,v128
v_dot2_f32_bf16 v131,v30,s99,v131
v_dot2_f32_bf16 v130,v30,v30,v130
v_dot2_f32_bf16 v129,v27,s99,v129
v_dot2_f32_bf16 v128,v27,v27,v128
v_dot2_f32_bf16 v131,v31,s99,v131
v_dot2_f32_bf16 v130,v31,v31,v130
s_waitcnt vmcnt(12)
v_dot2_f32_bf16 v129,v32,s99,v129
v_dot2_f32_bf16 v128,v32,v32,v128
v_dot2_f32_bf16 v131,v36,s99,v131
v_dot2_f32_bf16 v130,v36,v36,v130
v_dot2_f32_bf16 v129,v33,s99,v129
v_dot2_f32_bf16 v128,v33,v33,v128
v_dot2_f32_bf16 v131,v37,s99,v131
v_dot2_f32_bf16 v130,v37,v37,v130
v_dot2_f32_bf16 v129,v34,s99,v129
v_dot2_f32_bf16 v128,v34,v34,v128
v_dot2_f32_bf16 v131,v38,s99,v131
v_dot2_f32_bf16 v130,v38,v38,v130
v_dot2_f32_bf16 v129,v35,s99,v129
v_dot2_f32_bf16 v128,v35,v35,v128
v_dot2_f32_bf16 v131,v39,s99,v131
v_dot2_f32_bf16 v130,v39,v39,v130
s_waitcnt vmcnt(10)
v_dot2_f32_bf16 v129,v40,s99,v129
v_dot2_f32_bf16 v128,v40,v40,v128
v_dot2_f32_bf16 v131,v44,s99,v131
v_dot2_f32_bf16 v130,v44,v44,v130
v_dot2_f32_bf16 v129,v41,s99,v129
v_dot2_f32_bf16 v128,v41,v41,v128
v_dot2_f32_bf16 v131,v45,s99,v131
v_dot2_f32_bf16 v130,v45,v45,v130
v_dot2_f32_bf16 v129,v42,s99,v129
v_dot2_f32_bf16 v128,v42,v42,v128
v_dot2_f32_bf16 v131,v46,s99,v131
v_dot2_f32_bf16 v130,v46,v46,v130
v_dot2_f32_bf16 v129,v43,s99,v129
v_dot2_f32_bf16 v128,v43,v43,v128
v_dot2_f32_bf16 v131,v47,s99,v131
v_dot2_f32_bf16 v130,v47,v47,v130
s_waitcnt vmcnt(8)
v_dot2_f32_bf16 v129,v48,s99,v129
v_dot2_f32_bf16 v128,v48,v48,v128
v_dot2_f32_bf16 v131,v52,s99,v131
v_dot2_f32_bf16 v130,v52,v52,v130
v_dot2_f32_bf16 v129,v49,s99,v129
v_dot2_f32_bf16 v128,v49,v49,v128
v_dot2_f32_bf16 v131,v53,s99,v131
v_dot2_f32_bf16 v130,v53,v53,v130
v_dot2_f32_bf16 v129,v50,s99,v129
v_dot2_f32_bf16 v128,v50,v50,v128
v_dot2_f32_bf16 v131,v54,s99,v131
v_dot2_f32_bf16 v130,v54,v54,v130
v_dot2_f32_bf16 v129,v51,s99,v129
v_dot2_f32_bf16 v128,v51,v51,v128
v_dot2_f32_bf16 v131,v55,s99,v131
v_dot2_f32_bf16 v130,v55,v55,v130
s_nop 0
v_add_f32_dpp v128,v128,v128 quad_perm:[1,0,3,2] row_mask:0xf bank_mask:0xf
v_add_f32_dpp v129,v129,v129 quad_perm:[1,0,3,2] row_mask:0xf bank_mask:0xf
v_add_f32_dpp v130,v130,v130 quad_perm:[1,0,3,2] row_mask:0xf bank_mask:0xf
v_add_f32_dpp v131,v131,v131 quad_perm:[1,0,3,2] row_mask:0xf bank_mask:0xf
v_add_f32_dpp v128,v128,v128 quad_perm:[2,3,0,1] row_mask:0xf bank_mask:0xf
v_add_f32_dpp v129,v129,v129 quad_perm:[2,3,0,1] row_mask:0xf bank_mask:0xf
v_add_f32_dpp v130,v130,v130 quad_perm:[2,3,0,1] row_mask:0xf bank_mask:0xf
v_add_f32_dpp v131,v131,v131 quad_perm:[2,3,0,1] row_mask:0xf bank_mask:0xf
v_add_f32_dpp v128,v128,v128 row_half_mirror row_mask:0xf bank_mask:0xf
v_add_f32_dpp v129,v129,v129 row_half_mirror row_mask:0xf bank_mask:0xf
v_add_f32_dpp v130,v130,v130 row_half_mirror row_mask:0xf bank_mask:0xf
v_add_f32_dpp v131,v131,v131 row_half_mirror row_mask:0xf bank_mask:0xf
s_mov_b64 s[72:73],s[76:77]
s_mov_b32 s74,0x37e400
s_mov_b32 s75,0x20000
s_mov_b64 s[80:81],s[76:77]
s_add_u32 s80,s80,0xf800
s_addc_u32 s81,s81,0
s_mov_b32 s82,0x36ec00
s_mov_b32 s83,0x20000
s_mov_b64 s[84:85],s[76:77]
s_add_u32 s84,s84,0xf480
s_addc_u32 s85,s85,0
s_mov_b32 s86,0x36ef80
s_mov_b32 s87,0x20000
s_add_u32 s76,s76,0xfffffc80
s_addc_u32 s77,s77,0xffffffff
s_mov_b32 s78,0x37e780
s_mov_b32 s79,0x20000
v_mul_f32 v129,0x3b124925,v129
v_mul_f32 v131,0x3b124925,v131
v_mul_f32 v128,0x3b124925,v128
v_mul_f32 v130,0x3b124925,v130
v_fma_f32 v128,-v129,v129,v128
v_fma_f32 v130,-v131,v131,v130
v_max_f32 v128,0,v128
v_max_f32 v130,0,v130
v_cvt_pk_bf16_f32 v128,0,v128
v_cvt_pk_bf16_f32 v130,0,v130
v_add_f32 v128,0x3727c5ac,v128
v_add_f32 v130,0x3727c5ac,v130
v_cvt_pk_bf16_f32 v128,0,v128
v_cvt_pk_bf16_f32 v130,0,v130
v_sqrt_f32 v128,v128
v_sqrt_f32 v130,v130
v_cvt_pk_bf16_f32 v129,0,-v129
v_cvt_pk_bf16_f32 v131,0,-v131
)"
R"(v_cvt_pk_bf16_f32 v128,0,v128
v_cvt_pk_bf16_f32 v130,0,v130
v_rcp_f32 v128,v128
v_rcp_f32 v130,v130
s_add_u32 m0,s92,0x0
s_mov_b32 s95,0x70100
buffer_load_dwordx4 v230,s[56:59],s95 offen lds
v_dot2_f32_bf16 v136,v0,s98,v129
v_dot2_f32_bf16 v137,v0,s101,v129
v_dot2_f32_bf16 v138,v1,s98,v129
v_dot2_f32_bf16 v139,v1,s101,v129
v_dot2_f32_bf16 v140,v2,s98,v129
v_dot2_f32_bf16 v141,v2,s101,v129
v_dot2_f32_bf16 v142,v3,s98,v129
v_dot2_f32_bf16 v143,v3,s101,v129
v_cvt_pk_bf16_f32 v136,0,v136
v_cvt_pk_bf16_f32 v137,0,v137
v_cvt_pk_bf16_f32 v138,0,v138
v_cvt_pk_bf16_f32 v139,0,v139
buffer_load_dwordx4 v230,s[60:63],s95 offen offset:1024 lds
v_cvt_pk_bf16_f32 v140,0,v140
v_cvt_pk_bf16_f32 v141,0,v141
v_cvt_pk_bf16_f32 v142,0,v142
v_cvt_pk_bf16_f32 v143,0,v143
v_pk_mul_f32 v[136:137],v[136:137],v[128:129] op_sel_hi:[1,0]
v_pk_mul_f32 v[138:139],v[138:139],v[128:129] op_sel_hi:[1,0]
v_pk_mul_f32 v[140:141],v[140:141],v[128:129] op_sel_hi:[1,0]
v_pk_mul_f32 v[142:143],v[142:143],v[128:129] op_sel_hi:[1,0]
v_cvt_pk_bf16_f32 v0,v136,v137
v_cvt_pk_bf16_f32 v1,v138,v139
v_cvt_pk_bf16_f32 v2,v140,v141
v_cvt_pk_bf16_f32 v3,v142,v143
ds_write_b128 v150,v[0:3]
buffer_load_dwordx4 v231,s[64:67],s95 offen offset:2048 lds
v_dot2_f32_bf16 v136,v4,s98,v131
v_dot2_f32_bf16 v137,v4,s101,v131
v_dot2_f32_bf16 v138,v5,s98,v131
v_dot2_f32_bf16 v139,v5,s101,v131
v_dot2_f32_bf16 v140,v6,s98,v131
v_dot2_f32_bf16 v141,v6,s101,v131
v_dot2_f32_bf16 v142,v7,s98,v131
v_dot2_f32_bf16 v143,v7,s101,v131
v_cvt_pk_bf16_f32 v136,0,v136
v_cvt_pk_bf16_f32 v137,0,v137
v_cvt_pk_bf16_f32 v138,0,v138
v_cvt_pk_bf16_f32 v139,0,v139
buffer_load_dwordx4 v231,s[68:71],s95 offen offset:3072 lds
v_cvt_pk_bf16_f32 v140,0,v140
v_cvt_pk_bf16_f32 v141,0,v141
v_cvt_pk_bf16_f32 v142,0,v142
v_cvt_pk_bf16_f32 v143,0,v143
v_pk_mul_f32 v[136:137],v[136:137],v[130:131] op_sel_hi:[1,0]
v_pk_mul_f32 v[138:139],v[138:139],v[130:131] op_sel_hi:[1,0]
v_pk_mul_f32 v[140:141],v[140:141],v[130:131] op_sel_hi:[1,0]
v_pk_mul_f32 v[142:143],v[142:143],v[130:131] op_sel_hi:[1,0]
v_cvt_pk_bf16_f32 v4,v136,v137
v_cvt_pk_bf16_f32 v5,v138,v139
v_cvt_pk_bf16_f32 v6,v140,v141
v_cvt_pk_bf16_f32 v7,v142,v143
ds_write_b128 v150,v[4:7] offset:1024
s_add_u32 m0,s92,0x4000
s_mov_b32 s95,0xa8100
buffer_load_dwordx4 v230,s[56:59],s95 offen lds
v_dot2_f32_bf16 v136,v8,s98,v129
v_dot2_f32_bf16 v137,v8,s101,v129
v_dot2_f32_bf16 v138,v9,s98,v129
v_dot2_f32_bf16 v139,v9,s101,v129
v_dot2_f32_bf16 v140,v10,s98,v129
v_dot2_f32_bf16 v141,v10,s101,v129
v_dot2_f32_bf16 v142,v11,s98,v129
v_dot2_f32_bf16 v143,v11,s101,v129
v_cvt_pk_bf16_f32 v136,0,v136
v_cvt_pk_bf16_f32 v137,0,v137
v_cvt_pk_bf16_f32 v138,0,v138
v_cvt_pk_bf16_f32 v139,0,v139
buffer_load_dwordx4 v230,s[60:63],s95 offen offset:1024 lds
v_cvt_pk_bf16_f32 v140,0,v140
v_cvt_pk_bf16_f32 v141,0,v141
v_cvt_pk_bf16_f32 v142,0,v142
v_cvt_pk_bf16_f32 v143,0,v143
v_pk_mul_f32 v[136:137],v[136:137],v[128:129] op_sel_hi:[1,0]
v_pk_mul_f32 v[138:139],v[138:139],v[128:129] op_sel_hi:[1,0]
v_pk_mul_f32 v[140:141],v[140:141],v[128:129] op_sel_hi:[1,0]
v_pk_mul_f32 v[142:143],v[142:143],v[128:129] op_sel_hi:[1,0]
v_cvt_pk_bf16_f32 v8,v136,v137
v_cvt_pk_bf16_f32 v9,v138,v139
v_cvt_pk_bf16_f32 v10,v140,v141
v_cvt_pk_bf16_f32 v11,v142,v143
ds_write_b128 v150,v[8:11] offset:2048
buffer_load_dwordx4 v231,s[64:67],s95 offen offset:2048 lds
v_dot2_f32_bf16 v136,v12,s98,v131
v_dot2_f32_bf16 v137,v12,s101,v131
v_dot2_f32_bf16 v138,v13,s98,v131
v_dot2_f32_bf16 v139,v13,s101,v131
v_dot2_f32_bf16 v140,v14,s98,v131
v_dot2_f32_bf16 v141,v14,s101,v131
v_dot2_f32_bf16 v142,v15,s98,v131
v_dot2_f32_bf16 v143,v15,s101,v131
v_cvt_pk_bf16_f32 v136,0,v136
v_cvt_pk_bf16_f32 v137,0,v137
v_cvt_pk_bf16_f32 v138,0,v138
v_cvt_pk_bf16_f32 v139,0,v139
buffer_load_dwordx4 v231,s[68:71],s95 offen offset:3072 lds
v_cvt_pk_bf16_f32 v140,0,v140
v_cvt_pk_bf16_f32 v141,0,v141
v_cvt_pk_bf16_f32 v142,0,v142
v_cvt_pk_bf16_f32 v143,0,v143
v_pk_mul_f32 v[136:137],v[136:137],v[130:131] op_sel_hi:[1,0]
v_pk_mul_f32 v[138:139],v[138:139],v[130:131] op_sel_hi:[1,0]
v_pk_mul_f32 v[140:141],v[140:141],v[130:131] op_sel_hi:[1,0]
v_pk_mul_f32 v[142:143],v[142:143],v[130:131] op_sel_hi:[1,0]
v_cvt_pk_bf16_f32 v12,v136,v137
v_cvt_pk_bf16_f32 v13,v138,v139
v_cvt_pk_bf16_f32 v14,v140,v141
v_cvt_pk_bf16_f32 v15,v142,v143
ds_write_b128 v150,v[12:15] offset:3072
s_add_u32 m0,s92,0x8000
s_mov_b32 s95,0xe0100
buffer_load_dwordx4 v230,s[56:59],s95 offen lds
v_dot2_f32_bf16 v136,v16,s98,v129
v_dot2_f32_bf16 v137,v16,s101,v129
v_dot2_f32_bf16 v138,v17,s98,v129
v_dot2_f32_bf16 v139,v17,s101,v129
v_dot2_f32_bf16 v140,v18,s98,v129
v_dot2_f32_bf16 v141,v18,s101,v129
v_dot2_f32_bf16 v142,v19,s98,v129
v_dot2_f32_bf16 v143,v19,s101,v129
v_cvt_pk_bf16_f32 v136,0,v136
v_cvt_pk_bf16_f32 v137,0,v137
v_cvt_pk_bf16_f32 v138,0,v138
v_cvt_pk_bf16_f32 v139,0,v139
buffer_load_dwordx4 v230,s[60:63],s95 offen offset:1024 lds
v_cvt_pk_bf16_f32 v140,0,v140
v_cvt_pk_bf16_f32 v141,0,v141
v_cvt_pk_bf16_f32 v142,0,v142
v_cvt_pk_bf16_f32 v143,0,v143
v_pk_mul_f32 v[136:137],v[136:137],v[128:129] op_sel_hi:[1,0]
v_pk_mul_f32 v[138:139],v[138:139],v[128:129] op_sel_hi:[1,0]
v_pk_mul_f32 v[140:141],v[140:141],v[128:129] op_sel_hi:[1,0]
v_pk_mul_f32 v[142:143],v[142:143],v[128:129] op_sel_hi:[1,0]
v_cvt_pk_bf16_f32 v16,v136,v137
v_cvt_pk_bf16_f32 v17,v138,v139
v_cvt_pk_bf16_f32 v18,v140,v141
v_cvt_pk_bf16_f32 v19,v142,v143
ds_write_b128 v150,v[16:19] offset:4096
buffer_load_dwordx4 v231,s[64:67],s95 offen offset:2048 lds
v_dot2_f32_bf16 v136,v20,s98,v131
v_dot2_f32_bf16 v137,v20,s101,v131
v_dot2_f32_bf16 v138,v21,s98,v131
v_dot2_f32_bf16 v139,v21,s101,v131
v_dot2_f32_bf16 v140,v22,s98,v131
v_dot2_f32_bf16 v141,v22,s101,v131
v_dot2_f32_bf16 v142,v23,s98,v131
v_dot2_f32_bf16 v143,v23,s101,v131
v_cvt_pk_bf16_f32 v136,0,v136
v_cvt_pk_bf16_f32 v137,0,v137
v_cvt_pk_bf16_f32 v138,0,v138
v_cvt_pk_bf16_f32 v139,0,v139
buffer_load_dwordx4 v231,s[68:71],s95 offen offset:3072 lds
v_cvt_pk_bf16_f32 v140,0,v140
v_cvt_pk_bf16_f32 v141,0,v141
v_cvt_pk_bf16_f32 v142,0,v142
v_cvt_pk_bf16_f32 v143,0,v143
v_pk_mul_f32 v[136:137],v[136:137],v[130:131] op_sel_hi:[1,0]
v_pk_mul_f32 v[138:139],v[138:139],v[130:131] op_sel_hi:[1,0]
v_pk_mul_f32 v[140:141],v[140:141],v[130:131] op_sel_hi:[1,0]
v_pk_mul_f32 v[142:143],v[142:143],v[130:131] op_sel_hi:[1,0]
v_cvt_pk_bf16_f32 v20,v136,v137
v_cvt_pk_bf16_f32 v21,v138,v139
v_cvt_pk_bf16_f32 v22,v140,v141
v_cvt_pk_bf16_f32 v23,v142,v143
ds_write_b128 v150,v[20:23] offset:5120
s_add_u32 m0,s92,0xc000
s_mov_b32 s95,0x118100
buffer_load_dwordx4 v230,s[56:59],s95 offen lds
v_dot2_f32_bf16 v136,v24,s98,v129
v_dot2_f32_bf16 v137,v24,s101,v129
v_dot2_f32_bf16 v138,v25,s98,v129
v_dot2_f32_bf16 v139,v25,s101,v129
v_dot2_f32_bf16 v140,v26,s98,v129
v_dot2_f32_bf16 v141,v26,s101,v129
v_dot2_f32_bf16 v142,v27,s98,v129
v_dot2_f32_bf16 v143,v27,s101,v129
v_cvt_pk_bf16_f32 v136,0,v136
v_cvt_pk_bf16_f32 v137,0,v137
v_cvt_pk_bf16_f32 v138,0,v138
v_cvt_pk_bf16_f32 v139,0,v139
buffer_load_dwordx4 v230,s[60:63],s95 offen offset:1024 lds
v_cvt_pk_bf16_f32 v140,0,v140
v_cvt_pk_bf16_f32 v141,0,v141
v_cvt_pk_bf16_f32 v142,0,v142
v_cvt_pk_bf16_f32 v143,0,v143
v_pk_mul_f32 v[136:137],v[136:137],v[128:129] op_sel_hi:[1,0]
v_pk_mul_f32 v[138:139],v[138:139],v[128:129] op_sel_hi:[1,0]
v_pk_mul_f32 v[140:141],v[140:141],v[128:129] op_sel_hi:[1,0]
v_pk_mul_f32 v[142:143],v[142:143],v[128:129] op_sel_hi:[1,0]
v_cvt_pk_bf16_f32 v24,v136,v137
v_cvt_pk_bf16_f32 v25,v138,v139
v_cvt_pk_bf16_f32 v26,v140,v141
v_cvt_pk_bf16_f32 v27,v142,v143
)"
R"(ds_write_b128 v150,v[24:27] offset:6144
buffer_load_dwordx4 v231,s[64:67],s95 offen offset:2048 lds
v_dot2_f32_bf16 v136,v28,s98,v131
v_dot2_f32_bf16 v137,v28,s101,v131
v_dot2_f32_bf16 v138,v29,s98,v131
v_dot2_f32_bf16 v139,v29,s101,v131
v_dot2_f32_bf16 v140,v30,s98,v131
v_dot2_f32_bf16 v141,v30,s101,v131
v_dot2_f32_bf16 v142,v31,s98,v131
v_dot2_f32_bf16 v143,v31,s101,v131
v_cvt_pk_bf16_f32 v136,0,v136
v_cvt_pk_bf16_f32 v137,0,v137
v_cvt_pk_bf16_f32 v138,0,v138
v_cvt_pk_bf16_f32 v139,0,v139
buffer_load_dwordx4 v231,s[68:71],s95 offen offset:3072 lds
v_cvt_pk_bf16_f32 v140,0,v140
v_cvt_pk_bf16_f32 v141,0,v141
v_cvt_pk_bf16_f32 v142,0,v142
v_cvt_pk_bf16_f32 v143,0,v143
v_pk_mul_f32 v[136:137],v[136:137],v[130:131] op_sel_hi:[1,0]
v_pk_mul_f32 v[138:139],v[138:139],v[130:131] op_sel_hi:[1,0]
v_pk_mul_f32 v[140:141],v[140:141],v[130:131] op_sel_hi:[1,0]
v_pk_mul_f32 v[142:143],v[142:143],v[130:131] op_sel_hi:[1,0]
v_cvt_pk_bf16_f32 v28,v136,v137
v_cvt_pk_bf16_f32 v29,v138,v139
v_cvt_pk_bf16_f32 v30,v140,v141
v_cvt_pk_bf16_f32 v31,v142,v143
ds_write_b128 v150,v[28:31] offset:7168
buffer_load_dword v60,v176,s[56:59],0 offen
v_dot2_f32_bf16 v136,v32,s98,v129
v_dot2_f32_bf16 v137,v32,s101,v129
v_dot2_f32_bf16 v138,v33,s98,v129
v_dot2_f32_bf16 v139,v33,s101,v129
v_dot2_f32_bf16 v140,v34,s98,v129
v_dot2_f32_bf16 v141,v34,s101,v129
v_dot2_f32_bf16 v142,v35,s98,v129
v_dot2_f32_bf16 v143,v35,s101,v129
v_cvt_pk_bf16_f32 v136,0,v136
v_cvt_pk_bf16_f32 v137,0,v137
v_cvt_pk_bf16_f32 v138,0,v138
v_cvt_pk_bf16_f32 v139,0,v139
buffer_load_dword v61,v176,s[60:63],0 offen offset:1024
v_cvt_pk_bf16_f32 v140,0,v140
v_cvt_pk_bf16_f32 v141,0,v141
v_cvt_pk_bf16_f32 v142,0,v142
v_cvt_pk_bf16_f32 v143,0,v143
v_pk_mul_f32 v[136:137],v[136:137],v[128:129] op_sel_hi:[1,0]
v_pk_mul_f32 v[138:139],v[138:139],v[128:129] op_sel_hi:[1,0]
v_pk_mul_f32 v[140:141],v[140:141],v[128:129] op_sel_hi:[1,0]
v_pk_mul_f32 v[142:143],v[142:143],v[128:129] op_sel_hi:[1,0]
v_cvt_pk_bf16_f32 v32,v136,v137
v_cvt_pk_bf16_f32 v33,v138,v139
v_cvt_pk_bf16_f32 v34,v140,v141
v_cvt_pk_bf16_f32 v35,v142,v143
ds_write_b128 v150,v[32:35] offset:8192
buffer_load_dword v62,v177,s[72:75],0 offen
v_dot2_f32_bf16 v136,v36,s98,v131
v_dot2_f32_bf16 v137,v36,s101,v131
v_dot2_f32_bf16 v138,v37,s98,v131
v_dot2_f32_bf16 v139,v37,s101,v131
v_dot2_f32_bf16 v140,v38,s98,v131
v_dot2_f32_bf16 v141,v38,s101,v131
v_dot2_f32_bf16 v142,v39,s98,v131
v_dot2_f32_bf16 v143,v39,s101,v131
v_cvt_pk_bf16_f32 v136,0,v136
v_cvt_pk_bf16_f32 v137,0,v137
v_cvt_pk_bf16_f32 v138,0,v138
v_cvt_pk_bf16_f32 v139,0,v139
v_cvt_pk_bf16_f32 v140,0,v140
v_cvt_pk_bf16_f32 v141,0,v141
v_cvt_pk_bf16_f32 v142,0,v142
v_cvt_pk_bf16_f32 v143,0,v143
v_pk_mul_f32 v[136:137],v[136:137],v[130:131] op_sel_hi:[1,0]
v_pk_mul_f32 v[138:139],v[138:139],v[130:131] op_sel_hi:[1,0]
v_pk_mul_f32 v[140:141],v[140:141],v[130:131] op_sel_hi:[1,0]
v_pk_mul_f32 v[142:143],v[142:143],v[130:131] op_sel_hi:[1,0]
v_cvt_pk_bf16_f32 v36,v136,v137
v_cvt_pk_bf16_f32 v37,v138,v139
v_cvt_pk_bf16_f32 v38,v140,v141
v_cvt_pk_bf16_f32 v39,v142,v143
ds_write_b128 v150,v[36:39] offset:9216
v_dot2_f32_bf16 v136,v40,s98,v129
v_dot2_f32_bf16 v137,v40,s101,v129
v_dot2_f32_bf16 v138,v41,s98,v129
v_dot2_f32_bf16 v139,v41,s101,v129
v_dot2_f32_bf16 v140,v42,s98,v129
v_dot2_f32_bf16 v141,v42,s101,v129
v_dot2_f32_bf16 v142,v43,s98,v129
v_dot2_f32_bf16 v143,v43,s101,v129
v_cvt_pk_bf16_f32 v136,0,v136
v_cvt_pk_bf16_f32 v137,0,v137
v_cvt_pk_bf16_f32 v138,0,v138
v_cvt_pk_bf16_f32 v139,0,v139
v_cvt_pk_bf16_f32 v140,0,v140
v_cvt_pk_bf16_f32 v141,0,v141
v_cvt_pk_bf16_f32 v142,0,v142
v_cvt_pk_bf16_f32 v143,0,v143
v_pk_mul_f32 v[136:137],v[136:137],v[128:129] op_sel_hi:[1,0]
v_pk_mul_f32 v[138:139],v[138:139],v[128:129] op_sel_hi:[1,0]
v_pk_mul_f32 v[140:141],v[140:141],v[128:129] op_sel_hi:[1,0]
v_pk_mul_f32 v[142:143],v[142:143],v[128:129] op_sel_hi:[1,0]
v_cvt_pk_bf16_f32 v40,v136,v137
v_cvt_pk_bf16_f32 v41,v138,v139
v_cvt_pk_bf16_f32 v42,v140,v141
v_cvt_pk_bf16_f32 v43,v142,v143
ds_write_b128 v150,v[40:43] offset:10240
v_dot2_f32_bf16 v136,v44,s98,v131
v_dot2_f32_bf16 v137,v44,s101,v131
v_dot2_f32_bf16 v138,v45,s98,v131
v_dot2_f32_bf16 v139,v45,s101,v131
v_dot2_f32_bf16 v140,v46,s98,v131
v_dot2_f32_bf16 v141,v46,s101,v131
v_dot2_f32_bf16 v142,v47,s98,v131
v_dot2_f32_bf16 v143,v47,s101,v131
v_cvt_pk_bf16_f32 v136,0,v136
v_cvt_pk_bf16_f32 v137,0,v137
v_cvt_pk_bf16_f32 v138,0,v138
v_cvt_pk_bf16_f32 v139,0,v139
v_cvt_pk_bf16_f32 v140,0,v140
v_cvt_pk_bf16_f32 v141,0,v141
v_cvt_pk_bf16_f32 v142,0,v142
v_cvt_pk_bf16_f32 v143,0,v143
v_pk_mul_f32 v[136:137],v[136:137],v[130:131] op_sel_hi:[1,0]
v_pk_mul_f32 v[138:139],v[138:139],v[130:131] op_sel_hi:[1,0]
v_pk_mul_f32 v[140:141],v[140:141],v[130:131] op_sel_hi:[1,0]
v_pk_mul_f32 v[142:143],v[142:143],v[130:131] op_sel_hi:[1,0]
v_cvt_pk_bf16_f32 v44,v136,v137
v_cvt_pk_bf16_f32 v45,v138,v139
v_cvt_pk_bf16_f32 v46,v140,v141
v_cvt_pk_bf16_f32 v47,v142,v143
ds_write_b128 v150,v[44:47] offset:11264
v_dot2_f32_bf16 v136,v48,s98,v129
v_dot2_f32_bf16 v137,v48,s101,v129
v_dot2_f32_bf16 v138,v49,s98,v129
v_dot2_f32_bf16 v139,v49,s101,v129
v_dot2_f32_bf16 v140,v50,s98,v129
v_dot2_f32_bf16 v141,v50,s101,v129
v_dot2_f32_bf16 v142,v51,s98,v129
v_dot2_f32_bf16 v143,v51,s101,v129
v_cvt_pk_bf16_f32 v136,0,v136
v_cvt_pk_bf16_f32 v137,0,v137
v_cvt_pk_bf16_f32 v138,0,v138
v_cvt_pk_bf16_f32 v139,0,v139
v_cvt_pk_bf16_f32 v140,0,v140
v_cvt_pk_bf16_f32 v141,0,v141
v_cvt_pk_bf16_f32 v142,0,v142
v_cvt_pk_bf16_f32 v143,0,v143
v_pk_mul_f32 v[136:137],v[136:137],v[128:129] op_sel_hi:[1,0]
v_pk_mul_f32 v[138:139],v[138:139],v[128:129] op_sel_hi:[1,0]
v_pk_mul_f32 v[140:141],v[140:141],v[128:129] op_sel_hi:[1,0]
v_pk_mul_f32 v[142:143],v[142:143],v[128:129] op_sel_hi:[1,0]
v_cvt_pk_bf16_f32 v48,v136,v137
v_cvt_pk_bf16_f32 v49,v138,v139
v_cvt_pk_bf16_f32 v50,v140,v141
v_cvt_pk_bf16_f32 v51,v142,v143
ds_write_b128 v150,v[48:51] offset:12288
v_dot2_f32_bf16 v136,v52,s98,v131
v_dot2_f32_bf16 v137,v52,s101,v131
v_dot2_f32_bf16 v138,v53,s98,v131
v_dot2_f32_bf16 v139,v53,s101,v131
v_dot2_f32_bf16 v140,v54,s98,v131
v_dot2_f32_bf16 v141,v54,s101,v131
v_dot2_f32_bf16 v142,v55,s98,v131
v_dot2_f32_bf16 v143,v55,s101,v131
v_cvt_pk_bf16_f32 v136,0,v136
v_cvt_pk_bf16_f32 v137,0,v137
v_cvt_pk_bf16_f32 v138,0,v138
v_cvt_pk_bf16_f32 v139,0,v139
v_cvt_pk_bf16_f32 v140,0,v140
v_cvt_pk_bf16_f32 v141,0,v141
v_cvt_pk_bf16_f32 v142,0,v142
v_cvt_pk_bf16_f32 v143,0,v143
v_pk_mul_f32 v[136:137],v[136:137],v[130:131] op_sel_hi:[1,0]
v_pk_mul_f32 v[138:139],v[138:139],v[130:131] op_sel_hi:[1,0]
v_pk_mul_f32 v[140:141],v[140:141],v[130:131] op_sel_hi:[1,0]
v_pk_mul_f32 v[142:143],v[142:143],v[130:131] op_sel_hi:[1,0]
v_cvt_pk_bf16_f32 v52,v136,v137
v_cvt_pk_bf16_f32 v53,v138,v139
v_cvt_pk_bf16_f32 v54,v140,v141
v_cvt_pk_bf16_f32 v55,v142,v143
ds_write_b128 v150,v[52:55] offset:13312
s_lshr_b32 s99,%[tb],2
s_lshl_b32 s99,s99,10
s_add_u32 s99,s99,0x2b00000
v_lshlrev_b32 v255,3,v254
v_and_b32 v255,896,v255
buffer_load_dword v255,v255,s[88:91],s99 offen sc1
s_lshr_b32 s99,%[tb],2
s_lshl_b32 s99,s99,3
s_lshr_b32 s101,%[h],1
s_add_u32 s99,s99,s101
s_lshl_b32 s99,s99,7
s_lshl_b32 s101,%[w],15
s_add_u32 s99,s99,s101
s_add_u32 s99,s99,0x2b20000
v_mov_b32 v237,0
buffer_store_dword v237,off,s[88:91],s99 sc1
buffer_store_dword v237,off,s[88:91],s99 offset:64 sc1
s_sub_u32 s99,s99,131072
buffer_store_dword v237,off,s[88:91],s99 offset:4 sc1
)"
R"(s_waitcnt vmcnt(23) lgkmcnt(0)
s_barrier
ds_read_b64_tr_b16 v[192:193],v229
ds_read_b64_tr_b16 v[194:195],v229 offset:256
ds_read_b64_tr_b16 v[196:197],v229 offset:8192
ds_read_b64_tr_b16 v[198:199],v229 offset:8448
ds_read_b64_tr_b16 v[200:201],v229 offset:16384
ds_read_b64_tr_b16 v[202:203],v229 offset:16640
ds_read_b64_tr_b16 v[204:205],v229 offset:24576
ds_read_b64_tr_b16 v[206:207],v229 offset:24832
ds_read_b128 a[0:3],v146
ds_read_b128 a[56:59],v146 offset:16384
ds_read_b128 a[112:115],v148
ds_read_b128 a[168:171],v148 offset:16384
ds_read_b128 a[4:7],v147
ds_read_b128 a[60:63],v147 offset:16384
ds_read_b128 a[116:119],v149
ds_read_b128 a[172:175],v149 offset:16384
ds_read_b128 a[8:11],v146 offset:2048
ds_read_b128 a[64:67],v146 offset:18432
ds_read_b128 a[120:123],v148 offset:2048
ds_read_b128 a[176:179],v148 offset:18432
ds_read_b128 a[12:15],v147 offset:2048
ds_read_b128 a[68:71],v147 offset:18432
ds_read_b128 a[124:127],v149 offset:2048
ds_read_b128 a[180:183],v149 offset:18432
ds_read_b128 a[16:19],v146 offset:4096
ds_read_b128 a[72:75],v146 offset:20480
ds_read_b128 a[128:131],v148 offset:4096
ds_read_b128 a[184:187],v148 offset:20480
ds_read_b128 a[20:23],v147 offset:4096
ds_read_b128 a[76:79],v147 offset:20480
ds_read_b128 a[132:135],v149 offset:4096
ds_read_b128 a[188:191],v149 offset:20480
ds_read_b128 a[24:27],v146 offset:6144
ds_read_b128 a[80:83],v146 offset:22528
ds_read_b128 a[136:139],v148 offset:6144
ds_read_b128 a[192:195],v148 offset:22528
ds_read_b128 a[28:31],v147 offset:6144
ds_read_b128 a[84:87],v147 offset:22528
ds_read_b128 a[140:143],v149 offset:6144
ds_read_b128 a[196:199],v149 offset:22528
ds_read_b128 a[32:35],v146 offset:8192
ds_read_b128 a[88:91],v146 offset:24576
ds_read_b128 a[144:147],v148 offset:8192
ds_read_b128 a[200:203],v148 offset:24576
ds_read_b128 a[36:39],v147 offset:8192
ds_read_b128 a[92:95],v147 offset:24576
ds_read_b128 a[148:151],v149 offset:8192
ds_read_b128 a[204:207],v149 offset:24576
ds_read_b128 a[40:43],v146 offset:10240
ds_read_b128 a[96:99],v146 offset:26624
ds_read_b128 a[152:155],v148 offset:10240
ds_read_b128 a[208:211],v148 offset:26624
ds_read_b128 a[44:47],v147 offset:10240
ds_read_b128 a[100:103],v147 offset:26624
ds_read_b128 a[156:159],v149 offset:10240
ds_read_b128 a[212:215],v149 offset:26624
ds_read_b128 a[48:51],v146 offset:12288
ds_read_b128 a[104:107],v146 offset:28672
ds_read_b128 a[160:163],v148 offset:12288
ds_read_b128 a[216:219],v148 offset:28672
ds_read_b128 a[52:55],v147 offset:12288
ds_read_b128 a[108:111],v147 offset:28672
ds_read_b128 a[164:167],v149 offset:12288
ds_read_b128 a[220:223],v149 offset:28672
s_waitcnt lgkmcnt(0)
s_barrier
v_mfma_f32_16x16x32_bf16 v[64:67],v[192:195],a[0:3],0
s_add_u32 m0,s92,0x18000
ds_read_b64_tr_b16 v[208:209],v229 offset:1024
s_mov_b32 s95,0x150100
v_mfma_f32_16x16x32_bf16 v[64:67],v[196:199],a[4:7],v[64:67]
s_lshr_b32 s98,%[h],2
ds_read_b64_tr_b16 v[210:211],v229 offset:1280
v_mfma_f32_16x16x32_bf16 v[64:67],v[200:203],a[8:11],v[64:67]
s_mul_i32 s98,s98,28
buffer_load_dwordx4 v230,s[56:59],s95 offen lds
v_mfma_f32_16x16x32_bf16 v[64:67],v[204:207],a[12:15],v[64:67]
s_add_u32 s98,s98,%[w]
ds_read_b64_tr_b16 v[212:213],v229 offset:9216
ds_read_b64_tr_b16 v[214:215],v229 offset:9472
v_mfma_f32_16x16x32_bf16 v[68:71],v[192:195],a[56:59],0
s_and_b32 s101,%[h],2
ds_read_b64_tr_b16 v[216:217],v229 offset:17408
v_mfma_f32_16x16x32_bf16 v[68:71],v[196:199],a[60:63],v[68:71]
s_mul_i32 s101,s101,8
ds_read_b64_tr_b16 v[218:219],v229 offset:17664
v_mfma_f32_16x16x32_bf16 v[68:71],v[200:203],a[64:67],v[68:71]
s_add_u32 s98,s98,s101
buffer_load_dwordx4 v230,s[60:63],s95 offen offset:1024 lds
v_mfma_f32_16x16x32_bf16 v[68:71],v[204:207],a[68:71],v[68:71]
s_add_u32 s98,s98,8
ds_read_b64_tr_b16 v[220:221],v229 offset:25600
ds_read_b64_tr_b16 v[222:223],v229 offset:25856
v_mfma_f32_16x16x32_bf16 v[72:75],v[192:195],a[112:115],0
s_lshl_b32 s98,s98,10
v_mfma_f32_16x16x32_bf16 v[72:75],v[196:199],a[116:119],v[72:75]
s_add_u32 s99,s96,s98
v_mfma_f32_16x16x32_bf16 v[72:75],v[200:203],a[120:123],v[72:75]
s_and_b32 s101,%[w],1
buffer_load_dwordx4 v231,s[64:67],s95 offen offset:2048 lds
v_mfma_f32_16x16x32_bf16 v[72:75],v[204:207],a[124:127],v[72:75]
s_lshl_b32 s101,s101,2
v_mfma_f32_16x16x32_bf16 v[76:79],v[192:195],a[168:171],0
v_mfma_f32_16x16x32_bf16 v[76:79],v[196:199],a[172:175],v[76:79]
v_mfma_f32_16x16x32_bf16 v[76:79],v[200:203],a[176:179],v[76:79]
buffer_load_dwordx4 v231,s[68:71],s95 offen offset:3072 lds
v_mfma_f32_16x16x32_bf16 v[76:79],v[204:207],a[180:183],v[76:79]
s_waitcnt vmcnt(19) lgkmcnt(0)
v_mfma_f32_16x16x32_bf16 v[80:83],v[208:211],a[0:3],0
s_barrier
ds_read_b64_tr_b16 v[192:193],v226
s_add_u32 m0,s92,0x10000
v_mfma_f32_16x16x32_bf16 v[80:83],v[212:215],a[4:7],v[80:83]
s_mov_b32 s95,0x0
ds_read_b64_tr_b16 v[194:195],v226 offset:256
v_mfma_f32_16x16x32_bf16 v[80:83],v[216:219],a[8:11],v[80:83]
buffer_load_dwordx4 v230,s[56:59],s95 offen lds
v_mfma_f32_16x16x32_bf16 v[80:83],v[220:223],a[12:15],v[80:83]
ds_read_b64_tr_b16 v[196:197],v226 offset:8192
ds_read_b64_tr_b16 v[198:199],v226 offset:8448
v_mfma_f32_16x16x32_bf16 v[84:87],v[208:211],a[56:59],0
ds_read_b64_tr_b16 v[200:201],v226 offset:16384
v_mfma_f32_16x16x32_bf16 v[84:87],v[212:215],a[60:63],v[84:87]
ds_read_b64_tr_b16 v[202:203],v226 offset:16640
v_mfma_f32_16x16x32_bf16 v[84:87],v[216:219],a[64:67],v[84:87]
buffer_load_dwordx4 v230,s[60:63],s95 offen offset:1024 lds
v_mfma_f32_16x16x32_bf16 v[84:87],v[220:223],a[68:71],v[84:87]
ds_read_b64_tr_b16 v[204:205],v226 offset:24576
ds_read_b64_tr_b16 v[206:207],v226 offset:24832
v_mfma_f32_16x16x32_bf16 v[88:91],v[208:211],a[112:115],0
v_mfma_f32_16x16x32_bf16 v[88:91],v[212:215],a[116:119],v[88:91]
v_mfma_f32_16x16x32_bf16 v[88:91],v[216:219],a[120:123],v[88:91]
buffer_load_dwordx4 v231,s[64:67],s95 offen offset:2048 lds
v_mfma_f32_16x16x32_bf16 v[88:91],v[220:223],a[124:127],v[88:91]
v_mfma_f32_16x16x32_bf16 v[92:95],v[208:211],a[168:171],0
v_mfma_f32_16x16x32_bf16 v[92:95],v[212:215],a[172:175],v[92:95]
v_mfma_f32_16x16x32_bf16 v[92:95],v[216:219],a[176:179],v[92:95]
buffer_load_dwordx4 v231,s[68:71],s95 offen offset:3072 lds
v_mfma_f32_16x16x32_bf16 v[92:95],v[220:223],a[180:183],v[92:95]
s_waitcnt lgkmcnt(0)
v_mfma_f32_16x16x32_bf16 v[64:67],v[192:195],a[16:19],v[64:67]
s_add_u32 m0,s92,0x14000
ds_read_b64_tr_b16 v[208:209],v226 offset:1024
s_mov_b32 s95,0x80
v_mfma_f32_16x16x32_bf16 v[64:67],v[196:199],a[20:23],v[64:67]
ds_read_b64_tr_b16 v[210:211],v226 offset:1280
v_mfma_f32_16x16x32_bf16 v[64:67],v[200:203],a[24:27],v[64:67]
buffer_load_dwordx4 v230,s[56:59],s95 offen lds
v_mfma_f32_16x16x32_bf16 v[64:67],v[204:207],a[28:31],v[64:67]
ds_read_b64_tr_b16 v[212:213],v226 offset:9216
ds_read_b64_tr_b16 v[214:215],v226 offset:9472
v_mfma_f32_16x16x32_bf16 v[68:71],v[192:195],a[72:75],v[68:71]
ds_read_b64_tr_b16 v[216:217],v226 offset:17408
v_mfma_f32_16x16x32_bf16 v[68:71],v[196:199],a[76:79],v[68:71]
ds_read_b64_tr_b16 v[218:219],v226 offset:17664
v_mfma_f32_16x16x32_bf16 v[68:71],v[200:203],a[80:83],v[68:71]
buffer_load_dwordx4 v230,s[60:63],s95 offen offset:1024 lds
v_mfma_f32_16x16x32_bf16 v[68:71],v[204:207],a[84:87],v[68:71]
ds_read_b64_tr_b16 v[220:221],v226 offset:25600
ds_read_b64_tr_b16 v[222:223],v226 offset:25856
v_mfma_f32_16x16x32_bf16 v[72:75],v[192:195],a[128:131],v[72:75]
v_mfma_f32_16x16x32_bf16 v[72:75],v[196:199],a[132:135],v[72:75]
v_mfma_f32_16x16x32_bf16 v[72:75],v[200:203],a[136:139],v[72:75]
buffer_load_dwordx4 v231,s[64:67],s95 offen offset:2048 lds
v_mfma_f32_16x16x32_bf16 v[72:75],v[204:207],a[140:143],v[72:75]
v_mfma_f32_16x16x32_bf16 v[76:79],v[192:195],a[184:187],v[76:79]
v_mfma_f32_16x16x32_bf16 v[76:79],v[196:199],a[188:191],v[76:79]
v_mfma_f32_16x16x32_bf16 v[76:79],v[200:203],a[192:195],v[76:79]
buffer_load_dwordx4 v231,s[68:71],s95 offen offset:3072 lds
v_mfma_f32_16x16x32_bf16 v[76:79],v[204:207],a[196:199],v[76:79]
s_waitcnt vmcnt(19) lgkmcnt(0)
v_mfma_f32_16x16x32_bf16 v[80:83],v[208:211],a[16:19],v[80:83]
s_barrier
ds_read_b64_tr_b16 v[192:193],v226 offset:32768
s_add_u32 m0,s92,0x0
v_mfma_f32_16x16x32_bf16 v[80:83],v[212:215],a[20:23],v[80:83]
s_mov_b32 s95,0x38000
ds_read_b64_tr_b16 v[194:195],v226 offset:33024
v_mfma_f32_16x16x32_bf16 v[80:83],v[216:219],a[24:27],v[80:83]
buffer_load_dwordx4 v230,s[56:59],s95 offen lds
v_mfma_f32_16x16x32_bf16 v[80:83],v[220:223],a[28:31],v[80:83]
ds_read_b64_tr_b16 v[196:197],v226 offset:40960
ds_read_b64_tr_b16 v[198:199],v226 offset:41216
v_mfma_f32_16x16x32_bf16 v[84:87],v[208:211],a[72:75],v[84:87]
ds_read_b64_tr_b16 v[200:201],v226 offset:49152
v_mfma_f32_16x16x32_bf16 v[84:87],v[212:215],a[76:79],v[84:87]
ds_read_b64_tr_b16 v[202:203],v226 offset:49408
v_mfma_f32_16x16x32_bf16 v[84:87],v[216:219],a[80:83],v[84:87]
buffer_load_dwordx4 v230,s[60:63],s95 offen offset:1024 lds
v_mfma_f32_16x16x32_bf16 v[84:87],v[220:223],a[84:87],v[84:87]
ds_read_b64_tr_b16 v[204:205],v226 offset:57344
ds_read_b64_tr_b16 v[206:207],v226 offset:57600
v_mfma_f32_16x16x32_bf16 v[88:91],v[208:211],a[128:131],v[88:91]
v_mfma_f32_16x16x32_bf16 v[88:91],v[212:215],a[132:135],v[88:91]
v_mfma_f32_16x16x32_bf16 v[88:91],v[216:219],a[136:139],v[88:91]
buffer_load_dwordx4 v231,s[64:67],s95 offen offset:2048 lds
v_mfma_f32_16x16x32_bf16 v[88:91],v[220:223],a[140:143],v[88:91]
v_mfma_f32_16x16x32_bf16 v[92:95],v[208:211],a[184:187],v[92:95]
)"
R"(v_mfma_f32_16x16x32_bf16 v[92:95],v[212:215],a[188:191],v[92:95]
v_mfma_f32_16x16x32_bf16 v[92:95],v[216:219],a[192:195],v[92:95]
buffer_load_dwordx4 v231,s[68:71],s95 offen offset:3072 lds
v_mfma_f32_16x16x32_bf16 v[92:95],v[220:223],a[196:199],v[92:95]
s_waitcnt lgkmcnt(0)
v_mfma_f32_16x16x32_bf16 v[64:67],v[192:195],a[32:35],v[64:67]
s_add_u32 m0,s92,0x4000
ds_read_b64_tr_b16 v[208:209],v226 offset:33792
s_mov_b32 s95,0x38080
v_mfma_f32_16x16x32_bf16 v[64:67],v[196:199],a[36:39],v[64:67]
ds_read_b64_tr_b16 v[210:211],v226 offset:34048
v_mfma_f32_16x16x32_bf16 v[64:67],v[200:203],a[40:43],v[64:67]
buffer_load_dwordx4 v230,s[56:59],s95 offen lds
v_mfma_f32_16x16x32_bf16 v[64:67],v[204:207],a[44:47],v[64:67]
ds_read_b64_tr_b16 v[212:213],v226 offset:41984
ds_read_b64_tr_b16 v[214:215],v226 offset:42240
v_mfma_f32_16x16x32_bf16 v[68:71],v[192:195],a[88:91],v[68:71]
ds_read_b64_tr_b16 v[216:217],v226 offset:50176
v_mfma_f32_16x16x32_bf16 v[68:71],v[196:199],a[92:95],v[68:71]
ds_read_b64_tr_b16 v[218:219],v226 offset:50432
v_mfma_f32_16x16x32_bf16 v[68:71],v[200:203],a[96:99],v[68:71]
buffer_load_dwordx4 v230,s[60:63],s95 offen offset:1024 lds
v_mfma_f32_16x16x32_bf16 v[68:71],v[204:207],a[100:103],v[68:71]
ds_read_b64_tr_b16 v[220:221],v226 offset:58368
ds_read_b64_tr_b16 v[222:223],v226 offset:58624
v_mfma_f32_16x16x32_bf16 v[72:75],v[192:195],a[144:147],v[72:75]
v_and_b32 v226,48,v254
v_mfma_f32_16x16x32_bf16 v[72:75],v[196:199],a[148:151],v[72:75]
v_mfma_f32_16x16x32_bf16 v[72:75],v[200:203],a[152:155],v[72:75]
buffer_load_dwordx4 v231,s[64:67],s95 offen offset:2048 lds
v_mfma_f32_16x16x32_bf16 v[72:75],v[204:207],a[156:159],v[72:75]
v_mfma_f32_16x16x32_bf16 v[76:79],v[192:195],a[200:203],v[76:79]
v_mfma_f32_16x16x32_bf16 v[76:79],v[196:199],a[204:207],v[76:79]
v_mfma_f32_16x16x32_bf16 v[76:79],v[200:203],a[208:211],v[76:79]
buffer_load_dwordx4 v231,s[68:71],s95 offen offset:3072 lds
v_mfma_f32_16x16x32_bf16 v[76:79],v[204:207],a[212:215],v[76:79]
s_waitcnt vmcnt(16) lgkmcnt(0)
v_mfma_f32_16x16x32_bf16 v[80:83],v[208:211],a[32:35],v[80:83]
s_barrier
ds_read_b64_tr_b16 v[192:193],v229 offset:32768
s_add_u32 m0,s92,0x8000
v_mfma_f32_16x16x32_bf16 v[80:83],v[212:215],a[36:39],v[80:83]
s_mov_b32 s95,0x70000
v_mfma_f32_16x16x32_bf16 v[80:83],v[216:219],a[40:43],v[80:83]
buffer_load_dwordx4 v230,s[56:59],s95 offen lds
v_mfma_f32_16x16x32_bf16 v[80:83],v[220:223],a[44:47],v[80:83]
ds_read_b64_tr_b16 v[194:195],v229 offset:33024
v_mfma_f32_16x16x32_bf16 v[84:87],v[208:211],a[88:91],v[84:87]
ds_read_b64_tr_b16 v[196:197],v229 offset:40960
v_mfma_f32_16x16x32_bf16 v[84:87],v[212:215],a[92:95],v[84:87]
v_mfma_f32_16x16x32_bf16 v[84:87],v[216:219],a[96:99],v[84:87]
buffer_load_dwordx4 v230,s[60:63],s95 offen offset:1024 lds
v_mfma_f32_16x16x32_bf16 v[84:87],v[220:223],a[100:103],v[84:87]
ds_read_b64_tr_b16 v[198:199],v229 offset:41216
v_mfma_f32_16x16x32_bf16 v[88:91],v[208:211],a[144:147],v[88:91]
v_mfma_f32_16x16x32_bf16 v[88:91],v[212:215],a[148:151],v[88:91]
v_mfma_f32_16x16x32_bf16 v[88:91],v[216:219],a[152:155],v[88:91]
buffer_load_dwordx4 v231,s[64:67],s95 offen offset:2048 lds
v_mfma_f32_16x16x32_bf16 v[88:91],v[220:223],a[156:159],v[88:91]
v_mfma_f32_16x16x32_bf16 v[92:95],v[208:211],a[200:203],v[92:95]
v_mfma_f32_16x16x32_bf16 v[92:95],v[212:215],a[204:207],v[92:95]
v_mfma_f32_16x16x32_bf16 v[92:95],v[216:219],a[208:211],v[92:95]
buffer_load_dwordx4 v231,s[68:71],s95 offen offset:3072 lds
v_mfma_f32_16x16x32_bf16 v[92:95],v[220:223],a[212:215],v[92:95]
s_waitcnt lgkmcnt(0)
v_mfma_f32_16x16x32_bf16 v[64:67],v[192:195],a[48:51],v[64:67]
s_add_u32 m0,s92,0xc000
ds_read_b64_tr_b16 v[208:209],v229 offset:33792
s_mov_b32 s95,0x70080
v_mfma_f32_16x16x32_bf16 v[64:67],v[196:199],a[52:55],v[64:67]
ds_read_b64_tr_b16 v[210:211],v229 offset:34048
v_mfma_f32_16x16x32_bf16 v[68:71],v[192:195],a[104:107],v[68:71]
buffer_load_dwordx4 v230,s[56:59],s95 offen lds
v_mfma_f32_16x16x32_bf16 v[68:71],v[196:199],a[108:111],v[68:71]
ds_read_b64_tr_b16 v[212:213],v229 offset:41984
ds_read_b64_tr_b16 v[214:215],v229 offset:42240
v_mfma_f32_16x16x32_bf16 v[72:75],v[192:195],a[160:163],v[72:75]
buffer_load_dwordx4 v230,s[60:63],s95 offen offset:1024 lds
v_mfma_f32_16x16x32_bf16 v[72:75],v[196:199],a[164:167],v[72:75]
v_mul_f32 v237,v64,v64
v_fma_f32 v237,v237,s100,v234
v_mfma_f32_16x16x32_bf16 v[76:79],v[192:195],a[216:219],v[76:79]
buffer_load_dwordx4 v231,s[64:67],s95 offen offset:2048 lds
buffer_load_dwordx4 v231,s[68:71],s95 offen offset:3072 lds
v_mfma_f32_16x16x32_bf16 v[76:79],v[196:199],a[220:223],v[76:79]
s_waitcnt vmcnt(16) lgkmcnt(0)
v_mul_f32 v237,v237,v64
v_mul_f32 v238,v65,v65
v_mfma_f32_16x16x32_bf16 v[80:83],v[208:211],a[48:51],v[80:83]
s_barrier
ds_read_b64_tr_b16 v[192:193],v227
ds_read_b64_tr_b16 v[194:195],v227 offset:256
v_mfma_f32_16x16x32_bf16 v[80:83],v[212:215],a[52:55],v[80:83]
s_add_u32 m0,s92,0x18000
ds_read_b64_tr_b16 v[196:197],v227 offset:8192
ds_read_b64_tr_b16 v[198:199],v227 offset:8448
v_mfma_f32_16x16x32_bf16 v[84:87],v[208:211],a[104:107],v[84:87]
s_mov_b32 s95,0xa8000
buffer_load_dwordx4 v230,s[56:59],s95 offen lds
v_mfma_f32_16x16x32_bf16 v[84:87],v[212:215],a[108:111],v[84:87]
ds_read_b64_tr_b16 v[200:201],v228
ds_read_b64_tr_b16 v[202:203],v228 offset:256
v_mfma_f32_16x16x32_bf16 v[88:91],v[208:211],a[160:163],v[88:91]
buffer_load_dwordx4 v230,s[60:63],s95 offen offset:1024 lds
v_mfma_f32_16x16x32_bf16 v[88:91],v[212:215],a[164:167],v[88:91]
ds_read_b64_tr_b16 v[204:205],v228 offset:8192
ds_read_b64_tr_b16 v[206:207],v228 offset:8448
v_mfma_f32_16x16x32_bf16 v[92:95],v[208:211],a[216:219],v[92:95]
buffer_load_dwordx4 v231,s[64:67],s95 offen offset:2048 lds
buffer_load_dwordx4 v231,s[68:71],s95 offen offset:3072 lds
v_mfma_f32_16x16x32_bf16 v[92:95],v[212:215],a[220:223],v[92:95]
s_waitcnt lgkmcnt(0)
v_exp_f32 v237,v237
v_mfma_f32_16x16x32_bf16 v[128:131],v[192:195],a[0:3],0
s_add_u32 m0,s92,0x1c000
ds_read_b64_tr_b16 v[208:209],v227 offset:1024
s_mov_b32 s95,0xa8080
v_mfma_f32_16x16x32_bf16 v[128:131],v[196:199],a[4:7],v[128:131]
ds_read_b64_tr_b16 v[210:211],v227 offset:1280
v_mul_f32 v80,v64,v80
v_mfma_f32_16x16x32_bf16 v[132:135],v[192:195],a[56:59],0
buffer_load_dwordx4 v230,s[56:59],s95 offen lds
v_mfma_f32_16x16x32_bf16 v[132:135],v[196:199],a[60:63],v[132:135]
ds_read_b64_tr_b16 v[212:213],v227 offset:9216
ds_read_b64_tr_b16 v[214:215],v227 offset:9472
v_mfma_f32_16x16x32_bf16 v[136:139],v[192:195],a[112:115],0
ds_read_b64_tr_b16 v[216:217],v228 offset:1024
v_add_f32 v237,1.0,v237
v_mfma_f32_16x16x32_bf16 v[136:139],v[196:199],a[116:119],v[136:139]
ds_read_b64_tr_b16 v[218:219],v228 offset:1280
v_fma_f32 v238,v238,s100,v234
v_mfma_f32_16x16x32_bf16 v[140:143],v[192:195],a[168:171],0
buffer_load_dwordx4 v230,s[60:63],s95 offen offset:1024 lds
v_mfma_f32_16x16x32_bf16 v[140:143],v[196:199],a[172:175],v[140:143]
ds_read_b64_tr_b16 v[220:221],v228 offset:9216
ds_read_b64_tr_b16 v[222:223],v228 offset:9472
v_mfma_f32_16x16x32_bf16 v[144:147],v[200:203],a[0:3],0
v_rcp_f32 v237,v237
v_mfma_f32_16x16x32_bf16 v[144:147],v[204:207],a[4:7],v[144:147]
v_mul_f32 v80,v80,v237
v_mul_f32 v238,v238,v65
v_mfma_f32_16x16x32_bf16 v[148:151],v[200:203],a[56:59],0
buffer_load_dwordx4 v231,s[64:67],s95 offen offset:2048 lds
v_mfma_f32_16x16x32_bf16 v[148:151],v[204:207],a[60:63],v[148:151]
v_exp_f32 v238,v238
v_mfma_f32_16x16x32_bf16 v[152:155],v[200:203],a[112:115],0
v_mul_f32 v81,v65,v81
v_add_f32 v238,1.0,v238
v_mfma_f32_16x16x32_bf16 v[152:155],v[204:207],a[116:119],v[152:155]
v_rcp_f32 v238,v238
v_mfma_f32_16x16x32_bf16 v[156:159],v[200:203],a[168:171],0
buffer_load_dwordx4 v231,s[68:71],s95 offen offset:3072 lds
v_mfma_f32_16x16x32_bf16 v[156:159],v[204:207],a[172:175],v[156:159]
s_waitcnt vmcnt(16) lgkmcnt(0)
v_mul_f32 v81,v81,v238
v_mul_f32 v239,v66,v66
v_mfma_f32_16x16x32_bf16 v[160:163],v[208:211],a[0:3],0
s_barrier
ds_read_b64_tr_b16 v[192:193],v224
s_add_u32 m0,s92,0x10000
v_mfma_f32_16x16x32_bf16 v[160:163],v[212:215],a[4:7],v[160:163]
s_mov_b32 s95,0xe0000
ds_read_b64_tr_b16 v[194:195],v224 offset:256
v_fma_f32 v239,v239,s100,v234
v_mfma_f32_16x16x32_bf16 v[164:167],v[208:211],a[56:59],0
buffer_load_dwordx4 v230,s[56:59],s95 offen lds
v_mfma_f32_16x16x32_bf16 v[164:167],v[212:215],a[60:63],v[164:167]
ds_read_b64_tr_b16 v[196:197],v224 offset:8192
ds_read_b64_tr_b16 v[198:199],v224 offset:8448
v_mfma_f32_16x16x32_bf16 v[168:171],v[208:211],a[112:115],0
ds_read_b64_tr_b16 v[200:201],v225
v_mul_f32 v239,v239,v66
v_mfma_f32_16x16x32_bf16 v[168:171],v[212:215],a[116:119],v[168:171]
ds_read_b64_tr_b16 v[202:203],v225 offset:256
v_mul_f32 v82,v66,v82
v_mfma_f32_16x16x32_bf16 v[172:175],v[208:211],a[168:171],0
buffer_load_dwordx4 v230,s[60:63],s95 offen offset:1024 lds
v_mfma_f32_16x16x32_bf16 v[172:175],v[212:215],a[172:175],v[172:175]
ds_read_b64_tr_b16 v[204:205],v225 offset:8192
ds_read_b64_tr_b16 v[206:207],v225 offset:8448
v_mfma_f32_16x16x32_bf16 v[176:179],v[216:219],a[0:3],0
v_exp_f32 v239,v239
v_mfma_f32_16x16x32_bf16 v[176:179],v[220:223],a[4:7],v[176:179]
v_add_f32 v239,1.0,v239
v_mul_f32 v240,v67,v67
v_mfma_f32_16x16x32_bf16 v[180:183],v[216:219],a[56:59],0
buffer_load_dwordx4 v231,s[64:67],s95 offen offset:2048 lds
v_mfma_f32_16x16x32_bf16 v[180:183],v[220:223],a[60:63],v[180:183]
v_rcp_f32 v239,v239
v_mfma_f32_16x16x32_bf16 v[184:187],v[216:219],a[112:115],0
v_mul_f32 v82,v82,v239
v_fma_f32 v240,v240,s100,v234
v_mfma_f32_16x16x32_bf16 v[184:187],v[220:223],a[116:119],v[184:187]
v_mul_f32 v240,v240,v67
v_mul_f32 v83,v67,v83
v_mfma_f32_16x16x32_bf16 v[188:191],v[216:219],a[168:171],0
buffer_load_dwordx4 v231,s[68:71],s95 offen offset:3072 lds
v_mfma_f32_16x16x32_bf16 v[188:191],v[220:223],a[172:175],v[188:191]
s_waitcnt lgkmcnt(0)
v_exp_f32 v240,v240
)"
R"(v_mfma_f32_16x16x32_bf16 v[128:131],v[192:195],a[8:11],v[128:131]
s_add_u32 m0,s92,0x14000
ds_read_b64_tr_b16 v[208:209],v224 offset:1024
s_mov_b32 s95,0xe0080
v_mfma_f32_16x16x32_bf16 v[128:131],v[196:199],a[12:15],v[128:131]
ds_read_b64_tr_b16 v[210:211],v224 offset:1280
v_add_f32 v240,1.0,v240
v_mfma_f32_16x16x32_bf16 v[132:135],v[192:195],a[64:67],v[132:135]
buffer_load_dwordx4 v230,s[56:59],s95 offen lds
v_mfma_f32_16x16x32_bf16 v[132:135],v[196:199],a[68:71],v[132:135]
ds_read_b64_tr_b16 v[212:213],v224 offset:9216
ds_read_b64_tr_b16 v[214:215],v224 offset:9472
v_mfma_f32_16x16x32_bf16 v[136:139],v[192:195],a[120:123],v[136:139]
ds_read_b64_tr_b16 v[216:217],v225 offset:1024
v_cvt_pk_bf16_f32 v80,v80,v81
v_mfma_f32_16x16x32_bf16 v[136:139],v[196:199],a[124:127],v[136:139]
ds_read_b64_tr_b16 v[218:219],v225 offset:1280
v_mul_f32 v241,v68,v68
v_mfma_f32_16x16x32_bf16 v[140:143],v[192:195],a[176:179],v[140:143]
buffer_load_dwordx4 v230,s[60:63],s95 offen offset:1024 lds
v_mfma_f32_16x16x32_bf16 v[140:143],v[196:199],a[180:183],v[140:143]
ds_read_b64_tr_b16 v[220:221],v225 offset:9216
ds_read_b64_tr_b16 v[222:223],v225 offset:9472
v_mfma_f32_16x16x32_bf16 v[144:147],v[200:203],a[8:11],v[144:147]
v_rcp_f32 v240,v240
v_mfma_f32_16x16x32_bf16 v[144:147],v[204:207],a[12:15],v[144:147]
v_mul_f32 v83,v83,v240
v_cvt_pk_bf16_f32 v81,v82,v83
v_mfma_f32_16x16x32_bf16 v[148:151],v[200:203],a[64:67],v[148:151]
buffer_load_dwordx4 v231,s[64:67],s95 offen offset:2048 lds
v_mfma_f32_16x16x32_bf16 v[148:151],v[204:207],a[68:71],v[148:151]
v_fma_f32 v241,v241,s100,v234
v_mul_f32 v241,v241,v68
v_mfma_f32_16x16x32_bf16 v[152:155],v[200:203],a[120:123],v[152:155]
v_exp_f32 v241,v241
v_mfma_f32_16x16x32_bf16 v[152:155],v[204:207],a[124:127],v[152:155]
v_mul_f32 v84,v68,v84
v_add_f32 v241,1.0,v241
v_mfma_f32_16x16x32_bf16 v[156:159],v[200:203],a[176:179],v[156:159]
buffer_load_dwordx4 v231,s[68:71],s95 offen offset:3072 lds
v_mfma_f32_16x16x32_bf16 v[156:159],v[204:207],a[180:183],v[156:159]
s_waitcnt vmcnt(16) lgkmcnt(0)
v_rcp_f32 v241,v241
v_mfma_f32_16x16x32_bf16 v[160:163],v[208:211],a[8:11],v[160:163]
s_barrier
ds_read_b64_tr_b16 v[192:193],v224 offset:32768
s_add_u32 m0,s92,0x0
v_mfma_f32_16x16x32_bf16 v[160:163],v[212:215],a[12:15],v[160:163]
s_mov_b32 s95,0x118000
ds_read_b64_tr_b16 v[194:195],v224 offset:33024
v_mul_f32 v84,v84,v241
v_mfma_f32_16x16x32_bf16 v[164:167],v[208:211],a[64:67],v[164:167]
buffer_load_dwordx4 v230,s[56:59],s95 offen lds
v_mfma_f32_16x16x32_bf16 v[164:167],v[212:215],a[68:71],v[164:167]
ds_read_b64_tr_b16 v[196:197],v224 offset:40960
ds_read_b64_tr_b16 v[198:199],v224 offset:41216
v_mfma_f32_16x16x32_bf16 v[168:171],v[208:211],a[120:123],v[168:171]
ds_read_b64_tr_b16 v[200:201],v225 offset:32768
v_mul_f32 v242,v69,v69
v_mfma_f32_16x16x32_bf16 v[168:171],v[212:215],a[124:127],v[168:171]
ds_read_b64_tr_b16 v[202:203],v225 offset:33024
v_fma_f32 v242,v242,s100,v234
v_mfma_f32_16x16x32_bf16 v[172:175],v[208:211],a[176:179],v[172:175]
buffer_load_dwordx4 v230,s[60:63],s95 offen offset:1024 lds
v_mfma_f32_16x16x32_bf16 v[172:175],v[212:215],a[180:183],v[172:175]
ds_read_b64_tr_b16 v[204:205],v225 offset:40960
ds_read_b64_tr_b16 v[206:207],v225 offset:41216
v_mfma_f32_16x16x32_bf16 v[176:179],v[216:219],a[8:11],v[176:179]
v_mul_f32 v242,v242,v69
v_mul_f32 v85,v69,v85
v_mfma_f32_16x16x32_bf16 v[176:179],v[220:223],a[12:15],v[176:179]
v_exp_f32 v242,v242
v_mfma_f32_16x16x32_bf16 v[180:183],v[216:219],a[64:67],v[180:183]
buffer_load_dwordx4 v231,s[64:67],s95 offen offset:2048 lds
v_mfma_f32_16x16x32_bf16 v[180:183],v[220:223],a[68:71],v[180:183]
v_add_f32 v242,1.0,v242
v_mul_f32 v243,v70,v70
v_mfma_f32_16x16x32_bf16 v[184:187],v[216:219],a[120:123],v[184:187]
v_rcp_f32 v242,v242
v_mfma_f32_16x16x32_bf16 v[184:187],v[220:223],a[124:127],v[184:187]
v_mul_f32 v85,v85,v242
v_fma_f32 v243,v243,s100,v234
v_mfma_f32_16x16x32_bf16 v[188:191],v[216:219],a[176:179],v[188:191]
buffer_load_dwordx4 v231,s[68:71],s95 offen offset:3072 lds
v_mfma_f32_16x16x32_bf16 v[188:191],v[220:223],a[180:183],v[188:191]
s_waitcnt lgkmcnt(0)
v_mul_f32 v243,v243,v70
v_mul_f32 v86,v70,v86
v_mfma_f32_16x16x32_bf16 v[128:131],v[192:195],a[16:19],v[128:131]
s_add_u32 m0,s92,0x4000
ds_read_b64_tr_b16 v[208:209],v224 offset:33792
s_mov_b32 s95,0x118080
v_mfma_f32_16x16x32_bf16 v[128:131],v[196:199],a[20:23],v[128:131]
ds_read_b64_tr_b16 v[210:211],v224 offset:34048
v_mul_f32 v244,v71,v71
v_mfma_f32_16x16x32_bf16 v[132:135],v[192:195],a[72:75],v[132:135]
buffer_load_dwordx4 v230,s[56:59],s95 offen lds
v_mfma_f32_16x16x32_bf16 v[132:135],v[196:199],a[76:79],v[132:135]
ds_read_b64_tr_b16 v[212:213],v224 offset:41984
ds_read_b64_tr_b16 v[214:215],v224 offset:42240
v_mfma_f32_16x16x32_bf16 v[136:139],v[192:195],a[128:131],v[136:139]
ds_read_b64_tr_b16 v[216:217],v225 offset:33792
v_fma_f32 v244,v244,s100,v234
v_mfma_f32_16x16x32_bf16 v[136:139],v[196:199],a[132:135],v[136:139]
ds_read_b64_tr_b16 v[218:219],v225 offset:34048
v_mul_f32 v244,v244,v71
v_mfma_f32_16x16x32_bf16 v[140:143],v[192:195],a[184:187],v[140:143]
buffer_load_dwordx4 v230,s[60:63],s95 offen offset:1024 lds
v_mfma_f32_16x16x32_bf16 v[140:143],v[196:199],a[188:191],v[140:143]
ds_read_b64_tr_b16 v[220:221],v225 offset:41984
ds_read_b64_tr_b16 v[222:223],v225 offset:42240
v_mfma_f32_16x16x32_bf16 v[144:147],v[200:203],a[16:19],v[144:147]
v_exp_f32 v243,v243
v_mfma_f32_16x16x32_bf16 v[144:147],v[204:207],a[20:23],v[144:147]
v_add_f32 v243,1.0,v243
v_mul_f32 v87,v71,v87
v_mfma_f32_16x16x32_bf16 v[148:151],v[200:203],a[72:75],v[148:151]
buffer_load_dwordx4 v231,s[64:67],s95 offen offset:2048 lds
v_mfma_f32_16x16x32_bf16 v[148:151],v[204:207],a[76:79],v[148:151]
v_rcp_f32 v243,v243
v_mfma_f32_16x16x32_bf16 v[152:155],v[200:203],a[128:131],v[152:155]
v_mul_f32 v86,v86,v243
v_cvt_pk_bf16_f32 v82,v84,v85
v_mfma_f32_16x16x32_bf16 v[152:155],v[204:207],a[132:135],v[152:155]
v_exp_f32 v244,v244
v_mfma_f32_16x16x32_bf16 v[156:159],v[200:203],a[184:187],v[156:159]
buffer_load_dwordx4 v231,s[68:71],s95 offen offset:3072 lds
v_mfma_f32_16x16x32_bf16 v[156:159],v[204:207],a[188:191],v[156:159]
s_waitcnt vmcnt(16) lgkmcnt(0)
v_add_f32 v244,1.0,v244
v_mul_f32 v245,v72,v72
v_mfma_f32_16x16x32_bf16 v[160:163],v[208:211],a[16:19],v[160:163]
s_barrier
ds_read_b64_tr_b16 v[192:193],v227 offset:32768
s_add_u32 m0,s92,0x8000
v_mfma_f32_16x16x32_bf16 v[160:163],v[212:215],a[20:23],v[160:163]
s_mov_b32 s95,0x150000
ds_read_b64_tr_b16 v[194:195],v227 offset:33024
v_fma_f32 v245,v245,s100,v234
v_mfma_f32_16x16x32_bf16 v[164:167],v[208:211],a[72:75],v[164:167]
buffer_load_dwordx4 v230,s[56:59],s95 offen lds
v_mfma_f32_16x16x32_bf16 v[164:167],v[212:215],a[76:79],v[164:167]
ds_read_b64_tr_b16 v[196:197],v227 offset:40960
ds_read_b64_tr_b16 v[198:199],v227 offset:41216
v_mfma_f32_16x16x32_bf16 v[168:171],v[208:211],a[128:131],v[168:171]
ds_read_b64_tr_b16 v[200:201],v228 offset:32768
v_mul_f32 v245,v245,v72
v_mfma_f32_16x16x32_bf16 v[168:171],v[212:215],a[132:135],v[168:171]
ds_read_b64_tr_b16 v[202:203],v228 offset:33024
v_mul_f32 v88,v72,v88
v_mfma_f32_16x16x32_bf16 v[172:175],v[208:211],a[184:187],v[172:175]
buffer_load_dwordx4 v230,s[60:63],s95 offen offset:1024 lds
v_mfma_f32_16x16x32_bf16 v[172:175],v[212:215],a[188:191],v[172:175]
ds_read_b64_tr_b16 v[204:205],v228 offset:40960
ds_read_b64_tr_b16 v[206:207],v228 offset:41216
v_mfma_f32_16x16x32_bf16 v[176:179],v[216:219],a[16:19],v[176:179]
v_rcp_f32 v244,v244
v_mfma_f32_16x16x32_bf16 v[176:179],v[220:223],a[20:23],v[176:179]
v_mul_f32 v87,v87,v244
v_cvt_pk_bf16_f32 v83,v86,v87
v_mfma_f32_16x16x32_bf16 v[180:183],v[216:219],a[72:75],v[180:183]
buffer_load_dwordx4 v231,s[64:67],s95 offen offset:2048 lds
v_mfma_f32_16x16x32_bf16 v[180:183],v[220:223],a[76:79],v[180:183]
v_exp_f32 v245,v245
v_mfma_f32_16x16x32_bf16 v[184:187],v[216:219],a[128:131],v[184:187]
v_add_f32 v245,1.0,v245
v_mul_f32 v246,v73,v73
v_mfma_f32_16x16x32_bf16 v[184:187],v[220:223],a[132:135],v[184:187]
v_rcp_f32 v245,v245
v_mfma_f32_16x16x32_bf16 v[188:191],v[216:219],a[184:187],v[188:191]
buffer_load_dwordx4 v231,s[68:71],s95 offen offset:3072 lds
v_mfma_f32_16x16x32_bf16 v[188:191],v[220:223],a[188:191],v[188:191]
s_waitcnt lgkmcnt(0)
v_mul_f32 v88,v88,v245
v_fma_f32 v246,v246,s100,v234
v_mfma_f32_16x16x32_bf16 v[128:131],v[192:195],a[24:27],v[128:131]
s_add_u32 m0,s92,0xc000
ds_read_b64_tr_b16 v[208:209],v227 offset:33792
s_mov_b32 s95,0x150080
v_mfma_f32_16x16x32_bf16 v[128:131],v[196:199],a[28:31],v[128:131]
ds_read_b64_tr_b16 v[210:211],v227 offset:34048
v_mul_f32 v246,v246,v73
v_mfma_f32_16x16x32_bf16 v[132:135],v[192:195],a[80:83],v[132:135]
buffer_load_dwordx4 v230,s[56:59],s95 offen lds
v_mfma_f32_16x16x32_bf16 v[132:135],v[196:199],a[84:87],v[132:135]
s_mov_b64 s[56:57],%[wo]
ds_read_b64_tr_b16 v[212:213],v227 offset:41984
ds_read_b64_tr_b16 v[214:215],v227 offset:42240
v_mfma_f32_16x16x32_bf16 v[136:139],v[192:195],a[136:139],v[136:139]
s_mov_b32 s58,458752
ds_read_b64_tr_b16 v[216:217],v228 offset:33792
v_mul_f32 v89,v73,v89
v_mfma_f32_16x16x32_bf16 v[136:139],v[196:199],a[140:143],v[136:139]
ds_read_b64_tr_b16 v[218:219],v228 offset:34048
v_mul_f32 v247,v74,v74
v_mfma_f32_16x16x32_bf16 v[140:143],v[192:195],a[192:195],v[140:143]
buffer_load_dwordx4 v230,s[60:63],s95 offen offset:1024 lds
v_mfma_f32_16x16x32_bf16 v[140:143],v[196:199],a[196:199],v[140:143]
s_mov_b64 s[60:61],%[wd]
ds_read_b64_tr_b16 v[220:221],v228 offset:41984
)"
R"(ds_read_b64_tr_b16 v[222:223],v228 offset:42240
v_mfma_f32_16x16x32_bf16 v[144:147],v[200:203],a[24:27],v[144:147]
s_mov_b32 s62,401408
v_exp_f32 v246,v246
v_mfma_f32_16x16x32_bf16 v[144:147],v[204:207],a[28:31],v[144:147]
v_add_f32 v246,1.0,v246
v_fma_f32 v247,v247,s100,v234
v_mfma_f32_16x16x32_bf16 v[148:151],v[200:203],a[80:83],v[148:151]
buffer_load_dwordx4 v231,s[64:67],s95 offen offset:2048 lds
v_mfma_f32_16x16x32_bf16 v[148:151],v[204:207],a[84:87],v[148:151]
s_and_b32 s64,%[tb],124
v_rcp_f32 v246,v246
v_mfma_f32_16x16x32_bf16 v[152:155],v[200:203],a[136:139],v[152:155]
s_add_u32 s64,s64,%[w]
v_mul_f32 v89,v89,v246
v_mul_f32 v247,v247,v74
v_mfma_f32_16x16x32_bf16 v[152:155],v[204:207],a[140:143],v[152:155]
s_lshl_b32 s64,s64,10
v_exp_f32 v247,v247
v_mfma_f32_16x16x32_bf16 v[156:159],v[200:203],a[192:195],v[156:159]
s_lshr_b32 s66,s64,1
buffer_load_dwordx4 v231,s[68:71],s95 offen offset:3072 lds
v_mfma_f32_16x16x32_bf16 v[156:159],v[204:207],a[196:199],v[156:159]
s_waitcnt vmcnt(16) lgkmcnt(0)
v_lshl_add_u32 v226,v226,3,s66
v_mul_f32 v90,v74,v90
v_mfma_f32_16x16x32_bf16 v[160:163],v[208:211],a[24:27],v[160:163]
s_barrier
ds_read_b64_tr_b16 v[192:193],v227
s_add_u32 m0,s92,0x18000
v_mfma_f32_16x16x32_bf16 v[160:163],v[212:215],a[28:31],v[160:163]
s_mov_b32 s95,0x0
ds_read_b64_tr_b16 v[194:195],v227 offset:256
v_add_f32 v247,1.0,v247
v_mfma_f32_16x16x32_bf16 v[164:167],v[208:211],a[80:83],v[164:167]
s_mul_i32 s64,s64,7
buffer_load_dwordx4 v232,s[72:75],s95 offen lds
v_mfma_f32_16x16x32_bf16 v[164:167],v[212:215],a[84:87],v[164:167]
s_lshr_b32 s65,s64,1
ds_read_b64_tr_b16 v[196:197],v227 offset:8192
ds_read_b64_tr_b16 v[198:199],v227 offset:8448
v_mfma_f32_16x16x32_bf16 v[168:171],v[208:211],a[136:139],v[168:171]
s_lshr_b32 s64,s64,11
ds_read_b64_tr_b16 v[200:201],v228
v_mul_f32 v237,v75,v75
v_mfma_f32_16x16x32_bf16 v[168:171],v[212:215],a[140:143],v[168:171]
s_mul_i32 s64,s64,896
ds_read_b64_tr_b16 v[202:203],v228 offset:256
v_fma_f32 v237,v237,s100,v234
v_mfma_f32_16x16x32_bf16 v[172:175],v[208:211],a[192:195],v[172:175]
buffer_load_dwordx4 v232,s[76:79],s95 offen offset:1024 lds
v_mfma_f32_16x16x32_bf16 v[172:175],v[212:215],a[196:199],v[172:175]
ds_read_b64_tr_b16 v[204:205],v228 offset:8192
ds_read_b64_tr_b16 v[206:207],v228 offset:8448
v_mfma_f32_16x16x32_bf16 v[176:179],v[216:219],a[24:27],v[176:179]
v_rcp_f32 v247,v247
v_mfma_f32_16x16x32_bf16 v[176:179],v[220:223],a[28:31],v[176:179]
v_mul_f32 v90,v90,v247
v_mul_f32 v237,v237,v75
v_mfma_f32_16x16x32_bf16 v[180:183],v[216:219],a[80:83],v[180:183]
buffer_load_dwordx4 v233,s[80:83],s95 offen offset:2048 lds
v_mfma_f32_16x16x32_bf16 v[180:183],v[220:223],a[84:87],v[180:183]
v_exp_f32 v237,v237
v_mfma_f32_16x16x32_bf16 v[184:187],v[216:219],a[136:139],v[184:187]
v_mul_f32 v91,v75,v91
v_add_f32 v237,1.0,v237
v_mfma_f32_16x16x32_bf16 v[184:187],v[220:223],a[140:143],v[184:187]
v_rcp_f32 v237,v237
v_mfma_f32_16x16x32_bf16 v[188:191],v[216:219],a[192:195],v[188:191]
buffer_load_dwordx4 v233,s[84:87],s95 offen offset:3072 lds
v_mfma_f32_16x16x32_bf16 v[188:191],v[220:223],a[196:199],v[188:191]
s_waitcnt lgkmcnt(0)
v_mul_f32 v91,v91,v237
v_cvt_pk_bf16_f32 v88,v88,v89
v_mfma_f32_16x16x32_bf16 v[128:131],v[192:195],a[32:35],v[128:131]
s_add_u32 m0,s92,0x1c000
ds_read_b64_tr_b16 v[208:209],v227 offset:1024
s_mov_b32 s95,0x200
v_mfma_f32_16x16x32_bf16 v[128:131],v[196:199],a[36:39],v[128:131]
ds_read_b64_tr_b16 v[210:211],v227 offset:1280
v_cvt_pk_bf16_f32 v89,v90,v91
v_mfma_f32_16x16x32_bf16 v[132:135],v[192:195],a[88:91],v[132:135]
buffer_load_dwordx4 v232,s[72:75],s95 offen lds
v_mfma_f32_16x16x32_bf16 v[132:135],v[196:199],a[92:95],v[132:135]
ds_read_b64_tr_b16 v[212:213],v227 offset:9216
ds_read_b64_tr_b16 v[214:215],v227 offset:9472
v_mfma_f32_16x16x32_bf16 v[136:139],v[192:195],a[144:147],v[136:139]
ds_read_b64_tr_b16 v[216:217],v228 offset:1024
v_mul_f32 v238,v76,v76
v_mfma_f32_16x16x32_bf16 v[136:139],v[196:199],a[148:151],v[136:139]
ds_read_b64_tr_b16 v[218:219],v228 offset:1280
v_fma_f32 v238,v238,s100,v234
v_mfma_f32_16x16x32_bf16 v[140:143],v[192:195],a[200:203],v[140:143]
buffer_load_dwordx4 v232,s[76:79],s95 offen offset:1024 lds
v_mfma_f32_16x16x32_bf16 v[140:143],v[196:199],a[204:207],v[140:143]
ds_read_b64_tr_b16 v[220:221],v228 offset:9216
ds_read_b64_tr_b16 v[222:223],v228 offset:9472
v_mfma_f32_16x16x32_bf16 v[144:147],v[200:203],a[32:35],v[144:147]
v_mul_f32 v238,v238,v76
v_mul_f32 v92,v76,v92
v_mfma_f32_16x16x32_bf16 v[144:147],v[204:207],a[36:39],v[144:147]
v_exp_f32 v238,v238
v_mfma_f32_16x16x32_bf16 v[148:151],v[200:203],a[88:91],v[148:151]
buffer_load_dwordx4 v233,s[80:83],s95 offen offset:2048 lds
v_mfma_f32_16x16x32_bf16 v[148:151],v[204:207],a[92:95],v[148:151]
v_add_f32 v238,1.0,v238
v_mul_f32 v239,v77,v77
v_mfma_f32_16x16x32_bf16 v[152:155],v[200:203],a[144:147],v[152:155]
v_rcp_f32 v238,v238
v_mfma_f32_16x16x32_bf16 v[152:155],v[204:207],a[148:151],v[152:155]
v_mul_f32 v92,v92,v238
v_fma_f32 v239,v239,s100,v234
v_mfma_f32_16x16x32_bf16 v[156:159],v[200:203],a[200:203],v[156:159]
buffer_load_dwordx4 v233,s[84:87],s95 offen offset:3072 lds
v_mfma_f32_16x16x32_bf16 v[156:159],v[204:207],a[204:207],v[156:159]
s_waitcnt vmcnt(16) lgkmcnt(0)
v_mul_f32 v239,v239,v77
v_mul_f32 v93,v77,v93
v_mfma_f32_16x16x32_bf16 v[160:163],v[208:211],a[32:35],v[160:163]
s_barrier
ds_read_b64_tr_b16 v[192:193],v224
s_add_u32 m0,s92,0x10000
v_mfma_f32_16x16x32_bf16 v[160:163],v[212:215],a[36:39],v[160:163]
s_mov_b32 s95,0x80000
ds_read_b64_tr_b16 v[194:195],v224 offset:256
v_mul_f32 v240,v78,v78
v_mfma_f32_16x16x32_bf16 v[164:167],v[208:211],a[88:91],v[164:167]
buffer_load_dwordx4 v232,s[72:75],s95 offen lds
v_mfma_f32_16x16x32_bf16 v[164:167],v[212:215],a[92:95],v[164:167]
ds_read_b64_tr_b16 v[196:197],v224 offset:8192
ds_read_b64_tr_b16 v[198:199],v224 offset:8448
v_mfma_f32_16x16x32_bf16 v[168:171],v[208:211],a[144:147],v[168:171]
ds_read_b64_tr_b16 v[200:201],v225
v_fma_f32 v240,v240,s100,v234
v_mfma_f32_16x16x32_bf16 v[168:171],v[212:215],a[148:151],v[168:171]
ds_read_b64_tr_b16 v[202:203],v225 offset:256
v_mul_f32 v240,v240,v78
v_mfma_f32_16x16x32_bf16 v[172:175],v[208:211],a[200:203],v[172:175]
buffer_load_dwordx4 v232,s[76:79],s95 offen offset:1024 lds
v_mfma_f32_16x16x32_bf16 v[172:175],v[212:215],a[204:207],v[172:175]
ds_read_b64_tr_b16 v[204:205],v225 offset:8192
ds_read_b64_tr_b16 v[206:207],v225 offset:8448
v_mfma_f32_16x16x32_bf16 v[176:179],v[216:219],a[32:35],v[176:179]
v_exp_f32 v239,v239
v_mfma_f32_16x16x32_bf16 v[176:179],v[220:223],a[36:39],v[176:179]
v_add_f32 v239,1.0,v239
v_mul_f32 v94,v78,v94
v_mfma_f32_16x16x32_bf16 v[180:183],v[216:219],a[88:91],v[180:183]
buffer_load_dwordx4 v233,s[80:83],s95 offen offset:2048 lds
v_mfma_f32_16x16x32_bf16 v[180:183],v[220:223],a[92:95],v[180:183]
v_rcp_f32 v239,v239
v_mfma_f32_16x16x32_bf16 v[184:187],v[216:219],a[144:147],v[184:187]
v_mul_f32 v93,v93,v239
v_mul_f32 v241,v79,v79
v_mfma_f32_16x16x32_bf16 v[184:187],v[220:223],a[148:151],v[184:187]
v_exp_f32 v240,v240
v_mfma_f32_16x16x32_bf16 v[188:191],v[216:219],a[200:203],v[188:191]
buffer_load_dwordx4 v233,s[84:87],s95 offen offset:3072 lds
v_mfma_f32_16x16x32_bf16 v[188:191],v[220:223],a[204:207],v[188:191]
s_waitcnt lgkmcnt(0)
v_add_f32 v240,1.0,v240
v_fma_f32 v241,v241,s100,v234
v_mfma_f32_16x16x32_bf16 v[128:131],v[192:195],a[40:43],v[128:131]
s_add_u32 m0,s92,0x14000
ds_read_b64_tr_b16 v[208:209],v224 offset:1024
s_mov_b32 s95,0x80200
v_mfma_f32_16x16x32_bf16 v[128:131],v[196:199],a[44:47],v[128:131]
ds_read_b64_tr_b16 v[210:211],v224 offset:1280
v_mul_f32 v241,v241,v79
v_mfma_f32_16x16x32_bf16 v[132:135],v[192:195],a[96:99],v[132:135]
buffer_load_dwordx4 v232,s[72:75],s95 offen lds
v_mfma_f32_16x16x32_bf16 v[132:135],v[196:199],a[100:103],v[132:135]
ds_read_b64_tr_b16 v[212:213],v224 offset:9216
ds_read_b64_tr_b16 v[214:215],v224 offset:9472
v_mfma_f32_16x16x32_bf16 v[136:139],v[192:195],a[152:155],v[136:139]
ds_read_b64_tr_b16 v[216:217],v225 offset:1024
v_mul_f32 v95,v79,v95
v_mfma_f32_16x16x32_bf16 v[136:139],v[196:199],a[156:159],v[136:139]
ds_read_b64_tr_b16 v[218:219],v225 offset:1280
v_cvt_pk_bf16_f32 v90,v92,v93
v_mfma_f32_16x16x32_bf16 v[140:143],v[192:195],a[208:211],v[140:143]
buffer_load_dwordx4 v232,s[76:79],s95 offen offset:1024 lds
v_mfma_f32_16x16x32_bf16 v[140:143],v[196:199],a[212:215],v[140:143]
ds_read_b64_tr_b16 v[220:221],v225 offset:9216
ds_read_b64_tr_b16 v[222:223],v225 offset:9472
v_mfma_f32_16x16x32_bf16 v[144:147],v[200:203],a[40:43],v[144:147]
v_rcp_f32 v240,v240
v_mfma_f32_16x16x32_bf16 v[144:147],v[204:207],a[44:47],v[144:147]
v_mul_f32 v94,v94,v240
v_bfe_u32 v231,v254,7,2
v_mfma_f32_16x16x32_bf16 v[148:151],v[200:203],a[96:99],v[148:151]
buffer_load_dwordx4 v233,s[80:83],s95 offen offset:2048 lds
v_mfma_f32_16x16x32_bf16 v[148:151],v[204:207],a[100:103],v[148:151]
v_exp_f32 v241,v241
v_mfma_f32_16x16x32_bf16 v[152:155],v[200:203],a[152:155],v[152:155]
v_add_f32 v241,1.0,v241
v_mul_u32_u24 v231,896,v231
v_mfma_f32_16x16x32_bf16 v[152:155],v[204:207],a[156:159],v[152:155]
v_rcp_f32 v241,v241
v_mfma_f32_16x16x32_bf16 v[156:159],v[200:203],a[208:211],v[156:159]
)"
R"(buffer_load_dwordx4 v233,s[84:87],s95 offen offset:3072 lds
v_mfma_f32_16x16x32_bf16 v[156:159],v[204:207],a[212:215],v[156:159]
s_waitcnt vmcnt(16) lgkmcnt(0)
v_mul_f32 v95,v95,v241
v_cvt_pk_bf16_f32 v91,v94,v95
v_mfma_f32_16x16x32_bf16 v[160:163],v[208:211],a[40:43],v[160:163]
s_barrier
ds_read_b64_tr_b16 v[192:193],v224 offset:32768
s_add_u32 m0,s92,0x0
v_mfma_f32_16x16x32_bf16 v[160:163],v[212:215],a[44:47],v[160:163]
s_mov_b32 s95,0x100000
ds_read_b64_tr_b16 v[194:195],v224 offset:33024
v_and_b32 v230,112,v254
v_mfma_f32_16x16x32_bf16 v[164:167],v[208:211],a[96:99],v[164:167]
buffer_load_dwordx4 v232,s[72:75],s95 offen lds
v_mfma_f32_16x16x32_bf16 v[164:167],v[212:215],a[100:103],v[164:167]
ds_read_b64_tr_b16 v[196:197],v224 offset:40960
ds_read_b64_tr_b16 v[198:199],v224 offset:41216
v_mfma_f32_16x16x32_bf16 v[168:171],v[208:211],a[152:155],v[168:171]
ds_read_b64_tr_b16 v[200:201],v225 offset:32768
v_lshl_add_u32 v231,v230,2,v231
v_mfma_f32_16x16x32_bf16 v[168:171],v[212:215],a[156:159],v[168:171]
ds_read_b64_tr_b16 v[202:203],v225 offset:33024
v_and_b32 v230,496,v254
v_mfma_f32_16x16x32_bf16 v[172:175],v[208:211],a[208:211],v[172:175]
buffer_load_dwordx4 v232,s[76:79],s95 offen offset:1024 lds
v_mfma_f32_16x16x32_bf16 v[172:175],v[212:215],a[212:215],v[172:175]
ds_read_b64_tr_b16 v[204:205],v225 offset:40960
ds_read_b64_tr_b16 v[206:207],v225 offset:41216
v_mfma_f32_16x16x32_bf16 v[176:179],v[216:219],a[40:43],v[176:179]
v_lshl_add_u32 v230,v230,3,s65
v_min_u32 v230,458624,v230
v_mfma_f32_16x16x32_bf16 v[176:179],v[220:223],a[44:47],v[176:179]
v_add_u32 v231,s64,v231
v_mfma_f32_16x16x32_bf16 v[180:183],v[216:219],a[96:99],v[180:183]
s_and_b32 s64,%[h],2
buffer_load_dwordx4 v233,s[80:83],s95 offen offset:2048 lds
v_mfma_f32_16x16x32_bf16 v[180:183],v[220:223],a[100:103],v[180:183]
s_mul_i32 s64,s64,192
v_add_u32 v231,s64,v231
v_mfma_f32_16x16x32_bf16 v[184:187],v[216:219],a[152:155],v[184:187]
v_mfma_f32_16x16x32_bf16 v[184:187],v[220:223],a[156:159],v[184:187]
v_mfma_f32_16x16x32_bf16 v[188:191],v[216:219],a[208:211],v[188:191]
buffer_load_dwordx4 v233,s[84:87],s95 offen offset:3072 lds
v_mfma_f32_16x16x32_bf16 v[188:191],v[220:223],a[212:215],v[188:191]
s_waitcnt lgkmcnt(0)
v_mfma_f32_16x16x32_bf16 v[128:131],v[192:195],a[48:51],v[128:131]
s_add_u32 m0,s92,0x4000
ds_read_b64_tr_b16 v[208:209],v224 offset:33792
s_mov_b32 s95,0x100200
v_mfma_f32_16x16x32_bf16 v[128:131],v[196:199],a[52:55],v[128:131]
ds_read_b64_tr_b16 v[210:211],v224 offset:34048
v_mfma_f32_16x16x32_bf16 v[132:135],v[192:195],a[104:107],v[132:135]
buffer_load_dwordx4 v232,s[72:75],s95 offen lds
v_mfma_f32_16x16x32_bf16 v[132:135],v[196:199],a[108:111],v[132:135]
ds_read_b64_tr_b16 v[212:213],v224 offset:41984
ds_read_b64_tr_b16 v[214:215],v224 offset:42240
v_mfma_f32_16x16x32_bf16 v[136:139],v[192:195],a[160:163],v[136:139]
buffer_load_dwordx4 v232,s[76:79],s95 offen offset:1024 lds
v_mfma_f32_16x16x32_bf16 v[136:139],v[196:199],a[164:167],v[136:139]
ds_read_b64_tr_b16 v[216:217],v225 offset:33792
ds_read_b64_tr_b16 v[218:219],v225 offset:34048
v_mfma_f32_16x16x32_bf16 v[140:143],v[192:195],a[216:219],v[140:143]
ds_read_b64_tr_b16 v[220:221],v225 offset:41984
v_mul_f32 v242,v128,v128
v_mfma_f32_16x16x32_bf16 v[140:143],v[196:199],a[220:223],v[140:143]
buffer_load_dwordx4 v233,s[80:83],s95 offen offset:2048 lds
v_mfma_f32_16x16x32_bf16 v[144:147],v[200:203],a[48:51],v[144:147]
ds_read_b64_tr_b16 v[222:223],v225 offset:42240
v_fma_f32 v242,v242,s100,v234
v_mfma_f32_16x16x32_bf16 v[144:147],v[204:207],a[52:55],v[144:147]
v_mul_f32 v242,v242,v128
v_mul_f32 v243,v129,v129
v_mfma_f32_16x16x32_bf16 v[148:151],v[200:203],a[104:107],v[148:151]
buffer_load_dwordx4 v233,s[84:87],s95 offen offset:3072 lds
v_mfma_f32_16x16x32_bf16 v[148:151],v[204:207],a[108:111],v[148:151]
v_fma_f32 v243,v243,s100,v234
v_mul_f32 v243,v243,v129
v_mfma_f32_16x16x32_bf16 v[152:155],v[200:203],a[160:163],v[152:155]
buffer_load_dword v229,v226,s[24:27],0 offen
v_mfma_f32_16x16x32_bf16 v[152:155],v[204:207],a[164:167],v[152:155]
v_mul_f32 v244,v130,v130
v_fma_f32 v244,v244,s100,v234
v_mfma_f32_16x16x32_bf16 v[156:159],v[200:203],a[216:219],v[156:159]
buffer_load_dword v226,v226,s[40:43],0 offen
v_mfma_f32_16x16x32_bf16 v[156:159],v[204:207],a[220:223],v[156:159]
s_waitcnt vmcnt(18) lgkmcnt(0)
v_mul_f32 v244,v244,v130
v_mul_f32 v245,v131,v131
v_mfma_f32_16x16x32_bf16 v[160:163],v[208:211],a[48:51],v[160:163]
s_barrier
ds_read_b64_tr_b16 v[192:193],v227 offset:32768
s_add_u32 m0,s92,0x8000
v_mfma_f32_16x16x32_bf16 v[160:163],v[212:215],a[52:55],v[160:163]
s_mov_b32 s95,0x180000
ds_read_b64_tr_b16 v[194:195],v227 offset:33024
v_fma_f32 v245,v245,s100,v234
v_mfma_f32_16x16x32_bf16 v[164:167],v[208:211],a[104:107],v[164:167]
buffer_load_dwordx4 v232,s[72:75],s95 offen lds
v_mfma_f32_16x16x32_bf16 v[164:167],v[212:215],a[108:111],v[164:167]
ds_read_b64_tr_b16 v[196:197],v227 offset:40960
ds_read_b64_tr_b16 v[198:199],v227 offset:41216
v_mfma_f32_16x16x32_bf16 v[168:171],v[208:211],a[160:163],v[168:171]
ds_read_b64_tr_b16 v[200:201],v228 offset:32768
v_mul_f32 v160,v128,v160
v_mfma_f32_16x16x32_bf16 v[168:171],v[212:215],a[164:167],v[168:171]
ds_read_b64_tr_b16 v[202:203],v228 offset:33024
v_mul_f32 v161,v129,v161
v_mfma_f32_16x16x32_bf16 v[172:175],v[208:211],a[216:219],v[172:175]
buffer_load_dwordx4 v232,s[76:79],s95 offen offset:1024 lds
v_mfma_f32_16x16x32_bf16 v[172:175],v[212:215],a[220:223],v[172:175]
ds_read_b64_tr_b16 v[204:205],v228 offset:40960
ds_read_b64_tr_b16 v[206:207],v228 offset:41216
v_mfma_f32_16x16x32_bf16 v[176:179],v[216:219],a[48:51],v[176:179]
v_mul_f32 v162,v130,v162
v_mul_f32 v245,v245,v131
v_mfma_f32_16x16x32_bf16 v[176:179],v[220:223],a[52:55],v[176:179]
v_mul_f32 v163,v131,v163
v_mul_f32 v239,v136,v136
v_mfma_f32_16x16x32_bf16 v[180:183],v[216:219],a[104:107],v[180:183]
buffer_load_dwordx4 v233,s[80:83],s95 offen offset:2048 lds
v_mfma_f32_16x16x32_bf16 v[180:183],v[220:223],a[108:111],v[180:183]
v_exp_f32 v242,v242
v_mfma_f32_16x16x32_bf16 v[184:187],v[216:219],a[160:163],v[184:187]
v_add_f32 v242,1.0,v242
v_fma_f32 v239,v239,s100,v234
v_mfma_f32_16x16x32_bf16 v[184:187],v[220:223],a[164:167],v[184:187]
v_rcp_f32 v242,v242
v_mfma_f32_16x16x32_bf16 v[188:191],v[216:219],a[216:219],v[188:191]
buffer_load_dwordx4 v233,s[84:87],s95 offen offset:3072 lds
v_mfma_f32_16x16x32_bf16 v[188:191],v[220:223],a[220:223],v[188:191]
s_waitcnt lgkmcnt(0)
v_mul_f32 v160,v160,v242
v_mul_f32 v239,v239,v136
v_mfma_f32_16x16x32_bf16 v[0:3],v[192:195],a[0:3],0
s_add_u32 m0,s92,0xc000
ds_read_b64_tr_b16 v[208:209],v227 offset:33792
s_mov_b32 s95,0x180200
v_mfma_f32_16x16x32_bf16 v[0:3],v[196:199],a[4:7],v[0:3]
ds_read_b64_tr_b16 v[210:211],v227 offset:34048
v_mul_f32 v168,v136,v168
v_mfma_f32_16x16x32_bf16 v[8:11],v[192:195],a[56:59],0
buffer_load_dwordx4 v232,s[72:75],s95 offen lds
v_mfma_f32_16x16x32_bf16 v[8:11],v[196:199],a[60:63],v[8:11]
ds_read_b64_tr_b16 v[212:213],v227 offset:41984
ds_read_b64_tr_b16 v[214:215],v227 offset:42240
v_mfma_f32_16x16x32_bf16 v[16:19],v[192:195],a[112:115],0
ds_read_b64_tr_b16 v[216:217],v228 offset:33792
v_mul_f32 v240,v137,v137
v_mfma_f32_16x16x32_bf16 v[16:19],v[196:199],a[116:119],v[16:19]
ds_read_b64_tr_b16 v[218:219],v228 offset:34048
v_fma_f32 v240,v240,s100,v234
v_mfma_f32_16x16x32_bf16 v[24:27],v[192:195],a[168:171],0
buffer_load_dwordx4 v232,s[76:79],s95 offen offset:1024 lds
v_mfma_f32_16x16x32_bf16 v[24:27],v[196:199],a[172:175],v[24:27]
ds_read_b64_tr_b16 v[220:221],v228 offset:41984
ds_read_b64_tr_b16 v[222:223],v228 offset:42240
v_mfma_f32_16x16x32_bf16 v[4:7],v[200:203],a[0:3],0
v_mul_f32 v240,v240,v137
v_mul_f32 v169,v137,v169
v_mfma_f32_16x16x32_bf16 v[4:7],v[204:207],a[4:7],v[4:7]
v_mul_f32 v241,v138,v138
v_fma_f32 v241,v241,s100,v234
v_mfma_f32_16x16x32_bf16 v[12:15],v[200:203],a[56:59],0
buffer_load_dwordx4 v233,s[80:83],s95 offen offset:2048 lds
v_mfma_f32_16x16x32_bf16 v[12:15],v[204:207],a[60:63],v[12:15]
v_mul_f32 v241,v241,v138
v_mul_f32 v170,v138,v170
v_mfma_f32_16x16x32_bf16 v[20:23],v[200:203],a[112:115],0
v_mul_f32 v242,v139,v139
v_fma_f32 v242,v242,s100,v234
v_mfma_f32_16x16x32_bf16 v[20:23],v[204:207],a[116:119],v[20:23]
v_mul_f32 v242,v242,v139
v_mul_f32 v171,v139,v171
v_mfma_f32_16x16x32_bf16 v[28:31],v[200:203],a[168:171],0
buffer_load_dwordx4 v233,s[84:87],s95 offen offset:3072 lds
v_mfma_f32_16x16x32_bf16 v[28:31],v[204:207],a[172:175],v[28:31]
s_waitcnt vmcnt(18) lgkmcnt(0)
v_mul_f32 v247,v133,v133
v_fma_f32 v247,v247,s100,v234
v_mfma_f32_16x16x32_bf16 v[32:35],v[208:211],a[0:3],0
s_barrier
ds_read_b64_tr_b16 v[192:193],v227
s_add_u32 m0,s92,0x18000
v_mfma_f32_16x16x32_bf16 v[32:35],v[212:215],a[4:7],v[32:35]
s_mov_b32 s95,0x200000
ds_read_b64_tr_b16 v[194:195],v227 offset:256
v_mul_f32 v247,v247,v133
v_mfma_f32_16x16x32_bf16 v[40:43],v[208:211],a[56:59],0
buffer_load_dwordx4 v232,s[72:75],s95 offen lds
v_mfma_f32_16x16x32_bf16 v[40:43],v[212:215],a[60:63],v[40:43]
ds_read_b64_tr_b16 v[196:197],v227 offset:8192
ds_read_b64_tr_b16 v[198:199],v227 offset:8448
v_mfma_f32_16x16x32_bf16 v[48:51],v[208:211],a[112:115],0
ds_read_b64_tr_b16 v[200:201],v228
v_mul_f32 v165,v133,v165
v_mfma_f32_16x16x32_bf16 v[48:51],v[212:215],a[116:119],v[48:51]
ds_read_b64_tr_b16 v[202:203],v228 offset:256
v_mul_f32 v237,v134,v134
v_mfma_f32_16x16x32_bf16 v[56:59],v[208:211],a[168:171],0
)"
R"(buffer_load_dwordx4 v232,s[76:79],s95 offen offset:1024 lds
v_mfma_f32_16x16x32_bf16 v[56:59],v[212:215],a[172:175],v[56:59]
ds_read_b64_tr_b16 v[204:205],v228 offset:8192
ds_read_b64_tr_b16 v[206:207],v228 offset:8448
v_mfma_f32_16x16x32_bf16 v[36:39],v[216:219],a[0:3],0
v_exp_f32 v247,v247
v_mfma_f32_16x16x32_bf16 v[36:39],v[220:223],a[4:7],v[36:39]
v_add_f32 v247,1.0,v247
v_fma_f32 v237,v237,s100,v234
v_mfma_f32_16x16x32_bf16 v[44:47],v[216:219],a[56:59],0
buffer_load_dwordx4 v233,s[80:83],s95 offen offset:2048 lds
v_mfma_f32_16x16x32_bf16 v[44:47],v[220:223],a[60:63],v[44:47]
v_rcp_f32 v247,v247
v_mfma_f32_16x16x32_bf16 v[52:55],v[216:219],a[112:115],0
v_mul_f32 v165,v165,v247
v_mul_f32 v237,v237,v134
v_mfma_f32_16x16x32_bf16 v[52:55],v[220:223],a[116:119],v[52:55]
v_exp_f32 v237,v237
v_mfma_f32_16x16x32_bf16 v[60:63],v[216:219],a[168:171],0
buffer_load_dwordx4 v233,s[84:87],s95 offen offset:3072 lds
v_mfma_f32_16x16x32_bf16 v[60:63],v[220:223],a[172:175],v[60:63]
s_waitcnt lgkmcnt(0)
v_mul_f32 v166,v134,v166
v_add_f32 v237,1.0,v237
v_mfma_f32_16x16x32_bf16 v[0:3],v[192:195],a[8:11],v[0:3]
s_add_u32 m0,s92,0x1c000
ds_read_b64_tr_b16 v[208:209],v227 offset:1024
s_mov_b32 s95,0x200200
v_mfma_f32_16x16x32_bf16 v[0:3],v[196:199],a[12:15],v[0:3]
ds_read_b64_tr_b16 v[210:211],v227 offset:1280
v_mul_f32 v238,v135,v135
v_mfma_f32_16x16x32_bf16 v[8:11],v[192:195],a[64:67],v[8:11]
buffer_load_dwordx4 v232,s[72:75],s95 offen lds
v_mfma_f32_16x16x32_bf16 v[8:11],v[196:199],a[68:71],v[8:11]
ds_read_b64_tr_b16 v[212:213],v227 offset:9216
ds_read_b64_tr_b16 v[214:215],v227 offset:9472
v_mfma_f32_16x16x32_bf16 v[16:19],v[192:195],a[120:123],v[16:19]
ds_read_b64_tr_b16 v[216:217],v228 offset:1024
v_fma_f32 v238,v238,s100,v234
v_mfma_f32_16x16x32_bf16 v[16:19],v[196:199],a[124:127],v[16:19]
ds_read_b64_tr_b16 v[218:219],v228 offset:1280
v_mul_f32 v238,v238,v135
v_mfma_f32_16x16x32_bf16 v[24:27],v[192:195],a[176:179],v[24:27]
buffer_load_dwordx4 v232,s[76:79],s95 offen offset:1024 lds
v_mfma_f32_16x16x32_bf16 v[24:27],v[196:199],a[180:183],v[24:27]
ds_read_b64_tr_b16 v[220:221],v228 offset:9216
ds_read_b64_tr_b16 v[222:223],v228 offset:9472
v_mfma_f32_16x16x32_bf16 v[4:7],v[200:203],a[8:11],v[4:7]
v_rcp_f32 v237,v237
v_mfma_f32_16x16x32_bf16 v[4:7],v[204:207],a[12:15],v[4:7]
v_mul_f32 v166,v166,v237
v_mul_f32 v167,v135,v167
v_mfma_f32_16x16x32_bf16 v[12:15],v[200:203],a[64:67],v[12:15]
buffer_load_dwordx4 v233,s[80:83],s95 offen offset:2048 lds
v_mfma_f32_16x16x32_bf16 v[12:15],v[204:207],a[68:71],v[12:15]
v_exp_f32 v238,v238
v_mfma_f32_16x16x32_bf16 v[20:23],v[200:203],a[120:123],v[20:23]
v_add_f32 v238,1.0,v238
v_mul_f32 v247,v144,v144
v_mfma_f32_16x16x32_bf16 v[20:23],v[204:207],a[124:127],v[20:23]
v_rcp_f32 v238,v238
v_mfma_f32_16x16x32_bf16 v[28:31],v[200:203],a[176:179],v[28:31]
buffer_load_dwordx4 v233,s[84:87],s95 offen offset:3072 lds
v_mfma_f32_16x16x32_bf16 v[28:31],v[204:207],a[180:183],v[28:31]
s_waitcnt vmcnt(18) lgkmcnt(0)
v_mul_f32 v167,v167,v238
v_fma_f32 v247,v247,s100,v234
v_mfma_f32_16x16x32_bf16 v[32:35],v[208:211],a[8:11],v[32:35]
s_barrier
ds_read_b64_tr_b16 v[192:193],v224
s_add_u32 m0,s92,0x10000
v_mfma_f32_16x16x32_bf16 v[32:35],v[212:215],a[12:15],v[32:35]
s_mov_b32 s95,0x280000
ds_read_b64_tr_b16 v[194:195],v224 offset:256
v_mul_f32 v247,v247,v144
v_mfma_f32_16x16x32_bf16 v[40:43],v[208:211],a[64:67],v[40:43]
buffer_load_dwordx4 v232,s[72:75],s95 offen lds
v_mfma_f32_16x16x32_bf16 v[40:43],v[212:215],a[68:71],v[40:43]
ds_read_b64_tr_b16 v[196:197],v224 offset:8192
ds_read_b64_tr_b16 v[198:199],v224 offset:8448
v_mfma_f32_16x16x32_bf16 v[48:51],v[208:211],a[120:123],v[48:51]
ds_read_b64_tr_b16 v[200:201],v225
v_mul_f32 v176,v144,v176
v_mfma_f32_16x16x32_bf16 v[48:51],v[212:215],a[124:127],v[48:51]
ds_read_b64_tr_b16 v[202:203],v225 offset:256
v_mul_f32 v237,v145,v145
v_mfma_f32_16x16x32_bf16 v[56:59],v[208:211],a[176:179],v[56:59]
buffer_load_dwordx4 v232,s[76:79],s95 offen offset:1024 lds
v_mfma_f32_16x16x32_bf16 v[56:59],v[212:215],a[180:183],v[56:59]
ds_read_b64_tr_b16 v[204:205],v225 offset:8192
ds_read_b64_tr_b16 v[206:207],v225 offset:8448
v_mfma_f32_16x16x32_bf16 v[36:39],v[216:219],a[8:11],v[36:39]
v_exp_f32 v239,v239
v_mfma_f32_16x16x32_bf16 v[36:39],v[220:223],a[12:15],v[36:39]
v_add_f32 v239,1.0,v239
v_fma_f32 v237,v237,s100,v234
v_mfma_f32_16x16x32_bf16 v[44:47],v[216:219],a[64:67],v[44:47]
buffer_load_dwordx4 v233,s[80:83],s95 offen offset:2048 lds
v_mfma_f32_16x16x32_bf16 v[44:47],v[220:223],a[68:71],v[44:47]
v_rcp_f32 v239,v239
v_mfma_f32_16x16x32_bf16 v[52:55],v[216:219],a[120:123],v[52:55]
v_mul_f32 v168,v168,v239
v_mul_f32 v237,v237,v145
v_mfma_f32_16x16x32_bf16 v[52:55],v[220:223],a[124:127],v[52:55]
v_mul_f32 v177,v145,v177
v_mul_f32 v238,v146,v146
v_mfma_f32_16x16x32_bf16 v[60:63],v[216:219],a[176:179],v[60:63]
buffer_load_dwordx4 v233,s[84:87],s95 offen offset:3072 lds
v_mfma_f32_16x16x32_bf16 v[60:63],v[220:223],a[180:183],v[60:63]
s_waitcnt lgkmcnt(0)
v_fma_f32 v238,v238,s100,v234
v_mul_f32 v238,v238,v146
v_mfma_f32_16x16x32_bf16 v[0:3],v[192:195],a[16:19],v[0:3]
s_add_u32 m0,s92,0x14000
ds_read_b64_tr_b16 v[208:209],v224 offset:1024
s_mov_b32 s95,0x280200
v_mfma_f32_16x16x32_bf16 v[0:3],v[196:199],a[20:23],v[0:3]
ds_read_b64_tr_b16 v[210:211],v224 offset:1280
v_mul_f32 v178,v146,v178
v_mfma_f32_16x16x32_bf16 v[8:11],v[192:195],a[72:75],v[8:11]
buffer_load_dwordx4 v232,s[72:75],s95 offen lds
v_mfma_f32_16x16x32_bf16 v[8:11],v[196:199],a[76:79],v[8:11]
ds_read_b64_tr_b16 v[212:213],v224 offset:9216
ds_read_b64_tr_b16 v[214:215],v224 offset:9472
v_mfma_f32_16x16x32_bf16 v[16:19],v[192:195],a[128:131],v[16:19]
ds_read_b64_tr_b16 v[216:217],v225 offset:1024
v_mul_f32 v239,v147,v147
v_mfma_f32_16x16x32_bf16 v[16:19],v[196:199],a[132:135],v[16:19]
ds_read_b64_tr_b16 v[218:219],v225 offset:1280
v_fma_f32 v239,v239,s100,v234
v_mfma_f32_16x16x32_bf16 v[24:27],v[192:195],a[184:187],v[24:27]
buffer_load_dwordx4 v232,s[76:79],s95 offen offset:1024 lds
v_mfma_f32_16x16x32_bf16 v[24:27],v[196:199],a[188:191],v[24:27]
ds_read_b64_tr_b16 v[220:221],v225 offset:9216
ds_read_b64_tr_b16 v[222:223],v225 offset:9472
v_mfma_f32_16x16x32_bf16 v[4:7],v[200:203],a[16:19],v[4:7]
v_mul_f32 v239,v239,v147
v_mul_f32 v179,v147,v179
v_mfma_f32_16x16x32_bf16 v[4:7],v[204:207],a[20:23],v[4:7]
v_exp_f32 v244,v244
v_mfma_f32_16x16x32_bf16 v[12:15],v[200:203],a[72:75],v[12:15]
buffer_load_dwordx4 v233,s[80:83],s95 offen offset:2048 lds
v_mfma_f32_16x16x32_bf16 v[12:15],v[204:207],a[76:79],v[12:15]
v_add_f32 v244,1.0,v244
v_mul_f32 v246,v132,v132
v_mfma_f32_16x16x32_bf16 v[20:23],v[200:203],a[128:131],v[20:23]
v_rcp_f32 v244,v244
v_mfma_f32_16x16x32_bf16 v[20:23],v[204:207],a[132:135],v[20:23]
v_mul_f32 v162,v162,v244
v_fma_f32 v246,v246,s100,v234
v_mfma_f32_16x16x32_bf16 v[28:31],v[200:203],a[184:187],v[28:31]
buffer_load_dwordx4 v233,s[84:87],s95 offen offset:3072 lds
v_mfma_f32_16x16x32_bf16 v[28:31],v[204:207],a[188:191],v[28:31]
s_waitcnt vmcnt(16) lgkmcnt(0)
v_exp_f32 v245,v245
v_mfma_f32_16x16x32_bf16 v[32:35],v[208:211],a[16:19],v[32:35]
s_barrier
ds_read_b64_tr_b16 v[192:193],v224 offset:32768
s_add_u32 m0,s92,0x0
v_mfma_f32_16x16x32_bf16 v[32:35],v[212:215],a[20:23],v[32:35]
s_mov_b32 s95,0x300000
ds_read_b64_tr_b16 v[194:195],v224 offset:33024
v_add_f32 v245,1.0,v245
v_mfma_f32_16x16x32_bf16 v[40:43],v[208:211],a[72:75],v[40:43]
buffer_load_dwordx4 v232,s[72:75],s95 offen lds
v_mfma_f32_16x16x32_bf16 v[40:43],v[212:215],a[76:79],v[40:43]
ds_read_b64_tr_b16 v[196:197],v224 offset:40960
ds_read_b64_tr_b16 v[198:199],v224 offset:41216
v_mfma_f32_16x16x32_bf16 v[48:51],v[208:211],a[128:131],v[48:51]
ds_read_b64_tr_b16 v[200:201],v225 offset:32768
v_mul_f32 v246,v246,v132
v_mfma_f32_16x16x32_bf16 v[48:51],v[212:215],a[132:135],v[48:51]
ds_read_b64_tr_b16 v[202:203],v225 offset:33024
v_mul_f32 v164,v132,v164
v_mfma_f32_16x16x32_bf16 v[56:59],v[208:211],a[184:187],v[56:59]
buffer_load_dwordx4 v232,s[76:79],s95 offen offset:1024 lds
v_mfma_f32_16x16x32_bf16 v[56:59],v[212:215],a[188:191],v[56:59]
ds_read_b64_tr_b16 v[204:205],v225 offset:40960
ds_read_b64_tr_b16 v[206:207],v225 offset:41216
v_mfma_f32_16x16x32_bf16 v[36:39],v[216:219],a[16:19],v[36:39]
v_rcp_f32 v245,v245
v_mfma_f32_16x16x32_bf16 v[36:39],v[220:223],a[20:23],v[36:39]
v_mul_f32 v163,v163,v245
v_mul_f32 v244,v141,v141
v_mfma_f32_16x16x32_bf16 v[44:47],v[216:219],a[72:75],v[44:47]
buffer_load_dwordx4 v233,s[80:83],s95 offen offset:2048 lds
v_mfma_f32_16x16x32_bf16 v[44:47],v[220:223],a[76:79],v[44:47]
v_exp_f32 v246,v246
v_mfma_f32_16x16x32_bf16 v[52:55],v[216:219],a[128:131],v[52:55]
v_add_f32 v246,1.0,v246
v_fma_f32 v244,v244,s100,v234
v_mfma_f32_16x16x32_bf16 v[52:55],v[220:223],a[132:135],v[52:55]
v_rcp_f32 v246,v246
v_mfma_f32_16x16x32_bf16 v[60:63],v[216:219],a[184:187],v[60:63]
buffer_load_dwordx4 v233,s[84:87],s95 offen offset:3072 lds
v_mfma_f32_16x16x32_bf16 v[60:63],v[220:223],a[188:191],v[60:63]
s_waitcnt lgkmcnt(0)
v_mul_f32 v164,v164,v246
v_mul_f32 v244,v244,v141
v_mfma_f32_16x16x32_bf16 v[0:3],v[192:195],a[24:27],v[0:3]
)"
R"(s_add_u32 m0,s92,0x4000
ds_read_b64_tr_b16 v[208:209],v224 offset:33792
s_mov_b32 s95,0x300200
v_mfma_f32_16x16x32_bf16 v[0:3],v[196:199],a[28:31],v[0:3]
ds_read_b64_tr_b16 v[210:211],v224 offset:34048
v_mul_f32 v173,v141,v173
v_mfma_f32_16x16x32_bf16 v[8:11],v[192:195],a[80:83],v[8:11]
buffer_load_dwordx4 v232,s[72:75],s95 offen lds
v_mfma_f32_16x16x32_bf16 v[8:11],v[196:199],a[84:87],v[8:11]
ds_read_b64_tr_b16 v[212:213],v224 offset:41984
ds_read_b64_tr_b16 v[214:215],v224 offset:42240
v_mfma_f32_16x16x32_bf16 v[16:19],v[192:195],a[136:139],v[16:19]
ds_read_b64_tr_b16 v[216:217],v225 offset:33792
v_mul_f32 v245,v142,v142
v_mfma_f32_16x16x32_bf16 v[16:19],v[196:199],a[140:143],v[16:19]
ds_read_b64_tr_b16 v[218:219],v225 offset:34048
v_fma_f32 v245,v245,s100,v234
v_mfma_f32_16x16x32_bf16 v[24:27],v[192:195],a[192:195],v[24:27]
buffer_load_dwordx4 v232,s[76:79],s95 offen offset:1024 lds
v_mfma_f32_16x16x32_bf16 v[24:27],v[196:199],a[196:199],v[24:27]
ds_read_b64_tr_b16 v[220:221],v225 offset:41984
ds_read_b64_tr_b16 v[222:223],v225 offset:42240
v_mfma_f32_16x16x32_bf16 v[4:7],v[200:203],a[24:27],v[4:7]
v_exp_f32 v244,v244
v_mfma_f32_16x16x32_bf16 v[4:7],v[204:207],a[28:31],v[4:7]
v_add_f32 v244,1.0,v244
v_mul_f32 v245,v245,v142
v_mfma_f32_16x16x32_bf16 v[12:15],v[200:203],a[80:83],v[12:15]
buffer_load_dwordx4 v233,s[80:83],s95 offen offset:2048 lds
v_mfma_f32_16x16x32_bf16 v[12:15],v[204:207],a[84:87],v[12:15]
v_rcp_f32 v244,v244
v_mfma_f32_16x16x32_bf16 v[20:23],v[200:203],a[136:139],v[20:23]
v_mul_f32 v173,v173,v244
v_mul_f32 v174,v142,v174
v_mfma_f32_16x16x32_bf16 v[20:23],v[204:207],a[140:143],v[20:23]
v_exp_f32 v245,v245
v_mfma_f32_16x16x32_bf16 v[28:31],v[200:203],a[192:195],v[28:31]
buffer_load_dwordx4 v233,s[84:87],s95 offen offset:3072 lds
v_mfma_f32_16x16x32_bf16 v[28:31],v[204:207],a[196:199],v[28:31]
s_waitcnt vmcnt(16) lgkmcnt(0)
v_add_f32 v245,1.0,v245
v_mul_f32 v246,v143,v143
v_mfma_f32_16x16x32_bf16 v[32:35],v[208:211],a[24:27],v[32:35]
s_barrier
ds_read_b64_tr_b16 v[192:193],v227 offset:32768
s_add_u32 m0,s92,0x8000
v_mfma_f32_16x16x32_bf16 v[32:35],v[212:215],a[28:31],v[32:35]
s_mov_b32 s95,0x100
ds_read_b64_tr_b16 v[194:195],v227 offset:33024
v_fma_f32 v246,v246,s100,v234
v_mfma_f32_16x16x32_bf16 v[40:43],v[208:211],a[80:83],v[40:43]
buffer_load_dwordx4 v232,s[72:75],s95 offen lds
v_mfma_f32_16x16x32_bf16 v[40:43],v[212:215],a[84:87],v[40:43]
ds_read_b64_tr_b16 v[196:197],v227 offset:40960
ds_read_b64_tr_b16 v[198:199],v227 offset:41216
v_mfma_f32_16x16x32_bf16 v[48:51],v[208:211],a[136:139],v[48:51]
ds_read_b64_tr_b16 v[200:201],v228 offset:32768
v_mul_f32 v246,v246,v143
v_mfma_f32_16x16x32_bf16 v[48:51],v[212:215],a[140:143],v[48:51]
ds_read_b64_tr_b16 v[202:203],v228 offset:33024
v_mul_f32 v175,v143,v175
v_mfma_f32_16x16x32_bf16 v[56:59],v[208:211],a[192:195],v[56:59]
buffer_load_dwordx4 v232,s[76:79],s95 offen offset:1024 lds
v_mfma_f32_16x16x32_bf16 v[56:59],v[212:215],a[196:199],v[56:59]
ds_read_b64_tr_b16 v[204:205],v228 offset:40960
ds_read_b64_tr_b16 v[206:207],v228 offset:41216
v_mfma_f32_16x16x32_bf16 v[36:39],v[216:219],a[24:27],v[36:39]
v_rcp_f32 v245,v245
v_mfma_f32_16x16x32_bf16 v[36:39],v[220:223],a[28:31],v[36:39]
v_mul_f32 v174,v174,v245
v_mul_f32 v244,v152,v152
v_mfma_f32_16x16x32_bf16 v[44:47],v[216:219],a[80:83],v[44:47]
buffer_load_dwordx4 v233,s[80:83],s95 offen offset:2048 lds
v_mfma_f32_16x16x32_bf16 v[44:47],v[220:223],a[84:87],v[44:47]
v_exp_f32 v246,v246
v_mfma_f32_16x16x32_bf16 v[52:55],v[216:219],a[136:139],v[52:55]
v_add_f32 v246,1.0,v246
v_fma_f32 v244,v244,s100,v234
v_mfma_f32_16x16x32_bf16 v[52:55],v[220:223],a[140:143],v[52:55]
v_rcp_f32 v246,v246
v_mfma_f32_16x16x32_bf16 v[60:63],v[216:219],a[192:195],v[60:63]
buffer_load_dwordx4 v233,s[84:87],s95 offen offset:3072 lds
v_mfma_f32_16x16x32_bf16 v[60:63],v[220:223],a[196:199],v[60:63]
s_waitcnt lgkmcnt(0)
v_mul_f32 v175,v175,v246
v_mul_f32 v244,v244,v152
v_mfma_f32_16x16x32_bf16 v[0:3],v[192:195],a[32:35],v[0:3]
s_add_u32 m0,s92,0xc000
ds_read_b64_tr_b16 v[208:209],v227 offset:33792
s_mov_b32 s95,0x300
v_mfma_f32_16x16x32_bf16 v[0:3],v[196:199],a[36:39],v[0:3]
ds_read_b64_tr_b16 v[210:211],v227 offset:34048
v_mul_f32 v184,v152,v184
v_mfma_f32_16x16x32_bf16 v[8:11],v[192:195],a[88:91],v[8:11]
buffer_load_dwordx4 v232,s[72:75],s95 offen lds
v_mfma_f32_16x16x32_bf16 v[8:11],v[196:199],a[92:95],v[8:11]
ds_read_b64_tr_b16 v[212:213],v227 offset:41984
ds_read_b64_tr_b16 v[214:215],v227 offset:42240
v_mfma_f32_16x16x32_bf16 v[16:19],v[192:195],a[144:147],v[16:19]
ds_read_b64_tr_b16 v[216:217],v228 offset:33792
v_mul_f32 v245,v153,v153
v_mfma_f32_16x16x32_bf16 v[16:19],v[196:199],a[148:151],v[16:19]
ds_read_b64_tr_b16 v[218:219],v228 offset:34048
v_fma_f32 v245,v245,s100,v234
v_mfma_f32_16x16x32_bf16 v[24:27],v[192:195],a[200:203],v[24:27]
buffer_load_dwordx4 v232,s[76:79],s95 offen offset:1024 lds
v_mfma_f32_16x16x32_bf16 v[24:27],v[196:199],a[204:207],v[24:27]
ds_read_b64_tr_b16 v[220:221],v228 offset:41984
ds_read_b64_tr_b16 v[222:223],v228 offset:42240
v_mfma_f32_16x16x32_bf16 v[4:7],v[200:203],a[32:35],v[4:7]
v_exp_f32 v247,v247
v_mfma_f32_16x16x32_bf16 v[4:7],v[204:207],a[36:39],v[4:7]
v_add_f32 v247,1.0,v247
v_mul_f32 v245,v245,v153
v_mfma_f32_16x16x32_bf16 v[12:15],v[200:203],a[88:91],v[12:15]
buffer_load_dwordx4 v233,s[80:83],s95 offen offset:2048 lds
v_mfma_f32_16x16x32_bf16 v[12:15],v[204:207],a[92:95],v[12:15]
v_rcp_f32 v247,v247
v_mfma_f32_16x16x32_bf16 v[20:23],v[200:203],a[144:147],v[20:23]
v_mul_f32 v176,v176,v247
v_mul_f32 v185,v153,v185
v_mfma_f32_16x16x32_bf16 v[20:23],v[204:207],a[148:151],v[20:23]
v_mul_f32 v246,v154,v154
v_fma_f32 v246,v246,s100,v234
v_mfma_f32_16x16x32_bf16 v[28:31],v[200:203],a[200:203],v[28:31]
buffer_load_dwordx4 v233,s[84:87],s95 offen offset:3072 lds
v_mfma_f32_16x16x32_bf16 v[28:31],v[204:207],a[204:207],v[28:31]
s_waitcnt vmcnt(16) lgkmcnt(0)
v_mul_f32 v246,v246,v154
v_mul_f32 v186,v154,v186
v_mfma_f32_16x16x32_bf16 v[32:35],v[208:211],a[32:35],v[32:35]
s_barrier
ds_read_b64_tr_b16 v[192:193],v227
s_add_u32 m0,s92,0x18000
v_mfma_f32_16x16x32_bf16 v[32:35],v[212:215],a[36:39],v[32:35]
s_mov_b32 s95,0x80100
ds_read_b64_tr_b16 v[194:195],v227 offset:256
v_mul_f32 v247,v155,v155
v_mfma_f32_16x16x32_bf16 v[40:43],v[208:211],a[88:91],v[40:43]
buffer_load_dwordx4 v232,s[72:75],s95 offen lds
v_mfma_f32_16x16x32_bf16 v[40:43],v[212:215],a[92:95],v[40:43]
ds_read_b64_tr_b16 v[196:197],v227 offset:8192
ds_read_b64_tr_b16 v[198:199],v227 offset:8448
v_mfma_f32_16x16x32_bf16 v[48:51],v[208:211],a[144:147],v[48:51]
ds_read_b64_tr_b16 v[200:201],v228
v_fma_f32 v247,v247,s100,v234
v_mfma_f32_16x16x32_bf16 v[48:51],v[212:215],a[148:151],v[48:51]
ds_read_b64_tr_b16 v[202:203],v228 offset:256
v_mul_f32 v247,v247,v155
v_mfma_f32_16x16x32_bf16 v[56:59],v[208:211],a[200:203],v[56:59]
buffer_load_dwordx4 v232,s[76:79],s95 offen offset:1024 lds
v_mfma_f32_16x16x32_bf16 v[56:59],v[212:215],a[204:207],v[56:59]
ds_read_b64_tr_b16 v[204:205],v228 offset:8192
ds_read_b64_tr_b16 v[206:207],v228 offset:8448
v_mfma_f32_16x16x32_bf16 v[36:39],v[216:219],a[32:35],v[36:39]
v_mul_f32 v187,v155,v187
v_mul_f32 v172,v140,v172
v_mfma_f32_16x16x32_bf16 v[36:39],v[220:223],a[36:39],v[36:39]
v_exp_f32 v243,v243
v_mfma_f32_16x16x32_bf16 v[44:47],v[216:219],a[88:91],v[44:47]
buffer_load_dwordx4 v233,s[80:83],s95 offen offset:2048 lds
v_mfma_f32_16x16x32_bf16 v[44:47],v[220:223],a[92:95],v[44:47]
v_add_f32 v243,1.0,v243
v_mul_f32 v180,v148,v180
v_mfma_f32_16x16x32_bf16 v[52:55],v[216:219],a[144:147],v[52:55]
v_rcp_f32 v243,v243
v_mfma_f32_16x16x32_bf16 v[52:55],v[220:223],a[148:151],v[52:55]
v_mul_f32 v161,v161,v243
v_mul_f32 v243,v140,v140
v_mfma_f32_16x16x32_bf16 v[60:63],v[216:219],a[200:203],v[60:63]
buffer_load_dwordx4 v233,s[84:87],s95 offen offset:3072 lds
v_mfma_f32_16x16x32_bf16 v[60:63],v[220:223],a[204:207],v[60:63]
s_waitcnt lgkmcnt(0)
v_fma_f32 v243,v243,s100,v234
v_mul_f32 v243,v243,v140
v_mfma_f32_16x16x32_bf16 v[0:3],v[192:195],a[40:43],v[0:3]
s_add_u32 m0,s92,0x1c000
ds_read_b64_tr_b16 v[208:209],v227 offset:1024
s_mov_b32 s95,0x80300
v_mfma_f32_16x16x32_bf16 v[0:3],v[196:199],a[44:47],v[0:3]
ds_read_b64_tr_b16 v[210:211],v227 offset:1280
v_mul_f32 v181,v149,v181
v_mfma_f32_16x16x32_bf16 v[8:11],v[192:195],a[96:99],v[8:11]
buffer_load_dwordx4 v232,s[72:75],s95 offen lds
v_mfma_f32_16x16x32_bf16 v[8:11],v[196:199],a[100:103],v[8:11]
ds_read_b64_tr_b16 v[212:213],v227 offset:9216
ds_read_b64_tr_b16 v[214:215],v227 offset:9472
v_mfma_f32_16x16x32_bf16 v[16:19],v[192:195],a[152:155],v[16:19]
ds_read_b64_tr_b16 v[216:217],v228 offset:1024
v_mul_f32 v182,v150,v182
v_mfma_f32_16x16x32_bf16 v[16:19],v[196:199],a[156:159],v[16:19]
ds_read_b64_tr_b16 v[218:219],v228 offset:1280
v_mul_f32 v183,v151,v183
v_mfma_f32_16x16x32_bf16 v[24:27],v[192:195],a[208:211],v[24:27]
buffer_load_dwordx4 v232,s[76:79],s95 offen offset:1024 lds
v_mfma_f32_16x16x32_bf16 v[24:27],v[196:199],a[212:215],v[24:27]
ds_read_b64_tr_b16 v[220:221],v228 offset:9216
ds_read_b64_tr_b16 v[222:223],v228 offset:9472
v_mfma_f32_16x16x32_bf16 v[4:7],v[200:203],a[40:43],v[4:7]
v_exp_f32 v240,v240
)"
R"(v_mfma_f32_16x16x32_bf16 v[4:7],v[204:207],a[44:47],v[4:7]
v_add_f32 v240,1.0,v240
v_mul_f32 v188,v156,v188
v_mfma_f32_16x16x32_bf16 v[12:15],v[200:203],a[96:99],v[12:15]
buffer_load_dwordx4 v233,s[80:83],s95 offen offset:2048 lds
v_mfma_f32_16x16x32_bf16 v[12:15],v[204:207],a[100:103],v[12:15]
v_rcp_f32 v240,v240
v_mfma_f32_16x16x32_bf16 v[20:23],v[200:203],a[152:155],v[20:23]
v_mul_f32 v169,v169,v240
v_mul_f32 v240,v148,v148
v_mfma_f32_16x16x32_bf16 v[20:23],v[204:207],a[156:159],v[20:23]
v_exp_f32 v241,v241
v_mfma_f32_16x16x32_bf16 v[28:31],v[200:203],a[208:211],v[28:31]
buffer_load_dwordx4 v233,s[84:87],s95 offen offset:3072 lds
v_mfma_f32_16x16x32_bf16 v[28:31],v[204:207],a[212:215],v[28:31]
s_waitcnt vmcnt(16) lgkmcnt(0)
v_add_f32 v241,1.0,v241
v_fma_f32 v240,v240,s100,v234
v_mfma_f32_16x16x32_bf16 v[32:35],v[208:211],a[40:43],v[32:35]
s_barrier
ds_read_b64_tr_b16 v[192:193],v224
s_add_u32 m0,s92,0x10000
v_mfma_f32_16x16x32_bf16 v[32:35],v[212:215],a[44:47],v[32:35]
s_mov_b32 s95,0x100100
ds_read_b64_tr_b16 v[194:195],v224 offset:256
v_mul_f32 v240,v240,v148
v_mfma_f32_16x16x32_bf16 v[40:43],v[208:211],a[96:99],v[40:43]
buffer_load_dwordx4 v232,s[72:75],s95 offen lds
v_mfma_f32_16x16x32_bf16 v[40:43],v[212:215],a[100:103],v[40:43]
ds_read_b64_tr_b16 v[196:197],v224 offset:8192
ds_read_b64_tr_b16 v[198:199],v224 offset:8448
v_mfma_f32_16x16x32_bf16 v[48:51],v[208:211],a[152:155],v[48:51]
ds_read_b64_tr_b16 v[200:201],v225
v_mul_f32 v189,v157,v189
v_mfma_f32_16x16x32_bf16 v[48:51],v[212:215],a[156:159],v[48:51]
ds_read_b64_tr_b16 v[202:203],v225 offset:256
v_mul_f32 v190,v158,v190
v_mfma_f32_16x16x32_bf16 v[56:59],v[208:211],a[208:211],v[56:59]
buffer_load_dwordx4 v232,s[76:79],s95 offen offset:1024 lds
v_mfma_f32_16x16x32_bf16 v[56:59],v[212:215],a[212:215],v[56:59]
ds_read_b64_tr_b16 v[204:205],v225 offset:8192
ds_read_b64_tr_b16 v[206:207],v225 offset:8448
v_mfma_f32_16x16x32_bf16 v[36:39],v[216:219],a[40:43],v[36:39]
v_rcp_f32 v241,v241
v_mfma_f32_16x16x32_bf16 v[36:39],v[220:223],a[44:47],v[36:39]
v_mul_f32 v170,v170,v241
v_mul_f32 v241,v149,v149
v_mfma_f32_16x16x32_bf16 v[44:47],v[216:219],a[96:99],v[44:47]
buffer_load_dwordx4 v233,s[80:83],s95 offen offset:2048 lds
v_mfma_f32_16x16x32_bf16 v[44:47],v[220:223],a[100:103],v[44:47]
v_exp_f32 v242,v242
v_mfma_f32_16x16x32_bf16 v[52:55],v[216:219],a[152:155],v[52:55]
v_add_f32 v242,1.0,v242
v_fma_f32 v241,v241,s100,v234
v_mfma_f32_16x16x32_bf16 v[52:55],v[220:223],a[156:159],v[52:55]
v_rcp_f32 v242,v242
v_mfma_f32_16x16x32_bf16 v[60:63],v[216:219],a[208:211],v[60:63]
buffer_load_dwordx4 v233,s[84:87],s95 offen offset:3072 lds
v_mfma_f32_16x16x32_bf16 v[60:63],v[220:223],a[212:215],v[60:63]
s_waitcnt lgkmcnt(0)
v_mul_f32 v171,v171,v242
v_mul_f32 v241,v241,v149
v_mfma_f32_16x16x32_bf16 v[0:3],v[192:195],a[48:51],v[0:3]
s_add_u32 m0,s92,0x14000
ds_read_b64_tr_b16 v[208:209],v224 offset:1024
s_mov_b32 s95,0x100300
v_mfma_f32_16x16x32_bf16 v[0:3],v[196:199],a[52:55],v[0:3]
ds_read_b64_tr_b16 v[210:211],v224 offset:1280
v_mul_f32 v242,v150,v150
v_mfma_f32_16x16x32_bf16 v[8:11],v[192:195],a[104:107],v[8:11]
buffer_load_dwordx4 v232,s[72:75],s95 offen lds
v_mfma_f32_16x16x32_bf16 v[8:11],v[196:199],a[108:111],v[8:11]
ds_read_b64_tr_b16 v[212:213],v224 offset:9216
ds_read_b64_tr_b16 v[214:215],v224 offset:9472
v_mfma_f32_16x16x32_bf16 v[16:19],v[192:195],a[160:163],v[16:19]
ds_read_b64_tr_b16 v[216:217],v225 offset:1024
v_cvt_pk_bf16_f32 v0,v0,v1
v_mfma_f32_16x16x32_bf16 v[16:19],v[196:199],a[164:167],v[16:19]
ds_read_b64_tr_b16 v[218:219],v225 offset:1280
v_cvt_pk_bf16_f32 v1,v2,v3
v_mfma_f32_16x16x32_bf16 v[24:27],v[192:195],a[216:219],v[24:27]
buffer_store_dwordx4 v[80:83],v254,s[88:91],s99 offen sc1
buffer_load_dwordx4 v232,s[76:79],s95 offen offset:1024 lds
v_mfma_f32_16x16x32_bf16 v[24:27],v[196:199],a[220:223],v[24:27]
s_add_u32 s99,s97,s98
buffer_store_dwordx4 v[88:91],v254,s[88:91],s99 offen sc1
v_mfma_f32_16x16x32_bf16 v[4:7],v[200:203],a[48:51],v[4:7]
s_lshr_b32 s98,%[h],2
ds_read_b64_tr_b16 v[220:221],v225 offset:9216
ds_read_b64_tr_b16 v[222:223],v225 offset:9472
v_mfma_f32_16x16x32_bf16 v[4:7],v[204:207],a[52:55],v[4:7]
s_mul_i32 s98,s98,28
v_cvt_pk_bf16_f32 v8,v8,v9
v_cvt_pk_bf16_f32 v9,v10,v11
v_mfma_f32_16x16x32_bf16 v[12:15],v[200:203],a[104:107],v[12:15]
s_add_u32 s98,s98,s101
buffer_load_dwordx4 v233,s[80:83],s95 offen offset:2048 lds
v_mfma_f32_16x16x32_bf16 v[12:15],v[204:207],a[108:111],v[12:15]
s_and_b32 s101,%[w],2
v_cvt_pk_bf16_f32 v2,v4,v5
v_cvt_pk_bf16_f32 v3,v6,v7
v_mfma_f32_16x16x32_bf16 v[20:23],v[200:203],a[160:163],v[20:23]
s_add_u32 s98,s98,s101
v_permlane16_swap_b32 v0,v2
v_permlane16_swap_b32 v1,v3
v_mfma_f32_16x16x32_bf16 v[20:23],v[204:207],a[164:167],v[20:23]
s_and_b32 s101,%[h],2
ds_write_b128 v235,v[0:3] offset:32768
v_cvt_pk_bf16_f32 v10,v12,v13
v_mfma_f32_16x16x32_bf16 v[28:31],v[200:203],a[216:219],v[28:31]
s_mul_i32 s101,s101,8
buffer_load_dwordx4 v233,s[84:87],s95 offen offset:3072 lds
v_mfma_f32_16x16x32_bf16 v[28:31],v[204:207],a[220:223],v[28:31]
s_waitcnt vmcnt(18) lgkmcnt(0)
v_cvt_pk_bf16_f32 v11,v14,v15
v_permlane16_swap_b32 v8,v10
v_mfma_f32_16x16x32_bf16 v[32:35],v[208:211],a[48:51],v[32:35]
s_barrier
ds_read_b64_tr_b16 v[192:193],v224 offset:32768
s_add_u32 m0,s92,0x0
v_mfma_f32_16x16x32_bf16 v[32:35],v[212:215],a[52:55],v[32:35]
s_mov_b32 s95,0x180100
ds_read_b64_tr_b16 v[194:195],v224 offset:33024
v_permlane16_swap_b32 v9,v11
v_mfma_f32_16x16x32_bf16 v[40:43],v[208:211],a[104:107],v[40:43]
s_add_u32 s98,s98,s101
buffer_load_dwordx4 v232,s[72:75],s95 offen lds
v_mfma_f32_16x16x32_bf16 v[40:43],v[212:215],a[108:111],v[40:43]
s_add_u32 s98,s98,0
ds_read_b64_tr_b16 v[196:197],v224 offset:40960
ds_read_b64_tr_b16 v[198:199],v224 offset:41216
v_mfma_f32_16x16x32_bf16 v[48:51],v[208:211],a[160:163],v[48:51]
s_lshl_b32 s98,s98,10
ds_read_b64_tr_b16 v[200:201],v225 offset:32768
ds_write_b128 v235,v[8:11] offset:33792
v_mfma_f32_16x16x32_bf16 v[48:51],v[212:215],a[164:167],v[48:51]
s_add_u32 s99,s96,s98
ds_read_b64_tr_b16 v[202:203],v225 offset:33024
v_cvt_pk_bf16_f32 v16,v16,v17
v_mfma_f32_16x16x32_bf16 v[56:59],v[208:211],a[216:219],v[56:59]
s_and_b32 s101,%[w],1
buffer_load_dwordx4 v232,s[76:79],s95 offen offset:1024 lds
v_mfma_f32_16x16x32_bf16 v[56:59],v[212:215],a[220:223],v[56:59]
s_lshl_b32 s101,s101,2
ds_read_b64_tr_b16 v[204:205],v225 offset:40960
ds_read_b64_tr_b16 v[206:207],v225 offset:41216
v_mfma_f32_16x16x32_bf16 v[36:39],v[216:219],a[48:51],v[36:39]
v_cvt_pk_bf16_f32 v17,v18,v19
v_cvt_pk_bf16_f32 v18,v20,v21
v_mfma_f32_16x16x32_bf16 v[36:39],v[220:223],a[52:55],v[36:39]
v_cvt_pk_bf16_f32 v19,v22,v23
v_permlane16_swap_b32 v16,v18
v_mfma_f32_16x16x32_bf16 v[44:47],v[216:219],a[104:107],v[44:47]
buffer_load_dwordx4 v233,s[80:83],s95 offen offset:2048 lds
v_mfma_f32_16x16x32_bf16 v[44:47],v[220:223],a[108:111],v[44:47]
v_permlane16_swap_b32 v17,v19
v_cvt_pk_bf16_f32 v24,v24,v25
v_mfma_f32_16x16x32_bf16 v[52:55],v[216:219],a[160:163],v[52:55]
ds_write_b128 v235,v[16:19] offset:34816
v_cvt_pk_bf16_f32 v25,v26,v27
v_mfma_f32_16x16x32_bf16 v[52:55],v[220:223],a[164:167],v[52:55]
v_cvt_pk_bf16_f32 v26,v28,v29
v_cvt_pk_bf16_f32 v27,v30,v31
v_mfma_f32_16x16x32_bf16 v[60:63],v[216:219],a[216:219],v[60:63]
buffer_load_dwordx4 v233,s[84:87],s95 offen offset:3072 lds
v_mfma_f32_16x16x32_bf16 v[60:63],v[220:223],a[220:223],v[60:63]
s_waitcnt lgkmcnt(1)
v_permlane16_swap_b32 v24,v26
v_permlane16_swap_b32 v25,v27
v_mfma_f32_16x16x32_bf16 v[64:67],v[192:195],a[0:3],0
s_add_u32 m0,s92,0x4000
ds_read_b64_tr_b16 v[208:209],v224 offset:33792
s_mov_b32 s95,0x180300
v_mfma_f32_16x16x32_bf16 v[64:67],v[196:199],a[4:7],v[64:67]
ds_read_b64_tr_b16 v[210:211],v224 offset:34048
ds_write_b128 v235,v[24:27] offset:35840
v_mfma_f32_16x16x32_bf16 v[72:75],v[192:195],a[56:59],0
buffer_load_dwordx4 v232,s[72:75],s95 offen lds
v_mfma_f32_16x16x32_bf16 v[72:75],v[196:199],a[60:63],v[72:75]
ds_read_b64_tr_b16 v[212:213],v224 offset:41984
ds_read_b64_tr_b16 v[214:215],v224 offset:42240
v_mfma_f32_16x16x32_bf16 v[80:83],v[192:195],a[112:115],0
ds_read_b64_tr_b16 v[216:217],v225 offset:33792
v_cvt_pk_bf16_f32 v32,v32,v33
v_mfma_f32_16x16x32_bf16 v[80:83],v[196:199],a[116:119],v[80:83]
ds_read_b64_tr_b16 v[218:219],v225 offset:34048
v_cvt_pk_bf16_f32 v33,v34,v35
v_mfma_f32_16x16x32_bf16 v[88:91],v[192:195],a[168:171],0
buffer_load_dwordx4 v232,s[76:79],s95 offen offset:1024 lds
v_mfma_f32_16x16x32_bf16 v[88:91],v[196:199],a[172:175],v[88:91]
ds_read_b64_tr_b16 v[220:221],v225 offset:41984
ds_read_b64_tr_b16 v[222:223],v225 offset:42240
v_mfma_f32_16x16x32_bf16 v[68:71],v[200:203],a[0:3],0
v_cvt_pk_bf16_f32 v34,v36,v37
v_cvt_pk_bf16_f32 v35,v38,v39
v_mfma_f32_16x16x32_bf16 v[68:71],v[204:207],a[4:7],v[68:71]
v_permlane16_swap_b32 v32,v34
v_permlane16_swap_b32 v33,v35
v_mfma_f32_16x16x32_bf16 v[76:79],v[200:203],a[56:59],0
buffer_load_dwordx4 v233,s[80:83],s95 offen offset:2048 lds
)"
R"(v_mfma_f32_16x16x32_bf16 v[76:79],v[204:207],a[60:63],v[76:79]
ds_write_b128 v235,v[32:35] offset:36864
v_cvt_pk_bf16_f32 v40,v40,v41
v_mfma_f32_16x16x32_bf16 v[84:87],v[200:203],a[112:115],0
v_cvt_pk_bf16_f32 v41,v42,v43
v_cvt_pk_bf16_f32 v42,v44,v45
v_mfma_f32_16x16x32_bf16 v[84:87],v[204:207],a[116:119],v[84:87]
v_cvt_pk_bf16_f32 v43,v46,v47
v_permlane16_swap_b32 v40,v42
v_mfma_f32_16x16x32_bf16 v[92:95],v[200:203],a[168:171],0
buffer_load_dwordx4 v233,s[84:87],s95 offen offset:3072 lds
v_mfma_f32_16x16x32_bf16 v[92:95],v[204:207],a[172:175],v[92:95]
s_waitcnt vmcnt(18) lgkmcnt(0)
v_permlane16_swap_b32 v41,v43
v_cvt_pk_bf16_f32 v48,v48,v49
v_mfma_f32_16x16x32_bf16 v[96:99],v[208:211],a[0:3],0
s_barrier
ds_read_b64_tr_b16 v[192:193],v227 offset:32768
s_add_u32 m0,s92,0x8000
v_mfma_f32_16x16x32_bf16 v[96:99],v[212:215],a[4:7],v[96:99]
s_mov_b32 s95,0x200100
ds_read_b64_tr_b16 v[194:195],v227 offset:33024
ds_write_b128 v235,v[40:43] offset:37888
v_mfma_f32_16x16x32_bf16 v[104:107],v[208:211],a[56:59],0
buffer_load_dwordx4 v232,s[72:75],s95 offen lds
v_mfma_f32_16x16x32_bf16 v[104:107],v[212:215],a[60:63],v[104:107]
ds_read_b64_tr_b16 v[196:197],v227 offset:40960
ds_read_b64_tr_b16 v[198:199],v227 offset:41216
v_mfma_f32_16x16x32_bf16 v[112:115],v[208:211],a[112:115],0
ds_read_b64_tr_b16 v[200:201],v228 offset:32768
v_cvt_pk_bf16_f32 v49,v50,v51
v_mfma_f32_16x16x32_bf16 v[112:115],v[212:215],a[116:119],v[112:115]
ds_read_b64_tr_b16 v[202:203],v228 offset:33024
v_cvt_pk_bf16_f32 v50,v52,v53
v_mfma_f32_16x16x32_bf16 v[120:123],v[208:211],a[168:171],0
buffer_load_dwordx4 v232,s[76:79],s95 offen offset:1024 lds
v_mfma_f32_16x16x32_bf16 v[120:123],v[212:215],a[172:175],v[120:123]
ds_read_b64_tr_b16 v[204:205],v228 offset:40960
ds_read_b64_tr_b16 v[206:207],v228 offset:41216
v_mfma_f32_16x16x32_bf16 v[100:103],v[216:219],a[0:3],0
v_cvt_pk_bf16_f32 v51,v54,v55
v_permlane16_swap_b32 v48,v50
v_mfma_f32_16x16x32_bf16 v[100:103],v[220:223],a[4:7],v[100:103]
v_permlane16_swap_b32 v49,v51
v_cvt_pk_bf16_f32 v56,v56,v57
v_mfma_f32_16x16x32_bf16 v[108:111],v[216:219],a[56:59],0
buffer_load_dwordx4 v233,s[80:83],s95 offen offset:2048 lds
v_mfma_f32_16x16x32_bf16 v[108:111],v[220:223],a[60:63],v[108:111]
ds_write_b128 v235,v[48:51] offset:38912
v_cvt_pk_bf16_f32 v57,v58,v59
v_mfma_f32_16x16x32_bf16 v[116:119],v[216:219],a[112:115],0
v_cvt_pk_bf16_f32 v58,v60,v61
v_cvt_pk_bf16_f32 v59,v62,v63
v_mfma_f32_16x16x32_bf16 v[116:119],v[220:223],a[116:119],v[116:119]
v_permlane16_swap_b32 v56,v58
v_permlane16_swap_b32 v57,v59
v_mfma_f32_16x16x32_bf16 v[124:127],v[216:219],a[168:171],0
buffer_load_dwordx4 v233,s[84:87],s95 offen offset:3072 lds
v_mfma_f32_16x16x32_bf16 v[124:127],v[220:223],a[172:175],v[124:127]
s_waitcnt lgkmcnt(1)
ds_write_b128 v235,v[56:59] offset:39936
v_fma_f32 v242,v242,s100,v234
v_mfma_f32_16x16x32_bf16 v[64:67],v[192:195],a[8:11],v[64:67]
s_add_u32 m0,s92,0xc000
ds_read_b64_tr_b16 v[208:209],v227 offset:33792
s_mov_b32 s95,0x200300
v_mfma_f32_16x16x32_bf16 v[64:67],v[196:199],a[12:15],v[64:67]
ds_read_b64_tr_b16 v[210:211],v227 offset:34048
v_mul_f32 v242,v242,v150
v_mfma_f32_16x16x32_bf16 v[72:75],v[192:195],a[64:67],v[72:75]
buffer_load_dwordx4 v232,s[72:75],s95 offen lds
v_mfma_f32_16x16x32_bf16 v[72:75],v[196:199],a[68:71],v[72:75]
ds_read_b64_tr_b16 v[212:213],v227 offset:41984
ds_read_b64_tr_b16 v[214:215],v227 offset:42240
v_mfma_f32_16x16x32_bf16 v[80:83],v[192:195],a[120:123],v[80:83]
ds_read_b64_tr_b16 v[216:217],v228 offset:33792
v_mul_f32 v191,v159,v191
v_mfma_f32_16x16x32_bf16 v[80:83],v[196:199],a[124:127],v[80:83]
ds_read_b64_tr_b16 v[218:219],v228 offset:34048
v_cvt_pk_bf16_f32 v160,v160,v161
v_mfma_f32_16x16x32_bf16 v[88:91],v[192:195],a[176:179],v[88:91]
buffer_load_dwordx4 v232,s[76:79],s95 offen offset:1024 lds
v_mfma_f32_16x16x32_bf16 v[88:91],v[196:199],a[180:183],v[88:91]
ds_read_b64_tr_b16 v[220:221],v228 offset:41984
ds_read_b64_tr_b16 v[222:223],v228 offset:42240
v_mfma_f32_16x16x32_bf16 v[68:71],v[200:203],a[8:11],v[68:71]
v_exp_f32 v243,v243
v_mfma_f32_16x16x32_bf16 v[68:71],v[204:207],a[12:15],v[68:71]
v_add_f32 v243,1.0,v243
v_cvt_pk_bf16_f32 v161,v162,v163
v_mfma_f32_16x16x32_bf16 v[76:79],v[200:203],a[64:67],v[76:79]
buffer_load_dwordx4 v233,s[80:83],s95 offen offset:2048 lds
v_mfma_f32_16x16x32_bf16 v[76:79],v[204:207],a[68:71],v[76:79]
v_rcp_f32 v243,v243
v_mfma_f32_16x16x32_bf16 v[84:87],v[200:203],a[120:123],v[84:87]
v_mul_f32 v172,v172,v243
v_mul_f32 v243,v151,v151
v_mfma_f32_16x16x32_bf16 v[84:87],v[204:207],a[124:127],v[84:87]
v_fma_f32 v243,v243,s100,v234
v_mul_f32 v243,v243,v151
v_mfma_f32_16x16x32_bf16 v[92:95],v[200:203],a[176:179],v[92:95]
buffer_load_dwordx4 v233,s[84:87],s95 offen offset:3072 lds
v_mfma_f32_16x16x32_bf16 v[92:95],v[204:207],a[180:183],v[92:95]
s_waitcnt vmcnt(16) lgkmcnt(0)
v_exp_f32 v237,v237
v_mfma_f32_16x16x32_bf16 v[96:99],v[208:211],a[8:11],v[96:99]
s_barrier
ds_read_b64_tr_b16 v[192:193],v227
s_add_u32 m0,s92,0x18000
v_mfma_f32_16x16x32_bf16 v[96:99],v[212:215],a[12:15],v[96:99]
s_mov_b32 s95,0x280100
ds_read_b64_tr_b16 v[194:195],v227 offset:256
v_add_f32 v237,1.0,v237
v_mfma_f32_16x16x32_bf16 v[104:107],v[208:211],a[64:67],v[104:107]
buffer_load_dwordx4 v232,s[72:75],s95 offen lds
v_mfma_f32_16x16x32_bf16 v[104:107],v[212:215],a[68:71],v[104:107]
ds_read_b64_tr_b16 v[196:197],v227 offset:8192
ds_read_b64_tr_b16 v[198:199],v227 offset:8448
v_mfma_f32_16x16x32_bf16 v[112:115],v[208:211],a[120:123],v[112:115]
ds_read_b64_tr_b16 v[200:201],v228
v_cvt_pk_bf16_f32 v162,v164,v165
v_mfma_f32_16x16x32_bf16 v[112:115],v[212:215],a[124:127],v[112:115]
ds_read_b64_tr_b16 v[202:203],v228 offset:256
v_cvt_pk_bf16_f32 v163,v166,v167
v_mfma_f32_16x16x32_bf16 v[120:123],v[208:211],a[176:179],v[120:123]
buffer_load_dwordx4 v232,s[76:79],s95 offen offset:1024 lds
v_mfma_f32_16x16x32_bf16 v[120:123],v[212:215],a[180:183],v[120:123]
ds_read_b64_tr_b16 v[204:205],v228 offset:8192
ds_read_b64_tr_b16 v[206:207],v228 offset:8448
v_mfma_f32_16x16x32_bf16 v[100:103],v[216:219],a[8:11],v[100:103]
v_rcp_f32 v237,v237
v_mfma_f32_16x16x32_bf16 v[100:103],v[220:223],a[12:15],v[100:103]
v_mul_f32 v177,v177,v237
v_mul_f32 v237,v156,v156
v_mfma_f32_16x16x32_bf16 v[108:111],v[216:219],a[64:67],v[108:111]
buffer_load_dwordx4 v233,s[80:83],s95 offen offset:2048 lds
v_mfma_f32_16x16x32_bf16 v[108:111],v[220:223],a[68:71],v[108:111]
v_exp_f32 v238,v238
v_mfma_f32_16x16x32_bf16 v[116:119],v[216:219],a[120:123],v[116:119]
v_add_f32 v238,1.0,v238
v_fma_f32 v237,v237,s100,v234
v_mfma_f32_16x16x32_bf16 v[116:119],v[220:223],a[124:127],v[116:119]
v_rcp_f32 v238,v238
v_mfma_f32_16x16x32_bf16 v[124:127],v[216:219],a[176:179],v[124:127]
buffer_load_dwordx4 v233,s[84:87],s95 offen offset:3072 lds
v_mfma_f32_16x16x32_bf16 v[124:127],v[220:223],a[180:183],v[124:127]
s_waitcnt lgkmcnt(0)
v_mul_f32 v178,v178,v238
v_mul_f32 v237,v237,v156
v_mfma_f32_16x16x32_bf16 v[64:67],v[192:195],a[16:19],v[64:67]
s_add_u32 m0,s92,0x1c000
ds_read_b64_tr_b16 v[208:209],v227 offset:1024
s_mov_b32 s95,0x280300
v_mfma_f32_16x16x32_bf16 v[64:67],v[196:199],a[20:23],v[64:67]
ds_read_b64_tr_b16 v[210:211],v227 offset:1280
v_mul_f32 v238,v157,v157
v_mfma_f32_16x16x32_bf16 v[72:75],v[192:195],a[72:75],v[72:75]
buffer_load_dwordx4 v232,s[72:75],s95 offen lds
v_mfma_f32_16x16x32_bf16 v[72:75],v[196:199],a[76:79],v[72:75]
ds_read_b64_tr_b16 v[212:213],v227 offset:9216
ds_read_b64_tr_b16 v[214:215],v227 offset:9472
v_mfma_f32_16x16x32_bf16 v[80:83],v[192:195],a[128:131],v[80:83]
ds_read_b64_tr_b16 v[216:217],v228 offset:1024
v_fma_f32 v238,v238,s100,v234
v_mfma_f32_16x16x32_bf16 v[80:83],v[196:199],a[132:135],v[80:83]
ds_read_b64_tr_b16 v[218:219],v228 offset:1280
v_mul_f32 v238,v238,v157
v_mfma_f32_16x16x32_bf16 v[88:91],v[192:195],a[184:187],v[88:91]
buffer_load_dwordx4 v232,s[76:79],s95 offen offset:1024 lds
v_mfma_f32_16x16x32_bf16 v[88:91],v[196:199],a[188:191],v[88:91]
ds_read_b64_tr_b16 v[220:221],v228 offset:9216
ds_read_b64_tr_b16 v[222:223],v228 offset:9472
v_mfma_f32_16x16x32_bf16 v[68:71],v[200:203],a[16:19],v[68:71]
v_exp_f32 v239,v239
v_mfma_f32_16x16x32_bf16 v[68:71],v[204:207],a[20:23],v[68:71]
v_add_f32 v239,1.0,v239
v_cvt_pk_bf16_f32 v168,v168,v169
v_mfma_f32_16x16x32_bf16 v[76:79],v[200:203],a[72:75],v[76:79]
buffer_load_dwordx4 v233,s[80:83],s95 offen offset:2048 lds
v_mfma_f32_16x16x32_bf16 v[76:79],v[204:207],a[76:79],v[76:79]
v_rcp_f32 v239,v239
v_mfma_f32_16x16x32_bf16 v[84:87],v[200:203],a[128:131],v[84:87]
v_mul_f32 v179,v179,v239
v_mul_f32 v239,v158,v158
v_mfma_f32_16x16x32_bf16 v[84:87],v[204:207],a[132:135],v[84:87]
v_exp_f32 v240,v240
v_mfma_f32_16x16x32_bf16 v[92:95],v[200:203],a[184:187],v[92:95]
buffer_load_dwordx4 v233,s[84:87],s95 offen offset:3072 lds
v_mfma_f32_16x16x32_bf16 v[92:95],v[204:207],a[188:191],v[92:95]
s_waitcnt vmcnt(16) lgkmcnt(0)
v_add_f32 v240,1.0,v240
v_fma_f32 v239,v239,s100,v234
v_mfma_f32_16x16x32_bf16 v[96:99],v[208:211],a[16:19],v[96:99]
s_barrier
ds_read_b64_tr_b16 v[192:193],v224
s_add_u32 m0,s92,0x10000
v_mfma_f32_16x16x32_bf16 v[96:99],v[212:215],a[20:23],v[96:99]
s_mov_b32 s95,0x300100
ds_read_b64_tr_b16 v[194:195],v224 offset:256
ds_read_b128 v[4:7],v236 offset:34816
)"
R"(v_mfma_f32_16x16x32_bf16 v[104:107],v[208:211],a[72:75],v[104:107]
buffer_load_dwordx4 v232,s[72:75],s95 offen lds
v_mfma_f32_16x16x32_bf16 v[104:107],v[212:215],a[76:79],v[104:107]
ds_read_b64_tr_b16 v[196:197],v224 offset:8192
ds_read_b64_tr_b16 v[198:199],v224 offset:8448
v_mfma_f32_16x16x32_bf16 v[112:115],v[208:211],a[128:131],v[112:115]
ds_read_b64_tr_b16 v[200:201],v225
ds_read_b128 v[12:15],v236 offset:35840
v_mfma_f32_16x16x32_bf16 v[112:115],v[212:215],a[132:135],v[112:115]
ds_read_b64_tr_b16 v[202:203],v225 offset:256
ds_read_b128 v[20:23],v236 offset:32768
v_mfma_f32_16x16x32_bf16 v[120:123],v[208:211],a[184:187],v[120:123]
buffer_load_dwordx4 v232,s[76:79],s95 offen offset:1024 lds
v_mfma_f32_16x16x32_bf16 v[120:123],v[212:215],a[188:191],v[120:123]
ds_read_b64_tr_b16 v[204:205],v225 offset:8192
ds_read_b64_tr_b16 v[206:207],v225 offset:8448
v_mfma_f32_16x16x32_bf16 v[100:103],v[216:219],a[16:19],v[100:103]
ds_read_b128 v[28:31],v236 offset:33792
ds_read_b128 v[36:39],v236 offset:38912
v_mfma_f32_16x16x32_bf16 v[100:103],v[220:223],a[20:23],v[100:103]
ds_read_b128 v[44:47],v236 offset:39936
ds_read_b128 v[52:55],v236 offset:36864
v_mfma_f32_16x16x32_bf16 v[108:111],v[216:219],a[72:75],v[108:111]
buffer_load_dwordx4 v233,s[80:83],s95 offen offset:2048 lds
v_mfma_f32_16x16x32_bf16 v[108:111],v[220:223],a[76:79],v[108:111]
ds_read_b128 v[60:63],v236 offset:37888
v_mul_f32 v239,v239,v158
v_mfma_f32_16x16x32_bf16 v[116:119],v[216:219],a[128:131],v[116:119]
v_rcp_f32 v240,v240
v_mfma_f32_16x16x32_bf16 v[116:119],v[220:223],a[132:135],v[116:119]
v_mul_f32 v180,v180,v240
v_mul_f32 v240,v159,v159
v_mfma_f32_16x16x32_bf16 v[124:127],v[216:219],a[184:187],v[124:127]
buffer_load_dwordx4 v233,s[84:87],s95 offen offset:3072 lds
v_mfma_f32_16x16x32_bf16 v[124:127],v[220:223],a[188:191],v[124:127]
s_waitcnt lgkmcnt(5)
v_fma_f32 v240,v240,s100,v234
v_mul_f32 v240,v240,v159
v_mfma_f32_16x16x32_bf16 v[64:67],v[192:195],a[24:27],v[64:67]
s_add_u32 m0,s92,0x14000
ds_read_b64_tr_b16 v[208:209],v224 offset:1024
s_mov_b32 s95,0x300300
v_mfma_f32_16x16x32_bf16 v[64:67],v[196:199],a[28:31],v[64:67]
ds_read_b64_tr_b16 v[210:211],v224 offset:1280
v_cvt_pk_bf16_f32 v169,v170,v171
v_mfma_f32_16x16x32_bf16 v[72:75],v[192:195],a[80:83],v[72:75]
buffer_load_dwordx4 v232,s[72:75],s95 offen lds
v_mfma_f32_16x16x32_bf16 v[72:75],v[196:199],a[84:87],v[72:75]
ds_read_b64_tr_b16 v[212:213],v224 offset:9216
ds_read_b64_tr_b16 v[214:215],v224 offset:9472
v_mfma_f32_16x16x32_bf16 v[80:83],v[192:195],a[136:139],v[80:83]
ds_read_b64_tr_b16 v[216:217],v225 offset:1024
v_cvt_pk_bf16_f32 v170,v172,v173
v_mfma_f32_16x16x32_bf16 v[80:83],v[196:199],a[140:143],v[80:83]
ds_read_b64_tr_b16 v[218:219],v225 offset:1280
v_cvt_pk_bf16_f32 v171,v174,v175
v_mfma_f32_16x16x32_bf16 v[88:91],v[192:195],a[192:195],v[88:91]
buffer_load_dwordx4 v232,s[76:79],s95 offen offset:1024 lds
v_mfma_f32_16x16x32_bf16 v[88:91],v[196:199],a[196:199],v[88:91]
ds_read_b64_tr_b16 v[220:221],v225 offset:9216
ds_read_b64_tr_b16 v[222:223],v225 offset:9472
v_mfma_f32_16x16x32_bf16 v[68:71],v[200:203],a[24:27],v[68:71]
buffer_store_dwordx4 v[160:163],v254,s[88:91],s99 offen sc1
v_mfma_f32_16x16x32_bf16 v[68:71],v[204:207],a[28:31],v[68:71]
s_add_u32 s99,s97,s98
buffer_store_dwordx4 v[168:171],v254,s[88:91],s99 offen sc1
v_mfma_f32_16x16x32_bf16 v[76:79],v[200:203],a[80:83],v[76:79]
s_lshr_b32 s98,%[h],2
buffer_load_dwordx4 v233,s[80:83],s95 offen offset:2048 lds
v_mfma_f32_16x16x32_bf16 v[76:79],v[204:207],a[84:87],v[76:79]
s_mul_i32 s98,s98,28
v_cvt_pk_bf16_f32 v176,v176,v177
v_cvt_pk_bf16_f32 v177,v178,v179
v_mfma_f32_16x16x32_bf16 v[84:87],v[200:203],a[136:139],v[84:87]
s_add_u32 s98,s98,s101
v_exp_f32 v241,v241
v_mfma_f32_16x16x32_bf16 v[84:87],v[204:207],a[140:143],v[84:87]
s_and_b32 s101,%[w],2
v_exp_f32 v242,v242
v_mfma_f32_16x16x32_bf16 v[92:95],v[200:203],a[192:195],v[92:95]
s_add_u32 s98,s98,s101
buffer_load_dwordx4 v233,s[84:87],s95 offen offset:3072 lds
v_mfma_f32_16x16x32_bf16 v[92:95],v[204:207],a[196:199],v[92:95]
s_waitcnt vmcnt(18) lgkmcnt(0)
v_add_f32 v241,1.0,v241
v_add_f32 v242,1.0,v242
v_mfma_f32_16x16x32_bf16 v[96:99],v[208:211],a[24:27],v[96:99]
s_barrier
ds_read_b64_tr_b16 v[192:193],v224 offset:32768
s_add_u32 m0,s92,0x0
v_mfma_f32_16x16x32_bf16 v[96:99],v[212:215],a[28:31],v[96:99]
s_and_b32 s101,%[h],2
ds_read_b64_tr_b16 v[194:195],v224 offset:33024
v_mfma_f32_16x16x32_bf16 v[104:107],v[208:211],a[80:83],v[104:107]
s_mul_i32 s101,s101,8
buffer_load_dwordx4 v252,s[24:27],s93 offen lds
v_mfma_f32_16x16x32_bf16 v[104:107],v[212:215],a[84:87],v[104:107]
s_add_u32 s98,s98,s101
ds_read_b64_tr_b16 v[196:197],v224 offset:40960
ds_read_b64_tr_b16 v[198:199],v224 offset:41216
v_mfma_f32_16x16x32_bf16 v[112:115],v[208:211],a[136:139],v[112:115]
s_add_u32 s98,s98,1
ds_read_b64_tr_b16 v[200:201],v225 offset:32768
v_mfma_f32_16x16x32_bf16 v[112:115],v[212:215],a[140:143],v[112:115]
s_lshl_b32 s98,s98,10
ds_read_b64_tr_b16 v[202:203],v225 offset:33024
v_mfma_f32_16x16x32_bf16 v[120:123],v[208:211],a[192:195],v[120:123]
s_add_u32 s99,s96,s98
buffer_load_dwordx4 v252,s[28:31],s93 offen offset:1024 lds
v_mfma_f32_16x16x32_bf16 v[120:123],v[212:215],a[196:199],v[120:123]
ds_read_b64_tr_b16 v[204:205],v225 offset:40960
ds_read_b64_tr_b16 v[206:207],v225 offset:41216
v_mfma_f32_16x16x32_bf16 v[100:103],v[216:219],a[24:27],v[100:103]
v_rcp_f32 v241,v241
v_mfma_f32_16x16x32_bf16 v[100:103],v[220:223],a[28:31],v[100:103]
v_mul_f32 v181,v181,v241
v_cvt_pk_bf16_f32 v178,v180,v181
v_mfma_f32_16x16x32_bf16 v[108:111],v[216:219],a[80:83],v[108:111]
buffer_load_dwordx4 v252,s[32:35],s93 offen offset:2048 lds
v_mfma_f32_16x16x32_bf16 v[108:111],v[220:223],a[84:87],v[108:111]
v_rcp_f32 v242,v242
v_mfma_f32_16x16x32_bf16 v[116:119],v[216:219],a[136:139],v[116:119]
v_mul_f32 v182,v182,v242
v_mov_b32 v241,0x3f803f80
v_mfma_f32_16x16x32_bf16 v[116:119],v[220:223],a[140:143],v[116:119]
v_exp_f32 v243,v243
v_mfma_f32_16x16x32_bf16 v[124:127],v[216:219],a[192:195],v[124:127]
buffer_load_dwordx4 v252,s[36:39],s93 offen offset:3072 lds
v_mfma_f32_16x16x32_bf16 v[124:127],v[220:223],a[196:199],v[124:127]
s_add_u32 s93,s93,16384
s_waitcnt lgkmcnt(0)
v_add_f32 v243,1.0,v243
v_mfma_f32_16x16x32_bf16 v[64:67],v[192:195],a[32:35],v[64:67]
s_add_u32 m0,s92,0x4000
ds_read_b64_tr_b16 v[208:209],v224 offset:33792
v_mov_b32 v242,0x3f803f80
v_mfma_f32_16x16x32_bf16 v[64:67],v[196:199],a[36:39],v[64:67]
ds_read_b64_tr_b16 v[210:211],v224 offset:34048
v_mfma_f32_16x16x32_bf16 v[72:75],v[192:195],a[88:91],v[72:75]
buffer_load_dwordx4 v252,s[24:27],s93 offen lds
v_mfma_f32_16x16x32_bf16 v[72:75],v[196:199],a[92:95],v[72:75]
ds_read_b64_tr_b16 v[212:213],v224 offset:41984
ds_read_b64_tr_b16 v[214:215],v224 offset:42240
v_mfma_f32_16x16x32_bf16 v[80:83],v[192:195],a[144:147],v[80:83]
ds_read_b64_tr_b16 v[216:217],v225 offset:33792
v_mfma_f32_16x16x32_bf16 v[80:83],v[196:199],a[148:151],v[80:83]
ds_read_b64_tr_b16 v[218:219],v225 offset:34048
v_mfma_f32_16x16x32_bf16 v[88:91],v[192:195],a[200:203],v[88:91]
buffer_load_dwordx4 v252,s[28:31],s93 offen offset:1024 lds
v_mfma_f32_16x16x32_bf16 v[88:91],v[196:199],a[204:207],v[88:91]
ds_read_b64_tr_b16 v[220:221],v225 offset:41984
ds_read_b64_tr_b16 v[222:223],v225 offset:42240
v_mfma_f32_16x16x32_bf16 v[68:71],v[200:203],a[32:35],v[68:71]
v_rcp_f32 v243,v243
v_mfma_f32_16x16x32_bf16 v[68:71],v[204:207],a[36:39],v[68:71]
v_mul_f32 v183,v183,v243
v_cvt_pk_bf16_f32 v179,v182,v183
v_mfma_f32_16x16x32_bf16 v[76:79],v[200:203],a[88:91],v[76:79]
buffer_load_dwordx4 v252,s[32:35],s93 offen offset:2048 lds
v_mfma_f32_16x16x32_bf16 v[76:79],v[204:207],a[92:95],v[76:79]
buffer_store_dwordx4 v[176:179],v254,s[88:91],s99 offen sc1
v_mfma_f32_16x16x32_bf16 v[84:87],v[200:203],a[144:147],v[84:87]
s_add_u32 s99,s97,s98
v_exp_f32 v244,v244
v_mfma_f32_16x16x32_bf16 v[84:87],v[204:207],a[148:151],v[84:87]
v_add_f32 v244,1.0,v244
v_mov_b32 v243,0x3f803f80
v_mfma_f32_16x16x32_bf16 v[92:95],v[200:203],a[200:203],v[92:95]
buffer_load_dwordx4 v252,s[36:39],s93 offen offset:3072 lds
v_mfma_f32_16x16x32_bf16 v[92:95],v[204:207],a[204:207],v[92:95]
s_add_u32 s93,s93,16384
s_waitcnt vmcnt(19) lgkmcnt(0)
v_mfma_f32_16x16x32_bf16 v[96:99],v[208:211],a[32:35],v[96:99]
s_barrier
ds_read_b64_tr_b16 v[192:193],v227 offset:32768
s_add_u32 m0,s92,0x8000
v_mfma_f32_16x16x32_bf16 v[96:99],v[212:215],a[36:39],v[96:99]
ds_read_b64_tr_b16 v[194:195],v227 offset:33024
v_mfma_f32_16x16x32_bf16 v[104:107],v[208:211],a[88:91],v[104:107]
buffer_load_dwordx4 v252,s[24:27],s93 offen lds
v_mfma_f32_16x16x32_bf16 v[104:107],v[212:215],a[92:95],v[104:107]
ds_read_b64_tr_b16 v[196:197],v227 offset:40960
ds_read_b64_tr_b16 v[198:199],v227 offset:41216
v_mfma_f32_16x16x32_bf16 v[112:115],v[208:211],a[144:147],v[112:115]
ds_read_b64_tr_b16 v[200:201],v228 offset:32768
v_mfma_f32_16x16x32_bf16 v[112:115],v[212:215],a[148:151],v[112:115]
ds_read_b64_tr_b16 v[202:203],v228 offset:33024
v_mfma_f32_16x16x32_bf16 v[120:123],v[208:211],a[200:203],v[120:123]
buffer_load_dwordx4 v252,s[28:31],s93 offen offset:1024 lds
v_mfma_f32_16x16x32_bf16 v[120:123],v[212:215],a[204:207],v[120:123]
ds_read_b64_tr_b16 v[204:205],v228 offset:40960
ds_read_b64_tr_b16 v[206:207],v228 offset:41216
v_mfma_f32_16x16x32_bf16 v[100:103],v[216:219],a[32:35],v[100:103]
v_rcp_f32 v244,v244
v_mfma_f32_16x16x32_bf16 v[100:103],v[220:223],a[36:39],v[100:103]
v_exp_f32 v245,v245
v_mfma_f32_16x16x32_bf16 v[108:111],v[216:219],a[88:91],v[108:111]
buffer_load_dwordx4 v252,s[32:35],s93 offen offset:2048 lds
v_mfma_f32_16x16x32_bf16 v[108:111],v[220:223],a[92:95],v[108:111]
v_mul_f32 v184,v184,v244
)"
R"(v_add_f32 v245,1.0,v245
v_mfma_f32_16x16x32_bf16 v[116:119],v[216:219],a[144:147],v[116:119]
v_rcp_f32 v245,v245
v_mfma_f32_16x16x32_bf16 v[116:119],v[220:223],a[148:151],v[116:119]
v_mul_f32 v185,v185,v245
v_cvt_pk_bf16_f32 v184,v184,v185
v_mfma_f32_16x16x32_bf16 v[124:127],v[216:219],a[200:203],v[124:127]
buffer_load_dwordx4 v252,s[36:39],s93 offen offset:3072 lds
v_mfma_f32_16x16x32_bf16 v[124:127],v[220:223],a[204:207],v[124:127]
s_add_u32 s93,s93,16384
s_waitcnt lgkmcnt(0)
v_mfma_f32_16x16x32_bf16 v[64:67],v[192:195],a[40:43],v[64:67]
s_add_u32 m0,s92,0xc000
ds_read_b64_tr_b16 v[208:209],v227 offset:33792
v_mfma_f32_16x16x32_bf16 v[64:67],v[196:199],a[44:47],v[64:67]
ds_read_b64_tr_b16 v[210:211],v227 offset:34048
v_mfma_f32_16x16x32_bf16 v[72:75],v[192:195],a[96:99],v[72:75]
buffer_load_dwordx4 v252,s[24:27],s93 offen lds
v_mfma_f32_16x16x32_bf16 v[72:75],v[196:199],a[100:103],v[72:75]
ds_read_b64_tr_b16 v[212:213],v227 offset:41984
ds_read_b64_tr_b16 v[214:215],v227 offset:42240
v_mfma_f32_16x16x32_bf16 v[80:83],v[192:195],a[152:155],v[80:83]
ds_read_b64_tr_b16 v[216:217],v228 offset:33792
v_mfma_f32_16x16x32_bf16 v[80:83],v[196:199],a[156:159],v[80:83]
ds_read_b64_tr_b16 v[218:219],v228 offset:34048
v_mfma_f32_16x16x32_bf16 v[88:91],v[192:195],a[208:211],v[88:91]
buffer_load_dwordx4 v252,s[28:31],s93 offen offset:1024 lds
v_mfma_f32_16x16x32_bf16 v[88:91],v[196:199],a[212:215],v[88:91]
ds_read_b64_tr_b16 v[220:221],v228 offset:41984
ds_read_b64_tr_b16 v[222:223],v228 offset:42240
v_mfma_f32_16x16x32_bf16 v[68:71],v[200:203],a[40:43],v[68:71]
v_exp_f32 v246,v246
v_mfma_f32_16x16x32_bf16 v[68:71],v[204:207],a[44:47],v[68:71]
v_exp_f32 v247,v247
v_mfma_f32_16x16x32_bf16 v[76:79],v[200:203],a[96:99],v[76:79]
buffer_load_dwordx4 v252,s[32:35],s93 offen offset:2048 lds
v_mfma_f32_16x16x32_bf16 v[76:79],v[204:207],a[100:103],v[76:79]
v_add_f32 v246,1.0,v246
v_add_f32 v247,1.0,v247
v_mfma_f32_16x16x32_bf16 v[84:87],v[200:203],a[152:155],v[84:87]
v_rcp_f32 v246,v246
v_mfma_f32_16x16x32_bf16 v[84:87],v[204:207],a[156:159],v[84:87]
v_rcp_f32 v247,v247
v_mfma_f32_16x16x32_bf16 v[92:95],v[200:203],a[208:211],v[92:95]
buffer_load_dwordx4 v252,s[36:39],s93 offen offset:3072 lds
v_mfma_f32_16x16x32_bf16 v[92:95],v[204:207],a[212:215],v[92:95]
s_add_u32 s93,s93,16384
s_waitcnt vmcnt(17) lgkmcnt(0)
v_mul_f32 v186,v186,v246
v_mfma_f32_16x16x32_bf16 v[96:99],v[208:211],a[40:43],v[96:99]
s_barrier
ds_read_b64_tr_b16 v[192:193],v227
v_mul_f32 v187,v187,v247
v_mfma_f32_16x16x32_bf16 v[96:99],v[212:215],a[44:47],v[96:99]
ds_read_b64_tr_b16 v[194:195],v227 offset:256
v_cvt_pk_bf16_f32 v185,v186,v187
v_mfma_f32_16x16x32_bf16 v[104:107],v[208:211],a[96:99],v[104:107]
ds_read_b64_tr_b16 v[196:197],v227 offset:8192
v_mfma_f32_16x16x32_bf16 v[104:107],v[212:215],a[100:103],v[104:107]
ds_read_b64_tr_b16 v[198:199],v227 offset:8448
v_mfma_f32_16x16x32_bf16 v[112:115],v[208:211],a[152:155],v[112:115]
ds_read_b64_tr_b16 v[200:201],v228
v_mfma_f32_16x16x32_bf16 v[112:115],v[212:215],a[156:159],v[112:115]
ds_read_b64_tr_b16 v[202:203],v228 offset:256
v_mfma_f32_16x16x32_bf16 v[120:123],v[208:211],a[208:211],v[120:123]
ds_read_b64_tr_b16 v[204:205],v228 offset:8192
v_mfma_f32_16x16x32_bf16 v[120:123],v[212:215],a[212:215],v[120:123]
ds_read_b64_tr_b16 v[206:207],v228 offset:8448
v_mfma_f32_16x16x32_bf16 v[100:103],v[216:219],a[40:43],v[100:103]
v_exp_f32 v237,v237
v_mfma_f32_16x16x32_bf16 v[100:103],v[220:223],a[44:47],v[100:103]
buffer_load_dword v230,v230,s[56:59],0 offen
v_mfma_f32_16x16x32_bf16 v[108:111],v[216:219],a[96:99],v[108:111]
v_exp_f32 v238,v238
v_mfma_f32_16x16x32_bf16 v[108:111],v[220:223],a[100:103],v[108:111]
v_add_f32 v237,1.0,v237
v_add_f32 v238,1.0,v238
v_mfma_f32_16x16x32_bf16 v[116:119],v[216:219],a[152:155],v[116:119]
buffer_load_dword v231,v231,s[60:63],0 offen
v_mfma_f32_16x16x32_bf16 v[116:119],v[220:223],a[156:159],v[116:119]
v_rcp_f32 v237,v237
v_mfma_f32_16x16x32_bf16 v[124:127],v[216:219],a[208:211],v[124:127]
v_rcp_f32 v238,v238
v_mfma_f32_16x16x32_bf16 v[124:127],v[220:223],a[212:215],v[124:127]
s_waitcnt lgkmcnt(0)
v_mul_f32 v188,v188,v237
v_mul_f32 v189,v189,v238
v_mfma_f32_16x16x32_bf16 v[64:67],v[192:195],a[48:51],v[64:67]
ds_read_b64_tr_b16 v[208:209],v227 offset:1024
v_cvt_pk_bf16_f32 v186,v188,v189
v_mfma_f32_16x16x32_bf16 v[64:67],v[196:199],a[52:55],v[64:67]
ds_read_b64_tr_b16 v[210:211],v227 offset:1280
v_mfma_f32_16x16x32_bf16 v[72:75],v[192:195],a[104:107],v[72:75]
ds_read_b64_tr_b16 v[212:213],v227 offset:9216
v_mfma_f32_16x16x32_bf16 v[72:75],v[196:199],a[108:111],v[72:75]
ds_read_b64_tr_b16 v[214:215],v227 offset:9472
v_mfma_f32_16x16x32_bf16 v[80:83],v[192:195],a[160:163],v[80:83]
ds_read_b64_tr_b16 v[216:217],v228 offset:1024
v_mfma_f32_16x16x32_bf16 v[80:83],v[196:199],a[164:167],v[80:83]
ds_read_b64_tr_b16 v[218:219],v228 offset:1280
v_cvt_pk_bf16_f32 v64,v64,v65
v_mfma_f32_16x16x32_bf16 v[88:91],v[192:195],a[216:219],v[88:91]
ds_read_b64_tr_b16 v[220:221],v228 offset:9216
v_cvt_pk_bf16_f32 v65,v66,v67
v_mfma_f32_16x16x32_bf16 v[88:91],v[196:199],a[220:223],v[88:91]
ds_read_b64_tr_b16 v[222:223],v228 offset:9472
v_cvt_pk_bf16_f32 v72,v72,v73
v_mfma_f32_16x16x32_bf16 v[68:71],v[200:203],a[48:51],v[68:71]
v_cvt_pk_bf16_f32 v73,v74,v75
v_cvt_pk_bf16_f32 v80,v80,v81
v_mfma_f32_16x16x32_bf16 v[68:71],v[204:207],a[52:55],v[68:71]
v_exp_f32 v239,v239
v_mfma_f32_16x16x32_bf16 v[76:79],v[200:203],a[104:107],v[76:79]
v_cvt_pk_bf16_f32 v81,v82,v83
v_cvt_pk_bf16_f32 v88,v88,v89
v_mfma_f32_16x16x32_bf16 v[76:79],v[204:207],a[108:111],v[76:79]
v_cvt_pk_bf16_f32 v89,v90,v91
v_add_f32 v239,1.0,v239
v_mfma_f32_16x16x32_bf16 v[84:87],v[200:203],a[160:163],v[84:87]
v_cvt_pk_bf16_f32 v66,v68,v69
v_cvt_pk_bf16_f32 v67,v70,v71
v_mfma_f32_16x16x32_bf16 v[84:87],v[204:207],a[164:167],v[84:87]
v_rcp_f32 v239,v239
v_mfma_f32_16x16x32_bf16 v[92:95],v[200:203],a[216:219],v[92:95]
v_permlane16_swap_b32 v64,v66
v_permlane16_swap_b32 v65,v67
v_mfma_f32_16x16x32_bf16 v[92:95],v[204:207],a[220:223],v[92:95]
s_waitcnt vmcnt(10) lgkmcnt(0)
ds_write_b128 v235,v[64:67] offset:32768
v_cvt_pk_bf16_f32 v74,v76,v77
v_mfma_f32_16x16x32_bf16 v[96:99],v[208:211],a[48:51],v[96:99]
s_barrier
ds_read_b128 v[192:195],v248
v_cvt_pk_bf16_f32 v75,v78,v79
v_mfma_f32_16x16x32_bf16 v[96:99],v[212:215],a[52:55],v[96:99]
v_permlane16_swap_b32 v72,v74
v_permlane16_swap_b32 v73,v75
v_mfma_f32_16x16x32_bf16 v[104:107],v[208:211],a[104:107],v[104:107]
ds_read_b128 v[196:199],v249
ds_write_b128 v235,v[72:75] offset:33792
v_mfma_f32_16x16x32_bf16 v[104:107],v[212:215],a[108:111],v[104:107]
v_cvt_pk_bf16_f32 v82,v84,v85
v_cvt_pk_bf16_f32 v83,v86,v87
v_mfma_f32_16x16x32_bf16 v[112:115],v[208:211],a[160:163],v[112:115]
ds_read_b128 v[200:203],v248 offset:1024
v_permlane16_swap_b32 v80,v82
v_mfma_f32_16x16x32_bf16 v[112:115],v[212:215],a[164:167],v[112:115]
s_add_u32 m0,s92,0x10000
v_permlane16_swap_b32 v81,v83
v_cvt_pk_bf16_f32 v90,v92,v93
v_mfma_f32_16x16x32_bf16 v[120:123],v[208:211],a[216:219],v[120:123]
ds_read_b128 v[204:207],v249 offset:1024
ds_write_b128 v235,v[80:83] offset:34816
v_mfma_f32_16x16x32_bf16 v[120:123],v[212:215],a[220:223],v[120:123]
v_cvt_pk_bf16_f32 v91,v94,v95
v_permlane16_swap_b32 v88,v90
v_mfma_f32_16x16x32_bf16 v[100:103],v[216:219],a[48:51],v[100:103]
v_permlane16_swap_b32 v89,v91
v_cvt_pk_bf16_f32 v96,v96,v97
v_mfma_f32_16x16x32_bf16 v[100:103],v[220:223],a[52:55],v[100:103]
buffer_load_dwordx4 v253,s[40:43],s94 offen lds
v_mfma_f32_16x16x32_bf16 v[108:111],v[216:219],a[104:107],v[108:111]
ds_write_b128 v235,v[88:91] offset:35840
v_cvt_pk_bf16_f32 v97,v98,v99
v_mfma_f32_16x16x32_bf16 v[108:111],v[220:223],a[108:111],v[108:111]
v_cvt_pk_bf16_f32 v104,v104,v105
v_cvt_pk_bf16_f32 v105,v106,v107
v_mfma_f32_16x16x32_bf16 v[116:119],v[216:219],a[160:163],v[116:119]
buffer_load_dwordx4 v253,s[44:47],s94 offen offset:1024 lds
v_mfma_f32_16x16x32_bf16 v[116:119],v[220:223],a[164:167],v[116:119]
v_cvt_pk_bf16_f32 v98,v100,v101
v_cvt_pk_bf16_f32 v99,v102,v103
v_mfma_f32_16x16x32_bf16 v[124:127],v[216:219],a[216:219],v[124:127]
v_permlane16_swap_b32 v96,v98
v_permlane16_swap_b32 v97,v99
v_mfma_f32_16x16x32_bf16 v[124:127],v[220:223],a[220:223],v[124:127]
s_waitcnt lgkmcnt(2)
ds_write_b128 v235,v[96:99] offset:36864
v_cvt_pk_bf16_f32 v106,v108,v109
v_mfma_f32_16x16x32_bf16 v[128:131],v[192:195],v[0:3],0
ds_read_b128 v[208:211],v248 offset:8192
v_cvt_pk_bf16_f32 v107,v110,v111
v_mfma_f32_16x16x32_bf16 v[128:131],v[196:199],v[4:7],v[128:131]
v_permlane16_swap_b32 v104,v106
v_permlane16_swap_b32 v105,v107
v_mfma_f32_16x16x32_bf16 v[128:131],v[200:203],v[32:35],v[128:131]
ds_read_b128 v[212:215],v249 offset:8192
ds_write_b128 v235,v[104:107] offset:37888
v_mfma_f32_16x16x32_bf16 v[128:131],v[204:207],v[36:39],v[128:131]
v_cvt_pk_bf16_f32 v112,v112,v113
v_cvt_pk_bf16_f32 v113,v114,v115
v_mfma_f32_16x16x32_bf16 v[136:139],v[192:195],v[8:11],0
ds_read_b128 v[216:219],v248 offset:9216
v_cvt_pk_bf16_f32 v114,v116,v117
v_mfma_f32_16x16x32_bf16 v[136:139],v[196:199],v[12:15],v[136:139]
v_cvt_pk_bf16_f32 v115,v118,v119
v_permlane16_swap_b32 v112,v114
v_mfma_f32_16x16x32_bf16 v[136:139],v[200:203],v[40:43],v[136:139]
ds_read_b128 v[220:223],v249 offset:9216
v_permlane16_swap_b32 v113,v115
)"
R"(v_mfma_f32_16x16x32_bf16 v[136:139],v[204:207],v[44:47],v[136:139]
ds_write_b128 v235,v[112:115] offset:38912
v_cvt_pk_bf16_f32 v120,v120,v121
v_mfma_f32_16x16x32_bf16 v[144:147],v[192:195],v[16:19],0
v_cvt_pk_bf16_f32 v121,v122,v123
v_cvt_pk_bf16_f32 v122,v124,v125
v_mfma_f32_16x16x32_bf16 v[144:147],v[196:199],v[20:23],v[144:147]
buffer_load_dwordx4 v253,s[48:51],s94 offen offset:2048 lds
v_mfma_f32_16x16x32_bf16 v[144:147],v[200:203],v[48:51],v[144:147]
v_cvt_pk_bf16_f32 v123,v126,v127
v_permlane16_swap_b32 v120,v122
v_mfma_f32_16x16x32_bf16 v[144:147],v[204:207],v[52:55],v[144:147]
v_permlane16_swap_b32 v121,v123
v_mul_f32 v190,v190,v239
v_mfma_f32_16x16x32_bf16 v[152:155],v[192:195],v[24:27],0
buffer_load_dwordx4 v253,s[52:55],s94 offen offset:3072 lds
v_mfma_f32_16x16x32_bf16 v[152:155],v[196:199],v[28:31],v[152:155]
s_add_u32 s94,s94,16384
ds_write_b128 v235,v[120:123] offset:39936
v_mfma_f32_16x16x32_bf16 v[152:155],v[200:203],v[56:59],v[152:155]
v_exp_f32 v240,v240
v_mfma_f32_16x16x32_bf16 v[152:155],v[204:207],v[60:63],v[152:155]
s_waitcnt vmcnt(6) lgkmcnt(0)
v_add_f32 v240,1.0,v240
v_mfma_f32_16x16x32_bf16 v[132:135],v[208:211],v[0:3],0
s_barrier
ds_read_b128 v[192:195],v248 offset:32768
v_mfma_f32_16x16x32_bf16 v[132:135],v[212:215],v[4:7],v[132:135]
v_rcp_f32 v240,v240
v_mfma_f32_16x16x32_bf16 v[132:135],v[216:219],v[32:35],v[132:135]
ds_read_b128 v[196:199],v249 offset:32768
v_mul_f32 v191,v191,v240
v_mfma_f32_16x16x32_bf16 v[132:135],v[220:223],v[36:39],v[132:135]
v_cvt_pk_bf16_f32 v187,v190,v191
v_mov_b32 v240,0x3f803f80
v_mfma_f32_16x16x32_bf16 v[140:143],v[208:211],v[8:11],0
ds_read_b128 v[200:203],v248 offset:33792
v_mfma_f32_16x16x32_bf16 v[140:143],v[212:215],v[12:15],v[140:143]
s_add_u32 m0,s92,0x14000
buffer_store_dwordx4 v[184:187],v254,s[88:91],s99 offen sc1
v_mfma_f32_16x16x32_bf16 v[140:143],v[216:219],v[40:43],v[140:143]
ds_read_b128 v[204:207],v249 offset:33792
v_mfma_f32_16x16x32_bf16 v[140:143],v[220:223],v[44:47],v[140:143]
v_mfma_f32_16x16x32_bf16 v[148:151],v[208:211],v[16:19],0
v_mfma_f32_16x16x32_bf16 v[148:151],v[212:215],v[20:23],v[148:151]
buffer_load_dwordx4 v253,s[40:43],s94 offen lds
v_mfma_f32_16x16x32_bf16 v[148:151],v[216:219],v[48:51],v[148:151]
v_mfma_f32_16x16x32_bf16 v[148:151],v[220:223],v[52:55],v[148:151]
v_mfma_f32_16x16x32_bf16 v[156:159],v[208:211],v[24:27],0
buffer_load_dwordx4 v253,s[44:47],s94 offen offset:1024 lds
v_mfma_f32_16x16x32_bf16 v[156:159],v[212:215],v[28:31],v[156:159]
v_mfma_f32_16x16x32_bf16 v[156:159],v[216:219],v[56:59],v[156:159]
v_mfma_f32_16x16x32_bf16 v[156:159],v[220:223],v[60:63],v[156:159]
s_waitcnt lgkmcnt(0)
v_mfma_f32_16x16x32_bf16 v[160:163],v[192:195],v[0:3],0
ds_read_b128 v[208:211],v248 offset:40960
v_mfma_f32_16x16x32_bf16 v[160:163],v[196:199],v[4:7],v[160:163]
v_mfma_f32_16x16x32_bf16 v[160:163],v[200:203],v[32:35],v[160:163]
ds_read_b128 v[212:215],v249 offset:40960
v_mfma_f32_16x16x32_bf16 v[160:163],v[204:207],v[36:39],v[160:163]
v_mfma_f32_16x16x32_bf16 v[168:171],v[192:195],v[8:11],0
ds_read_b128 v[216:219],v248 offset:41984
v_mfma_f32_16x16x32_bf16 v[168:171],v[196:199],v[12:15],v[168:171]
v_mfma_f32_16x16x32_bf16 v[168:171],v[200:203],v[40:43],v[168:171]
ds_read_b128 v[220:223],v249 offset:41984
v_mfma_f32_16x16x32_bf16 v[168:171],v[204:207],v[44:47],v[168:171]
v_mfma_f32_16x16x32_bf16 v[176:179],v[192:195],v[16:19],0
v_mfma_f32_16x16x32_bf16 v[176:179],v[196:199],v[20:23],v[176:179]
s_waitcnt lgkmcnt(0)
buffer_load_dwordx4 v253,s[48:51],s94 offen offset:2048 lds
v_mfma_f32_16x16x32_bf16 v[176:179],v[200:203],v[48:51],v[176:179]
s_barrier
ds_read_b128 v[68:71],v236 offset:34816
ds_read_b128 v[76:79],v236 offset:35840
v_mfma_f32_16x16x32_bf16 v[176:179],v[204:207],v[52:55],v[176:179]
ds_read_b128 v[84:87],v236 offset:32768
ds_read_b128 v[92:95],v236 offset:33792
v_mfma_f32_16x16x32_bf16 v[184:187],v[192:195],v[24:27],0
buffer_load_dwordx4 v253,s[52:55],s94 offen offset:3072 lds
v_mfma_f32_16x16x32_bf16 v[184:187],v[196:199],v[28:31],v[184:187]
s_add_u32 s94,s94,16384
ds_read_b128 v[100:103],v236 offset:38912
ds_read_b128 v[108:111],v236 offset:39936
v_mfma_f32_16x16x32_bf16 v[184:187],v[200:203],v[56:59],v[184:187]
ds_read_b128 v[116:119],v236 offset:36864
ds_read_b128 v[124:127],v236 offset:37888
v_mfma_f32_16x16x32_bf16 v[184:187],v[204:207],v[60:63],v[184:187]
s_waitcnt lgkmcnt(8)
v_mfma_f32_16x16x32_bf16 v[164:167],v[208:211],v[0:3],0
ds_read_b128 v[192:195],v248 offset:2048
v_mfma_f32_16x16x32_bf16 v[164:167],v[212:215],v[4:7],v[164:167]
v_mfma_f32_16x16x32_bf16 v[164:167],v[216:219],v[32:35],v[164:167]
ds_read_b128 v[196:199],v249 offset:2048
v_mfma_f32_16x16x32_bf16 v[164:167],v[220:223],v[36:39],v[164:167]
v_mfma_f32_16x16x32_bf16 v[172:175],v[208:211],v[8:11],0
ds_read_b128 v[200:203],v248 offset:3072
v_mfma_f32_16x16x32_bf16 v[172:175],v[212:215],v[12:15],v[172:175]
s_add_u32 m0,s92,0x18000
v_mfma_f32_16x16x32_bf16 v[172:175],v[216:219],v[40:43],v[172:175]
ds_read_b128 v[204:207],v249 offset:3072
v_mfma_f32_16x16x32_bf16 v[172:175],v[220:223],v[44:47],v[172:175]
v_mfma_f32_16x16x32_bf16 v[180:183],v[208:211],v[16:19],0
v_mfma_f32_16x16x32_bf16 v[180:183],v[212:215],v[20:23],v[180:183]
buffer_load_dwordx4 v253,s[40:43],s94 offen lds
v_mfma_f32_16x16x32_bf16 v[180:183],v[216:219],v[48:51],v[180:183]
v_mfma_f32_16x16x32_bf16 v[180:183],v[220:223],v[52:55],v[180:183]
v_mfma_f32_16x16x32_bf16 v[188:191],v[208:211],v[24:27],0
buffer_load_dwordx4 v253,s[44:47],s94 offen offset:1024 lds
v_mfma_f32_16x16x32_bf16 v[188:191],v[212:215],v[28:31],v[188:191]
v_mfma_f32_16x16x32_bf16 v[188:191],v[216:219],v[56:59],v[188:191]
v_mfma_f32_16x16x32_bf16 v[188:191],v[220:223],v[60:63],v[188:191]
s_waitcnt lgkmcnt(0)
v_mfma_f32_16x16x32_bf16 v[128:131],v[192:195],v[64:67],v[128:131]
s_waitcnt lgkmcnt(4)
ds_read_b128 v[208:211],v248 offset:10240
v_accvgpr_read_b32 v224,a255
v_mfma_f32_16x16x32_bf16 v[128:131],v[196:199],v[68:71],v[128:131]
s_waitcnt vmcnt(63)
v_add_u32 v255,v255,v224
v_mfma_f32_16x16x32_bf16 v[128:131],v[200:203],v[96:99],v[128:131]
s_mov_b32 s72,0x3db8aa3b
ds_read_b128 v[212:215],v249 offset:10240
v_mfma_f32_16x16x32_bf16 v[128:131],v[204:207],v[100:103],v[128:131]
s_mov_b32 s73,0xc2000000
v_mfma_f32_16x16x32_bf16 v[136:139],v[192:195],v[72:75],v[136:139]
s_mov_b32 s74,14
ds_read_b128 v[216:219],v248 offset:11264
v_mfma_f32_16x16x32_bf16 v[136:139],v[196:199],v[76:79],v[136:139]
s_lshr_b32 s78,%[h],1
v_readlane_b32 s75,v255,s78
v_mfma_f32_16x16x32_bf16 v[136:139],v[200:203],v[104:107],v[136:139]
s_lshr_b32 s79,%[tb],2
ds_read_b128 v[220:223],v249 offset:11264
v_max3_f32 v244,v128,v129,v130
v_mfma_f32_16x16x32_bf16 v[136:139],v[204:207],v[108:111],v[136:139]
s_lshl_b32 s79,s79,3
v_cvt_pk_bf16_f32 v128,0,v128
v_cvt_pk_bf16_f32 v129,0,v129
v_mfma_f32_16x16x32_bf16 v[144:147],v[192:195],v[80:83],v[144:147]
s_lshr_b32 s78,%[h],1
v_cvt_pk_bf16_f32 v130,0,v130
v_mfma_f32_16x16x32_bf16 v[144:147],v[196:199],v[84:87],v[144:147]
s_add_u32 s79,s79,s78
buffer_load_dwordx4 v253,s[48:51],s94 offen offset:2048 lds
v_mfma_f32_16x16x32_bf16 v[144:147],v[200:203],v[112:115],v[144:147]
s_lshl_b32 s79,s79,7
v_max3_f32 v245,v136,v137,v138
v_cvt_pk_bf16_f32 v136,0,v136
v_mfma_f32_16x16x32_bf16 v[144:147],v[204:207],v[116:119],v[144:147]
s_lshl_b32 s78,%[w],15
v_cvt_pk_bf16_f32 v137,0,v137
v_cvt_pk_bf16_f32 v138,0,v138
v_mfma_f32_16x16x32_bf16 v[152:155],v[192:195],v[88:91],v[152:155]
s_add_u32 s79,s79,s78
buffer_load_dwordx4 v253,s[52:55],s94 offen offset:3072 lds
v_mfma_f32_16x16x32_bf16 v[152:155],v[196:199],v[92:95],v[152:155]
s_add_u32 s94,s94,16384
v_max3_f32 v246,v144,v145,v146
v_cvt_pk_bf16_f32 v144,0,v144
v_mfma_f32_16x16x32_bf16 v[152:155],v[200:203],v[120:123],v[152:155]
s_add_u32 s79,s79,0x2b20000
v_cvt_pk_bf16_f32 v145,0,v145
v_cvt_pk_bf16_f32 v146,0,v146
v_mfma_f32_16x16x32_bf16 v[152:155],v[204:207],v[124:127],v[152:155]
s_waitcnt lgkmcnt(0)
v_mfma_f32_16x16x32_bf16 v[132:135],v[208:211],v[64:67],v[132:135]
s_sub_u32 s78,s79,131072
ds_read_b128 v[192:195],v248 offset:34816
v_mfma_f32_16x16x32_bf16 v[132:135],v[212:215],v[68:71],v[132:135]
s_mov_b64 s[56:57],%[wd]
v_mfma_f32_16x16x32_bf16 v[132:135],v[216:219],v[96:99],v[132:135]
s_mov_b32 s58,0x62000
ds_read_b128 v[196:199],v249 offset:34816
v_max3_f32 v247,v152,v153,v154
v_mfma_f32_16x16x32_bf16 v[132:135],v[220:223],v[100:103],v[132:135]
s_mov_b32 s59,0x20000
v_cvt_pk_bf16_f32 v152,0,v152
v_cvt_pk_bf16_f32 v153,0,v153
v_mfma_f32_16x16x32_bf16 v[140:143],v[208:211],v[72:75],v[140:143]
s_mov_b64 s[60:61],%[wd]
ds_read_b128 v[200:203],v248 offset:35840
v_cvt_pk_bf16_f32 v154,0,v154
v_mfma_f32_16x16x32_bf16 v[140:143],v[212:215],v[76:79],v[140:143]
s_add_u32 m0,s92,0x1c000
v_max3_f32 v244,v131,v132,v244
v_max3_f32 v244,v133,v134,v244
v_mfma_f32_16x16x32_bf16 v[140:143],v[216:219],v[104:107],v[140:143]
s_add_u32 s60,s60,0x1800
s_addc_u32 s61,s61,0
ds_read_b128 v[204:207],v249 offset:35840
v_max_f32 v244,v135,v244
v_mfma_f32_16x16x32_bf16 v[140:143],v[220:223],v[108:111],v[140:143]
s_mov_b32 s62,0x60800
v_cvt_pk_bf16_f32 v131,0,v131
v_cvt_pk_bf16_f32 v132,0,v132
v_mfma_f32_16x16x32_bf16 v[148:151],v[208:211],v[80:83],v[148:151]
s_mov_b32 s63,0x20000
v_cvt_pk_bf16_f32 v133,0,v133
v_cvt_pk_bf16_f32 v134,0,v134
v_mfma_f32_16x16x32_bf16 v[148:151],v[212:215],v[84:87],v[148:151]
)"
R"(s_mov_b64 s[64:65],%[wd]
buffer_load_dwordx4 v253,s[40:43],s94 offen lds
v_mfma_f32_16x16x32_bf16 v[148:151],v[216:219],v[112:115],v[148:151]
s_add_u32 s64,s64,0x3000
s_addc_u32 s65,s65,0
v_max3_f32 v245,v139,v140,v245
v_max3_f32 v245,v141,v142,v245
v_mfma_f32_16x16x32_bf16 v[148:151],v[220:223],v[116:119],v[148:151]
s_mov_b32 s66,0x5f000
v_max_f32 v245,v143,v245
v_cvt_pk_bf16_f32 v139,0,v139
v_mfma_f32_16x16x32_bf16 v[156:159],v[208:211],v[88:91],v[156:159]
s_mov_b32 s67,0x20000
buffer_load_dwordx4 v253,s[44:47],s94 offen offset:1024 lds
v_mfma_f32_16x16x32_bf16 v[156:159],v[212:215],v[92:95],v[156:159]
s_mov_b64 s[68:69],%[wd]
v_max3_f32 v246,v147,v148,v246
v_max3_f32 v246,v149,v150,v246
v_mfma_f32_16x16x32_bf16 v[156:159],v[216:219],v[120:123],v[156:159]
s_add_u32 s68,s68,0x4800
s_addc_u32 s69,s69,0
v_max_f32 v246,v151,v246
v_cvt_pk_bf16_f32 v147,0,v147
v_mfma_f32_16x16x32_bf16 v[156:159],v[220:223],v[124:127],v[156:159]
s_waitcnt lgkmcnt(0)
v_cvt_pk_bf16_f32 v135,0,v135
v_cvt_pk_bf16_f32 v140,0,v140
v_mfma_f32_16x16x32_bf16 v[160:163],v[192:195],v[64:67],v[160:163]
s_mov_b32 s70,0x5d800
ds_read_b128 v[208:211],v248 offset:43008
v_cvt_pk_bf16_f32 v141,0,v141
v_mfma_f32_16x16x32_bf16 v[160:163],v[196:199],v[68:71],v[160:163]
s_mov_b32 s71,0x20000
v_max3_f32 v247,v155,v156,v247
v_max3_f32 v247,v157,v158,v247
v_mfma_f32_16x16x32_bf16 v[160:163],v[200:203],v[96:99],v[160:163]
ds_read_b128 v[212:215],v249 offset:43008
v_max_f32 v247,v159,v247
v_mfma_f32_16x16x32_bf16 v[160:163],v[204:207],v[100:103],v[160:163]
v_cvt_pk_bf16_f32 v155,0,v155
v_cvt_pk_bf16_f32 v142,0,v142
v_mfma_f32_16x16x32_bf16 v[168:171],v[192:195],v[72:75],v[168:171]
ds_read_b128 v[216:219],v248 offset:44032
v_cvt_pk_bf16_f32 v143,0,v143
v_mfma_f32_16x16x32_bf16 v[168:171],v[196:199],v[76:79],v[168:171]
v_cvt_pk_bf16_f32 v148,0,v148
v_cvt_pk_bf16_f32 v149,0,v149
v_mfma_f32_16x16x32_bf16 v[168:171],v[200:203],v[104:107],v[168:171]
ds_read_b128 v[220:223],v249 offset:44032
v_cvt_pk_bf16_f32 v150,0,v150
v_mfma_f32_16x16x32_bf16 v[168:171],v[204:207],v[108:111],v[168:171]
v_cvt_pk_bf16_f32 v151,0,v151
v_cvt_pk_bf16_f32 v156,0,v156
v_mfma_f32_16x16x32_bf16 v[176:179],v[192:195],v[80:83],v[176:179]
v_cvt_pk_bf16_f32 v157,0,v157
v_cvt_pk_bf16_f32 v158,0,v158
v_mfma_f32_16x16x32_bf16 v[176:179],v[196:199],v[84:87],v[176:179]
buffer_load_dwordx4 v253,s[48:51],s94 offen offset:2048 lds
v_mfma_f32_16x16x32_bf16 v[176:179],v[200:203],v[112:115],v[176:179]
v_cvt_pk_bf16_f32 v159,0,v159
v_max3_f32 v244,v160,v161,v244
v_mfma_f32_16x16x32_bf16 v[176:179],v[204:207],v[116:119],v[176:179]
v_max3_f32 v244,v162,v163,v244
v_max3_f32 v245,v168,v169,v245
v_mfma_f32_16x16x32_bf16 v[184:187],v[192:195],v[88:91],v[184:187]
buffer_load_dwordx4 v253,s[52:55],s94 offen offset:3072 lds
v_mfma_f32_16x16x32_bf16 v[184:187],v[196:199],v[92:95],v[184:187]
s_add_u32 s94,s94,16384
v_max3_f32 v245,v170,v171,v245
v_mfma_f32_16x16x32_bf16 v[184:187],v[200:203],v[120:123],v[184:187]
v_max3_f32 v246,v176,v177,v246
v_max3_f32 v246,v178,v179,v246
v_mfma_f32_16x16x32_bf16 v[184:187],v[204:207],v[124:127],v[184:187]
s_waitcnt vmcnt(8) lgkmcnt(0)
v_mfma_f32_16x16x32_bf16 v[164:167],v[208:211],v[64:67],v[164:167]
s_barrier
ds_read_b64_tr_b16 v[192:193],v250
v_mfma_f32_16x16x32_bf16 v[164:167],v[212:215],v[68:71],v[164:167]
ds_read_b64_tr_b16 v[194:195],v250 offset:8192
v_mfma_f32_16x16x32_bf16 v[164:167],v[216:219],v[96:99],v[164:167]
ds_read_b64_tr_b16 v[196:197],v251
v_max3_f32 v247,v184,v185,v247
v_mfma_f32_16x16x32_bf16 v[164:167],v[220:223],v[100:103],v[164:167]
ds_read_b64_tr_b16 v[198:199],v251 offset:8192
v_max3_f32 v247,v186,v187,v247
v_mfma_f32_16x16x32_bf16 v[172:175],v[208:211],v[72:75],v[172:175]
ds_read_b64_tr_b16 v[200:201],v250 offset:512
v_mfma_f32_16x16x32_bf16 v[172:175],v[212:215],v[76:79],v[172:175]
s_add_u32 m0,s92,0x0
ds_read_b64_tr_b16 v[202:203],v250 offset:8704
v_mfma_f32_16x16x32_bf16 v[172:175],v[216:219],v[104:107],v[172:175]
ds_read_b64_tr_b16 v[204:205],v251 offset:512
v_max3_f32 v244,v164,v165,v244
v_mfma_f32_16x16x32_bf16 v[172:175],v[220:223],v[108:111],v[172:175]
ds_read_b64_tr_b16 v[206:207],v251 offset:8704
v_max3_f32 v244,v166,v167,v244
v_mfma_f32_16x16x32_bf16 v[180:183],v[208:211],v[80:83],v[180:183]
v_mfma_f32_16x16x32_bf16 v[180:183],v[212:215],v[84:87],v[180:183]
buffer_load_dwordx4 v252,s[24:27],s93 offen lds
v_mfma_f32_16x16x32_bf16 v[180:183],v[216:219],v[112:115],v[180:183]
v_mfma_f32_16x16x32_bf16 v[180:183],v[220:223],v[116:119],v[180:183]
v_mfma_f32_16x16x32_bf16 v[188:191],v[208:211],v[88:91],v[188:191]
buffer_load_dwordx4 v252,s[28:31],s93 offen offset:1024 lds
v_mfma_f32_16x16x32_bf16 v[188:191],v[212:215],v[92:95],v[188:191]
v_max3_f32 v245,v172,v173,v245
v_max3_f32 v245,v174,v175,v245
v_mfma_f32_16x16x32_bf16 v[188:191],v[216:219],v[120:123],v[188:191]
v_mfma_f32_16x16x32_bf16 v[188:191],v[220:223],v[124:127],v[188:191]
s_waitcnt lgkmcnt(0)
v_max3_f32 v246,v180,v181,v246
v_max3_f32 v246,v182,v183,v246
s_nop 4
v_max3_f32 v247,v188,v189,v247
v_max3_f32 v247,v190,v191,v247
v_mov_b32 v224,v244
v_mov_b32 v225,v245
v_mov_b32 v226,v246
v_mov_b32 v227,v247
v_permlane16_swap_b32 v244,v224
v_permlane16_swap_b32 v245,v225
v_permlane16_swap_b32 v246,v226
v_permlane16_swap_b32 v247,v227
v_max_f32 v244,v244,v224
v_max_f32 v245,v245,v225
v_max_f32 v246,v246,v226
v_max_f32 v247,v247,v227
v_mov_b32 v224,v244
v_mov_b32 v225,v245
v_mov_b32 v226,v246
v_mov_b32 v227,v247
v_permlane32_swap_b32 v244,v224
v_permlane32_swap_b32 v245,v225
v_permlane32_swap_b32 v246,v226
v_permlane32_swap_b32 v247,v227
v_max_f32 v244,v244,v224
v_max_f32 v245,v245,v225
v_max_f32 v246,v246,v226
v_max_f32 v247,v247,v227
ds_write_b128 v235,v[244:247] offset:32768
s_waitcnt lgkmcnt(0)
s_barrier
ds_read_b128 v[224:227],v236 offset:32768
buffer_load_dwordx4 v252,s[32:35],s93 offen offset:2048 lds
buffer_load_dwordx4 v252,s[36:39],s93 offen offset:3072 lds
s_add_u32 s93,s93,16384
s_waitcnt lgkmcnt(0)
v_max_f32 v244,v244,v226
v_max_f32 v245,v245,v227
v_max_f32 v246,v246,v224
v_max_f32 v247,v247,v225
v_cvt_pk_bf16_f32 v244,0,v244
v_cvt_pk_bf16_f32 v245,0,v245
v_cvt_pk_bf16_f32 v246,0,v246
v_cvt_pk_bf16_f32 v247,0,v247
v_mul_f32 v244,s72,v244
v_mul_f32 v245,s72,v245
v_mul_f32 v246,s72,v246
v_mul_f32 v247,s72,v247
v_sub_f32 v244,0xc2c00000,v244
v_sub_f32 v245,0xc2c00000,v245
v_sub_f32 v246,0xc2c00000,v246
v_sub_f32 v247,0xc2c00000,v247
s_add_u32 m0,s92,0x4000
s_nop 0
buffer_load_dwordx4 v252,s[24:27],s93 offen lds
v_fma_f32 v128,v128,s72,v244
v_fma_f32 v129,v129,s72,v244
v_fma_f32 v130,v130,s72,v244
v_fma_f32 v131,v131,s72,v244
v_fma_f32 v136,v136,s72,v245
v_fma_f32 v137,v137,s72,v245
v_fma_f32 v138,v138,s72,v245
v_fma_f32 v139,v139,s72,v245
v_fma_f32 v144,v144,s72,v246
v_fma_f32 v145,v145,s72,v246
v_fma_f32 v146,v146,s72,v246
v_fma_f32 v147,v147,s72,v246
v_fma_f32 v152,v152,s72,v247
buffer_load_dwordx4 v252,s[28:31],s93 offen offset:1024 lds
v_fma_f32 v153,v153,s72,v247
v_fma_f32 v154,v154,s72,v247
v_fma_f32 v155,v155,s72,v247
v_fma_f32 v132,v132,s72,v244
v_fma_f32 v133,v133,s72,v244
v_fma_f32 v134,v134,s72,v244
v_fma_f32 v135,v135,s72,v244
v_fma_f32 v140,v140,s72,v245
v_fma_f32 v141,v141,s72,v245
v_fma_f32 v142,v142,s72,v245
v_fma_f32 v143,v143,s72,v245
v_fma_f32 v148,v148,s72,v246
v_fma_f32 v149,v149,s72,v246
buffer_load_dwordx4 v252,s[32:35],s93 offen offset:2048 lds
v_fma_f32 v150,v150,s72,v246
v_fma_f32 v151,v151,s72,v246
v_fma_f32 v156,v156,s72,v247
v_fma_f32 v157,v157,s72,v247
v_fma_f32 v158,v158,s72,v247
v_fma_f32 v159,v159,s72,v247
v_exp_f32 v128,v128
)"
R"(v_exp_f32 v129,v129
v_exp_f32 v130,v130
v_exp_f32 v131,v131
v_exp_f32 v132,v132
v_exp_f32 v133,v133
v_exp_f32 v134,v134
v_exp_f32 v135,v135
buffer_load_dwordx4 v252,s[36:39],s93 offen offset:3072 lds
s_add_u32 s93,s93,16384
v_exp_f32 v136,v136
v_exp_f32 v137,v137
v_exp_f32 v138,v138
v_exp_f32 v139,v139
v_exp_f32 v140,v140
v_exp_f32 v141,v141
v_exp_f32 v142,v142
v_exp_f32 v143,v143
v_exp_f32 v144,v144
v_exp_f32 v145,v145
v_exp_f32 v146,v146
v_exp_f32 v147,v147
v_exp_f32 v148,v148
s_add_u32 m0,s92,0x8000
s_nop 0
buffer_load_dwordx4 v252,s[24:27],s93 offen lds
v_exp_f32 v149,v149
v_exp_f32 v150,v150
v_exp_f32 v151,v151
v_exp_f32 v152,v152
v_exp_f32 v153,v153
v_exp_f32 v154,v154
v_exp_f32 v155,v155
v_exp_f32 v156,v156
v_exp_f32 v157,v157
v_exp_f32 v158,v158
v_exp_f32 v159,v159
v_cvt_pk_bf16_f32 v128,v128,v129
v_cvt_pk_bf16_f32 v129,v130,v131
buffer_load_dwordx4 v252,s[28:31],s93 offen offset:1024 lds
v_cvt_pk_bf16_f32 v130,v132,v133
v_cvt_pk_bf16_f32 v131,v134,v135
v_cvt_pk_bf16_f32 v136,v136,v137
v_cvt_pk_bf16_f32 v137,v138,v139
v_cvt_pk_bf16_f32 v138,v140,v141
v_cvt_pk_bf16_f32 v139,v142,v143
v_cvt_pk_bf16_f32 v144,v144,v145
v_cvt_pk_bf16_f32 v145,v146,v147
v_cvt_pk_bf16_f32 v146,v148,v149
v_cvt_pk_bf16_f32 v147,v150,v151
v_cvt_pk_bf16_f32 v152,v152,v153
v_cvt_pk_bf16_f32 v153,v154,v155
v_cvt_pk_bf16_f32 v154,v156,v157
v_cvt_pk_bf16_f32 v155,v158,v159
v_mfma_f32_16x16x32_bf16 a[0:3],v[192:195],v[128:131],0
ds_read_b64_tr_b16 v[208:209],v250 offset:1024
v_cvt_pk_bf16_f32 v160,0,v160
v_mfma_f32_16x16x32_bf16 a[4:7],v[196:199],v[128:131],0
ds_read_b64_tr_b16 v[210:211],v250 offset:9216
v_fma_f32 v160,v160,s72,v244
v_mfma_f32_16x16x32_bf16 a[8:11],v[200:203],v[128:131],0
ds_read_b64_tr_b16 v[212:213],v251 offset:1024
v_cvt_pk_bf16_f32 v161,0,v161
v_mfma_f32_16x16x32_bf16 a[12:15],v[204:207],v[128:131],0
ds_read_b64_tr_b16 v[214:215],v251 offset:9216
v_fma_f32 v161,v161,s72,v244
v_mfma_f32_16x16x32_bf16 a[64:67],v[192:195],v[136:139],0
ds_read_b64_tr_b16 v[216:217],v250 offset:1536
v_cvt_pk_bf16_f32 v162,0,v162
v_mfma_f32_16x16x32_bf16 a[68:71],v[196:199],v[136:139],0
ds_read_b64_tr_b16 v[218:219],v250 offset:9728
v_fma_f32 v162,v162,s72,v244
v_mfma_f32_16x16x32_bf16 a[72:75],v[200:203],v[136:139],0
ds_read_b64_tr_b16 v[220:221],v251 offset:1536
v_cvt_pk_bf16_f32 v163,0,v163
v_mfma_f32_16x16x32_bf16 a[76:79],v[204:207],v[136:139],0
ds_read_b64_tr_b16 v[222:223],v251 offset:9728
v_fma_f32 v163,v163,s72,v244
v_mfma_f32_16x16x32_bf16 a[128:131],v[192:195],v[144:147],0
v_cvt_pk_bf16_f32 v168,0,v168
v_fma_f32 v168,v168,s72,v245
v_mfma_f32_16x16x32_bf16 a[132:135],v[196:199],v[144:147],0
buffer_load_dwordx4 v252,s[32:35],s93 offen offset:2048 lds
v_mfma_f32_16x16x32_bf16 a[136:139],v[200:203],v[144:147],0
v_cvt_pk_bf16_f32 v169,0,v169
v_fma_f32 v169,v169,s72,v245
v_mfma_f32_16x16x32_bf16 a[140:143],v[204:207],v[144:147],0
v_cvt_pk_bf16_f32 v170,0,v170
v_fma_f32 v170,v170,s72,v245
v_mfma_f32_16x16x32_bf16 a[192:195],v[192:195],v[152:155],0
v_cvt_pk_bf16_f32 v171,0,v171
v_fma_f32 v171,v171,s72,v245
v_mfma_f32_16x16x32_bf16 a[196:199],v[196:199],v[152:155],0
buffer_load_dwordx4 v252,s[36:39],s93 offen offset:3072 lds
v_mfma_f32_16x16x32_bf16 a[200:203],v[200:203],v[152:155],0
s_add_u32 s93,s93,16384
v_cvt_pk_bf16_f32 v176,0,v176
v_fma_f32 v176,v176,s72,v246
v_mfma_f32_16x16x32_bf16 a[204:207],v[204:207],v[152:155],0
v_cvt_pk_bf16_f32 v177,0,v177
v_fma_f32 v177,v177,s72,v246
v_mfma_f32_16x16x32_bf16 v[224:227],v[240:243],v[128:131],0
s_waitcnt lgkmcnt(0)
v_cvt_pk_bf16_f32 v178,0,v178
v_fma_f32 v178,v178,s72,v246
v_mfma_f32_16x16x32_bf16 a[16:19],v[208:211],v[128:131],0
ds_read_b64_tr_b16 v[192:193],v250 offset:2048
v_cvt_pk_bf16_f32 v179,0,v179
v_mfma_f32_16x16x32_bf16 a[80:83],v[208:211],v[136:139],0
ds_read_b64_tr_b16 v[194:195],v250 offset:10240
v_fma_f32 v179,v179,s72,v246
v_mfma_f32_16x16x32_bf16 a[144:147],v[208:211],v[144:147],0
ds_read_b64_tr_b16 v[196:197],v251 offset:2048
v_cvt_pk_bf16_f32 v184,0,v184
v_mfma_f32_16x16x32_bf16 a[208:211],v[208:211],v[152:155],0
ds_read_b64_tr_b16 v[198:199],v251 offset:10240
v_fma_f32 v184,v184,s72,v247
v_mfma_f32_16x16x32_bf16 a[20:23],v[212:215],v[128:131],0
ds_read_b64_tr_b16 v[200:201],v250 offset:2560
v_cvt_pk_bf16_f32 v185,0,v185
v_mfma_f32_16x16x32_bf16 a[84:87],v[212:215],v[136:139],0
s_add_u32 m0,s92,0xc000
ds_read_b64_tr_b16 v[202:203],v250 offset:10752
v_fma_f32 v185,v185,s72,v247
v_mfma_f32_16x16x32_bf16 a[148:151],v[212:215],v[144:147],0
ds_read_b64_tr_b16 v[204:205],v251 offset:2560
v_cvt_pk_bf16_f32 v186,0,v186
v_mfma_f32_16x16x32_bf16 a[212:215],v[212:215],v[152:155],0
ds_read_b64_tr_b16 v[206:207],v251 offset:10752
v_fma_f32 v186,v186,s72,v247
v_mfma_f32_16x16x32_bf16 a[24:27],v[216:219],v[128:131],0
v_cvt_pk_bf16_f32 v187,0,v187
v_fma_f32 v187,v187,s72,v247
v_mfma_f32_16x16x32_bf16 a[88:91],v[216:219],v[136:139],0
buffer_load_dwordx4 v252,s[24:27],s93 offen lds
v_mfma_f32_16x16x32_bf16 a[152:155],v[216:219],v[144:147],0
v_cvt_pk_bf16_f32 v164,0,v164
v_fma_f32 v164,v164,s72,v244
v_mfma_f32_16x16x32_bf16 a[216:219],v[216:219],v[152:155],0
v_cvt_pk_bf16_f32 v165,0,v165
v_fma_f32 v165,v165,s72,v244
v_mfma_f32_16x16x32_bf16 a[28:31],v[220:223],v[128:131],0
v_cvt_pk_bf16_f32 v166,0,v166
v_fma_f32 v166,v166,s72,v244
v_mfma_f32_16x16x32_bf16 a[92:95],v[220:223],v[136:139],0
buffer_load_dwordx4 v252,s[28:31],s93 offen offset:1024 lds
v_mfma_f32_16x16x32_bf16 a[156:159],v[220:223],v[144:147],0
v_cvt_pk_bf16_f32 v167,0,v167
v_fma_f32 v167,v167,s72,v244
v_mfma_f32_16x16x32_bf16 a[220:223],v[220:223],v[152:155],0
v_cvt_pk_bf16_f32 v172,0,v172
v_fma_f32 v172,v172,s72,v245
v_mfma_f32_16x16x32_bf16 v[228:231],v[240:243],v[136:139],0
s_waitcnt lgkmcnt(0)
v_cvt_pk_bf16_f32 v173,0,v173
v_fma_f32 v173,v173,s72,v245
v_mfma_f32_16x16x32_bf16 a[32:35],v[192:195],v[128:131],0
ds_read_b64_tr_b16 v[208:209],v250 offset:3072
v_cvt_pk_bf16_f32 v174,0,v174
v_mfma_f32_16x16x32_bf16 a[96:99],v[192:195],v[136:139],0
ds_read_b64_tr_b16 v[210:211],v250 offset:11264
v_fma_f32 v174,v174,s72,v245
v_mfma_f32_16x16x32_bf16 a[160:163],v[192:195],v[144:147],0
ds_read_b64_tr_b16 v[212:213],v251 offset:3072
v_cvt_pk_bf16_f32 v175,0,v175
v_mfma_f32_16x16x32_bf16 a[224:227],v[192:195],v[152:155],0
ds_read_b64_tr_b16 v[214:215],v251 offset:11264
v_fma_f32 v175,v175,s72,v245
v_mfma_f32_16x16x32_bf16 a[36:39],v[196:199],v[128:131],0
ds_read_b64_tr_b16 v[216:217],v250 offset:3584
v_cvt_pk_bf16_f32 v180,0,v180
v_mfma_f32_16x16x32_bf16 a[100:103],v[196:199],v[136:139],0
ds_read_b64_tr_b16 v[218:219],v250 offset:11776
v_fma_f32 v180,v180,s72,v246
v_mfma_f32_16x16x32_bf16 a[164:167],v[196:199],v[144:147],0
ds_read_b64_tr_b16 v[220:221],v251 offset:3584
v_cvt_pk_bf16_f32 v181,0,v181
v_mfma_f32_16x16x32_bf16 a[228:231],v[196:199],v[152:155],0
ds_read_b64_tr_b16 v[222:223],v251 offset:11776
v_fma_f32 v181,v181,s72,v246
v_mfma_f32_16x16x32_bf16 a[40:43],v[200:203],v[128:131],0
v_cvt_pk_bf16_f32 v182,0,v182
v_fma_f32 v182,v182,s72,v246
v_mfma_f32_16x16x32_bf16 a[104:107],v[200:203],v[136:139],0
buffer_load_dwordx4 v252,s[32:35],s93 offen offset:2048 lds
v_mfma_f32_16x16x32_bf16 a[168:171],v[200:203],v[144:147],0
v_cvt_pk_bf16_f32 v183,0,v183
v_fma_f32 v183,v183,s72,v246
v_mfma_f32_16x16x32_bf16 a[232:235],v[200:203],v[152:155],0
v_cvt_pk_bf16_f32 v188,0,v188
v_fma_f32 v188,v188,s72,v247
v_mfma_f32_16x16x32_bf16 a[44:47],v[204:207],v[128:131],0
v_cvt_pk_bf16_f32 v189,0,v189
v_fma_f32 v189,v189,s72,v247
v_mfma_f32_16x16x32_bf16 a[108:111],v[204:207],v[136:139],0
buffer_load_dwordx4 v252,s[36:39],s93 offen offset:3072 lds
v_mfma_f32_16x16x32_bf16 a[172:175],v[204:207],v[144:147],0
s_add_u32 s93,s93,16384
v_cvt_pk_bf16_f32 v190,0,v190
v_fma_f32 v190,v190,s72,v247
v_mfma_f32_16x16x32_bf16 a[236:239],v[204:207],v[152:155],0
)"
R"(v_cvt_pk_bf16_f32 v191,0,v191
v_fma_f32 v191,v191,s72,v247
v_mfma_f32_16x16x32_bf16 v[232:235],v[240:243],v[144:147],0
s_waitcnt vmcnt(8) lgkmcnt(0)
v_mfma_f32_16x16x32_bf16 a[48:51],v[208:211],v[128:131],0
s_barrier
ds_read_b128 v[192:195],v248
v_mfma_f32_16x16x32_bf16 a[112:115],v[208:211],v[136:139],0
s_add_u32 s75,s75,1
v_mov_b32 v254,s75
v_mfma_f32_16x16x32_bf16 a[176:179],v[208:211],v[144:147],0
ds_read_b128 v[196:199],v249
v_mfma_f32_16x16x32_bf16 a[240:243],v[208:211],v[152:155],0
buffer_store_dword v254,off,s[88:91],s78 sc1
v_mfma_f32_16x16x32_bf16 a[52:55],v[212:215],v[128:131],0
ds_read_b128 v[200:203],v248 offset:1024
v_mfma_f32_16x16x32_bf16 a[116:119],v[212:215],v[136:139],0
s_add_u32 m0,s92,0x10000
v_mfma_f32_16x16x32_bf16 a[180:183],v[212:215],v[144:147],0
ds_read_b128 v[204:207],v249 offset:1024
v_mfma_f32_16x16x32_bf16 a[244:247],v[212:215],v[152:155],0
v_mfma_f32_16x16x32_bf16 a[56:59],v[216:219],v[128:131],0
v_mfma_f32_16x16x32_bf16 a[120:123],v[216:219],v[136:139],0
buffer_load_dwordx4 v253,s[40:43],s94 offen lds
v_mfma_f32_16x16x32_bf16 a[184:187],v[216:219],v[144:147],0
v_mfma_f32_16x16x32_bf16 a[248:251],v[216:219],v[152:155],0
v_mfma_f32_16x16x32_bf16 a[60:63],v[220:223],v[128:131],0
v_mfma_f32_16x16x32_bf16 a[124:127],v[220:223],v[136:139],0
buffer_load_dwordx4 v253,s[44:47],s94 offen offset:1024 lds
v_mfma_f32_16x16x32_bf16 a[188:191],v[220:223],v[144:147],0
v_mfma_f32_16x16x32_bf16 a[252:255],v[220:223],v[152:155],0
v_mfma_f32_16x16x32_bf16 v[236:239],v[240:243],v[152:155],0
s_waitcnt lgkmcnt(0)
.p2align 5
s_nop 0
s_nop 0
Lloopa_%=:
v_mfma_f32_16x16x32_bf16 v[128:131],v[192:195],v[0:3],0
ds_read_b128 v[208:211],v248 offset:2048
v_mfma_f32_16x16x32_bf16 v[128:131],v[196:199],v[4:7],v[128:131]
v_exp_f32 v160,v160
v_mfma_f32_16x16x32_bf16 v[128:131],v[200:203],v[32:35],v[128:131]
ds_read_b128 v[212:215],v249 offset:2048
v_mfma_f32_16x16x32_bf16 v[128:131],v[204:207],v[36:39],v[128:131]
v_exp_f32 v161,v161
v_mfma_f32_16x16x32_bf16 v[136:139],v[192:195],v[8:11],0
ds_read_b128 v[216:219],v248 offset:3072
v_cvt_pk_bf16_f32 v160,v160,v161
v_mfma_f32_16x16x32_bf16 v[136:139],v[196:199],v[12:15],v[136:139]
v_exp_f32 v162,v162
v_mfma_f32_16x16x32_bf16 v[136:139],v[200:203],v[40:43],v[136:139]
ds_read_b128 v[220:223],v249 offset:3072
v_mfma_f32_16x16x32_bf16 v[136:139],v[204:207],v[44:47],v[136:139]
v_exp_f32 v163,v163
v_mfma_f32_16x16x32_bf16 v[144:147],v[192:195],v[16:19],0
v_exp_f32 v164,v164
v_mfma_f32_16x16x32_bf16 v[144:147],v[196:199],v[20:23],v[144:147]
buffer_load_dwordx4 v253,s[48:51],s94 offen offset:2048 lds
v_mfma_f32_16x16x32_bf16 v[144:147],v[200:203],v[48:51],v[144:147]
v_exp_f32 v165,v165
v_mfma_f32_16x16x32_bf16 v[144:147],v[204:207],v[52:55],v[144:147]
v_exp_f32 v166,v166
v_mfma_f32_16x16x32_bf16 v[152:155],v[192:195],v[24:27],0
buffer_load_dwordx4 v253,s[52:55],s94 offen offset:3072 lds
v_mfma_f32_16x16x32_bf16 v[152:155],v[196:199],v[28:31],v[152:155]
s_add_u32 s94,s94,16384
v_exp_f32 v167,v167
v_mfma_f32_16x16x32_bf16 v[152:155],v[200:203],v[56:59],v[152:155]
v_exp_f32 v168,v168
v_mfma_f32_16x16x32_bf16 v[152:155],v[204:207],v[60:63],v[152:155]
s_waitcnt lgkmcnt(0)
v_exp_f32 v169,v169
v_mfma_f32_16x16x32_bf16 v[128:131],v[208:211],v[64:67],v[128:131]
ds_read_b128 v[192:195],v248 offset:8192
v_cvt_pk_bf16_f32 v161,v162,v163
v_mfma_f32_16x16x32_bf16 v[128:131],v[212:215],v[68:71],v[128:131]
v_exp_f32 v170,v170
v_mfma_f32_16x16x32_bf16 v[128:131],v[216:219],v[96:99],v[128:131]
ds_read_b128 v[196:199],v249 offset:8192
v_cvt_pk_bf16_f32 v162,v164,v165
v_mfma_f32_16x16x32_bf16 v[128:131],v[220:223],v[100:103],v[128:131]
v_exp_f32 v171,v171
v_mfma_f32_16x16x32_bf16 v[136:139],v[208:211],v[72:75],v[136:139]
ds_read_b128 v[200:203],v248 offset:9216
v_cvt_pk_bf16_f32 v163,v166,v167
v_mfma_f32_16x16x32_bf16 v[136:139],v[212:215],v[76:79],v[136:139]
s_add_u32 m0,s92,0x14000
v_exp_f32 v172,v172
v_mfma_f32_16x16x32_bf16 v[136:139],v[216:219],v[104:107],v[136:139]
ds_read_b128 v[204:207],v249 offset:9216
v_cvt_pk_bf16_f32 v168,v168,v169
v_mfma_f32_16x16x32_bf16 v[136:139],v[220:223],v[108:111],v[136:139]
v_exp_f32 v173,v173
v_mfma_f32_16x16x32_bf16 v[144:147],v[208:211],v[80:83],v[144:147]
v_exp_f32 v174,v174
v_mfma_f32_16x16x32_bf16 v[144:147],v[212:215],v[84:87],v[144:147]
buffer_load_dwordx4 v253,s[40:43],s94 offen lds
v_mfma_f32_16x16x32_bf16 v[144:147],v[216:219],v[112:115],v[144:147]
v_exp_f32 v175,v175
v_mfma_f32_16x16x32_bf16 v[144:147],v[220:223],v[116:119],v[144:147]
v_exp_f32 v176,v176
v_mfma_f32_16x16x32_bf16 v[152:155],v[208:211],v[88:91],v[152:155]
buffer_load_dwordx4 v253,s[44:47],s94 offen offset:1024 lds
v_mfma_f32_16x16x32_bf16 v[152:155],v[212:215],v[92:95],v[152:155]
v_exp_f32 v177,v177
v_mfma_f32_16x16x32_bf16 v[152:155],v[216:219],v[120:123],v[152:155]
v_exp_f32 v178,v178
v_mfma_f32_16x16x32_bf16 v[152:155],v[220:223],v[124:127],v[152:155]
s_waitcnt lgkmcnt(0)
v_exp_f32 v179,v179
v_mfma_f32_16x16x32_bf16 v[132:135],v[192:195],v[0:3],0
ds_read_b128 v[208:211],v248 offset:10240
v_cvt_pk_bf16_f32 v169,v170,v171
v_mfma_f32_16x16x32_bf16 v[132:135],v[196:199],v[4:7],v[132:135]
v_exp_f32 v180,v180
v_mfma_f32_16x16x32_bf16 v[132:135],v[200:203],v[32:35],v[132:135]
ds_read_b128 v[212:215],v249 offset:10240
v_cvt_pk_bf16_f32 v170,v172,v173
v_mfma_f32_16x16x32_bf16 v[132:135],v[204:207],v[36:39],v[132:135]
v_exp_f32 v181,v181
v_mfma_f32_16x16x32_bf16 v[140:143],v[192:195],v[8:11],0
ds_read_b128 v[216:219],v248 offset:11264
v_cvt_pk_bf16_f32 v171,v174,v175
v_mfma_f32_16x16x32_bf16 v[140:143],v[196:199],v[12:15],v[140:143]
v_exp_f32 v182,v182
v_mfma_f32_16x16x32_bf16 v[140:143],v[200:203],v[40:43],v[140:143]
ds_read_b128 v[220:223],v249 offset:11264
v_cvt_pk_bf16_f32 v176,v176,v177
v_mfma_f32_16x16x32_bf16 v[140:143],v[204:207],v[44:47],v[140:143]
v_exp_f32 v183,v183
v_mfma_f32_16x16x32_bf16 v[148:151],v[192:195],v[16:19],0
v_exp_f32 v184,v184
v_mfma_f32_16x16x32_bf16 v[148:151],v[196:199],v[20:23],v[148:151]
buffer_load_dwordx4 v253,s[48:51],s94 offen offset:2048 lds
v_mfma_f32_16x16x32_bf16 v[148:151],v[200:203],v[48:51],v[148:151]
v_exp_f32 v185,v185
v_mfma_f32_16x16x32_bf16 v[148:151],v[204:207],v[52:55],v[148:151]
v_exp_f32 v186,v186
v_mfma_f32_16x16x32_bf16 v[156:159],v[192:195],v[24:27],0
buffer_load_dwordx4 v253,s[52:55],s94 offen offset:3072 lds
v_mfma_f32_16x16x32_bf16 v[156:159],v[196:199],v[28:31],v[156:159]
s_add_u32 s94,s94,16384
v_exp_f32 v187,v187
v_mfma_f32_16x16x32_bf16 v[156:159],v[200:203],v[56:59],v[156:159]
v_exp_f32 v188,v188
v_mfma_f32_16x16x32_bf16 v[156:159],v[204:207],v[60:63],v[156:159]
s_waitcnt vmcnt(16) lgkmcnt(0)
v_exp_f32 v189,v189
v_mfma_f32_16x16x32_bf16 v[132:135],v[208:211],v[64:67],v[132:135]
s_barrier
ds_read_b64_tr_b16 v[192:193],v250 offset:32768
v_cvt_pk_bf16_f32 v177,v178,v179
v_mfma_f32_16x16x32_bf16 v[132:135],v[212:215],v[68:71],v[132:135]
ds_read_b64_tr_b16 v[194:195],v250 offset:40960
v_cvt_pk_bf16_f32 v178,v180,v181
v_mfma_f32_16x16x32_bf16 v[132:135],v[216:219],v[96:99],v[132:135]
ds_read_b64_tr_b16 v[196:197],v251 offset:32768
v_cvt_pk_bf16_f32 v179,v182,v183
v_mfma_f32_16x16x32_bf16 v[132:135],v[220:223],v[100:103],v[132:135]
ds_read_b64_tr_b16 v[198:199],v251 offset:40960
v_cvt_pk_bf16_f32 v184,v184,v185
v_mfma_f32_16x16x32_bf16 v[140:143],v[208:211],v[72:75],v[140:143]
ds_read_b64_tr_b16 v[200:201],v250 offset:33280
v_cvt_pk_bf16_f32 v185,v186,v187
v_mfma_f32_16x16x32_bf16 v[140:143],v[212:215],v[76:79],v[140:143]
s_add_u32 m0,s92,0x0
ds_read_b64_tr_b16 v[202:203],v250 offset:41472
v_cvt_pk_bf16_f32 v186,v188,v189
v_mfma_f32_16x16x32_bf16 v[140:143],v[216:219],v[104:107],v[140:143]
ds_read_b64_tr_b16 v[204:205],v251 offset:33280
v_cvt_pk_bf16_f32 v128,0,v128
v_mfma_f32_16x16x32_bf16 v[140:143],v[220:223],v[108:111],v[140:143]
ds_read_b64_tr_b16 v[206:207],v251 offset:41472
v_fma_f32 v128,v128,s72,v244
v_mfma_f32_16x16x32_bf16 v[148:151],v[208:211],v[80:83],v[148:151]
v_exp_f32 v190,v190
v_mfma_f32_16x16x32_bf16 v[148:151],v[212:215],v[84:87],v[148:151]
buffer_load_dwordx4 v252,s[24:27],s93 offen lds
v_mfma_f32_16x16x32_bf16 v[148:151],v[216:219],v[112:115],v[148:151]
v_exp_f32 v191,v191
v_mfma_f32_16x16x32_bf16 v[148:151],v[220:223],v[116:119],v[148:151]
v_cvt_pk_bf16_f32 v187,v190,v191
v_cvt_pk_bf16_f32 v129,0,v129
v_mfma_f32_16x16x32_bf16 v[156:159],v[208:211],v[88:91],v[156:159]
buffer_load_dwordx4 v252,s[28:31],s93 offen offset:1024 lds
v_mfma_f32_16x16x32_bf16 v[156:159],v[212:215],v[92:95],v[156:159]
v_fma_f32 v129,v129,s72,v244
v_cvt_pk_bf16_f32 v130,0,v130
v_mfma_f32_16x16x32_bf16 v[156:159],v[216:219],v[120:123],v[156:159]
v_fma_f32 v130,v130,s72,v244
v_cvt_pk_bf16_f32 v131,0,v131
v_mfma_f32_16x16x32_bf16 v[156:159],v[220:223],v[124:127],v[156:159]
s_waitcnt lgkmcnt(0)
v_fma_f32 v131,v131,s72,v244
v_max3_f32 v164,v128,v129,v129
v_mfma_f32_16x16x32_bf16 a[0:3],v[192:195],v[160:163],a[0:3]
ds_read_b64_tr_b16 v[208:209],v250 offset:33792
v_max3_f32 v164,v130,v131,v164
v_mfma_f32_16x16x32_bf16 a[4:7],v[196:199],v[160:163],a[4:7]
ds_read_b64_tr_b16 v[210:211],v250 offset:41984
)"
R"(v_cvt_pk_bf16_f32 v136,0,v136
v_mfma_f32_16x16x32_bf16 a[8:11],v[200:203],v[160:163],a[8:11]
ds_read_b64_tr_b16 v[212:213],v251 offset:33792
v_fma_f32 v136,v136,s72,v245
v_mfma_f32_16x16x32_bf16 a[12:15],v[204:207],v[160:163],a[12:15]
ds_read_b64_tr_b16 v[214:215],v251 offset:41984
v_cvt_pk_bf16_f32 v137,0,v137
v_mfma_f32_16x16x32_bf16 a[64:67],v[192:195],v[168:171],a[64:67]
ds_read_b64_tr_b16 v[216:217],v250 offset:34304
v_fma_f32 v137,v137,s72,v245
v_mfma_f32_16x16x32_bf16 a[68:71],v[196:199],v[168:171],a[68:71]
ds_read_b64_tr_b16 v[218:219],v250 offset:42496
v_cvt_pk_bf16_f32 v138,0,v138
v_mfma_f32_16x16x32_bf16 a[72:75],v[200:203],v[168:171],a[72:75]
ds_read_b64_tr_b16 v[220:221],v251 offset:34304
v_fma_f32 v138,v138,s72,v245
v_mfma_f32_16x16x32_bf16 a[76:79],v[204:207],v[168:171],a[76:79]
ds_read_b64_tr_b16 v[222:223],v251 offset:42496
v_cvt_pk_bf16_f32 v139,0,v139
v_mfma_f32_16x16x32_bf16 a[128:131],v[192:195],v[176:179],a[128:131]
v_fma_f32 v139,v139,s72,v245
v_max3_f32 v164,v136,v137,v164
v_mfma_f32_16x16x32_bf16 a[132:135],v[196:199],v[176:179],a[132:135]
buffer_load_dwordx4 v252,s[32:35],s93 offen offset:2048 lds
v_mfma_f32_16x16x32_bf16 a[136:139],v[200:203],v[176:179],a[136:139]
v_max3_f32 v164,v138,v139,v164
v_cvt_pk_bf16_f32 v144,0,v144
v_mfma_f32_16x16x32_bf16 a[140:143],v[204:207],v[176:179],a[140:143]
v_fma_f32 v144,v144,s72,v246
v_cvt_pk_bf16_f32 v145,0,v145
v_mfma_f32_16x16x32_bf16 a[192:195],v[192:195],v[184:187],a[192:195]
v_fma_f32 v145,v145,s72,v246
v_cvt_pk_bf16_f32 v146,0,v146
v_mfma_f32_16x16x32_bf16 a[196:199],v[196:199],v[184:187],a[196:199]
buffer_load_dwordx4 v252,s[36:39],s93 offen offset:3072 lds
v_mfma_f32_16x16x32_bf16 a[200:203],v[200:203],v[184:187],a[200:203]
s_add_u32 s93,s93,16384
v_fma_f32 v146,v146,s72,v246
v_cvt_pk_bf16_f32 v147,0,v147
v_mfma_f32_16x16x32_bf16 a[204:207],v[204:207],v[184:187],a[204:207]
v_fma_f32 v147,v147,s72,v246
v_max3_f32 v164,v144,v145,v164
v_mfma_f32_16x16x32_bf16 v[224:227],v[240:243],v[160:163],v[224:227]
s_waitcnt lgkmcnt(0)
v_max3_f32 v164,v146,v147,v164
v_cvt_pk_bf16_f32 v152,0,v152
v_mfma_f32_16x16x32_bf16 a[16:19],v[208:211],v[160:163],a[16:19]
ds_read_b64_tr_b16 v[192:193],v250 offset:34816
v_fma_f32 v152,v152,s72,v247
v_mfma_f32_16x16x32_bf16 a[80:83],v[208:211],v[168:171],a[80:83]
ds_read_b64_tr_b16 v[194:195],v250 offset:43008
v_cvt_pk_bf16_f32 v153,0,v153
v_mfma_f32_16x16x32_bf16 a[144:147],v[208:211],v[176:179],a[144:147]
ds_read_b64_tr_b16 v[196:197],v251 offset:34816
v_fma_f32 v153,v153,s72,v247
v_mfma_f32_16x16x32_bf16 a[208:211],v[208:211],v[184:187],a[208:211]
ds_read_b64_tr_b16 v[198:199],v251 offset:43008
v_cvt_pk_bf16_f32 v154,0,v154
v_mfma_f32_16x16x32_bf16 a[20:23],v[212:215],v[160:163],a[20:23]
ds_read_b64_tr_b16 v[200:201],v250 offset:35328
v_fma_f32 v154,v154,s72,v247
v_mfma_f32_16x16x32_bf16 a[84:87],v[212:215],v[168:171],a[84:87]
s_add_u32 m0,s92,0x4000
ds_read_b64_tr_b16 v[202:203],v250 offset:43520
v_cvt_pk_bf16_f32 v155,0,v155
v_mfma_f32_16x16x32_bf16 a[148:151],v[212:215],v[176:179],a[148:151]
ds_read_b64_tr_b16 v[204:205],v251 offset:35328
v_fma_f32 v155,v155,s72,v247
v_mfma_f32_16x16x32_bf16 a[212:215],v[212:215],v[184:187],a[212:215]
ds_read_b64_tr_b16 v[206:207],v251 offset:43520
v_max3_f32 v164,v152,v153,v164
v_mfma_f32_16x16x32_bf16 a[24:27],v[216:219],v[160:163],a[24:27]
v_max3_f32 v164,v154,v155,v164
v_cvt_pk_bf16_f32 v132,0,v132
v_mfma_f32_16x16x32_bf16 a[88:91],v[216:219],v[168:171],a[88:91]
buffer_load_dwordx4 v252,s[24:27],s93 offen lds
v_mfma_f32_16x16x32_bf16 a[152:155],v[216:219],v[176:179],a[152:155]
v_fma_f32 v132,v132,s72,v244
v_cvt_pk_bf16_f32 v133,0,v133
v_mfma_f32_16x16x32_bf16 a[216:219],v[216:219],v[184:187],a[216:219]
v_fma_f32 v133,v133,s72,v244
v_cvt_pk_bf16_f32 v134,0,v134
v_mfma_f32_16x16x32_bf16 a[28:31],v[220:223],v[160:163],a[28:31]
v_fma_f32 v134,v134,s72,v244
v_cvt_pk_bf16_f32 v135,0,v135
v_mfma_f32_16x16x32_bf16 a[92:95],v[220:223],v[168:171],a[92:95]
buffer_load_dwordx4 v252,s[28:31],s93 offen offset:1024 lds
v_mfma_f32_16x16x32_bf16 a[156:159],v[220:223],v[176:179],a[156:159]
v_fma_f32 v135,v135,s72,v244
v_max3_f32 v164,v132,v133,v164
v_mfma_f32_16x16x32_bf16 a[220:223],v[220:223],v[184:187],a[220:223]
v_max3_f32 v164,v134,v135,v164
v_cvt_pk_bf16_f32 v140,0,v140
v_mfma_f32_16x16x32_bf16 v[228:231],v[240:243],v[168:171],v[228:231]
s_waitcnt lgkmcnt(0)
v_fma_f32 v140,v140,s72,v245
v_cvt_pk_bf16_f32 v141,0,v141
v_mfma_f32_16x16x32_bf16 a[32:35],v[192:195],v[160:163],a[32:35]
ds_read_b64_tr_b16 v[208:209],v250 offset:35840
v_fma_f32 v141,v141,s72,v245
v_mfma_f32_16x16x32_bf16 a[96:99],v[192:195],v[168:171],a[96:99]
ds_read_b64_tr_b16 v[210:211],v250 offset:44032
v_cvt_pk_bf16_f32 v142,0,v142
v_mfma_f32_16x16x32_bf16 a[160:163],v[192:195],v[176:179],a[160:163]
ds_read_b64_tr_b16 v[212:213],v251 offset:35840
v_fma_f32 v142,v142,s72,v245
v_mfma_f32_16x16x32_bf16 a[224:227],v[192:195],v[184:187],a[224:227]
ds_read_b64_tr_b16 v[214:215],v251 offset:44032
v_cvt_pk_bf16_f32 v143,0,v143
v_mfma_f32_16x16x32_bf16 a[36:39],v[196:199],v[160:163],a[36:39]
ds_read_b64_tr_b16 v[216:217],v250 offset:36352
v_fma_f32 v143,v143,s72,v245
v_mfma_f32_16x16x32_bf16 a[100:103],v[196:199],v[168:171],a[100:103]
ds_read_b64_tr_b16 v[218:219],v250 offset:44544
v_max3_f32 v164,v140,v141,v164
v_mfma_f32_16x16x32_bf16 a[164:167],v[196:199],v[176:179],a[164:167]
ds_read_b64_tr_b16 v[220:221],v251 offset:36352
v_max3_f32 v164,v142,v143,v164
v_mfma_f32_16x16x32_bf16 a[228:231],v[196:199],v[184:187],a[228:231]
ds_read_b64_tr_b16 v[222:223],v251 offset:44544
v_cvt_pk_bf16_f32 v148,0,v148
v_mfma_f32_16x16x32_bf16 a[40:43],v[200:203],v[160:163],a[40:43]
v_fma_f32 v148,v148,s72,v246
v_cvt_pk_bf16_f32 v149,0,v149
v_mfma_f32_16x16x32_bf16 a[104:107],v[200:203],v[168:171],a[104:107]
buffer_load_dwordx4 v252,s[32:35],s93 offen offset:2048 lds
v_mfma_f32_16x16x32_bf16 a[168:171],v[200:203],v[176:179],a[168:171]
v_fma_f32 v149,v149,s72,v246
v_cvt_pk_bf16_f32 v150,0,v150
v_mfma_f32_16x16x32_bf16 a[232:235],v[200:203],v[184:187],a[232:235]
v_fma_f32 v150,v150,s72,v246
v_cvt_pk_bf16_f32 v151,0,v151
v_mfma_f32_16x16x32_bf16 a[44:47],v[204:207],v[160:163],a[44:47]
v_fma_f32 v151,v151,s72,v246
v_max3_f32 v164,v148,v149,v164
v_mfma_f32_16x16x32_bf16 a[108:111],v[204:207],v[168:171],a[108:111]
buffer_load_dwordx4 v252,s[36:39],s93 offen offset:3072 lds
v_mfma_f32_16x16x32_bf16 a[172:175],v[204:207],v[176:179],a[172:175]
s_add_u32 s93,s93,16384
v_max3_f32 v164,v150,v151,v164
v_cvt_pk_bf16_f32 v156,0,v156
v_mfma_f32_16x16x32_bf16 a[236:239],v[204:207],v[184:187],a[236:239]
v_fma_f32 v156,v156,s72,v247
v_cvt_pk_bf16_f32 v157,0,v157
v_mfma_f32_16x16x32_bf16 v[232:235],v[240:243],v[176:179],v[232:235]
s_waitcnt vmcnt(16) lgkmcnt(0)
v_fma_f32 v157,v157,s72,v247
v_cvt_pk_bf16_f32 v158,0,v158
v_mfma_f32_16x16x32_bf16 a[48:51],v[208:211],v[160:163],a[48:51]
s_barrier
ds_read_b128 v[192:195],v248 offset:32768
v_fma_f32 v158,v158,s72,v247
v_mfma_f32_16x16x32_bf16 a[112:115],v[208:211],v[168:171],a[112:115]
v_cvt_pk_bf16_f32 v159,0,v159
v_fma_f32 v159,v159,s72,v247
v_mfma_f32_16x16x32_bf16 a[176:179],v[208:211],v[176:179],a[176:179]
ds_read_b128 v[196:199],v249 offset:32768
v_max3_f32 v164,v156,v157,v164
v_mfma_f32_16x16x32_bf16 a[240:243],v[208:211],v[184:187],a[240:243]
v_max3_f32 v164,v158,v159,v164
v_cmp_lt_f32 vcc,s73,v164
v_mfma_f32_16x16x32_bf16 a[52:55],v[212:215],v[160:163],a[52:55]
ds_read_b128 v[200:203],v248 offset:33792
v_mfma_f32_16x16x32_bf16 a[116:119],v[212:215],v[168:171],a[116:119]
s_add_u32 m0,s92,0x18000
v_mfma_f32_16x16x32_bf16 a[180:183],v[212:215],v[176:179],a[180:183]
ds_read_b128 v[204:207],v249 offset:33792
v_mfma_f32_16x16x32_bf16 a[244:247],v[212:215],v[184:187],a[244:247]
v_mfma_f32_16x16x32_bf16 a[56:59],v[216:219],v[160:163],a[56:59]
v_mfma_f32_16x16x32_bf16 a[120:123],v[216:219],v[168:171],a[120:123]
buffer_load_dwordx4 v253,s[40:43],s94 offen lds
v_mfma_f32_16x16x32_bf16 a[184:187],v[216:219],v[176:179],a[184:187]
v_mfma_f32_16x16x32_bf16 a[248:251],v[216:219],v[184:187],a[248:251]
v_mfma_f32_16x16x32_bf16 a[60:63],v[220:223],v[160:163],a[60:63]
v_mfma_f32_16x16x32_bf16 a[124:127],v[220:223],v[168:171],a[124:127]
buffer_load_dwordx4 v253,s[44:47],s94 offen offset:1024 lds
v_mfma_f32_16x16x32_bf16 a[188:191],v[220:223],v[176:179],a[188:191]
v_mfma_f32_16x16x32_bf16 a[252:255],v[220:223],v[184:187],a[252:255]
v_mfma_f32_16x16x32_bf16 v[236:239],v[240:243],v[184:187],v[236:239]
s_waitcnt lgkmcnt(0)
s_cbranch_vccnz Lstub0a_%=
Lback0a_%=:
v_mfma_f32_16x16x32_bf16 v[160:163],v[192:195],v[0:3],0
s_sub_u32 s74,s74,1
ds_read_b128 v[208:211],v248 offset:34816
v_mfma_f32_16x16x32_bf16 v[160:163],v[196:199],v[4:7],v[160:163]
v_exp_f32 v128,v128
v_mfma_f32_16x16x32_bf16 v[160:163],v[200:203],v[32:35],v[160:163]
ds_read_b128 v[212:215],v249 offset:34816
v_mfma_f32_16x16x32_bf16 v[160:163],v[204:207],v[36:39],v[160:163]
v_exp_f32 v129,v129
v_mfma_f32_16x16x32_bf16 v[168:171],v[192:195],v[8:11],0
ds_read_b128 v[216:219],v248 offset:35840
v_cvt_pk_bf16_f32 v128,v128,v129
v_mfma_f32_16x16x32_bf16 v[168:171],v[196:199],v[12:15],v[168:171]
v_exp_f32 v130,v130
v_mfma_f32_16x16x32_bf16 v[168:171],v[200:203],v[40:43],v[168:171]
ds_read_b128 v[220:223],v249 offset:35840
v_mfma_f32_16x16x32_bf16 v[168:171],v[204:207],v[44:47],v[168:171]
v_exp_f32 v131,v131
)"
R"(v_mfma_f32_16x16x32_bf16 v[176:179],v[192:195],v[16:19],0
v_exp_f32 v132,v132
v_mfma_f32_16x16x32_bf16 v[176:179],v[196:199],v[20:23],v[176:179]
buffer_load_dwordx4 v253,s[48:51],s94 offen offset:2048 lds
v_mfma_f32_16x16x32_bf16 v[176:179],v[200:203],v[48:51],v[176:179]
v_exp_f32 v133,v133
v_mfma_f32_16x16x32_bf16 v[176:179],v[204:207],v[52:55],v[176:179]
v_exp_f32 v134,v134
v_mfma_f32_16x16x32_bf16 v[184:187],v[192:195],v[24:27],0
buffer_load_dwordx4 v253,s[52:55],s94 offen offset:3072 lds
v_mfma_f32_16x16x32_bf16 v[184:187],v[196:199],v[28:31],v[184:187]
s_add_u32 s94,s94,16384
v_exp_f32 v135,v135
v_mfma_f32_16x16x32_bf16 v[184:187],v[200:203],v[56:59],v[184:187]
v_exp_f32 v136,v136
v_mfma_f32_16x16x32_bf16 v[184:187],v[204:207],v[60:63],v[184:187]
s_waitcnt lgkmcnt(0)
v_exp_f32 v137,v137
v_mfma_f32_16x16x32_bf16 v[160:163],v[208:211],v[64:67],v[160:163]
ds_read_b128 v[192:195],v248 offset:40960
v_cvt_pk_bf16_f32 v129,v130,v131
v_mfma_f32_16x16x32_bf16 v[160:163],v[212:215],v[68:71],v[160:163]
v_exp_f32 v138,v138
v_mfma_f32_16x16x32_bf16 v[160:163],v[216:219],v[96:99],v[160:163]
ds_read_b128 v[196:199],v249 offset:40960
v_cvt_pk_bf16_f32 v130,v132,v133
v_mfma_f32_16x16x32_bf16 v[160:163],v[220:223],v[100:103],v[160:163]
v_exp_f32 v139,v139
v_mfma_f32_16x16x32_bf16 v[168:171],v[208:211],v[72:75],v[168:171]
ds_read_b128 v[200:203],v248 offset:41984
v_cvt_pk_bf16_f32 v131,v134,v135
v_mfma_f32_16x16x32_bf16 v[168:171],v[212:215],v[76:79],v[168:171]
s_add_u32 m0,s92,0x1c000
v_exp_f32 v140,v140
v_mfma_f32_16x16x32_bf16 v[168:171],v[216:219],v[104:107],v[168:171]
ds_read_b128 v[204:207],v249 offset:41984
v_cvt_pk_bf16_f32 v136,v136,v137
v_mfma_f32_16x16x32_bf16 v[168:171],v[220:223],v[108:111],v[168:171]
v_exp_f32 v141,v141
v_mfma_f32_16x16x32_bf16 v[176:179],v[208:211],v[80:83],v[176:179]
v_exp_f32 v142,v142
v_mfma_f32_16x16x32_bf16 v[176:179],v[212:215],v[84:87],v[176:179]
buffer_load_dwordx4 v253,s[40:43],s94 offen lds
v_mfma_f32_16x16x32_bf16 v[176:179],v[216:219],v[112:115],v[176:179]
v_exp_f32 v143,v143
v_mfma_f32_16x16x32_bf16 v[176:179],v[220:223],v[116:119],v[176:179]
v_exp_f32 v144,v144
v_mfma_f32_16x16x32_bf16 v[184:187],v[208:211],v[88:91],v[184:187]
buffer_load_dwordx4 v253,s[44:47],s94 offen offset:1024 lds
v_mfma_f32_16x16x32_bf16 v[184:187],v[212:215],v[92:95],v[184:187]
v_exp_f32 v145,v145
v_mfma_f32_16x16x32_bf16 v[184:187],v[216:219],v[120:123],v[184:187]
v_exp_f32 v146,v146
v_mfma_f32_16x16x32_bf16 v[184:187],v[220:223],v[124:127],v[184:187]
s_waitcnt lgkmcnt(0)
v_exp_f32 v147,v147
v_mfma_f32_16x16x32_bf16 v[164:167],v[192:195],v[0:3],0
ds_read_b128 v[208:211],v248 offset:43008
v_cvt_pk_bf16_f32 v137,v138,v139
v_mfma_f32_16x16x32_bf16 v[164:167],v[196:199],v[4:7],v[164:167]
v_exp_f32 v148,v148
v_mfma_f32_16x16x32_bf16 v[164:167],v[200:203],v[32:35],v[164:167]
ds_read_b128 v[212:215],v249 offset:43008
v_cvt_pk_bf16_f32 v138,v140,v141
v_mfma_f32_16x16x32_bf16 v[164:167],v[204:207],v[36:39],v[164:167]
v_exp_f32 v149,v149
v_mfma_f32_16x16x32_bf16 v[172:175],v[192:195],v[8:11],0
ds_read_b128 v[216:219],v248 offset:44032
v_cvt_pk_bf16_f32 v139,v142,v143
v_mfma_f32_16x16x32_bf16 v[172:175],v[196:199],v[12:15],v[172:175]
v_exp_f32 v150,v150
v_mfma_f32_16x16x32_bf16 v[172:175],v[200:203],v[40:43],v[172:175]
ds_read_b128 v[220:223],v249 offset:44032
v_cvt_pk_bf16_f32 v144,v144,v145
v_mfma_f32_16x16x32_bf16 v[172:175],v[204:207],v[44:47],v[172:175]
v_exp_f32 v151,v151
v_mfma_f32_16x16x32_bf16 v[180:183],v[192:195],v[16:19],0
v_exp_f32 v152,v152
v_mfma_f32_16x16x32_bf16 v[180:183],v[196:199],v[20:23],v[180:183]
buffer_load_dwordx4 v253,s[48:51],s94 offen offset:2048 lds
v_mfma_f32_16x16x32_bf16 v[180:183],v[200:203],v[48:51],v[180:183]
v_exp_f32 v153,v153
v_mfma_f32_16x16x32_bf16 v[180:183],v[204:207],v[52:55],v[180:183]
v_exp_f32 v154,v154
v_mfma_f32_16x16x32_bf16 v[188:191],v[192:195],v[24:27],0
buffer_load_dwordx4 v253,s[52:55],s94 offen offset:3072 lds
v_mfma_f32_16x16x32_bf16 v[188:191],v[196:199],v[28:31],v[188:191]
s_add_u32 s94,s94,16384
v_exp_f32 v155,v155
v_mfma_f32_16x16x32_bf16 v[188:191],v[200:203],v[56:59],v[188:191]
v_exp_f32 v156,v156
v_mfma_f32_16x16x32_bf16 v[188:191],v[204:207],v[60:63],v[188:191]
s_waitcnt vmcnt(16) lgkmcnt(0)
v_exp_f32 v157,v157
v_mfma_f32_16x16x32_bf16 v[164:167],v[208:211],v[64:67],v[164:167]
s_barrier
ds_read_b64_tr_b16 v[192:193],v250
v_cvt_pk_bf16_f32 v145,v146,v147
v_mfma_f32_16x16x32_bf16 v[164:167],v[212:215],v[68:71],v[164:167]
ds_read_b64_tr_b16 v[194:195],v250 offset:8192
v_cvt_pk_bf16_f32 v146,v148,v149
v_mfma_f32_16x16x32_bf16 v[164:167],v[216:219],v[96:99],v[164:167]
ds_read_b64_tr_b16 v[196:197],v251
v_cvt_pk_bf16_f32 v147,v150,v151
v_mfma_f32_16x16x32_bf16 v[164:167],v[220:223],v[100:103],v[164:167]
ds_read_b64_tr_b16 v[198:199],v251 offset:8192
v_cvt_pk_bf16_f32 v152,v152,v153
v_mfma_f32_16x16x32_bf16 v[172:175],v[208:211],v[72:75],v[172:175]
ds_read_b64_tr_b16 v[200:201],v250 offset:512
v_cvt_pk_bf16_f32 v153,v154,v155
v_mfma_f32_16x16x32_bf16 v[172:175],v[212:215],v[76:79],v[172:175]
s_add_u32 m0,s92,0x8000
ds_read_b64_tr_b16 v[202:203],v250 offset:8704
v_cvt_pk_bf16_f32 v154,v156,v157
v_mfma_f32_16x16x32_bf16 v[172:175],v[216:219],v[104:107],v[172:175]
ds_read_b64_tr_b16 v[204:205],v251 offset:512
v_cvt_pk_bf16_f32 v160,0,v160
v_mfma_f32_16x16x32_bf16 v[172:175],v[220:223],v[108:111],v[172:175]
ds_read_b64_tr_b16 v[206:207],v251 offset:8704
v_fma_f32 v160,v160,s72,v244
v_mfma_f32_16x16x32_bf16 v[180:183],v[208:211],v[80:83],v[180:183]
v_exp_f32 v158,v158
v_mfma_f32_16x16x32_bf16 v[180:183],v[212:215],v[84:87],v[180:183]
buffer_load_dwordx4 v252,s[24:27],s93 offen lds
v_mfma_f32_16x16x32_bf16 v[180:183],v[216:219],v[112:115],v[180:183]
v_exp_f32 v159,v159
v_mfma_f32_16x16x32_bf16 v[180:183],v[220:223],v[116:119],v[180:183]
v_cvt_pk_bf16_f32 v155,v158,v159
v_cvt_pk_bf16_f32 v161,0,v161
v_mfma_f32_16x16x32_bf16 v[188:191],v[208:211],v[88:91],v[188:191]
buffer_load_dwordx4 v252,s[28:31],s93 offen offset:1024 lds
v_mfma_f32_16x16x32_bf16 v[188:191],v[212:215],v[92:95],v[188:191]
v_fma_f32 v161,v161,s72,v244
v_cvt_pk_bf16_f32 v162,0,v162
v_mfma_f32_16x16x32_bf16 v[188:191],v[216:219],v[120:123],v[188:191]
v_fma_f32 v162,v162,s72,v244
v_cvt_pk_bf16_f32 v163,0,v163
v_mfma_f32_16x16x32_bf16 v[188:191],v[220:223],v[124:127],v[188:191]
s_waitcnt lgkmcnt(0)
v_fma_f32 v163,v163,s72,v244
v_max3_f32 v132,v160,v161,v161
v_mfma_f32_16x16x32_bf16 a[0:3],v[192:195],v[128:131],a[0:3]
ds_read_b64_tr_b16 v[208:209],v250 offset:1024
v_max3_f32 v132,v162,v163,v132
v_mfma_f32_16x16x32_bf16 a[4:7],v[196:199],v[128:131],a[4:7]
ds_read_b64_tr_b16 v[210:211],v250 offset:9216
v_cvt_pk_bf16_f32 v168,0,v168
v_mfma_f32_16x16x32_bf16 a[8:11],v[200:203],v[128:131],a[8:11]
ds_read_b64_tr_b16 v[212:213],v251 offset:1024
v_fma_f32 v168,v168,s72,v245
v_mfma_f32_16x16x32_bf16 a[12:15],v[204:207],v[128:131],a[12:15]
ds_read_b64_tr_b16 v[214:215],v251 offset:9216
v_cvt_pk_bf16_f32 v169,0,v169
v_mfma_f32_16x16x32_bf16 a[64:67],v[192:195],v[136:139],a[64:67]
ds_read_b64_tr_b16 v[216:217],v250 offset:1536
v_fma_f32 v169,v169,s72,v245
v_mfma_f32_16x16x32_bf16 a[68:71],v[196:199],v[136:139],a[68:71]
ds_read_b64_tr_b16 v[218:219],v250 offset:9728
v_cvt_pk_bf16_f32 v170,0,v170
v_mfma_f32_16x16x32_bf16 a[72:75],v[200:203],v[136:139],a[72:75]
ds_read_b64_tr_b16 v[220:221],v251 offset:1536
v_fma_f32 v170,v170,s72,v245
v_mfma_f32_16x16x32_bf16 a[76:79],v[204:207],v[136:139],a[76:79]
ds_read_b64_tr_b16 v[222:223],v251 offset:9728
v_cvt_pk_bf16_f32 v171,0,v171
v_mfma_f32_16x16x32_bf16 a[128:131],v[192:195],v[144:147],a[128:131]
v_fma_f32 v171,v171,s72,v245
v_max3_f32 v132,v168,v169,v132
v_mfma_f32_16x16x32_bf16 a[132:135],v[196:199],v[144:147],a[132:135]
buffer_load_dwordx4 v252,s[32:35],s93 offen offset:2048 lds
v_mfma_f32_16x16x32_bf16 a[136:139],v[200:203],v[144:147],a[136:139]
v_max3_f32 v132,v170,v171,v132
v_cvt_pk_bf16_f32 v176,0,v176
v_mfma_f32_16x16x32_bf16 a[140:143],v[204:207],v[144:147],a[140:143]
v_fma_f32 v176,v176,s72,v246
v_cvt_pk_bf16_f32 v177,0,v177
v_mfma_f32_16x16x32_bf16 a[192:195],v[192:195],v[152:155],a[192:195]
v_fma_f32 v177,v177,s72,v246
v_cvt_pk_bf16_f32 v178,0,v178
v_mfma_f32_16x16x32_bf16 a[196:199],v[196:199],v[152:155],a[196:199]
buffer_load_dwordx4 v252,s[36:39],s93 offen offset:3072 lds
v_mfma_f32_16x16x32_bf16 a[200:203],v[200:203],v[152:155],a[200:203]
s_add_u32 s93,s93,16384
v_fma_f32 v178,v178,s72,v246
v_cvt_pk_bf16_f32 v179,0,v179
v_mfma_f32_16x16x32_bf16 a[204:207],v[204:207],v[152:155],a[204:207]
v_fma_f32 v179,v179,s72,v246
v_max3_f32 v132,v176,v177,v132
v_mfma_f32_16x16x32_bf16 v[224:227],v[240:243],v[128:131],v[224:227]
s_waitcnt lgkmcnt(0)
v_max3_f32 v132,v178,v179,v132
v_cvt_pk_bf16_f32 v184,0,v184
v_mfma_f32_16x16x32_bf16 a[16:19],v[208:211],v[128:131],a[16:19]
ds_read_b64_tr_b16 v[192:193],v250 offset:2048
v_fma_f32 v184,v184,s72,v247
v_mfma_f32_16x16x32_bf16 a[80:83],v[208:211],v[136:139],a[80:83]
ds_read_b64_tr_b16 v[194:195],v250 offset:10240
v_cvt_pk_bf16_f32 v185,0,v185
v_mfma_f32_16x16x32_bf16 a[144:147],v[208:211],v[144:147],a[144:147]
ds_read_b64_tr_b16 v[196:197],v251 offset:2048
)"
R"(v_fma_f32 v185,v185,s72,v247
v_mfma_f32_16x16x32_bf16 a[208:211],v[208:211],v[152:155],a[208:211]
ds_read_b64_tr_b16 v[198:199],v251 offset:10240
v_cvt_pk_bf16_f32 v186,0,v186
v_mfma_f32_16x16x32_bf16 a[20:23],v[212:215],v[128:131],a[20:23]
ds_read_b64_tr_b16 v[200:201],v250 offset:2560
v_fma_f32 v186,v186,s72,v247
v_mfma_f32_16x16x32_bf16 a[84:87],v[212:215],v[136:139],a[84:87]
s_add_u32 m0,s92,0xc000
ds_read_b64_tr_b16 v[202:203],v250 offset:10752
v_cvt_pk_bf16_f32 v187,0,v187
v_mfma_f32_16x16x32_bf16 a[148:151],v[212:215],v[144:147],a[148:151]
ds_read_b64_tr_b16 v[204:205],v251 offset:2560
v_fma_f32 v187,v187,s72,v247
v_mfma_f32_16x16x32_bf16 a[212:215],v[212:215],v[152:155],a[212:215]
ds_read_b64_tr_b16 v[206:207],v251 offset:10752
v_max3_f32 v132,v184,v185,v132
v_mfma_f32_16x16x32_bf16 a[24:27],v[216:219],v[128:131],a[24:27]
v_max3_f32 v132,v186,v187,v132
v_cvt_pk_bf16_f32 v164,0,v164
v_mfma_f32_16x16x32_bf16 a[88:91],v[216:219],v[136:139],a[88:91]
buffer_load_dwordx4 v252,s[24:27],s93 offen lds
v_mfma_f32_16x16x32_bf16 a[152:155],v[216:219],v[144:147],a[152:155]
v_fma_f32 v164,v164,s72,v244
v_cvt_pk_bf16_f32 v165,0,v165
v_mfma_f32_16x16x32_bf16 a[216:219],v[216:219],v[152:155],a[216:219]
v_fma_f32 v165,v165,s72,v244
v_cvt_pk_bf16_f32 v166,0,v166
v_mfma_f32_16x16x32_bf16 a[28:31],v[220:223],v[128:131],a[28:31]
v_fma_f32 v166,v166,s72,v244
v_cvt_pk_bf16_f32 v167,0,v167
v_mfma_f32_16x16x32_bf16 a[92:95],v[220:223],v[136:139],a[92:95]
buffer_load_dwordx4 v252,s[28:31],s93 offen offset:1024 lds
v_mfma_f32_16x16x32_bf16 a[156:159],v[220:223],v[144:147],a[156:159]
v_fma_f32 v167,v167,s72,v244
v_max3_f32 v132,v164,v165,v132
v_mfma_f32_16x16x32_bf16 a[220:223],v[220:223],v[152:155],a[220:223]
v_max3_f32 v132,v166,v167,v132
v_cvt_pk_bf16_f32 v172,0,v172
v_mfma_f32_16x16x32_bf16 v[228:231],v[240:243],v[136:139],v[228:231]
s_waitcnt lgkmcnt(0)
v_fma_f32 v172,v172,s72,v245
v_cvt_pk_bf16_f32 v173,0,v173
v_mfma_f32_16x16x32_bf16 a[32:35],v[192:195],v[128:131],a[32:35]
ds_read_b64_tr_b16 v[208:209],v250 offset:3072
v_fma_f32 v173,v173,s72,v245
v_mfma_f32_16x16x32_bf16 a[96:99],v[192:195],v[136:139],a[96:99]
ds_read_b64_tr_b16 v[210:211],v250 offset:11264
v_cvt_pk_bf16_f32 v174,0,v174
v_mfma_f32_16x16x32_bf16 a[160:163],v[192:195],v[144:147],a[160:163]
ds_read_b64_tr_b16 v[212:213],v251 offset:3072
v_fma_f32 v174,v174,s72,v245
v_mfma_f32_16x16x32_bf16 a[224:227],v[192:195],v[152:155],a[224:227]
ds_read_b64_tr_b16 v[214:215],v251 offset:11264
v_cvt_pk_bf16_f32 v175,0,v175
v_mfma_f32_16x16x32_bf16 a[36:39],v[196:199],v[128:131],a[36:39]
ds_read_b64_tr_b16 v[216:217],v250 offset:3584
v_fma_f32 v175,v175,s72,v245
v_mfma_f32_16x16x32_bf16 a[100:103],v[196:199],v[136:139],a[100:103]
ds_read_b64_tr_b16 v[218:219],v250 offset:11776
v_max3_f32 v132,v172,v173,v132
v_mfma_f32_16x16x32_bf16 a[164:167],v[196:199],v[144:147],a[164:167]
ds_read_b64_tr_b16 v[220:221],v251 offset:3584
v_max3_f32 v132,v174,v175,v132
v_mfma_f32_16x16x32_bf16 a[228:231],v[196:199],v[152:155],a[228:231]
ds_read_b64_tr_b16 v[222:223],v251 offset:11776
v_cvt_pk_bf16_f32 v180,0,v180
v_mfma_f32_16x16x32_bf16 a[40:43],v[200:203],v[128:131],a[40:43]
v_fma_f32 v180,v180,s72,v246
v_cvt_pk_bf16_f32 v181,0,v181
v_mfma_f32_16x16x32_bf16 a[104:107],v[200:203],v[136:139],a[104:107]
buffer_load_dwordx4 v252,s[32:35],s93 offen offset:2048 lds
v_mfma_f32_16x16x32_bf16 a[168:171],v[200:203],v[144:147],a[168:171]
v_fma_f32 v181,v181,s72,v246
v_cvt_pk_bf16_f32 v182,0,v182
v_mfma_f32_16x16x32_bf16 a[232:235],v[200:203],v[152:155],a[232:235]
v_fma_f32 v182,v182,s72,v246
v_cvt_pk_bf16_f32 v183,0,v183
v_mfma_f32_16x16x32_bf16 a[44:47],v[204:207],v[128:131],a[44:47]
v_fma_f32 v183,v183,s72,v246
v_max3_f32 v132,v180,v181,v132
v_mfma_f32_16x16x32_bf16 a[108:111],v[204:207],v[136:139],a[108:111]
buffer_load_dwordx4 v252,s[36:39],s93 offen offset:3072 lds
v_mfma_f32_16x16x32_bf16 a[172:175],v[204:207],v[144:147],a[172:175]
s_add_u32 s93,s93,16384
v_max3_f32 v132,v182,v183,v132
v_cvt_pk_bf16_f32 v188,0,v188
v_mfma_f32_16x16x32_bf16 a[236:239],v[204:207],v[152:155],a[236:239]
v_fma_f32 v188,v188,s72,v247
v_cvt_pk_bf16_f32 v189,0,v189
v_mfma_f32_16x16x32_bf16 v[232:235],v[240:243],v[144:147],v[232:235]
s_waitcnt vmcnt(16) lgkmcnt(0)
v_fma_f32 v189,v189,s72,v247
v_cvt_pk_bf16_f32 v190,0,v190
v_mfma_f32_16x16x32_bf16 a[48:51],v[208:211],v[128:131],a[48:51]
s_barrier
ds_read_b128 v[192:195],v248
v_fma_f32 v190,v190,s72,v247
v_mfma_f32_16x16x32_bf16 a[112:115],v[208:211],v[136:139],a[112:115]
v_cvt_pk_bf16_f32 v191,0,v191
v_fma_f32 v191,v191,s72,v247
v_mfma_f32_16x16x32_bf16 a[176:179],v[208:211],v[144:147],a[176:179]
ds_read_b128 v[196:199],v249
v_max3_f32 v132,v188,v189,v132
v_mfma_f32_16x16x32_bf16 a[240:243],v[208:211],v[152:155],a[240:243]
v_max3_f32 v132,v190,v191,v132
v_cmp_lt_f32 vcc,s73,v132
v_mfma_f32_16x16x32_bf16 a[52:55],v[212:215],v[128:131],a[52:55]
ds_read_b128 v[200:203],v248 offset:1024
v_mfma_f32_16x16x32_bf16 a[116:119],v[212:215],v[136:139],a[116:119]
s_add_u32 m0,s92,0x10000
v_mfma_f32_16x16x32_bf16 a[180:183],v[212:215],v[144:147],a[180:183]
ds_read_b128 v[204:207],v249 offset:1024
v_mfma_f32_16x16x32_bf16 a[244:247],v[212:215],v[152:155],a[244:247]
v_mfma_f32_16x16x32_bf16 a[56:59],v[216:219],v[128:131],a[56:59]
v_mfma_f32_16x16x32_bf16 a[120:123],v[216:219],v[136:139],a[120:123]
buffer_load_dwordx4 v253,s[40:43],s94 offen lds
v_mfma_f32_16x16x32_bf16 a[184:187],v[216:219],v[144:147],a[184:187]
v_mfma_f32_16x16x32_bf16 a[248:251],v[216:219],v[152:155],a[248:251]
v_mfma_f32_16x16x32_bf16 a[60:63],v[220:223],v[128:131],a[60:63]
v_mfma_f32_16x16x32_bf16 a[124:127],v[220:223],v[136:139],a[124:127]
buffer_load_dwordx4 v253,s[44:47],s94 offen offset:1024 lds
v_mfma_f32_16x16x32_bf16 a[188:191],v[220:223],v[144:147],a[188:191]
v_mfma_f32_16x16x32_bf16 a[252:255],v[220:223],v[152:155],a[252:255]
v_mfma_f32_16x16x32_bf16 v[236:239],v[240:243],v[152:155],v[236:239]
s_waitcnt lgkmcnt(0)
s_cbranch_vccnz Lstub1a_%=
Lback1a_%=:
s_cmp_gt_u32 s74,1
s_cbranch_scc1 Lloopa_%=
s_cmp_eq_u32 s74,1
s_cbranch_scc1 Lf1la_%=
s_waitcnt vmcnt(16)
v_cmp_eq_u32 vcc,v254,v255
s_nop 1
s_cbranch_vccnz Lspin1a_%=
Lok1a_%=:
v_mfma_f32_16x16x32_bf16 v[128:131],v[192:195],v[0:3],0
s_and_b32 s99,%[w],1
ds_read_b128 v[208:211],v248 offset:2048
v_mbcnt_lo_u32_b32 v254,-1,0
v_mfma_f32_16x16x32_bf16 v[128:131],v[196:199],v[4:7],v[128:131]
s_mul_i32 s99,s99,28672
v_mbcnt_hi_u32_b32 v254,-1,v254
v_bfe_u32 v252,v254,2,3
v_mfma_f32_16x16x32_bf16 v[128:131],v[200:203],v[32:35],v[128:131]
s_lshr_b32 s101,%[w],1
ds_read_b128 v[212:215],v249 offset:2048
v_mul_u32_u24 v252,896,v252
v_mfma_f32_16x16x32_bf16 v[128:131],v[204:207],v[36:39],v[128:131]
s_lshl_b32 s101,s101,7
v_and_b32 v254,48,v254
v_lshl_add_u32 v252,v254,1,v252
v_mfma_f32_16x16x32_bf16 v[136:139],v[192:195],v[8:11],0
s_add_u32 s99,s99,s101
ds_read_b128 v[216:219],v248 offset:3072
v_mbcnt_lo_u32_b32 v254,-1,0
v_mfma_f32_16x16x32_bf16 v[136:139],v[196:199],v[12:15],v[136:139]
s_and_b32 s101,%[h],2
v_mbcnt_hi_u32_b32 v254,-1,v254
v_and_b32 v254,3,v254
v_mfma_f32_16x16x32_bf16 v[136:139],v[200:203],v[40:43],v[136:139]
s_mul_i32 s101,s101,256
ds_read_b128 v[220:223],v249 offset:3072
v_lshlrev_b32 v254,4,v254
v_mfma_f32_16x16x32_bf16 v[136:139],v[204:207],v[44:47],v[136:139]
s_add_u32 s99,s99,s101
v_xor_b32 v252,v252,v254
v_add_u32 v252,s99,v252
v_mfma_f32_16x16x32_bf16 v[144:147],v[192:195],v[16:19],0
s_add_u32 s75,s75,1
v_exp_f32 v160,v160
v_mfma_f32_16x16x32_bf16 v[144:147],v[196:199],v[20:23],v[144:147]
s_mul_i32 s86,%[tb],57344
buffer_load_dwordx4 v253,s[48:51],s94 offen offset:2048 lds
v_mfma_f32_16x16x32_bf16 v[144:147],v[200:203],v[48:51],v[144:147]
s_lshr_b32 s101,%[h],2
v_exp_f32 v161,v161
v_mfma_f32_16x16x32_bf16 v[144:147],v[204:207],v[52:55],v[144:147]
s_mul_i32 s101,s101,28672
v_exp_f32 v162,v162
v_mfma_f32_16x16x32_bf16 v[152:155],v[192:195],v[24:27],0
s_add_u32 s86,s86,s101
buffer_load_dwordx4 v253,s[52:55],s94 offen offset:3072 lds
v_mfma_f32_16x16x32_bf16 v[152:155],v[196:199],v[28:31],v[152:155]
s_add_u32 s94,s94,16384
v_exp_f32 v163,v163
v_mfma_f32_16x16x32_bf16 v[152:155],v[200:203],v[56:59],v[152:155]
s_add_u32 s86,s86,0x700000
v_exp_f32 v164,v164
v_mfma_f32_16x16x32_bf16 v[152:155],v[204:207],v[60:63],v[152:155]
s_waitcnt lgkmcnt(0)
v_exp_f32 v165,v165
v_mfma_f32_16x16x32_bf16 v[128:131],v[208:211],v[64:67],v[128:131]
ds_read_b128 v[192:195],v248 offset:8192
v_cvt_pk_bf16_f32 v160,v160,v161
v_mfma_f32_16x16x32_bf16 v[128:131],v[212:215],v[68:71],v[128:131]
v_exp_f32 v166,v166
v_mfma_f32_16x16x32_bf16 v[128:131],v[216:219],v[96:99],v[128:131]
ds_read_b128 v[196:199],v249 offset:8192
)"
R"(v_cvt_pk_bf16_f32 v161,v162,v163
v_mfma_f32_16x16x32_bf16 v[128:131],v[220:223],v[100:103],v[128:131]
v_exp_f32 v167,v167
v_mfma_f32_16x16x32_bf16 v[136:139],v[208:211],v[72:75],v[136:139]
ds_read_b128 v[200:203],v248 offset:9216
v_cvt_pk_bf16_f32 v162,v164,v165
v_mfma_f32_16x16x32_bf16 v[136:139],v[212:215],v[76:79],v[136:139]
s_add_u32 m0,s92,0x14000
v_exp_f32 v168,v168
v_mfma_f32_16x16x32_bf16 v[136:139],v[216:219],v[104:107],v[136:139]
ds_read_b128 v[204:207],v249 offset:9216
v_cvt_pk_bf16_f32 v163,v166,v167
v_mfma_f32_16x16x32_bf16 v[136:139],v[220:223],v[108:111],v[136:139]
v_exp_f32 v169,v169
v_mfma_f32_16x16x32_bf16 v[144:147],v[208:211],v[80:83],v[144:147]
v_exp_f32 v170,v170
v_mfma_f32_16x16x32_bf16 v[144:147],v[212:215],v[84:87],v[144:147]
buffer_load_dwordx4 v253,s[40:43],s94 offen lds
v_mfma_f32_16x16x32_bf16 v[144:147],v[216:219],v[112:115],v[144:147]
v_exp_f32 v171,v171
v_mfma_f32_16x16x32_bf16 v[144:147],v[220:223],v[116:119],v[144:147]
v_exp_f32 v172,v172
v_mfma_f32_16x16x32_bf16 v[152:155],v[208:211],v[88:91],v[152:155]
buffer_load_dwordx4 v253,s[44:47],s94 offen offset:1024 lds
v_mfma_f32_16x16x32_bf16 v[152:155],v[212:215],v[92:95],v[152:155]
v_exp_f32 v173,v173
v_mfma_f32_16x16x32_bf16 v[152:155],v[216:219],v[120:123],v[152:155]
v_exp_f32 v174,v174
v_mfma_f32_16x16x32_bf16 v[152:155],v[220:223],v[124:127],v[152:155]
s_waitcnt lgkmcnt(0)
v_exp_f32 v175,v175
v_mfma_f32_16x16x32_bf16 v[132:135],v[192:195],v[0:3],0
ds_read_b128 v[208:211],v248 offset:10240
v_cvt_pk_bf16_f32 v168,v168,v169
v_mfma_f32_16x16x32_bf16 v[132:135],v[196:199],v[4:7],v[132:135]
v_exp_f32 v176,v176
v_mfma_f32_16x16x32_bf16 v[132:135],v[200:203],v[32:35],v[132:135]
ds_read_b128 v[212:215],v249 offset:10240
v_cvt_pk_bf16_f32 v169,v170,v171
v_mfma_f32_16x16x32_bf16 v[132:135],v[204:207],v[36:39],v[132:135]
v_exp_f32 v177,v177
v_mfma_f32_16x16x32_bf16 v[140:143],v[192:195],v[8:11],0
ds_read_b128 v[216:219],v248 offset:11264
v_cvt_pk_bf16_f32 v170,v172,v173
v_mfma_f32_16x16x32_bf16 v[140:143],v[196:199],v[12:15],v[140:143]
v_exp_f32 v178,v178
v_mfma_f32_16x16x32_bf16 v[140:143],v[200:203],v[40:43],v[140:143]
ds_read_b128 v[220:223],v249 offset:11264
v_cvt_pk_bf16_f32 v171,v174,v175
v_mfma_f32_16x16x32_bf16 v[140:143],v[204:207],v[44:47],v[140:143]
v_exp_f32 v179,v179
v_mfma_f32_16x16x32_bf16 v[148:151],v[192:195],v[16:19],0
v_exp_f32 v180,v180
v_mfma_f32_16x16x32_bf16 v[148:151],v[196:199],v[20:23],v[148:151]
buffer_load_dwordx4 v253,s[48:51],s94 offen offset:2048 lds
v_mfma_f32_16x16x32_bf16 v[148:151],v[200:203],v[48:51],v[148:151]
v_exp_f32 v181,v181
v_mfma_f32_16x16x32_bf16 v[148:151],v[204:207],v[52:55],v[148:151]
v_exp_f32 v182,v182
v_mfma_f32_16x16x32_bf16 v[156:159],v[192:195],v[24:27],0
buffer_load_dwordx4 v253,s[52:55],s94 offen offset:3072 lds
v_mfma_f32_16x16x32_bf16 v[156:159],v[196:199],v[28:31],v[156:159]
s_add_u32 s94,s94,16384
v_exp_f32 v183,v183
v_mfma_f32_16x16x32_bf16 v[156:159],v[200:203],v[56:59],v[156:159]
v_exp_f32 v184,v184
v_mfma_f32_16x16x32_bf16 v[156:159],v[204:207],v[60:63],v[156:159]
s_waitcnt vmcnt(16) lgkmcnt(0)
v_exp_f32 v185,v185
v_mfma_f32_16x16x32_bf16 v[132:135],v[208:211],v[64:67],v[132:135]
s_barrier
ds_read_b64_tr_b16 v[192:193],v250 offset:32768
v_cvt_pk_bf16_f32 v176,v176,v177
v_mfma_f32_16x16x32_bf16 v[132:135],v[212:215],v[68:71],v[132:135]
ds_read_b64_tr_b16 v[194:195],v250 offset:40960
v_cvt_pk_bf16_f32 v177,v178,v179
v_mfma_f32_16x16x32_bf16 v[132:135],v[216:219],v[96:99],v[132:135]
ds_read_b64_tr_b16 v[196:197],v251 offset:32768
v_cvt_pk_bf16_f32 v178,v180,v181
v_mfma_f32_16x16x32_bf16 v[132:135],v[220:223],v[100:103],v[132:135]
ds_read_b64_tr_b16 v[198:199],v251 offset:40960
v_cvt_pk_bf16_f32 v179,v182,v183
v_mfma_f32_16x16x32_bf16 v[140:143],v[208:211],v[72:75],v[140:143]
ds_read_b64_tr_b16 v[200:201],v250 offset:33280
v_cvt_pk_bf16_f32 v184,v184,v185
v_mfma_f32_16x16x32_bf16 v[140:143],v[212:215],v[76:79],v[140:143]
s_add_u32 m0,s92,0x0
ds_read_b64_tr_b16 v[202:203],v250 offset:41472
v_mbcnt_lo_u32_b32 v255,-1,0
v_mfma_f32_16x16x32_bf16 v[140:143],v[216:219],v[104:107],v[140:143]
s_mov_b32 s95,0x0
ds_read_b64_tr_b16 v[204:205],v251 offset:33280
v_mbcnt_hi_u32_b32 v255,-1,v255
v_mfma_f32_16x16x32_bf16 v[140:143],v[220:223],v[108:111],v[140:143]
ds_read_b64_tr_b16 v[206:207],v251 offset:41472
v_and_b32 v255,7,v255
v_mfma_f32_16x16x32_bf16 v[148:151],v[208:211],v[80:83],v[148:151]
v_exp_f32 v186,v186
v_mfma_f32_16x16x32_bf16 v[148:151],v[212:215],v[84:87],v[148:151]
buffer_load_dwordx4 v252,s[56:59],s95 offen lds
v_mfma_f32_16x16x32_bf16 v[148:151],v[216:219],v[112:115],v[148:151]
v_exp_f32 v187,v187
v_mfma_f32_16x16x32_bf16 v[148:151],v[220:223],v[116:119],v[148:151]
v_exp_f32 v188,v188
v_mfma_f32_16x16x32_bf16 v[156:159],v[208:211],v[88:91],v[156:159]
buffer_load_dwordx4 v252,s[60:63],s95 offen offset:1024 lds
v_mfma_f32_16x16x32_bf16 v[156:159],v[212:215],v[92:95],v[156:159]
v_exp_f32 v189,v189
v_mfma_f32_16x16x32_bf16 v[156:159],v[216:219],v[120:123],v[156:159]
v_exp_f32 v190,v190
v_mfma_f32_16x16x32_bf16 v[156:159],v[220:223],v[124:127],v[156:159]
s_waitcnt lgkmcnt(0)
v_exp_f32 v191,v191
v_mfma_f32_16x16x32_bf16 a[0:3],v[192:195],v[160:163],a[0:3]
ds_read_b64_tr_b16 v[208:209],v250 offset:33792
v_cvt_pk_bf16_f32 v185,v186,v187
v_mfma_f32_16x16x32_bf16 a[4:7],v[196:199],v[160:163],a[4:7]
ds_read_b64_tr_b16 v[210:211],v250 offset:41984
v_cvt_pk_bf16_f32 v186,v188,v189
v_mfma_f32_16x16x32_bf16 a[8:11],v[200:203],v[160:163],a[8:11]
ds_read_b64_tr_b16 v[212:213],v251 offset:33792
v_cvt_pk_bf16_f32 v187,v190,v191
v_mfma_f32_16x16x32_bf16 a[12:15],v[204:207],v[160:163],a[12:15]
ds_read_b64_tr_b16 v[214:215],v251 offset:41984
v_lshlrev_b32 v255,7,v255
v_mfma_f32_16x16x32_bf16 a[64:67],v[192:195],v[168:171],a[64:67]
ds_read_b64_tr_b16 v[216:217],v250 offset:34304
v_mov_b32 v254,s75
v_mfma_f32_16x16x32_bf16 a[68:71],v[196:199],v[168:171],a[68:71]
ds_read_b64_tr_b16 v[218:219],v250 offset:42496
v_cvt_pk_bf16_f32 v128,0,v128
v_mfma_f32_16x16x32_bf16 a[72:75],v[200:203],v[168:171],a[72:75]
ds_read_b64_tr_b16 v[220:221],v251 offset:34304
v_fma_f32 v128,v128,s72,v244
v_mfma_f32_16x16x32_bf16 a[76:79],v[204:207],v[168:171],a[76:79]
ds_read_b64_tr_b16 v[222:223],v251 offset:42496
v_cvt_pk_bf16_f32 v129,0,v129
v_mfma_f32_16x16x32_bf16 a[128:131],v[192:195],v[176:179],a[128:131]
buffer_load_dword v255,v255,s[88:91],s97 offen offset:4 sc1
v_mfma_f32_16x16x32_bf16 a[132:135],v[196:199],v[176:179],a[132:135]
buffer_load_dwordx4 v252,s[64:67],s95 offen offset:2048 lds
v_mfma_f32_16x16x32_bf16 a[136:139],v[200:203],v[176:179],a[136:139]
buffer_store_dword v254,off,s[88:91],s78 sc1
v_mfma_f32_16x16x32_bf16 a[140:143],v[204:207],v[176:179],a[140:143]
v_fma_f32 v129,v129,s72,v244
v_cvt_pk_bf16_f32 v130,0,v130
v_mfma_f32_16x16x32_bf16 a[192:195],v[192:195],v[184:187],a[192:195]
v_fma_f32 v130,v130,s72,v244
v_cvt_pk_bf16_f32 v131,0,v131
v_mfma_f32_16x16x32_bf16 a[196:199],v[196:199],v[184:187],a[196:199]
buffer_load_dwordx4 v252,s[68:71],s95 offen offset:3072 lds
v_mfma_f32_16x16x32_bf16 a[200:203],v[200:203],v[184:187],a[200:203]
v_fma_f32 v131,v131,s72,v244
v_max3_f32 v164,v128,v129,v129
v_mfma_f32_16x16x32_bf16 a[204:207],v[204:207],v[184:187],a[204:207]
v_max3_f32 v164,v130,v131,v164
v_cvt_pk_bf16_f32 v136,0,v136
v_mfma_f32_16x16x32_bf16 v[224:227],v[240:243],v[160:163],v[224:227]
s_waitcnt lgkmcnt(0)
v_fma_f32 v136,v136,s72,v245
v_cvt_pk_bf16_f32 v137,0,v137
v_mfma_f32_16x16x32_bf16 a[16:19],v[208:211],v[160:163],a[16:19]
ds_read_b64_tr_b16 v[192:193],v250 offset:34816
v_fma_f32 v137,v137,s72,v245
v_mfma_f32_16x16x32_bf16 a[80:83],v[208:211],v[168:171],a[80:83]
ds_read_b64_tr_b16 v[194:195],v250 offset:43008
v_cvt_pk_bf16_f32 v138,0,v138
v_mfma_f32_16x16x32_bf16 a[144:147],v[208:211],v[176:179],a[144:147]
ds_read_b64_tr_b16 v[196:197],v251 offset:34816
v_fma_f32 v138,v138,s72,v245
v_mfma_f32_16x16x32_bf16 a[208:211],v[208:211],v[184:187],a[208:211]
ds_read_b64_tr_b16 v[198:199],v251 offset:43008
v_cvt_pk_bf16_f32 v139,0,v139
v_mfma_f32_16x16x32_bf16 a[20:23],v[212:215],v[160:163],a[20:23]
ds_read_b64_tr_b16 v[200:201],v250 offset:35328
v_fma_f32 v139,v139,s72,v245
v_mfma_f32_16x16x32_bf16 a[84:87],v[212:215],v[168:171],a[84:87]
s_add_u32 m0,s92,0x4000
ds_read_b64_tr_b16 v[202:203],v250 offset:43520
v_max3_f32 v164,v136,v137,v164
v_mfma_f32_16x16x32_bf16 a[148:151],v[212:215],v[176:179],a[148:151]
s_mov_b32 s95,0x100
ds_read_b64_tr_b16 v[204:205],v251 offset:35328
v_max3_f32 v164,v138,v139,v164
v_mfma_f32_16x16x32_bf16 a[212:215],v[212:215],v[184:187],a[212:215]
ds_read_b64_tr_b16 v[206:207],v251 offset:43520
v_cvt_pk_bf16_f32 v144,0,v144
v_mfma_f32_16x16x32_bf16 a[24:27],v[216:219],v[160:163],a[24:27]
v_fma_f32 v144,v144,s72,v246
v_cvt_pk_bf16_f32 v145,0,v145
v_mfma_f32_16x16x32_bf16 a[88:91],v[216:219],v[168:171],a[88:91]
buffer_load_dwordx4 v252,s[56:59],s95 offen lds
v_mfma_f32_16x16x32_bf16 a[152:155],v[216:219],v[176:179],a[152:155]
v_fma_f32 v145,v145,s72,v246
v_cvt_pk_bf16_f32 v146,0,v146
v_mfma_f32_16x16x32_bf16 a[216:219],v[216:219],v[184:187],a[216:219]
v_fma_f32 v146,v146,s72,v246
v_cvt_pk_bf16_f32 v147,0,v147
v_mfma_f32_16x16x32_bf16 a[28:31],v[220:223],v[160:163],a[28:31]
v_fma_f32 v147,v147,s72,v246
)"
R"(v_max3_f32 v164,v144,v145,v164
v_mfma_f32_16x16x32_bf16 a[92:95],v[220:223],v[168:171],a[92:95]
buffer_load_dwordx4 v252,s[60:63],s95 offen offset:1024 lds
v_mfma_f32_16x16x32_bf16 a[156:159],v[220:223],v[176:179],a[156:159]
v_max3_f32 v164,v146,v147,v164
v_cvt_pk_bf16_f32 v152,0,v152
v_mfma_f32_16x16x32_bf16 a[220:223],v[220:223],v[184:187],a[220:223]
v_fma_f32 v152,v152,s72,v247
v_cvt_pk_bf16_f32 v153,0,v153
v_mfma_f32_16x16x32_bf16 v[228:231],v[240:243],v[168:171],v[228:231]
s_waitcnt lgkmcnt(0)
v_fma_f32 v153,v153,s72,v247
v_cvt_pk_bf16_f32 v154,0,v154
v_mfma_f32_16x16x32_bf16 a[32:35],v[192:195],v[160:163],a[32:35]
ds_read_b64_tr_b16 v[208:209],v250 offset:35840
v_fma_f32 v154,v154,s72,v247
v_mfma_f32_16x16x32_bf16 a[96:99],v[192:195],v[168:171],a[96:99]
ds_read_b64_tr_b16 v[210:211],v250 offset:44032
v_cvt_pk_bf16_f32 v155,0,v155
v_mfma_f32_16x16x32_bf16 a[160:163],v[192:195],v[176:179],a[160:163]
ds_read_b64_tr_b16 v[212:213],v251 offset:35840
v_fma_f32 v155,v155,s72,v247
v_mfma_f32_16x16x32_bf16 a[224:227],v[192:195],v[184:187],a[224:227]
ds_read_b64_tr_b16 v[214:215],v251 offset:44032
v_max3_f32 v164,v152,v153,v164
v_mfma_f32_16x16x32_bf16 a[36:39],v[196:199],v[160:163],a[36:39]
ds_read_b64_tr_b16 v[216:217],v250 offset:36352
v_max3_f32 v164,v154,v155,v164
v_mfma_f32_16x16x32_bf16 a[100:103],v[196:199],v[168:171],a[100:103]
ds_read_b64_tr_b16 v[218:219],v250 offset:44544
v_cvt_pk_bf16_f32 v132,0,v132
v_mfma_f32_16x16x32_bf16 a[164:167],v[196:199],v[176:179],a[164:167]
ds_read_b64_tr_b16 v[220:221],v251 offset:36352
v_fma_f32 v132,v132,s72,v244
v_mfma_f32_16x16x32_bf16 a[228:231],v[196:199],v[184:187],a[228:231]
ds_read_b64_tr_b16 v[222:223],v251 offset:44544
v_cvt_pk_bf16_f32 v133,0,v133
v_mfma_f32_16x16x32_bf16 a[40:43],v[200:203],v[160:163],a[40:43]
v_fma_f32 v133,v133,s72,v244
v_cvt_pk_bf16_f32 v134,0,v134
v_mfma_f32_16x16x32_bf16 a[104:107],v[200:203],v[168:171],a[104:107]
buffer_load_dwordx4 v252,s[64:67],s95 offen offset:2048 lds
v_mfma_f32_16x16x32_bf16 a[168:171],v[200:203],v[176:179],a[168:171]
v_fma_f32 v134,v134,s72,v244
v_cvt_pk_bf16_f32 v135,0,v135
v_mfma_f32_16x16x32_bf16 a[232:235],v[200:203],v[184:187],a[232:235]
v_fma_f32 v135,v135,s72,v244
v_max3_f32 v164,v132,v133,v164
v_mfma_f32_16x16x32_bf16 a[44:47],v[204:207],v[160:163],a[44:47]
v_max3_f32 v164,v134,v135,v164
v_cvt_pk_bf16_f32 v140,0,v140
v_mfma_f32_16x16x32_bf16 a[108:111],v[204:207],v[168:171],a[108:111]
buffer_load_dwordx4 v252,s[68:71],s95 offen offset:3072 lds
v_mfma_f32_16x16x32_bf16 a[172:175],v[204:207],v[176:179],a[172:175]
v_fma_f32 v140,v140,s72,v245
v_cvt_pk_bf16_f32 v141,0,v141
v_mfma_f32_16x16x32_bf16 a[236:239],v[204:207],v[184:187],a[236:239]
v_fma_f32 v141,v141,s72,v245
v_cvt_pk_bf16_f32 v142,0,v142
v_mfma_f32_16x16x32_bf16 v[232:235],v[240:243],v[176:179],v[232:235]
s_waitcnt vmcnt(18) lgkmcnt(0)
v_fma_f32 v142,v142,s72,v245
v_cvt_pk_bf16_f32 v143,0,v143
v_mfma_f32_16x16x32_bf16 a[48:51],v[208:211],v[160:163],a[48:51]
s_barrier
ds_read_b128 v[192:195],v248 offset:32768
v_fma_f32 v143,v143,s72,v245
v_mfma_f32_16x16x32_bf16 a[112:115],v[208:211],v[168:171],a[112:115]
s_waitcnt vmcnt(7)
v_max3_f32 v164,v140,v141,v164
v_max3_f32 v164,v142,v143,v164
v_mfma_f32_16x16x32_bf16 a[176:179],v[208:211],v[176:179],a[176:179]
ds_read_b128 v[196:199],v249 offset:32768
v_cvt_pk_bf16_f32 v148,0,v148
v_mfma_f32_16x16x32_bf16 a[240:243],v[208:211],v[184:187],a[240:243]
v_fma_f32 v148,v148,s72,v246
v_cvt_pk_bf16_f32 v149,0,v149
v_mfma_f32_16x16x32_bf16 a[52:55],v[212:215],v[160:163],a[52:55]
ds_read_b128 v[200:203],v248 offset:33792
v_fma_f32 v149,v149,s72,v246
v_mfma_f32_16x16x32_bf16 a[116:119],v[212:215],v[168:171],a[116:119]
s_add_u32 m0,s92,0x18000
v_cvt_pk_bf16_f32 v150,0,v150
v_fma_f32 v150,v150,s72,v246
v_mfma_f32_16x16x32_bf16 a[180:183],v[212:215],v[176:179],a[180:183]
ds_read_b128 v[204:207],v249 offset:33792
v_cvt_pk_bf16_f32 v151,0,v151
v_mfma_f32_16x16x32_bf16 a[244:247],v[212:215],v[184:187],a[244:247]
v_fma_f32 v151,v151,s72,v246
v_max3_f32 v164,v148,v149,v164
v_mfma_f32_16x16x32_bf16 a[56:59],v[216:219],v[160:163],a[56:59]
v_max3_f32 v164,v150,v151,v164
v_cvt_pk_bf16_f32 v156,0,v156
v_mfma_f32_16x16x32_bf16 a[120:123],v[216:219],v[168:171],a[120:123]
buffer_load_dwordx4 v253,s[40:43],s94 offen lds
v_mfma_f32_16x16x32_bf16 a[184:187],v[216:219],v[176:179],a[184:187]
v_fma_f32 v156,v156,s72,v247
v_cvt_pk_bf16_f32 v157,0,v157
v_mfma_f32_16x16x32_bf16 a[248:251],v[216:219],v[184:187],a[248:251]
v_fma_f32 v157,v157,s72,v247
v_cvt_pk_bf16_f32 v158,0,v158
v_mfma_f32_16x16x32_bf16 a[60:63],v[220:223],v[160:163],a[60:63]
v_fma_f32 v158,v158,s72,v247
v_cvt_pk_bf16_f32 v159,0,v159
v_mfma_f32_16x16x32_bf16 a[124:127],v[220:223],v[168:171],a[124:127]
buffer_load_dwordx4 v253,s[44:47],s94 offen offset:1024 lds
v_mfma_f32_16x16x32_bf16 a[188:191],v[220:223],v[176:179],a[188:191]
v_fma_f32 v159,v159,s72,v247
v_max3_f32 v164,v156,v157,v164
v_mfma_f32_16x16x32_bf16 a[252:255],v[220:223],v[184:187],a[252:255]
v_max3_f32 v164,v158,v159,v164
v_cmp_lt_f32 vcc,s73,v164
v_mfma_f32_16x16x32_bf16 v[236:239],v[240:243],v[184:187],v[236:239]
s_waitcnt lgkmcnt(0)
v_cmp_ne_u32 s[84:85],0,v255
s_nop 0
s_lshr_b32 s85,s84,1
s_or_b32 s84,s84,s85
s_and_b32 s84,s84,85
s_cbranch_vccnz Lstub2a_%=
Lback2a_%=:
s_cmp_lg_u32 s84,85
s_cbranch_scc1 Lrolea_%=
Lok3a_%=:
s_getpc_b64 s[80:81]
Lpc1a_%=:
s_mov_b32 s82,Lenda_%=-Lpc1a_%=
s_mov_b32 s83,0x20000
v_mfma_f32_16x16x32_bf16 v[160:163],v[192:195],v[0:3],0
s_lshr_b32 s98,%[tb],2
ds_read_b128 v[208:211],v248 offset:34816
v_mbcnt_lo_u32_b32 v255,-1,0
v_mfma_f32_16x16x32_bf16 v[160:163],v[196:199],v[4:7],v[160:163]
s_lshl_b32 s98,s98,10
v_exp_f32 v128,v128
v_mfma_f32_16x16x32_bf16 v[160:163],v[200:203],v[32:35],v[160:163]
s_add_u32 s98,s98,0x2b20000
ds_read_b128 v[212:215],v249 offset:34816
v_mbcnt_hi_u32_b32 v255,-1,v255
v_mfma_f32_16x16x32_bf16 v[160:163],v[204:207],v[36:39],v[160:163]
s_lshl_b32 s100,%[w],15
v_exp_f32 v129,v129
v_mfma_f32_16x16x32_bf16 v[168:171],v[192:195],v[8:11],0
s_add_u32 s98,s98,s100
ds_read_b128 v[216:219],v248 offset:35840
v_cvt_pk_bf16_f32 v128,v128,v129
v_mfma_f32_16x16x32_bf16 v[168:171],v[196:199],v[12:15],v[168:171]
s_and_b32 s96,%[tb],124
v_exp_f32 v130,v130
v_mfma_f32_16x16x32_bf16 v[168:171],v[200:203],v[40:43],v[168:171]
s_add_u32 s96,s96,%[w]
ds_read_b128 v[220:223],v249 offset:35840
v_lshlrev_b32 v255,4,v255
v_mfma_f32_16x16x32_bf16 v[168:171],v[204:207],v[44:47],v[168:171]
s_lshl_b32 s96,s96,10
v_exp_f32 v131,v131
v_mfma_f32_16x16x32_bf16 v[176:179],v[192:195],v[16:19],0
s_lshr_b32 s96,s96,1
v_exp_f32 v132,v132
v_mfma_f32_16x16x32_bf16 v[176:179],v[196:199],v[20:23],v[176:179]
buffer_load_dwordx4 v253,s[48:51],s94 offen offset:2048 lds
v_mfma_f32_16x16x32_bf16 v[176:179],v[200:203],v[48:51],v[176:179]
v_exp_f32 v133,v133
v_mfma_f32_16x16x32_bf16 v[176:179],v[204:207],v[52:55],v[176:179]
v_exp_f32 v134,v134
v_mfma_f32_16x16x32_bf16 v[184:187],v[192:195],v[24:27],0
buffer_load_dwordx4 v253,s[52:55],s94 offen offset:3072 lds
v_mfma_f32_16x16x32_bf16 v[184:187],v[196:199],v[28:31],v[184:187]
s_add_u32 s94,s94,16384
v_exp_f32 v135,v135
v_mfma_f32_16x16x32_bf16 v[184:187],v[200:203],v[56:59],v[184:187]
v_exp_f32 v136,v136
v_mfma_f32_16x16x32_bf16 v[184:187],v[204:207],v[60:63],v[184:187]
s_waitcnt lgkmcnt(0)
v_exp_f32 v137,v137
v_mfma_f32_16x16x32_bf16 v[160:163],v[208:211],v[64:67],v[160:163]
ds_read_b128 v[192:195],v248 offset:40960
v_cvt_pk_bf16_f32 v129,v130,v131
v_mfma_f32_16x16x32_bf16 v[160:163],v[212:215],v[68:71],v[160:163]
v_exp_f32 v138,v138
v_mfma_f32_16x16x32_bf16 v[160:163],v[216:219],v[96:99],v[160:163]
ds_read_b128 v[196:199],v249 offset:40960
v_cvt_pk_bf16_f32 v130,v132,v133
v_mfma_f32_16x16x32_bf16 v[160:163],v[220:223],v[100:103],v[160:163]
v_exp_f32 v139,v139
v_mfma_f32_16x16x32_bf16 v[168:171],v[208:211],v[72:75],v[168:171]
ds_read_b128 v[200:203],v248 offset:41984
v_cvt_pk_bf16_f32 v131,v134,v135
v_mfma_f32_16x16x32_bf16 v[168:171],v[212:215],v[76:79],v[168:171]
s_add_u32 m0,s92,0x1c000
v_exp_f32 v140,v140
v_mfma_f32_16x16x32_bf16 v[168:171],v[216:219],v[104:107],v[168:171]
ds_read_b128 v[204:207],v249 offset:41984
v_cvt_pk_bf16_f32 v136,v136,v137
v_mfma_f32_16x16x32_bf16 v[168:171],v[220:223],v[108:111],v[168:171]
v_exp_f32 v141,v141
v_mfma_f32_16x16x32_bf16 v[176:179],v[208:211],v[80:83],v[176:179]
v_exp_f32 v142,v142
v_mfma_f32_16x16x32_bf16 v[176:179],v[212:215],v[84:87],v[176:179]
buffer_load_dwordx4 v253,s[40:43],s94 offen lds
)"
R"(v_mfma_f32_16x16x32_bf16 v[176:179],v[216:219],v[112:115],v[176:179]
v_exp_f32 v143,v143
v_mfma_f32_16x16x32_bf16 v[176:179],v[220:223],v[116:119],v[176:179]
v_exp_f32 v144,v144
v_mfma_f32_16x16x32_bf16 v[184:187],v[208:211],v[88:91],v[184:187]
buffer_load_dwordx4 v253,s[44:47],s94 offen offset:1024 lds
v_mfma_f32_16x16x32_bf16 v[184:187],v[212:215],v[92:95],v[184:187]
v_exp_f32 v145,v145
v_mfma_f32_16x16x32_bf16 v[184:187],v[216:219],v[120:123],v[184:187]
v_exp_f32 v146,v146
v_mfma_f32_16x16x32_bf16 v[184:187],v[220:223],v[124:127],v[184:187]
s_waitcnt lgkmcnt(0)
v_exp_f32 v147,v147
v_mfma_f32_16x16x32_bf16 v[164:167],v[192:195],v[0:3],0
ds_read_b128 v[208:211],v248 offset:43008
v_cvt_pk_bf16_f32 v137,v138,v139
v_mfma_f32_16x16x32_bf16 v[164:167],v[196:199],v[4:7],v[164:167]
v_exp_f32 v148,v148
v_mfma_f32_16x16x32_bf16 v[164:167],v[200:203],v[32:35],v[164:167]
ds_read_b128 v[212:215],v249 offset:43008
v_cvt_pk_bf16_f32 v138,v140,v141
v_mfma_f32_16x16x32_bf16 v[164:167],v[204:207],v[36:39],v[164:167]
v_exp_f32 v149,v149
v_mfma_f32_16x16x32_bf16 v[172:175],v[192:195],v[8:11],0
ds_read_b128 v[216:219],v248 offset:44032
v_cvt_pk_bf16_f32 v139,v142,v143
v_mfma_f32_16x16x32_bf16 v[172:175],v[196:199],v[12:15],v[172:175]
v_exp_f32 v150,v150
v_mfma_f32_16x16x32_bf16 v[172:175],v[200:203],v[40:43],v[172:175]
ds_read_b128 v[220:223],v249 offset:44032
v_cvt_pk_bf16_f32 v144,v144,v145
v_mfma_f32_16x16x32_bf16 v[172:175],v[204:207],v[44:47],v[172:175]
v_exp_f32 v151,v151
v_mfma_f32_16x16x32_bf16 v[180:183],v[192:195],v[16:19],0
v_exp_f32 v152,v152
v_mfma_f32_16x16x32_bf16 v[180:183],v[196:199],v[20:23],v[180:183]
buffer_load_dwordx4 v253,s[48:51],s94 offen offset:2048 lds
v_mfma_f32_16x16x32_bf16 v[180:183],v[200:203],v[48:51],v[180:183]
v_exp_f32 v153,v153
v_mfma_f32_16x16x32_bf16 v[180:183],v[204:207],v[52:55],v[180:183]
v_exp_f32 v154,v154
v_mfma_f32_16x16x32_bf16 v[188:191],v[192:195],v[24:27],0
buffer_load_dwordx4 v253,s[52:55],s94 offen offset:3072 lds
v_mfma_f32_16x16x32_bf16 v[188:191],v[196:199],v[28:31],v[188:191]
s_add_u32 s94,s94,16384
v_exp_f32 v155,v155
v_mfma_f32_16x16x32_bf16 v[188:191],v[200:203],v[56:59],v[188:191]
v_exp_f32 v156,v156
v_mfma_f32_16x16x32_bf16 v[188:191],v[204:207],v[60:63],v[188:191]
s_waitcnt vmcnt(18) lgkmcnt(0)
v_exp_f32 v157,v157
v_mfma_f32_16x16x32_bf16 v[164:167],v[208:211],v[64:67],v[164:167]
s_barrier
ds_read_b64_tr_b16 v[192:193],v250
v_cvt_pk_bf16_f32 v145,v146,v147
v_mfma_f32_16x16x32_bf16 v[164:167],v[212:215],v[68:71],v[164:167]
ds_read_b64_tr_b16 v[194:195],v250 offset:8192
v_cvt_pk_bf16_f32 v146,v148,v149
v_mfma_f32_16x16x32_bf16 v[164:167],v[216:219],v[96:99],v[164:167]
ds_read_b64_tr_b16 v[196:197],v251
v_cvt_pk_bf16_f32 v147,v150,v151
v_mfma_f32_16x16x32_bf16 v[164:167],v[220:223],v[100:103],v[164:167]
ds_read_b64_tr_b16 v[198:199],v251 offset:8192
v_cvt_pk_bf16_f32 v152,v152,v153
v_mfma_f32_16x16x32_bf16 v[172:175],v[208:211],v[72:75],v[172:175]
ds_read_b64_tr_b16 v[200:201],v250 offset:512
v_cvt_pk_bf16_f32 v153,v154,v155
v_mfma_f32_16x16x32_bf16 v[172:175],v[212:215],v[76:79],v[172:175]
s_add_u32 m0,s92,0x20000
ds_read_b64_tr_b16 v[202:203],v250 offset:8704
v_cvt_pk_bf16_f32 v154,v156,v157
v_mfma_f32_16x16x32_bf16 v[172:175],v[216:219],v[104:107],v[172:175]
s_mov_b32 s95,0xe000
ds_read_b64_tr_b16 v[204:205],v251 offset:512
v_cvt_pk_bf16_f32 v160,0,v160
v_mfma_f32_16x16x32_bf16 v[172:175],v[220:223],v[108:111],v[172:175]
ds_read_b64_tr_b16 v[206:207],v251 offset:8704
v_fma_f32 v160,v160,s72,v244
v_mfma_f32_16x16x32_bf16 v[180:183],v[208:211],v[80:83],v[180:183]
v_exp_f32 v158,v158
v_mfma_f32_16x16x32_bf16 v[180:183],v[212:215],v[84:87],v[180:183]
buffer_load_dwordx4 v252,s[56:59],s95 offen lds
v_mfma_f32_16x16x32_bf16 v[180:183],v[216:219],v[112:115],v[180:183]
v_exp_f32 v159,v159
v_mfma_f32_16x16x32_bf16 v[180:183],v[220:223],v[116:119],v[180:183]
buffer_load_dwordx4 v252,s[60:63],s95 offen offset:1024 lds
v_mfma_f32_16x16x32_bf16 v[188:191],v[208:211],v[88:91],v[188:191]
v_cvt_pk_bf16_f32 v155,v158,v159
v_cvt_pk_bf16_f32 v161,0,v161
v_mfma_f32_16x16x32_bf16 v[188:191],v[212:215],v[92:95],v[188:191]
buffer_load_dwordx4 v252,s[64:67],s95 offen offset:2048 lds
v_mfma_f32_16x16x32_bf16 v[188:191],v[216:219],v[120:123],v[188:191]
v_fma_f32 v161,v161,s72,v244
v_cvt_pk_bf16_f32 v162,0,v162
v_mfma_f32_16x16x32_bf16 v[188:191],v[220:223],v[124:127],v[188:191]
s_waitcnt lgkmcnt(0)
v_fma_f32 v162,v162,s72,v244
v_cvt_pk_bf16_f32 v163,0,v163
v_mfma_f32_16x16x32_bf16 a[0:3],v[192:195],v[128:131],a[0:3]
s_and_b32 s100,%[w],1
ds_read_b64_tr_b16 v[208:209],v250 offset:1024
v_mov_b32 v127,1
v_mfma_f32_16x16x32_bf16 a[4:7],v[196:199],v[128:131],a[4:7]
s_lshl_b32 s100,s100,5
s_mov_b64 exec,1
buffer_atomic_add v127,off,s[88:91],s98 offset:64 sc0
s_mov_b64 exec,-1
v_mfma_f32_16x16x32_bf16 a[8:11],v[200:203],v[128:131],a[8:11]
buffer_load_dwordx4 v252,s[68:71],s95 offen offset:3072 lds
v_mfma_f32_16x16x32_bf16 a[12:15],v[204:207],v[128:131],a[12:15]
ds_read_b64_tr_b16 v[210:211],v250 offset:9216
ds_read_b64_tr_b16 v[212:213],v251 offset:1024
v_mfma_f32_16x16x32_bf16 a[64:67],v[192:195],v[136:139],a[64:67]
buffer_load_dwordx4 v[48:51],v255,s[88:91],s86 offen sc1
v_mfma_f32_16x16x32_bf16 a[68:71],v[196:199],v[136:139],a[68:71]
ds_read_b64_tr_b16 v[214:215],v251 offset:9216
ds_read_b64_tr_b16 v[216:217],v250 offset:1536
v_mfma_f32_16x16x32_bf16 a[72:75],v[200:203],v[136:139],a[72:75]
ds_read_b64_tr_b16 v[218:219],v250 offset:9728
ds_read_b64_tr_b16 v[220:221],v251 offset:1536
v_mfma_f32_16x16x32_bf16 a[76:79],v[204:207],v[136:139],a[76:79]
buffer_load_dwordx4 v[52:55],v255,s[88:91],s86 offen offset:1024 sc1
v_mfma_f32_16x16x32_bf16 a[128:131],v[192:195],v[144:147],a[128:131]
ds_read_b64_tr_b16 v[222:223],v251 offset:9728
v_fma_f32 v163,v163,s72,v244
v_mfma_f32_16x16x32_bf16 a[132:135],v[196:199],v[144:147],a[132:135]
v_max3_f32 v132,v160,v161,v161
v_max3_f32 v132,v162,v163,v132
v_mfma_f32_16x16x32_bf16 a[136:139],v[200:203],v[144:147],a[136:139]
buffer_load_dwordx4 v[56:59],v255,s[88:91],s86 offen offset:2048 sc1
v_mfma_f32_16x16x32_bf16 a[140:143],v[204:207],v[144:147],a[140:143]
v_cvt_pk_bf16_f32 v168,0,v168
v_fma_f32 v168,v168,s72,v245
v_mfma_f32_16x16x32_bf16 a[192:195],v[192:195],v[152:155],a[192:195]
v_cvt_pk_bf16_f32 v169,0,v169
v_fma_f32 v169,v169,s72,v245
v_mfma_f32_16x16x32_bf16 a[196:199],v[196:199],v[152:155],a[196:199]
buffer_load_dwordx4 v[60:63],v255,s[88:91],s86 offen offset:3072 sc1
v_mfma_f32_16x16x32_bf16 a[200:203],v[200:203],v[152:155],a[200:203]
s_add_u32 s86,s86,4096
v_cvt_pk_bf16_f32 v170,0,v170
v_fma_f32 v170,v170,s72,v245
v_mfma_f32_16x16x32_bf16 a[204:207],v[204:207],v[152:155],a[204:207]
buffer_load_dwordx4 v[64:67],v255,s[88:91],s86 offen sc1
v_mfma_f32_16x16x32_bf16 v[224:227],v[240:243],v[128:131],v[224:227]
s_waitcnt lgkmcnt(0)
v_cvt_pk_bf16_f32 v171,0,v171
v_fma_f32 v171,v171,s72,v245
v_mfma_f32_16x16x32_bf16 a[16:19],v[208:211],v[128:131],a[16:19]
ds_read_b64_tr_b16 v[192:193],v250 offset:2048
v_max3_f32 v132,v168,v169,v132
v_mfma_f32_16x16x32_bf16 a[80:83],v[208:211],v[136:139],a[80:83]
ds_read_b64_tr_b16 v[194:195],v250 offset:10240
v_max3_f32 v132,v170,v171,v132
v_mfma_f32_16x16x32_bf16 a[144:147],v[208:211],v[144:147],a[144:147]
buffer_load_dwordx4 v[68:71],v255,s[88:91],s86 offen offset:1024 sc1
v_mfma_f32_16x16x32_bf16 a[208:211],v[208:211],v[152:155],a[208:211]
ds_read_b64_tr_b16 v[196:197],v251 offset:2048
ds_read_b64_tr_b16 v[198:199],v251 offset:10240
v_mfma_f32_16x16x32_bf16 a[20:23],v[212:215],v[128:131],a[20:23]
ds_read_b64_tr_b16 v[200:201],v250 offset:2560
v_cvt_pk_bf16_f32 v176,0,v176
v_mfma_f32_16x16x32_bf16 a[84:87],v[212:215],v[136:139],a[84:87]
buffer_load_dwordx4 v[72:75],v255,s[88:91],s86 offen offset:2048 sc1
v_mfma_f32_16x16x32_bf16 a[148:151],v[212:215],v[144:147],a[148:151]
ds_read_b64_tr_b16 v[202:203],v250 offset:10752
ds_read_b64_tr_b16 v[204:205],v251 offset:2560
v_mfma_f32_16x16x32_bf16 a[212:215],v[212:215],v[152:155],a[212:215]
ds_read_b64_tr_b16 v[206:207],v251 offset:10752
v_fma_f32 v176,v176,s72,v246
v_mfma_f32_16x16x32_bf16 a[24:27],v[216:219],v[128:131],a[24:27]
s_add_u32 m0,s92,0x24000
buffer_load_dwordx4 v[76:79],v255,s[88:91],s86 offen offset:3072 sc1
v_mfma_f32_16x16x32_bf16 a[88:91],v[216:219],v[136:139],a[88:91]
s_mov_b32 s95,0xe100
v_cvt_pk_bf16_f32 v177,0,v177
v_fma_f32 v177,v177,s72,v246
v_mfma_f32_16x16x32_bf16 a[152:155],v[216:219],v[144:147],a[152:155]
v_cvt_pk_bf16_f32 v178,0,v178
v_fma_f32 v178,v178,s72,v246
v_mfma_f32_16x16x32_bf16 a[216:219],v[216:219],v[152:155],a[216:219]
v_cvt_pk_bf16_f32 v179,0,v179
v_fma_f32 v179,v179,s72,v246
v_mfma_f32_16x16x32_bf16 a[28:31],v[220:223],v[128:131],a[28:31]
buffer_load_dwordx4 v252,s[56:59],s95 offen lds
v_mfma_f32_16x16x32_bf16 a[92:95],v[220:223],v[136:139],a[92:95]
v_max3_f32 v132,v176,v177,v132
v_max3_f32 v132,v178,v179,v132
v_mfma_f32_16x16x32_bf16 a[156:159],v[220:223],v[144:147],a[156:159]
v_cvt_pk_bf16_f32 v184,0,v184
v_fma_f32 v184,v184,s72,v247
v_mfma_f32_16x16x32_bf16 a[220:223],v[220:223],v[152:155],a[220:223]
buffer_load_dwordx4 v252,s[60:63],s95 offen offset:1024 lds
v_mfma_f32_16x16x32_bf16 v[228:231],v[240:243],v[136:139],v[228:231]
s_waitcnt lgkmcnt(0)
v_cvt_pk_bf16_f32 v185,0,v185
v_fma_f32 v185,v185,s72,v247
v_mfma_f32_16x16x32_bf16 a[32:35],v[192:195],v[128:131],a[32:35]
ds_read_b64_tr_b16 v[208:209],v250 offset:3072
v_cvt_pk_bf16_f32 v186,0,v186
)"
R"(v_mfma_f32_16x16x32_bf16 a[96:99],v[192:195],v[136:139],a[96:99]
ds_read_b64_tr_b16 v[210:211],v250 offset:11264
v_fma_f32 v186,v186,s72,v247
v_mfma_f32_16x16x32_bf16 a[160:163],v[192:195],v[144:147],a[160:163]
buffer_load_dwordx4 v252,s[64:67],s95 offen offset:2048 lds
v_mfma_f32_16x16x32_bf16 a[224:227],v[192:195],v[152:155],a[224:227]
ds_read_b64_tr_b16 v[212:213],v251 offset:3072
ds_read_b64_tr_b16 v[214:215],v251 offset:11264
v_mfma_f32_16x16x32_bf16 a[36:39],v[196:199],v[128:131],a[36:39]
ds_read_b64_tr_b16 v[216:217],v250 offset:3584
v_cvt_pk_bf16_f32 v187,0,v187
v_mfma_f32_16x16x32_bf16 a[100:103],v[196:199],v[136:139],a[100:103]
buffer_load_dwordx4 v252,s[68:71],s95 offen offset:3072 lds
v_mfma_f32_16x16x32_bf16 a[164:167],v[196:199],v[144:147],a[164:167]
s_add_u32 m0,s92,0x8000
ds_read_b64_tr_b16 v[218:219],v250 offset:11776
ds_read_b64_tr_b16 v[220:221],v251 offset:3584
v_mfma_f32_16x16x32_bf16 a[228:231],v[196:199],v[152:155],a[228:231]
s_mov_b32 s95,0x1c000
ds_read_b64_tr_b16 v[222:223],v251 offset:11776
v_fma_f32 v187,v187,s72,v247
v_mfma_f32_16x16x32_bf16 a[40:43],v[200:203],v[128:131],a[40:43]
buffer_load_dwordx4 v252,s[56:59],s95 offen lds
v_mfma_f32_16x16x32_bf16 a[104:107],v[200:203],v[136:139],a[104:107]
v_max3_f32 v132,v184,v185,v132
v_max3_f32 v132,v186,v187,v132
v_mfma_f32_16x16x32_bf16 a[168:171],v[200:203],v[144:147],a[168:171]
v_cvt_pk_bf16_f32 v164,0,v164
v_fma_f32 v164,v164,s72,v244
v_mfma_f32_16x16x32_bf16 a[232:235],v[200:203],v[152:155],a[232:235]
v_cvt_pk_bf16_f32 v165,0,v165
v_fma_f32 v165,v165,s72,v244
v_mfma_f32_16x16x32_bf16 a[44:47],v[204:207],v[128:131],a[44:47]
buffer_load_dwordx4 v252,s[60:63],s95 offen offset:1024 lds
v_mfma_f32_16x16x32_bf16 a[108:111],v[204:207],v[136:139],a[108:111]
v_cvt_pk_bf16_f32 v166,0,v166
v_fma_f32 v166,v166,s72,v244
v_mfma_f32_16x16x32_bf16 a[172:175],v[204:207],v[144:147],a[172:175]
v_cvt_pk_bf16_f32 v167,0,v167
v_fma_f32 v167,v167,s72,v244
v_mfma_f32_16x16x32_bf16 a[236:239],v[204:207],v[152:155],a[236:239]
buffer_load_dwordx4 v252,s[64:67],s95 offen offset:2048 lds
v_mfma_f32_16x16x32_bf16 v[232:235],v[240:243],v[144:147],v[232:235]
s_waitcnt vmcnt(28) lgkmcnt(0)
v_max3_f32 v132,v164,v165,v132
v_max3_f32 v132,v166,v167,v132
v_mfma_f32_16x16x32_bf16 a[48:51],v[208:211],v[128:131],a[48:51]
s_barrier
v_cvt_pk_bf16_f32 v172,0,v172
v_fma_f32 v172,v172,s72,v245
v_mfma_f32_16x16x32_bf16 a[112:115],v[208:211],v[136:139],a[112:115]
v_cvt_pk_bf16_f32 v173,0,v173
v_fma_f32 v173,v173,s72,v245
v_mfma_f32_16x16x32_bf16 a[176:179],v[208:211],v[144:147],a[176:179]
buffer_load_dwordx4 v252,s[68:71],s95 offen offset:3072 lds
v_mfma_f32_16x16x32_bf16 a[240:243],v[208:211],v[152:155],a[240:243]
s_add_u32 m0,s92,0xc000
s_mov_b32 s95,0x1c100
v_cvt_pk_bf16_f32 v174,0,v174
v_mfma_f32_16x16x32_bf16 a[52:55],v[212:215],v[128:131],a[52:55]
v_fma_f32 v174,v174,s72,v245
v_cvt_pk_bf16_f32 v175,0,v175
v_mfma_f32_16x16x32_bf16 a[116:119],v[212:215],v[136:139],a[116:119]
buffer_load_dwordx4 v252,s[56:59],s95 offen lds
v_mfma_f32_16x16x32_bf16 a[180:183],v[212:215],v[144:147],a[180:183]
v_fma_f32 v175,v175,s72,v245
v_max3_f32 v132,v172,v173,v132
v_mfma_f32_16x16x32_bf16 a[244:247],v[212:215],v[152:155],a[244:247]
v_max3_f32 v132,v174,v175,v132
v_cvt_pk_bf16_f32 v180,0,v180
v_mfma_f32_16x16x32_bf16 a[56:59],v[216:219],v[128:131],a[56:59]
buffer_load_dwordx4 v252,s[60:63],s95 offen offset:1024 lds
v_mfma_f32_16x16x32_bf16 a[120:123],v[216:219],v[136:139],a[120:123]
v_fma_f32 v180,v180,s72,v246
v_cvt_pk_bf16_f32 v181,0,v181
v_mfma_f32_16x16x32_bf16 a[184:187],v[216:219],v[144:147],a[184:187]
v_fma_f32 v181,v181,s72,v246
v_cvt_pk_bf16_f32 v182,0,v182
v_mfma_f32_16x16x32_bf16 a[248:251],v[216:219],v[152:155],a[248:251]
v_fma_f32 v182,v182,s72,v246
v_cvt_pk_bf16_f32 v183,0,v183
v_mfma_f32_16x16x32_bf16 a[60:63],v[220:223],v[128:131],a[60:63]
buffer_load_dwordx4 v252,s[64:67],s95 offen offset:2048 lds
v_mfma_f32_16x16x32_bf16 a[124:127],v[220:223],v[136:139],a[124:127]
v_fma_f32 v183,v183,s72,v246
v_max3_f32 v132,v180,v181,v132
v_mfma_f32_16x16x32_bf16 a[188:191],v[220:223],v[144:147],a[188:191]
v_max3_f32 v132,v182,v183,v132
v_cvt_pk_bf16_f32 v188,0,v188
v_mfma_f32_16x16x32_bf16 a[252:255],v[220:223],v[152:155],a[252:255]
buffer_load_dwordx4 v252,s[68:71],s95 offen offset:3072 lds
v_mfma_f32_16x16x32_bf16 v[236:239],v[240:243],v[152:155],v[236:239]
v_fma_f32 v188,v188,s72,v247
v_cvt_pk_bf16_f32 v189,0,v189
v_fma_f32 v189,v189,s72,v247
v_cvt_pk_bf16_f32 v190,0,v190
v_fma_f32 v190,v190,s72,v247
v_cvt_pk_bf16_f32 v191,0,v191
v_fma_f32 v191,v191,s72,v247
v_max3_f32 v132,v188,v189,v132
v_max3_f32 v132,v190,v191,v132
v_cmp_lt_f32 vcc,s73,v132
v_mbcnt_lo_u32_b32 v248,-1,0
v_mbcnt_hi_u32_b32 v248,-1,v248
v_bfe_u32 v249,v248,2,3
v_lshlrev_b32 v120,6,v249
v_and_b32 v249,3,v248
v_lshl_add_u32 v120,v249,3,v120
v_bfe_u32 v249,v248,4,1
v_lshl_add_u32 v120,v249,5,v120
v_lshrrev_b32 v249,5,v248
v_lshl_add_u32 v120,v249,10,v120
v_xor_b32 v120,s100,v120
v_add_u32 v122,65536,v120
v_add_u32 v124,131072,v120
v_and_b32 v126,48,v255
v_lshl_add_u32 v126,v126,3,s96
buffer_load_dword v126,v126,s[80:83],0 offen
s_cbranch_vccnz Lstub3a_%=
Lback3a_%=:
ds_read_b64_tr_b16 v[192:193],v120
ds_read_b64_tr_b16 v[194:195],v120 offset:2048
ds_read_b64_tr_b16 v[196:197],v120 offset:512
ds_read_b64_tr_b16 v[198:199],v120 offset:2560
ds_read_b64_tr_b16 v[200:201],v120 offset:8192
ds_read_b64_tr_b16 v[202:203],v120 offset:10240
ds_read_b64_tr_b16 v[204:205],v120 offset:8704
ds_read_b64_tr_b16 v[206:207],v120 offset:10752
ds_read_b64_tr_b16 v[208:209],v120 offset:16384
ds_read_b64_tr_b16 v[210:211],v120 offset:18432
ds_read_b64_tr_b16 v[212:213],v120 offset:16896
ds_read_b64_tr_b16 v[214:215],v120 offset:18944
s_add_u32 s86,s86,4096
buffer_load_dwordx4 v[80:83],v255,s[88:91],s86 offen sc1
v_exp_f32 v160,v160
s_mov_b64 s[28:29],%[wo]
s_add_u32 s28,s28,0x1800
s_addc_u32 s29,s29,0
buffer_load_dwordx4 v[84:87],v255,s[88:91],s86 offen offset:1024 sc1
v_exp_f32 v161,v161
v_exp_f32 v162,v162
s_mov_b32 s30,0x6e800
s_mov_b32 s31,0x20000
s_mov_b64 s[32:33],%[wo]
buffer_load_dwordx4 v[88:91],v255,s[88:91],s86 offen offset:2048 sc1
v_exp_f32 v163,v163
v_exp_f32 v164,v164
s_add_u32 s32,s32,0x3000
s_addc_u32 s33,s33,0
s_mov_b32 s34,0x6d000
buffer_load_dwordx4 v[92:95],v255,s[88:91],s86 offen offset:3072 sc1
v_exp_f32 v165,v165
v_exp_f32 v166,v166
s_mov_b32 s35,0x20000
s_mov_b64 s[36:37],%[wo]
s_add_u32 s36,s36,0x4800
s_addc_u32 s37,s37,0
s_add_u32 s86,s86,4096
buffer_load_dwordx4 v[96:99],v255,s[88:91],s86 offen sc1
v_exp_f32 v167,v167
s_mov_b32 s38,0x6b800
s_mov_b32 s39,0x20000
s_mov_b64 s[24:25],%[wo]
buffer_load_dwordx4 v[100:103],v255,s[88:91],s86 offen offset:1024 sc1
v_exp_f32 v168,v168
v_exp_f32 v169,v169
s_mov_b32 s26,0x70000
s_mov_b32 s27,0x20000
s_mov_b64 s[40:41],s[88:89]
buffer_load_dwordx4 v[104:107],v255,s[88:91],s86 offen offset:2048 sc1
v_exp_f32 v170,v170
v_exp_f32 v171,v171
s_mov_b32 s43,0x20000
s_mov_b64 s[44:45],%[x]
s_mov_b32 s47,0x20000
buffer_load_dwordx4 v[108:111],v255,s[88:91],s86 offen offset:3072 sc1
v_exp_f32 v172,v172
v_exp_f32 v173,v173
s_mov_b64 s[48:49],%[out]
s_mov_b32 s51,0x20000
s_waitcnt vmcnt(30)
s_add_u32 s86,s86,4096
buffer_load_dwordx4 v[112:115],v255,s[88:91],s86 offen sc1
v_exp_f32 v174,v174
v_exp_f32 v175,v175
v_readfirstlane_b32 s53,v127
s_and_b32 s85,%[w],1
s_sub_u32 s53,6,s53
buffer_load_dwordx4 v[116:119],v255,s[88:91],s86 offen offset:1024 sc1
v_exp_f32 v176,v176
s_and_b32 s53,s53,7
s_lshl_b32 s53,s53,1
s_add_u32 s53,s53,s85
s_add_u32 m0,s92,0x10000
s_mov_b32 s95,0x2a000
buffer_load_dwordx4 v252,s[56:59],s95 offen lds
v_exp_f32 v177,v177
v_exp_f32 v178,v178
s_cmp_lt_u32 s53,14
s_cselect_b32 s42,45350912,0
)"
R"(s_cselect_b32 s46,1835008,0
s_cselect_b32 s50,1835008,0
s_cselect_b32 s84,0,255
s_cselect_b32 s52,1,0
buffer_load_dwordx4 v252,s[60:63],s95 offen offset:1024 lds
v_exp_f32 v179,v179
v_exp_f32 v180,v180
s_lshr_b32 s81,s53,1
s_cmp_ge_u32 s81,0
s_addc_u32 s81,s81,1
buffer_load_dwordx4 v252,s[64:67],s95 offen offset:2048 lds
v_exp_f32 v181,v181
v_exp_f32 v182,v182
buffer_load_dwordx4 v252,s[68:71],s95 offen offset:3072 lds
v_exp_f32 v183,v183
s_add_u32 m0,s92,0x14000
s_mov_b32 s95,0x2a100
buffer_load_dwordx4 v252,s[56:59],s95 offen lds
v_exp_f32 v184,v184
v_exp_f32 v185,v185
buffer_load_dwordx4 v252,s[60:63],s95 offen offset:1024 lds
v_exp_f32 v186,v186
v_exp_f32 v187,v187
buffer_load_dwordx4 v252,s[64:67],s95 offen offset:2048 lds
v_exp_f32 v188,v188
v_exp_f32 v189,v189
buffer_load_dwordx4 v252,s[68:71],s95 offen offset:3072 lds
v_exp_f32 v190,v190
v_exp_f32 v191,v191
s_waitcnt vmcnt(37)
v_swap_b32 v50,v52
v_swap_b32 v51,v53
s_waitcnt lgkmcnt(6)
s_nop 0
v_mfma_f32_16x16x32_bf16 v[0:3],v[192:195],v[48:51],0
s_lshr_b32 s83,%[tb],3
ds_read_b64_tr_b16 v[128:129],v120 offset:4096
ds_read_b64_tr_b16 v[130:131],v120 offset:6144
v_mfma_f32_16x16x32_bf16 v[4:7],v[192:195],v[52:55],0
s_mul_i32 s83,s83,458752
ds_read_b64_tr_b16 v[132:133],v120 offset:4608
v_cvt_pk_bf16_f32 v160,v160,v161
v_mfma_f32_16x16x32_bf16 v[8:11],v[196:199],v[48:51],0
s_and_b32 s98,%[tb],6
ds_read_b64_tr_b16 v[134:135],v120 offset:6656
v_cvt_pk_bf16_f32 v161,v162,v163
v_mfma_f32_16x16x32_bf16 v[12:15],v[196:199],v[52:55],0
s_waitcnt lgkmcnt(4)
ds_read_b64_tr_b16 v[136:137],v120 offset:12288
ds_read_b64_tr_b16 v[138:139],v120 offset:14336
v_mfma_f32_16x16x32_bf16 v[16:19],v[200:203],v[48:51],0
s_lshl_b32 s98,s98,9
ds_read_b64_tr_b16 v[140:141],v120 offset:12800
v_cvt_pk_bf16_f32 v162,v164,v165
v_mfma_f32_16x16x32_bf16 v[20:23],v[200:203],v[52:55],0
s_add_u32 s83,s83,s98
ds_read_b64_tr_b16 v[142:143],v120 offset:14848
v_cvt_pk_bf16_f32 v163,v166,v167
v_mfma_f32_16x16x32_bf16 v[24:27],v[204:207],v[48:51],0
s_waitcnt vmcnt(35)
ds_read_b64_tr_b16 v[144:145],v120 offset:20480
ds_read_b64_tr_b16 v[146:147],v120 offset:22528
v_mfma_f32_16x16x32_bf16 v[28:31],v[204:207],v[52:55],0
s_lshr_b32 s54,%[h],2
ds_read_b64_tr_b16 v[148:149],v120 offset:20992
v_swap_b32 v58,v60
v_mfma_f32_16x16x32_bf16 v[32:35],v[208:211],v[48:51],0
s_mul_i32 s54,s54,114688
ds_read_b64_tr_b16 v[150:151],v120 offset:23040
v_swap_b32 v59,v61
v_mfma_f32_16x16x32_bf16 v[36:39],v[208:211],v[52:55],0
s_add_u32 s54,s54,s83
v_cvt_pk_bf16_f32 v168,v168,v169
v_cvt_pk_bf16_f32 v169,v170,v171
v_mfma_f32_16x16x32_bf16 v[40:43],v[212:215],v[48:51],0
s_and_b32 s98,%[h],1
buffer_load_dwordx4 v[48:51],v255,s[88:91],s86 offen offset:2048 sc1
v_mfma_f32_16x16x32_bf16 v[44:47],v[212:215],v[52:55],0
s_waitcnt lgkmcnt(6)
buffer_load_dwordx4 v[52:55],v255,s[88:91],s86 offen offset:3072 sc1
v_mfma_f32_16x16x32_bf16 v[0:3],v[128:131],v[56:59],v[0:3]
s_add_u32 s86,s86,4096
v_cvt_pk_bf16_f32 v170,v172,v173
v_cvt_pk_bf16_f32 v171,v174,v175
v_mfma_f32_16x16x32_bf16 v[4:7],v[128:131],v[60:63],v[4:7]
s_lshl_b32 s98,s98,12
v_cvt_pk_bf16_f32 v176,v176,v177
v_cvt_pk_bf16_f32 v177,v178,v179
v_mfma_f32_16x16x32_bf16 v[8:11],v[132:135],v[56:59],v[8:11]
s_add_u32 m0,s92,0x0
v_cvt_pk_bf16_f32 v178,v180,v181
v_cvt_pk_bf16_f32 v179,v182,v183
v_mfma_f32_16x16x32_bf16 v[12:15],v[132:135],v[60:63],v[12:15]
s_waitcnt vmcnt(29) lgkmcnt(0)
v_cvt_pk_bf16_f32 v184,v184,v185
v_cvt_pk_bf16_f32 v185,v186,v187
v_mfma_f32_16x16x32_bf16 v[16:19],v[136:139],v[56:59],v[16:19]
s_barrier
ds_read_b64_tr_b16 v[192:193],v124
ds_read_b64_tr_b16 v[194:195],v124 offset:2048
v_mfma_f32_16x16x32_bf16 v[20:23],v[136:139],v[60:63],v[20:23]
s_mov_b32 s95,0x38000
ds_read_b64_tr_b16 v[196:197],v124 offset:512
ds_read_b64_tr_b16 v[198:199],v124 offset:2560
v_mfma_f32_16x16x32_bf16 v[24:27],v[140:143],v[56:59],v[24:27]
s_waitcnt vmcnt(35)
buffer_load_dwordx4 v252,s[56:59],s95 offen lds
v_mfma_f32_16x16x32_bf16 v[28:31],v[140:143],v[60:63],v[28:31]
s_add_u32 s54,s54,s98
ds_read_b64_tr_b16 v[200:201],v124 offset:8192
ds_read_b64_tr_b16 v[202:203],v124 offset:10240
v_mfma_f32_16x16x32_bf16 v[32:35],v[144:147],v[56:59],v[32:35]
s_and_b32 s98,%[h],2
buffer_load_dwordx4 v252,s[60:63],s95 offen offset:1024 lds
v_mfma_f32_16x16x32_bf16 v[36:39],v[144:147],v[60:63],v[36:39]
s_mul_i32 s98,s98,32768
ds_read_b64_tr_b16 v[204:205],v124 offset:8704
ds_read_b64_tr_b16 v[206:207],v124 offset:10752
v_mfma_f32_16x16x32_bf16 v[40:43],v[148:151],v[56:59],v[40:43]
s_add_u32 s54,s54,s98
buffer_load_dwordx4 v252,s[64:67],s95 offen offset:2048 lds
buffer_load_dwordx4 v252,s[68:71],s95 offen offset:3072 lds
v_swap_b32 v66,v68
v_swap_b32 v67,v69
v_mfma_f32_16x16x32_bf16 v[44:47],v[148:151],v[60:63],v[44:47]
s_waitcnt lgkmcnt(2)
ds_read_b64_tr_b16 v[208:209],v124 offset:16384
ds_read_b64_tr_b16 v[210:211],v124 offset:18432
v_mfma_f32_16x16x32_bf16 v[0:3],v[192:195],v[64:67],v[0:3]
s_add_u32 m0,s92,0x4000
ds_read_b64_tr_b16 v[212:213],v124 offset:16896
ds_read_b64_tr_b16 v[214:215],v124 offset:18944
v_mfma_f32_16x16x32_bf16 v[4:7],v[192:195],v[68:71],v[4:7]
s_mov_b32 s95,0x38100
ds_read_b64_tr_b16 v[128:129],v124 offset:4096
ds_read_b64_tr_b16 v[130:131],v124 offset:6144
v_mfma_f32_16x16x32_bf16 v[8:11],v[196:199],v[64:67],v[8:11]
s_lshl_b32 s98,s53,1
buffer_load_dwordx4 v252,s[56:59],s95 offen lds
v_mfma_f32_16x16x32_bf16 v[12:15],v[196:199],v[68:71],v[12:15]
s_waitcnt lgkmcnt(2)
ds_read_b64_tr_b16 v[132:133],v124 offset:4608
ds_read_b64_tr_b16 v[134:135],v124 offset:6656
v_mfma_f32_16x16x32_bf16 v[16:19],v[200:203],v[64:67],v[16:19]
s_lshl_b32 s82,s98,5
ds_read_b64_tr_b16 v[136:137],v124 offset:12288
ds_read_b64_tr_b16 v[138:139],v124 offset:14336
v_mfma_f32_16x16x32_bf16 v[20:23],v[200:203],v[68:71],v[20:23]
s_lshl_b32 s98,s98,12
buffer_load_dwordx4 v252,s[60:63],s95 offen offset:1024 lds
v_mfma_f32_16x16x32_bf16 v[24:27],v[204:207],v[64:67],v[24:27]
s_waitcnt vmcnt(39)
ds_read_b64_tr_b16 v[140:141],v124 offset:12800
ds_read_b64_tr_b16 v[142:143],v124 offset:14848
v_mfma_f32_16x16x32_bf16 v[28:31],v[204:207],v[68:71],v[28:31]
s_add_u32 s83,s83,s98
buffer_load_dwordx4 v[56:59],v255,s[88:91],s86 offen sc1
v_mfma_f32_16x16x32_bf16 v[32:35],v[208:211],v[64:67],v[32:35]
s_mul_i32 s98,%[tb],14336
buffer_load_dwordx4 v252,s[64:67],s95 offen offset:2048 lds
v_mfma_f32_16x16x32_bf16 v[36:39],v[208:211],v[68:71],v[36:39]
s_add_u32 s82,s82,s98
buffer_load_dwordx4 v[60:63],v255,s[88:91],s86 offen offset:1024 sc1
v_mfma_f32_16x16x32_bf16 v[40:43],v[212:215],v[64:67],v[40:43]
s_mov_b32 s55,%[tb]
buffer_load_dwordx4 v252,s[68:71],s95 offen offset:3072 lds
v_swap_b32 v74,v76
v_swap_b32 v75,v77
v_mfma_f32_16x16x32_bf16 v[44:47],v[212:215],v[68:71],v[44:47]
s_waitcnt lgkmcnt(2)
ds_read_b64_tr_b16 v[144:145],v124 offset:20480
ds_read_b64_tr_b16 v[146:147],v124 offset:22528
v_mfma_f32_16x16x32_bf16 v[0:3],v[128:131],v[72:75],v[0:3]
s_mul_i32 s55,s55,14336
ds_read_b64_tr_b16 v[148:149],v124 offset:20992
ds_read_b64_tr_b16 v[150:151],v124 offset:23040
v_mfma_f32_16x16x32_bf16 v[4:7],v[128:131],v[76:79],v[4:7]
s_lshl_b32 s98,s53,11
buffer_load_dwordx4 v[64:67],v255,s[88:91],s86 offen offset:2048 sc1
v_mfma_f32_16x16x32_bf16 v[8:11],v[132:135],v[72:75],v[8:11]
s_add_u32 m0,s92,0x20000
buffer_load_dwordx4 v[68:71],v255,s[88:91],s86 offen offset:3072 sc1
v_mfma_f32_16x16x32_bf16 v[12:15],v[132:135],v[76:79],v[12:15]
s_waitcnt vmcnt(33) lgkmcnt(0)
v_cvt_pk_bf16_f32 v186,v188,v189
v_cvt_pk_bf16_f32 v187,v190,v191
v_mfma_f32_16x16x32_bf16 v[16:19],v[136:139],v[72:75],v[16:19]
s_barrier
ds_read_b64_tr_b16 v[192:193],v120 offset:32768
ds_read_b64_tr_b16 v[194:195],v120 offset:34816
v_mfma_f32_16x16x32_bf16 v[20:23],v[136:139],v[76:79],v[20:23]
s_mov_b32 s95,0x46000
ds_read_b64_tr_b16 v[196:197],v120 offset:33280
ds_read_b64_tr_b16 v[198:199],v120 offset:35328
v_mfma_f32_16x16x32_bf16 v[24:27],v[140:143],v[72:75],v[24:27]
s_waitcnt vmcnt(30)
buffer_load_dwordx4 v252,s[56:59],s95 offen lds
v_mfma_f32_16x16x32_bf16 v[28:31],v[140:143],v[76:79],v[28:31]
s_add_u32 s86,s86,4096
ds_read_b64_tr_b16 v[200:201],v120 offset:40960
)"
R"(ds_read_b64_tr_b16 v[202:203],v120 offset:43008
v_mfma_f32_16x16x32_bf16 v[32:35],v[144:147],v[72:75],v[32:35]
s_add_u32 s55,s55,s98
buffer_load_dwordx4 v252,s[60:63],s95 offen offset:1024 lds
v_mfma_f32_16x16x32_bf16 v[36:39],v[144:147],v[76:79],v[36:39]
s_add_u32 s55,s55,0xe00000
ds_read_b64_tr_b16 v[204:205],v120 offset:41472
ds_read_b64_tr_b16 v[206:207],v120 offset:43520
v_mfma_f32_16x16x32_bf16 v[40:43],v[148:151],v[72:75],v[40:43]
s_lshr_b32 s80,%[tb],2
buffer_load_dwordx4 v252,s[64:67],s95 offen offset:2048 lds
buffer_load_dwordx4 v252,s[68:71],s95 offen offset:3072 lds
v_swap_b32 v82,v84
v_swap_b32 v83,v85
v_mfma_f32_16x16x32_bf16 v[44:47],v[148:151],v[76:79],v[44:47]
s_waitcnt lgkmcnt(2)
ds_read_b64_tr_b16 v[208:209],v120 offset:49152
ds_read_b64_tr_b16 v[210:211],v120 offset:51200
v_mfma_f32_16x16x32_bf16 v[0:3],v[192:195],v[80:83],v[0:3]
s_add_u32 m0,s92,0x24000
ds_read_b64_tr_b16 v[212:213],v120 offset:49664
ds_read_b64_tr_b16 v[214:215],v120 offset:51712
v_mfma_f32_16x16x32_bf16 v[4:7],v[192:195],v[84:87],v[4:7]
s_mov_b32 s95,0x46100
ds_read_b64_tr_b16 v[128:129],v120 offset:36864
ds_read_b64_tr_b16 v[130:131],v120 offset:38912
v_mfma_f32_16x16x32_bf16 v[8:11],v[196:199],v[80:83],v[8:11]
s_lshl_b32 s80,s80,10
buffer_load_dwordx4 v252,s[56:59],s95 offen lds
v_mfma_f32_16x16x32_bf16 v[12:15],v[196:199],v[84:87],v[12:15]
s_waitcnt lgkmcnt(2)
ds_read_b64_tr_b16 v[132:133],v120 offset:37376
ds_read_b64_tr_b16 v[134:135],v120 offset:39424
v_mfma_f32_16x16x32_bf16 v[16:19],v[200:203],v[80:83],v[16:19]
s_add_u32 s80,s80,0x2b20000
ds_read_b64_tr_b16 v[136:137],v120 offset:45056
ds_read_b64_tr_b16 v[138:139],v120 offset:47104
v_mfma_f32_16x16x32_bf16 v[20:23],v[200:203],v[84:87],v[20:23]
s_add_u32 s101,s54,0
buffer_load_dwordx4 v252,s[60:63],s95 offen offset:1024 lds
v_mfma_f32_16x16x32_bf16 v[24:27],v[204:207],v[80:83],v[24:27]
s_waitcnt vmcnt(34)
ds_read_b64_tr_b16 v[140:141],v120 offset:45568
ds_read_b64_tr_b16 v[142:143],v120 offset:47616
v_mfma_f32_16x16x32_bf16 v[28:31],v[204:207],v[84:87],v[28:31]
s_and_b32 s100,%[w],1
buffer_load_dwordx4 v[72:75],v255,s[88:91],s86 offen sc1
v_mfma_f32_16x16x32_bf16 v[32:35],v[208:211],v[80:83],v[32:35]
s_lshl_b32 s100,s100,14
buffer_load_dwordx4 v252,s[64:67],s95 offen offset:2048 lds
v_mfma_f32_16x16x32_bf16 v[36:39],v[208:211],v[84:87],v[36:39]
s_lshr_b32 s96,%[h],1
buffer_load_dwordx4 v[76:79],v255,s[88:91],s86 offen offset:1024 sc1
v_mfma_f32_16x16x32_bf16 v[40:43],v[212:215],v[80:83],v[40:43]
s_mul_i32 s96,s96,1835008
buffer_load_dwordx4 v252,s[68:71],s95 offen offset:3072 lds
v_swap_b32 v90,v92
v_swap_b32 v91,v93
v_mfma_f32_16x16x32_bf16 v[44:47],v[212:215],v[84:87],v[44:47]
s_waitcnt lgkmcnt(2)
ds_read_b64_tr_b16 v[144:145],v120 offset:53248
ds_read_b64_tr_b16 v[146:147],v120 offset:55296
v_mfma_f32_16x16x32_bf16 v[0:3],v[128:131],v[88:91],v[0:3]
ds_read_b64_tr_b16 v[148:149],v120 offset:53760
ds_read_b64_tr_b16 v[150:151],v120 offset:55808
v_mfma_f32_16x16x32_bf16 v[4:7],v[128:131],v[92:95],v[4:7]
buffer_load_dwordx4 v[80:83],v255,s[88:91],s86 offen offset:2048 sc1
v_mfma_f32_16x16x32_bf16 v[8:11],v[132:135],v[88:91],v[8:11]
s_add_u32 m0,s92,0x8000
buffer_load_dwordx4 v[84:87],v255,s[88:91],s86 offen offset:3072 sc1
v_mfma_f32_16x16x32_bf16 v[12:15],v[132:135],v[92:95],v[12:15]
s_waitcnt vmcnt(26) lgkmcnt(0)
v_mfma_f32_16x16x32_bf16 v[16:19],v[136:139],v[88:91],v[16:19]
s_barrier
ds_read_b64_tr_b16 v[192:193],v122
ds_read_b64_tr_b16 v[194:195],v122 offset:2048
v_mfma_f32_16x16x32_bf16 v[20:23],v[136:139],v[92:95],v[20:23]
s_mov_b32 s95,0x54000
ds_read_b64_tr_b16 v[196:197],v122 offset:512
ds_read_b64_tr_b16 v[198:199],v122 offset:2560
v_mfma_f32_16x16x32_bf16 v[24:27],v[140:143],v[88:91],v[24:27]
s_waitcnt vmcnt(38)
buffer_load_dwordx4 v252,s[56:59],s95 offen lds
v_mfma_f32_16x16x32_bf16 v[28:31],v[140:143],v[92:95],v[28:31]
ds_read_b64_tr_b16 v[200:201],v122 offset:8192
ds_read_b64_tr_b16 v[202:203],v122 offset:10240
v_mfma_f32_16x16x32_bf16 v[32:35],v[144:147],v[88:91],v[32:35]
buffer_load_dwordx4 v252,s[60:63],s95 offen offset:1024 lds
v_mfma_f32_16x16x32_bf16 v[36:39],v[144:147],v[92:95],v[36:39]
ds_read_b64_tr_b16 v[204:205],v122 offset:8704
ds_read_b64_tr_b16 v[206:207],v122 offset:10752
v_mfma_f32_16x16x32_bf16 v[40:43],v[148:151],v[88:91],v[40:43]
buffer_load_dwordx4 v252,s[64:67],s95 offen offset:2048 lds
buffer_load_dwordx4 v252,s[68:71],s95 offen offset:3072 lds
v_swap_b32 v98,v100
v_swap_b32 v99,v101
v_mfma_f32_16x16x32_bf16 v[44:47],v[148:151],v[92:95],v[44:47]
s_waitcnt lgkmcnt(2)
ds_read_b64_tr_b16 v[208:209],v122 offset:16384
ds_read_b64_tr_b16 v[210:211],v122 offset:18432
v_mfma_f32_16x16x32_bf16 v[0:3],v[192:195],v[96:99],v[0:3]
s_add_u32 m0,s92,0xc000
ds_read_b64_tr_b16 v[212:213],v122 offset:16896
ds_read_b64_tr_b16 v[214:215],v122 offset:18944
v_mfma_f32_16x16x32_bf16 v[4:7],v[192:195],v[100:103],v[4:7]
s_mov_b32 s95,0x54100
ds_read_b64_tr_b16 v[128:129],v122 offset:4096
ds_read_b64_tr_b16 v[130:131],v122 offset:6144
v_mfma_f32_16x16x32_bf16 v[8:11],v[196:199],v[96:99],v[8:11]
buffer_load_dwordx4 v252,s[56:59],s95 offen lds
v_mfma_f32_16x16x32_bf16 v[12:15],v[196:199],v[100:103],v[12:15]
s_waitcnt lgkmcnt(2)
ds_read_b64_tr_b16 v[132:133],v122 offset:4608
ds_read_b64_tr_b16 v[134:135],v122 offset:6656
v_mfma_f32_16x16x32_bf16 v[16:19],v[200:203],v[96:99],v[16:19]
ds_read_b64_tr_b16 v[136:137],v122 offset:12288
ds_read_b64_tr_b16 v[138:139],v122 offset:14336
v_mfma_f32_16x16x32_bf16 v[20:23],v[200:203],v[100:103],v[20:23]
buffer_load_dwordx4 v252,s[60:63],s95 offen offset:1024 lds
v_mfma_f32_16x16x32_bf16 v[24:27],v[204:207],v[96:99],v[24:27]
s_waitcnt vmcnt(42)
ds_read_b64_tr_b16 v[140:141],v122 offset:12800
ds_read_b64_tr_b16 v[142:143],v122 offset:14848
v_mfma_f32_16x16x32_bf16 v[28:31],v[204:207],v[100:103],v[28:31]
ds_read_b64_tr_b16 v[144:145],v122 offset:20480
ds_read_b64_tr_b16 v[146:147],v122 offset:22528
v_mfma_f32_16x16x32_bf16 v[32:35],v[208:211],v[96:99],v[32:35]
buffer_load_dwordx4 v252,s[64:67],s95 offen offset:2048 lds
v_mfma_f32_16x16x32_bf16 v[36:39],v[208:211],v[100:103],v[36:39]
ds_read_b64_tr_b16 v[148:149],v122 offset:20992
ds_read_b64_tr_b16 v[150:151],v122 offset:23040
v_mfma_f32_16x16x32_bf16 v[40:43],v[212:215],v[96:99],v[40:43]
buffer_load_dwordx4 v252,s[68:71],s95 offen offset:3072 lds
v_swap_b32 v106,v108
v_swap_b32 v107,v109
v_mfma_f32_16x16x32_bf16 v[44:47],v[212:215],v[100:103],v[44:47]
s_waitcnt lgkmcnt(6)
v_mfma_f32_16x16x32_bf16 v[0:3],v[128:131],v[104:107],v[0:3]
s_lshr_b32 s98,%[w],1
v_mfma_f32_16x16x32_bf16 v[4:7],v[128:131],v[108:111],v[4:7]
s_mul_i32 s98,s98,57216
v_add_u32 v252,s98,v252
v_mfma_f32_16x16x32_bf16 v[8:11],v[132:135],v[104:107],v[8:11]
s_and_b32 s98,%[h],2
v_mfma_f32_16x16x32_bf16 v[12:15],v[132:135],v[108:111],v[12:15]
s_waitcnt vmcnt(22) lgkmcnt(0)
v_mfma_f32_16x16x32_bf16 v[16:19],v[136:139],v[104:107],v[16:19]
s_barrier
ds_read_b64_tr_b16 v[192:193],v120
ds_read_b64_tr_b16 v[194:195],v120 offset:2048
v_mfma_f32_16x16x32_bf16 v[20:23],v[136:139],v[108:111],v[20:23]
s_mul_i32 s98,s98,256
ds_read_b64_tr_b16 v[196:197],v120 offset:512
ds_read_b64_tr_b16 v[198:199],v120 offset:2560
v_mfma_f32_16x16x32_bf16 v[24:27],v[140:143],v[104:107],v[24:27]
s_waitcnt vmcnt(42)
ds_read_b64_tr_b16 v[200:201],v120 offset:8192
ds_read_b64_tr_b16 v[202:203],v120 offset:10240
v_mfma_f32_16x16x32_bf16 v[28:31],v[140:143],v[108:111],v[28:31]
ds_read_b64_tr_b16 v[204:205],v120 offset:8704
ds_read_b64_tr_b16 v[206:207],v120 offset:10752
v_mfma_f32_16x16x32_bf16 v[32:35],v[144:147],v[104:107],v[32:35]
ds_read_b64_tr_b16 v[208:209],v120 offset:16384
ds_read_b64_tr_b16 v[210:211],v120 offset:18432
v_mfma_f32_16x16x32_bf16 v[36:39],v[144:147],v[108:111],v[36:39]
ds_read_b64_tr_b16 v[212:213],v120 offset:16896
ds_read_b64_tr_b16 v[214:215],v120 offset:18944
v_mfma_f32_16x16x32_bf16 v[40:43],v[148:151],v[104:107],v[40:43]
v_swap_b32 v114,v116
v_swap_b32 v115,v117
v_mfma_f32_16x16x32_bf16 v[44:47],v[148:151],v[108:111],v[44:47]
s_waitcnt lgkmcnt(6)
v_subrev_u32 v252,s98,v252
v_mfma_f32_16x16x32_bf16 v[0:3],v[192:195],v[112:115],v[0:3]
s_and_b32 s98,%[w],2
ds_read_b64_tr_b16 v[128:129],v120 offset:4096
ds_read_b64_tr_b16 v[130:131],v120 offset:6144
v_mfma_f32_16x16x32_bf16 v[4:7],v[192:195],v[116:119],v[4:7]
s_lshl_b32 s98,s98,8
ds_read_b64_tr_b16 v[132:133],v120 offset:4608
v_mfma_f32_16x16x32_bf16 v[8:11],v[196:199],v[112:115],v[8:11]
s_add_u32 s98,s98,s100
ds_read_b64_tr_b16 v[134:135],v120 offset:6656
v_mfma_f32_16x16x32_bf16 v[12:15],v[196:199],v[116:119],v[12:15]
s_waitcnt lgkmcnt(4)
ds_read_b64_tr_b16 v[136:137],v120 offset:12288
ds_read_b64_tr_b16 v[138:139],v120 offset:14336
v_mfma_f32_16x16x32_bf16 v[16:19],v[200:203],v[112:115],v[16:19]
s_xor_b32 s100,s98,16384
ds_read_b64_tr_b16 v[140:141],v120 offset:12800
v_mfma_f32_16x16x32_bf16 v[20:23],v[200:203],v[116:119],v[20:23]
ds_read_b64_tr_b16 v[142:143],v120 offset:14848
v_mfma_f32_16x16x32_bf16 v[24:27],v[204:207],v[112:115],v[24:27]
s_waitcnt vmcnt(32)
ds_read_b64_tr_b16 v[144:145],v120 offset:20480
ds_read_b64_tr_b16 v[146:147],v120 offset:22528
v_mfma_f32_16x16x32_bf16 v[28:31],v[204:207],v[116:119],v[28:31]
ds_read_b64_tr_b16 v[148:149],v120 offset:20992
v_swap_b32 v50,v52
v_mfma_f32_16x16x32_bf16 v[32:35],v[208:211],v[112:115],v[32:35]
)"
R"(ds_read_b64_tr_b16 v[150:151],v120 offset:23040
v_swap_b32 v51,v53
v_mfma_f32_16x16x32_bf16 v[36:39],v[208:211],v[116:119],v[36:39]
v_mfma_f32_16x16x32_bf16 v[40:43],v[212:215],v[112:115],v[40:43]
v_mfma_f32_16x16x32_bf16 v[44:47],v[212:215],v[116:119],v[44:47]
s_waitcnt lgkmcnt(6)
v_mfma_f32_16x16x32_bf16 v[0:3],v[128:131],v[48:51],v[0:3]
v_mfma_f32_16x16x32_bf16 v[4:7],v[128:131],v[52:55],v[4:7]
v_mfma_f32_16x16x32_bf16 v[8:11],v[132:135],v[48:51],v[8:11]
v_mfma_f32_16x16x32_bf16 v[12:15],v[132:135],v[52:55],v[12:15]
s_waitcnt vmcnt(10) lgkmcnt(0)
v_mfma_f32_16x16x32_bf16 v[16:19],v[136:139],v[48:51],v[16:19]
s_barrier
ds_read_b64_tr_b16 v[192:193],v124
ds_read_b64_tr_b16 v[194:195],v124 offset:2048
v_mfma_f32_16x16x32_bf16 v[20:23],v[136:139],v[52:55],v[20:23]
s_add_u32 m0,s92,0x0
ds_read_b64_tr_b16 v[196:197],v124 offset:512
ds_read_b64_tr_b16 v[198:199],v124 offset:2560
v_mfma_f32_16x16x32_bf16 v[24:27],v[140:143],v[48:51],v[24:27]
s_mov_b32 s95,0x0
ds_read_b64_tr_b16 v[200:201],v124 offset:8192
ds_read_b64_tr_b16 v[202:203],v124 offset:10240
v_mfma_f32_16x16x32_bf16 v[28:31],v[140:143],v[52:55],v[28:31]
s_waitcnt vmcnt(23)
buffer_load_dwordx4 v252,s[24:27],s95 offen lds
v_mfma_f32_16x16x32_bf16 v[32:35],v[144:147],v[48:51],v[32:35]
ds_read_b64_tr_b16 v[204:205],v124 offset:8704
ds_read_b64_tr_b16 v[206:207],v124 offset:10752
v_mfma_f32_16x16x32_bf16 v[36:39],v[144:147],v[52:55],v[36:39]
buffer_load_dwordx4 v252,s[28:31],s95 offen offset:1024 lds
v_mfma_f32_16x16x32_bf16 v[40:43],v[148:151],v[48:51],v[40:43]
v_swap_b32 v58,v60
v_swap_b32 v59,v61
v_mfma_f32_16x16x32_bf16 v[44:47],v[148:151],v[52:55],v[44:47]
s_waitcnt lgkmcnt(2)
ds_read_b64_tr_b16 v[208:209],v124 offset:16384
ds_read_b64_tr_b16 v[210:211],v124 offset:18432
v_mfma_f32_16x16x32_bf16 v[0:3],v[192:195],v[56:59],v[0:3]
ds_read_b64_tr_b16 v[212:213],v124 offset:16896
ds_read_b64_tr_b16 v[214:215],v124 offset:18944
v_mfma_f32_16x16x32_bf16 v[4:7],v[192:195],v[60:63],v[4:7]
ds_read_b64_tr_b16 v[128:129],v124 offset:4096
ds_read_b64_tr_b16 v[130:131],v124 offset:6144
v_mfma_f32_16x16x32_bf16 v[8:11],v[196:199],v[56:59],v[8:11]
ds_read_b64_tr_b16 v[132:133],v124 offset:4608
ds_read_b64_tr_b16 v[134:135],v124 offset:6656
v_mfma_f32_16x16x32_bf16 v[12:15],v[196:199],v[60:63],v[12:15]
s_waitcnt lgkmcnt(4)
ds_read_b64_tr_b16 v[136:137],v124 offset:12288
ds_read_b64_tr_b16 v[138:139],v124 offset:14336
v_mfma_f32_16x16x32_bf16 v[16:19],v[200:203],v[56:59],v[16:19]
ds_read_b64_tr_b16 v[140:141],v124 offset:12800
v_mfma_f32_16x16x32_bf16 v[20:23],v[200:203],v[60:63],v[20:23]
ds_read_b64_tr_b16 v[142:143],v124 offset:14848
v_mfma_f32_16x16x32_bf16 v[24:27],v[204:207],v[56:59],v[24:27]
s_waitcnt vmcnt(22)
ds_read_b64_tr_b16 v[144:145],v124 offset:20480
ds_read_b64_tr_b16 v[146:147],v124 offset:22528
v_mfma_f32_16x16x32_bf16 v[28:31],v[204:207],v[60:63],v[28:31]
buffer_load_dwordx4 v252,s[32:35],s95 offen offset:2048 lds
v_mfma_f32_16x16x32_bf16 v[32:35],v[208:211],v[56:59],v[32:35]
ds_read_b64_tr_b16 v[148:149],v124 offset:20992
ds_read_b64_tr_b16 v[150:151],v124 offset:23040
v_mfma_f32_16x16x32_bf16 v[36:39],v[208:211],v[60:63],v[36:39]
buffer_load_dwordx4 v252,s[36:39],s95 offen offset:3072 lds
v_mfma_f32_16x16x32_bf16 v[40:43],v[212:215],v[56:59],v[40:43]
v_swap_b32 v66,v68
v_swap_b32 v67,v69
v_mfma_f32_16x16x32_bf16 v[44:47],v[212:215],v[60:63],v[44:47]
s_waitcnt lgkmcnt(6)
v_mfma_f32_16x16x32_bf16 v[0:3],v[128:131],v[64:67],v[0:3]
v_mfma_f32_16x16x32_bf16 v[4:7],v[128:131],v[68:71],v[4:7]
v_mfma_f32_16x16x32_bf16 v[8:11],v[132:135],v[64:67],v[8:11]
v_mfma_f32_16x16x32_bf16 v[12:15],v[132:135],v[68:71],v[12:15]
s_waitcnt vmcnt(4) lgkmcnt(0)
v_mfma_f32_16x16x32_bf16 v[16:19],v[136:139],v[64:67],v[16:19]
s_barrier
ds_read_b64_tr_b16 v[192:193],v120 offset:32768
ds_read_b64_tr_b16 v[194:195],v120 offset:34816
v_mfma_f32_16x16x32_bf16 v[20:23],v[136:139],v[68:71],v[20:23]
s_add_u32 m0,s92,0x4000
ds_read_b64_tr_b16 v[196:197],v120 offset:33280
ds_read_b64_tr_b16 v[198:199],v120 offset:35328
v_mfma_f32_16x16x32_bf16 v[24:27],v[140:143],v[64:67],v[24:27]
s_mov_b32 s95,0x38000
ds_read_b64_tr_b16 v[200:201],v120 offset:40960
ds_read_b64_tr_b16 v[202:203],v120 offset:43008
v_mfma_f32_16x16x32_bf16 v[28:31],v[140:143],v[68:71],v[28:31]
s_waitcnt vmcnt(15)
buffer_load_dwordx4 v252,s[24:27],s95 offen lds
v_mfma_f32_16x16x32_bf16 v[32:35],v[144:147],v[64:67],v[32:35]
ds_read_b64_tr_b16 v[204:205],v120 offset:41472
ds_read_b64_tr_b16 v[206:207],v120 offset:43520
v_mfma_f32_16x16x32_bf16 v[36:39],v[144:147],v[68:71],v[36:39]
buffer_load_dwordx4 v252,s[28:31],s95 offen offset:1024 lds
v_mfma_f32_16x16x32_bf16 v[40:43],v[148:151],v[64:67],v[40:43]
v_swap_b32 v74,v76
v_swap_b32 v75,v77
v_mfma_f32_16x16x32_bf16 v[44:47],v[148:151],v[68:71],v[44:47]
s_waitcnt lgkmcnt(2)
ds_read_b64_tr_b16 v[208:209],v120 offset:49152
ds_read_b64_tr_b16 v[210:211],v120 offset:51200
v_mfma_f32_16x16x32_bf16 v[0:3],v[192:195],v[72:75],v[0:3]
ds_read_b64_tr_b16 v[212:213],v120 offset:49664
ds_read_b64_tr_b16 v[214:215],v120 offset:51712
v_mfma_f32_16x16x32_bf16 v[4:7],v[192:195],v[76:79],v[4:7]
ds_read_b64_tr_b16 v[128:129],v120 offset:36864
ds_read_b64_tr_b16 v[130:131],v120 offset:38912
v_mfma_f32_16x16x32_bf16 v[8:11],v[196:199],v[72:75],v[8:11]
ds_read_b64_tr_b16 v[132:133],v120 offset:37376
ds_read_b64_tr_b16 v[134:135],v120 offset:39424
v_mfma_f32_16x16x32_bf16 v[12:15],v[196:199],v[76:79],v[12:15]
s_waitcnt lgkmcnt(4)
ds_read_b64_tr_b16 v[136:137],v120 offset:45056
ds_read_b64_tr_b16 v[138:139],v120 offset:47104
v_mfma_f32_16x16x32_bf16 v[16:19],v[200:203],v[72:75],v[16:19]
ds_read_b64_tr_b16 v[140:141],v120 offset:45568
v_mfma_f32_16x16x32_bf16 v[20:23],v[200:203],v[76:79],v[20:23]
ds_read_b64_tr_b16 v[142:143],v120 offset:47616
v_mfma_f32_16x16x32_bf16 v[24:27],v[204:207],v[72:75],v[24:27]
s_waitcnt vmcnt(14)
ds_read_b64_tr_b16 v[144:145],v120 offset:53248
ds_read_b64_tr_b16 v[146:147],v120 offset:55296
v_mfma_f32_16x16x32_bf16 v[28:31],v[204:207],v[76:79],v[28:31]
buffer_load_dwordx4 v252,s[32:35],s95 offen offset:2048 lds
v_mfma_f32_16x16x32_bf16 v[32:35],v[208:211],v[72:75],v[32:35]
ds_read_b64_tr_b16 v[148:149],v120 offset:53760
ds_read_b64_tr_b16 v[150:151],v120 offset:55808
v_mfma_f32_16x16x32_bf16 v[36:39],v[208:211],v[76:79],v[36:39]
buffer_load_dwordx4 v252,s[36:39],s95 offen offset:3072 lds
v_mfma_f32_16x16x32_bf16 v[40:43],v[212:215],v[72:75],v[40:43]
v_swap_b32 v82,v84
v_swap_b32 v83,v85
v_mfma_f32_16x16x32_bf16 v[44:47],v[212:215],v[76:79],v[44:47]
s_waitcnt lgkmcnt(6)
v_mfma_f32_16x16x32_bf16 v[0:3],v[128:131],v[80:83],v[0:3]
v_mfma_f32_16x16x32_bf16 v[4:7],v[128:131],v[84:87],v[4:7]
v_mov_b32 v128,v246
v_mov_b32 v130,v247
v_mfma_f32_16x16x32_bf16 v[8:11],v[132:135],v[80:83],v[8:11]
v_add_u32 v246,s98,v124
v_add_u32 v247,s100,v124
v_mfma_f32_16x16x32_bf16 v[12:15],v[132:135],v[84:87],v[12:15]
s_waitcnt vmcnt(63) lgkmcnt(0)
v_cvt_pk_bf16_f32 v0,v0,v1
v_cvt_pk_bf16_f32 v1,v2,v3
v_mfma_f32_16x16x32_bf16 v[16:19],v[136:139],v[80:83],v[16:19]
s_barrier
ds_read_b64_tr_b16 v[192:193],v250 offset:32768
ds_read_b64_tr_b16 v[194:195],v250 offset:40960
v_mfma_f32_16x16x32_bf16 v[20:23],v[136:139],v[84:87],v[20:23]
ds_read_b64_tr_b16 v[196:197],v251 offset:32768
ds_read_b64_tr_b16 v[198:199],v251 offset:40960
v_mfma_f32_16x16x32_bf16 v[24:27],v[140:143],v[80:83],v[24:27]
ds_read_b64_tr_b16 v[200:201],v250 offset:33280
ds_read_b64_tr_b16 v[202:203],v250 offset:41472
v_mfma_f32_16x16x32_bf16 v[28:31],v[140:143],v[84:87],v[28:31]
ds_read_b64_tr_b16 v[204:205],v251 offset:33280
ds_read_b64_tr_b16 v[206:207],v251 offset:41472
v_mfma_f32_16x16x32_bf16 v[32:35],v[144:147],v[80:83],v[32:35]
v_cvt_pk_bf16_f32 v2,v4,v5
v_cvt_pk_bf16_f32 v3,v6,v7
v_mfma_f32_16x16x32_bf16 v[36:39],v[144:147],v[84:87],v[36:39]
buffer_store_dwordx4 v[0:3],v255,s[88:91],s101 offen sc1
v_mfma_f32_16x16x32_bf16 v[40:43],v[148:151],v[80:83],v[40:43]
s_add_u32 s101,s54,8192
v_cvt_pk_bf16_f32 v8,v8,v9
v_cvt_pk_bf16_f32 v9,v10,v11
v_mfma_f32_16x16x32_bf16 v[44:47],v[148:151],v[84:87],v[44:47]
s_waitcnt lgkmcnt(0)
v_cvt_pk_bf16_f32 v10,v12,v13
v_cvt_pk_bf16_f32 v11,v14,v15
v_mfma_f32_16x16x32_bf16 v[224:227],v[240:243],v[160:163],v[224:227]
s_lshl_b32 s99,%[w],13
ds_read_b64_tr_b16 v[208:209],v250 offset:33792
v_cvt_pk_bf16_f32 v16,v16,v17
v_mfma_f32_16x16x32_bf16 v[228:231],v[240:243],v[168:171],v[228:231]
s_add_u32 s99,s99,32768
ds_read_b64_tr_b16 v[210:211],v250 offset:41984
v_add_u32 v248,s99,v255
v_mfma_f32_16x16x32_bf16 v[232:235],v[240:243],v[176:179],v[232:235]
ds_read_b64_tr_b16 v[212:213],v251 offset:33792
v_add_u32 v249,65536,v248
v_mfma_f32_16x16x32_bf16 v[236:239],v[240:243],v[184:187],v[236:239]
ds_read_b64_tr_b16 v[214:215],v251 offset:41984
v_xor_b32 v254,16384,v249
v_mfma_f32_16x16x32_bf16 a[0:3],v[192:195],v[160:163],a[0:3]
buffer_store_dwordx4 v[8:11],v255,s[88:91],s101 offen sc1
v_mfma_f32_16x16x32_bf16 a[4:7],v[196:199],v[160:163],a[4:7]
s_add_u32 s101,s54,16384
ds_read_b64_tr_b16 v[216:217],v250 offset:34304
v_mov_b32 v129,v232
v_mfma_f32_16x16x32_bf16 a[8:11],v[200:203],v[160:163],a[8:11]
ds_read_b64_tr_b16 v[218:219],v250 offset:42496
v_mov_b32 v131,v236
v_mfma_f32_16x16x32_bf16 a[12:15],v[204:207],v[160:163],a[12:15]
ds_read_b64_tr_b16 v[220:221],v251 offset:34304
ds_write_b128 v254,v[128:131] offset:32768
v_mfma_f32_16x16x32_bf16 a[64:67],v[192:195],v[168:171],a[64:67]
)"
R"(ds_read_b64_tr_b16 v[222:223],v251 offset:42496
v_cvt_pk_bf16_f32 v17,v18,v19
v_mfma_f32_16x16x32_bf16 a[68:71],v[196:199],v[168:171],a[68:71]
v_cvt_pk_bf16_f32 v18,v20,v21
v_cvt_pk_bf16_f32 v19,v22,v23
v_mfma_f32_16x16x32_bf16 a[72:75],v[200:203],v[168:171],a[72:75]
buffer_store_dwordx4 v[16:19],v255,s[88:91],s101 offen sc1
v_mfma_f32_16x16x32_bf16 a[76:79],v[204:207],v[168:171],a[76:79]
s_add_u32 s101,s54,24576
v_cvt_pk_bf16_f32 v24,v24,v25
v_cvt_pk_bf16_f32 v25,v26,v27
v_mfma_f32_16x16x32_bf16 a[128:131],v[192:195],v[176:179],a[128:131]
v_cvt_pk_bf16_f32 v26,v28,v29
v_cvt_pk_bf16_f32 v27,v30,v31
v_mfma_f32_16x16x32_bf16 a[132:135],v[196:199],v[176:179],a[132:135]
buffer_store_dwordx4 v[24:27],v255,s[88:91],s101 offen sc1
v_mfma_f32_16x16x32_bf16 a[136:139],v[200:203],v[176:179],a[136:139]
s_add_u32 s101,s54,32768
v_cvt_pk_bf16_f32 v32,v32,v33
v_cvt_pk_bf16_f32 v33,v34,v35
v_mfma_f32_16x16x32_bf16 a[140:143],v[204:207],v[176:179],a[140:143]
ds_write_b128 v248,a[128:131]
v_cvt_pk_bf16_f32 v34,v36,v37
v_mfma_f32_16x16x32_bf16 a[192:195],v[192:195],v[184:187],a[192:195]
ds_write_b128 v248,a[132:135] offset:1024
v_cvt_pk_bf16_f32 v35,v38,v39
v_mfma_f32_16x16x32_bf16 a[196:199],v[196:199],v[184:187],a[196:199]
buffer_store_dwordx4 v[32:35],v255,s[88:91],s101 offen sc1
v_mfma_f32_16x16x32_bf16 a[200:203],v[200:203],v[184:187],a[200:203]
s_add_u32 s101,s54,40960
ds_write_b128 v248,a[136:139] offset:2048
v_cvt_pk_bf16_f32 v40,v40,v41
v_mfma_f32_16x16x32_bf16 a[204:207],v[204:207],v[184:187],a[204:207]
s_waitcnt lgkmcnt(3)
ds_write_b128 v248,a[140:143] offset:3072
v_cvt_pk_bf16_f32 v41,v42,v43
v_mfma_f32_16x16x32_bf16 a[16:19],v[208:211],v[160:163],a[16:19]
ds_read_b64_tr_b16 v[192:193],v250 offset:34816
v_cvt_pk_bf16_f32 v42,v44,v45
v_mfma_f32_16x16x32_bf16 a[80:83],v[208:211],v[168:171],a[80:83]
ds_read_b64_tr_b16 v[194:195],v250 offset:43008
v_cvt_pk_bf16_f32 v43,v46,v47
v_mfma_f32_16x16x32_bf16 a[144:147],v[208:211],v[176:179],a[144:147]
ds_read_b64_tr_b16 v[196:197],v251 offset:34816
v_xor_b32 v253,16384,v248
v_mfma_f32_16x16x32_bf16 a[208:211],v[208:211],v[184:187],a[208:211]
ds_read_b64_tr_b16 v[198:199],v251 offset:43008
ds_write_b128 v248,a[192:195] offset:4096
v_mfma_f32_16x16x32_bf16 a[20:23],v[212:215],v[160:163],a[20:23]
ds_read_b64_tr_b16 v[200:201],v250 offset:35328
ds_write_b128 v248,a[196:199] offset:5120
v_mfma_f32_16x16x32_bf16 a[84:87],v[212:215],v[168:171],a[84:87]
ds_read_b64_tr_b16 v[202:203],v250 offset:43520
ds_write_b128 v248,a[200:203] offset:6144
v_mfma_f32_16x16x32_bf16 a[148:151],v[212:215],v[176:179],a[148:151]
ds_read_b64_tr_b16 v[204:205],v251 offset:35328
ds_write_b128 v248,a[204:207] offset:7168
v_mfma_f32_16x16x32_bf16 a[212:215],v[212:215],v[184:187],a[212:215]
ds_read_b64_tr_b16 v[206:207],v251 offset:43520
ds_write_b128 v248,a[144:147] offset:32768
v_mfma_f32_16x16x32_bf16 a[24:27],v[216:219],v[160:163],a[24:27]
buffer_store_dwordx4 v[40:43],v255,s[88:91],s101 offen sc1
v_mfma_f32_16x16x32_bf16 a[88:91],v[216:219],v[168:171],a[88:91]
ds_write_b128 v248,a[148:151] offset:33792
ds_write_b128 v248,a[208:211] offset:36864
v_mfma_f32_16x16x32_bf16 a[152:155],v[216:219],v[176:179],a[152:155]
ds_write_b128 v248,a[212:215] offset:37888
v_add_u32 v240,s98,v120
v_mfma_f32_16x16x32_bf16 a[216:219],v[216:219],v[184:187],a[216:219]
v_add_u32 v241,s100,v120
v_add_u32 v242,s98,v122
v_mfma_f32_16x16x32_bf16 a[28:31],v[220:223],v[160:163],a[28:31]
v_add_u32 v243,s100,v122
v_lshlrev_b32 v226,3,v255
v_mfma_f32_16x16x32_bf16 a[92:95],v[220:223],v[168:171],a[92:95]
s_lshl_b32 s100,%[w],10
ds_write_b128 v248,a[152:155] offset:34816
ds_write_b128 v248,a[216:219] offset:38912
v_mfma_f32_16x16x32_bf16 a[156:159],v[220:223],v[176:179],a[156:159]
s_add_u32 s96,s96,s100
v_and_b32 v226,896,v226
v_mov_b32 v231,v254
v_mfma_f32_16x16x32_bf16 a[220:223],v[220:223],v[184:187],a[220:223]
s_waitcnt lgkmcnt(6)
v_xor_b32 v225,8192,v254
v_mfma_f32_16x16x32_bf16 a[32:35],v[192:195],v[160:163],a[32:35]
s_add_u32 s96,s96,0xe00000
ds_read_b64_tr_b16 v[208:209],v250 offset:35840
ds_write_b128 v248,a[156:159] offset:35840
v_mfma_f32_16x16x32_bf16 a[96:99],v[192:195],v[168:171],a[96:99]
s_xor_b32 s100,%[tb],2
ds_read_b64_tr_b16 v[210:211],v250 offset:44032
ds_write_b128 v248,a[220:223] offset:39936
v_mfma_f32_16x16x32_bf16 a[160:163],v[192:195],v[176:179],a[160:163]
s_mul_i32 s100,s100,14336
ds_read_b64_tr_b16 v[212:213],v251 offset:35840
v_mfma_f32_16x16x32_bf16 a[224:227],v[192:195],v[184:187],a[224:227]
s_add_u32 s93,s96,s100
ds_read_b64_tr_b16 v[214:215],v251 offset:44032
v_mfma_f32_16x16x32_bf16 a[36:39],v[196:199],v[160:163],a[36:39]
s_mul_i32 s100,%[tb],14336
ds_read_b64_tr_b16 v[216:217],v250 offset:36352
v_mfma_f32_16x16x32_bf16 a[100:103],v[196:199],v[168:171],a[100:103]
s_add_u32 s96,s96,s100
ds_read_b64_tr_b16 v[218:219],v250 offset:44544
v_mfma_f32_16x16x32_bf16 a[164:167],v[196:199],v[176:179],a[164:167]
ds_read_b64_tr_b16 v[220:221],v251 offset:36352
v_mfma_f32_16x16x32_bf16 a[228:231],v[196:199],v[184:187],a[228:231]
ds_read_b64_tr_b16 v[222:223],v251 offset:44544
v_mfma_f32_16x16x32_bf16 a[40:43],v[200:203],v[160:163],a[40:43]
v_mfma_f32_16x16x32_bf16 a[104:107],v[200:203],v[168:171],a[104:107]
v_mfma_f32_16x16x32_bf16 a[168:171],v[200:203],v[176:179],a[168:171]
v_mfma_f32_16x16x32_bf16 a[232:235],v[200:203],v[184:187],a[232:235]
v_mfma_f32_16x16x32_bf16 a[44:47],v[204:207],v[160:163],a[44:47]
v_mfma_f32_16x16x32_bf16 a[108:111],v[204:207],v[168:171],a[108:111]
v_mfma_f32_16x16x32_bf16 a[172:175],v[204:207],v[176:179],a[172:175]
v_mfma_f32_16x16x32_bf16 a[236:239],v[204:207],v[184:187],a[236:239]
s_waitcnt lgkmcnt(0)
v_mfma_f32_16x16x32_bf16 a[48:51],v[208:211],v[160:163],a[48:51]
s_barrier
ds_read_b128 v[132:135],v249 offset:32768
ds_write_b128 v249,a[160:163] offset:32768
v_mfma_f32_16x16x32_bf16 a[112:115],v[208:211],v[168:171],a[112:115]
ds_write_b128 v249,a[164:167] offset:33792
ds_write_b128 v249,a[168:171] offset:34816
v_mfma_f32_16x16x32_bf16 a[176:179],v[208:211],v[176:179],a[176:179]
ds_write_b128 v249,a[172:175] offset:35840
ds_write_b128 v249,a[224:227] offset:36864
v_mfma_f32_16x16x32_bf16 a[240:243],v[208:211],v[184:187],a[240:243]
ds_write_b128 v249,a[228:231] offset:37888
ds_write_b128 v249,a[232:235] offset:38912
v_mfma_f32_16x16x32_bf16 a[52:55],v[212:215],v[160:163],a[52:55]
ds_write_b128 v249,a[236:239] offset:39936
v_mfma_f32_16x16x32_bf16 a[116:119],v[212:215],v[168:171],a[116:119]
ds_write_b128 v249,a[176:179]
v_mfma_f32_16x16x32_bf16 a[180:183],v[212:215],v[176:179],a[180:183]
v_mfma_f32_16x16x32_bf16 a[244:247],v[212:215],v[184:187],a[244:247]
ds_write_b128 v249,a[240:243] offset:4096
v_mfma_f32_16x16x32_bf16 a[56:59],v[216:219],v[160:163],a[56:59]
s_waitcnt lgkmcnt(10)
v_min_f32 v136,v244,v132
v_min_f32 v137,v245,v134
v_mfma_f32_16x16x32_bf16 a[120:123],v[216:219],v[168:171],a[120:123]
v_sub_f32 v232,v136,v244
v_sub_f32 v234,v137,v245
v_mfma_f32_16x16x32_bf16 a[184:187],v[216:219],v[176:179],a[184:187]
ds_write_b128 v249,a[180:183] offset:1024
v_sub_f32 v236,v136,v132
v_mfma_f32_16x16x32_bf16 a[248:251],v[216:219],v[184:187],a[248:251]
ds_write_b128 v249,a[244:247] offset:5120
v_sub_f32 v238,v137,v134
v_mfma_f32_16x16x32_bf16 a[60:63],v[220:223],v[160:163],a[60:63]
v_exp_f32 v232,v232
v_mfma_f32_16x16x32_bf16 a[124:127],v[220:223],v[168:171],a[124:127]
v_exp_f32 v234,v234
v_mfma_f32_16x16x32_bf16 a[188:191],v[220:223],v[176:179],a[188:191]
v_exp_f32 v236,v236
v_mfma_f32_16x16x32_bf16 a[252:255],v[220:223],v[184:187],a[252:255]
v_exp_f32 v238,v238
v_mul_f32 v136,v224,v232
v_mul_f32 v137,v228,v234
v_fma_f32 v136,v133,v236,v136
v_fma_f32 v137,v135,v238,v137
v_rcp_f32 v136,v136
v_rcp_f32 v137,v137
v_mul_f32 v232,v232,v136
v_mul_f32 v234,v234,v137
v_mul_f32 v236,v236,v136
v_mul_f32 v238,v238,v137
ds_write_b128 v249,a[184:187] offset:2048
ds_write_b128 v249,a[188:191] offset:3072
ds_write_b128 v249,a[248:251] offset:6144
ds_write_b128 v249,a[252:255] offset:7168
v_lshrrev_b32 v228,6,v255
v_mul_u32_u24 v227,896,v228
v_lshlrev_b32 v228,3,v228
v_xor_b32 v228,v228,v255
v_and_b32 v228,48,v228
v_add_u32 v227,v227,v228
v_xor_b32 v228,16384,v254
v_xor_b32 v224,16384,v225
s_waitcnt vmcnt(6) lgkmcnt(0)
s_barrier
ds_read_b128 v[0:3],v253
ds_read_b128 v[4:7],v253 offset:1024
ds_read_b128 v[8:11],v253 offset:2048
ds_read_b128 v[12:15],v253 offset:3072
ds_read_b128 v[16:19],v253 offset:32768
ds_read_b128 v[20:23],v253 offset:33792
ds_read_b128 v[24:27],v253 offset:34816
ds_read_b128 v[28:31],v253 offset:35840
ds_read_b128 v[64:67],v253 offset:4096
ds_read_b128 v[68:71],v253 offset:5120
ds_read_b128 v[72:75],v253 offset:6144
ds_read_b128 v[76:79],v253 offset:7168
ds_read_b128 v[80:83],v253 offset:36864
ds_read_b128 v[84:87],v253 offset:37888
ds_read_b128 v[88:91],v253 offset:38912
ds_read_b128 v[92:95],v253 offset:39936
ds_read_b128 v[32:35],v231 offset:32768
)"
R"(ds_read_b128 v[36:39],v231 offset:33792
ds_read_b128 v[40:43],v231 offset:34816
ds_read_b128 v[44:47],v231 offset:35840
ds_read_b128 v[48:51],v231
ds_read_b128 v[52:55],v231 offset:1024
ds_read_b128 v[56:59],v231 offset:2048
ds_read_b128 v[60:63],v231 offset:3072
ds_read_b128 v[96:99],v231 offset:36864
ds_read_b128 v[100:103],v231 offset:37888
ds_read_b128 v[104:107],v231 offset:38912
ds_read_b128 v[108:111],v231 offset:39936
ds_read_b128 v[112:115],v231 offset:4096
ds_read_b128 v[116:119],v231 offset:5120
ds_read_b128 v[120:123],v231 offset:6144
ds_read_b128 v[124:127],v231 offset:7168
s_waitcnt lgkmcnt(15)
v_accvgpr_read_b32 v248,a0
v_accvgpr_read_b32 v249,a1
v_accvgpr_read_b32 v250,a2
v_accvgpr_read_b32 v251,a3
v_pk_mul_f32 v[0:1],v[0:1],v[236:237] op_sel_hi:[1,0]
v_pk_mul_f32 v[2:3],v[2:3],v[236:237] op_sel_hi:[1,0]
v_pk_fma_f32 v[0:1],v[248:249],v[232:233],v[0:1] op_sel_hi:[1,0,1]
v_pk_fma_f32 v[2:3],v[250:251],v[232:233],v[2:3] op_sel_hi:[1,0,1]
v_accvgpr_read_b32 v248,a4
v_accvgpr_read_b32 v249,a5
v_accvgpr_read_b32 v250,a6
v_accvgpr_read_b32 v251,a7
v_pk_mul_f32 v[4:5],v[4:5],v[236:237] op_sel_hi:[1,0]
v_pk_mul_f32 v[6:7],v[6:7],v[236:237] op_sel_hi:[1,0]
v_pk_fma_f32 v[4:5],v[248:249],v[232:233],v[4:5] op_sel_hi:[1,0,1]
v_pk_fma_f32 v[6:7],v[250:251],v[232:233],v[6:7] op_sel_hi:[1,0,1]
v_cvt_pk_bf16_f32 v128,v0,v1
v_cvt_pk_bf16_f32 v129,v2,v3
v_cvt_pk_bf16_f32 v130,v4,v5
v_cvt_pk_bf16_f32 v131,v6,v7
v_accvgpr_read_b32 v248,a8
v_accvgpr_read_b32 v249,a9
v_accvgpr_read_b32 v250,a10
v_accvgpr_read_b32 v251,a11
v_pk_mul_f32 v[8:9],v[8:9],v[236:237] op_sel_hi:[1,0]
v_pk_mul_f32 v[10:11],v[10:11],v[236:237] op_sel_hi:[1,0]
v_pk_fma_f32 v[8:9],v[248:249],v[232:233],v[8:9] op_sel_hi:[1,0,1]
v_pk_fma_f32 v[10:11],v[250:251],v[232:233],v[10:11] op_sel_hi:[1,0,1]
v_accvgpr_read_b32 v248,a12
v_accvgpr_read_b32 v249,a13
v_accvgpr_read_b32 v250,a14
v_accvgpr_read_b32 v251,a15
v_pk_mul_f32 v[12:13],v[12:13],v[236:237] op_sel_hi:[1,0]
v_pk_mul_f32 v[14:15],v[14:15],v[236:237] op_sel_hi:[1,0]
v_pk_fma_f32 v[12:13],v[248:249],v[232:233],v[12:13] op_sel_hi:[1,0,1]
v_pk_fma_f32 v[14:15],v[250:251],v[232:233],v[14:15] op_sel_hi:[1,0,1]
v_cvt_pk_bf16_f32 v132,v8,v9
v_cvt_pk_bf16_f32 v133,v10,v11
v_cvt_pk_bf16_f32 v134,v12,v13
v_cvt_pk_bf16_f32 v135,v14,v15
s_waitcnt lgkmcnt(0)
s_barrier
s_add_u32 m0,s92,0x8000
s_mov_b32 s95,0x80
buffer_load_dwordx4 v252,s[24:27],s95 offen lds
v_accvgpr_read_b32 v248,a16
v_accvgpr_read_b32 v249,a17
v_accvgpr_read_b32 v250,a18
v_accvgpr_read_b32 v251,a19
v_pk_mul_f32 v[16:17],v[16:17],v[236:237] op_sel_hi:[1,0]
v_pk_mul_f32 v[18:19],v[18:19],v[236:237] op_sel_hi:[1,0]
v_pk_fma_f32 v[16:17],v[248:249],v[232:233],v[16:17] op_sel_hi:[1,0,1]
v_pk_fma_f32 v[18:19],v[250:251],v[232:233],v[18:19] op_sel_hi:[1,0,1]
v_accvgpr_read_b32 v248,a20
v_accvgpr_read_b32 v249,a21
v_accvgpr_read_b32 v250,a22
v_accvgpr_read_b32 v251,a23
v_pk_mul_f32 v[20:21],v[20:21],v[236:237] op_sel_hi:[1,0]
v_pk_mul_f32 v[22:23],v[22:23],v[236:237] op_sel_hi:[1,0]
v_pk_fma_f32 v[20:21],v[248:249],v[232:233],v[20:21] op_sel_hi:[1,0,1]
v_pk_fma_f32 v[22:23],v[250:251],v[232:233],v[22:23] op_sel_hi:[1,0,1]
v_cvt_pk_bf16_f32 v136,v16,v17
v_cvt_pk_bf16_f32 v137,v18,v19
v_cvt_pk_bf16_f32 v138,v20,v21
v_cvt_pk_bf16_f32 v139,v22,v23
ds_write_b128 v231,v[136:139] offset:2048
ds_write_b128 v231,v[128:131]
buffer_load_dwordx4 v252,s[28:31],s95 offen offset:1024 lds
v_accvgpr_read_b32 v248,a24
v_accvgpr_read_b32 v249,a25
v_accvgpr_read_b32 v250,a26
v_accvgpr_read_b32 v251,a27
v_pk_mul_f32 v[24:25],v[24:25],v[236:237] op_sel_hi:[1,0]
v_pk_mul_f32 v[26:27],v[26:27],v[236:237] op_sel_hi:[1,0]
v_pk_fma_f32 v[24:25],v[248:249],v[232:233],v[24:25] op_sel_hi:[1,0,1]
v_pk_fma_f32 v[26:27],v[250:251],v[232:233],v[26:27] op_sel_hi:[1,0,1]
v_accvgpr_read_b32 v248,a28
v_accvgpr_read_b32 v249,a29
v_accvgpr_read_b32 v250,a30
v_accvgpr_read_b32 v251,a31
v_pk_mul_f32 v[28:29],v[28:29],v[236:237] op_sel_hi:[1,0]
v_pk_mul_f32 v[30:31],v[30:31],v[236:237] op_sel_hi:[1,0]
v_pk_fma_f32 v[28:29],v[248:249],v[232:233],v[28:29] op_sel_hi:[1,0,1]
v_pk_fma_f32 v[30:31],v[250:251],v[232:233],v[30:31] op_sel_hi:[1,0,1]
v_cvt_pk_bf16_f32 v140,v24,v25
v_cvt_pk_bf16_f32 v141,v26,v27
v_cvt_pk_bf16_f32 v142,v28,v29
v_cvt_pk_bf16_f32 v143,v30,v31
ds_write_b128 v231,v[140:143] offset:3072
ds_write_b128 v231,v[132:135] offset:1024
buffer_load_dwordx4 v252,s[32:35],s95 offen offset:2048 lds
v_accvgpr_read_b32 v248,a64
v_accvgpr_read_b32 v249,a65
v_accvgpr_read_b32 v250,a66
v_accvgpr_read_b32 v251,a67
v_pk_mul_f32 v[64:65],v[64:65],v[238:239] op_sel_hi:[1,0]
v_pk_mul_f32 v[66:67],v[66:67],v[238:239] op_sel_hi:[1,0]
v_pk_fma_f32 v[64:65],v[248:249],v[234:235],v[64:65] op_sel_hi:[1,0,1]
v_pk_fma_f32 v[66:67],v[250:251],v[234:235],v[66:67] op_sel_hi:[1,0,1]
v_accvgpr_read_b32 v248,a68
v_accvgpr_read_b32 v249,a69
v_accvgpr_read_b32 v250,a70
v_accvgpr_read_b32 v251,a71
v_pk_mul_f32 v[68:69],v[68:69],v[238:239] op_sel_hi:[1,0]
v_pk_mul_f32 v[70:71],v[70:71],v[238:239] op_sel_hi:[1,0]
v_pk_fma_f32 v[68:69],v[248:249],v[234:235],v[68:69] op_sel_hi:[1,0,1]
v_pk_fma_f32 v[70:71],v[250:251],v[234:235],v[70:71] op_sel_hi:[1,0,1]
v_cvt_pk_bf16_f32 v160,v64,v65
v_cvt_pk_bf16_f32 v161,v66,v67
v_cvt_pk_bf16_f32 v162,v68,v69
v_cvt_pk_bf16_f32 v163,v70,v71
ds_write_b128 v231,v[160:163] offset:4096
buffer_load_dwordx4 v252,s[36:39],s95 offen offset:3072 lds
v_accvgpr_read_b32 v248,a72
v_accvgpr_read_b32 v249,a73
v_accvgpr_read_b32 v250,a74
v_accvgpr_read_b32 v251,a75
v_pk_mul_f32 v[72:73],v[72:73],v[238:239] op_sel_hi:[1,0]
v_pk_mul_f32 v[74:75],v[74:75],v[238:239] op_sel_hi:[1,0]
v_pk_fma_f32 v[72:73],v[248:249],v[234:235],v[72:73] op_sel_hi:[1,0,1]
v_pk_fma_f32 v[74:75],v[250:251],v[234:235],v[74:75] op_sel_hi:[1,0,1]
v_accvgpr_read_b32 v248,a76
v_accvgpr_read_b32 v249,a77
v_accvgpr_read_b32 v250,a78
v_accvgpr_read_b32 v251,a79
v_pk_mul_f32 v[76:77],v[76:77],v[238:239] op_sel_hi:[1,0]
v_pk_mul_f32 v[78:79],v[78:79],v[238:239] op_sel_hi:[1,0]
v_pk_fma_f32 v[76:77],v[248:249],v[234:235],v[76:77] op_sel_hi:[1,0,1]
v_pk_fma_f32 v[78:79],v[250:251],v[234:235],v[78:79] op_sel_hi:[1,0,1]
v_cvt_pk_bf16_f32 v164,v72,v73
v_cvt_pk_bf16_f32 v165,v74,v75
v_cvt_pk_bf16_f32 v166,v76,v77
v_cvt_pk_bf16_f32 v167,v78,v79
ds_write_b128 v231,v[164:167] offset:5120
s_add_u32 m0,s92,0xc000
s_mov_b32 s95,0x38080
buffer_load_dwordx4 v252,s[24:27],s95 offen lds
v_accvgpr_read_b32 v248,a80
v_accvgpr_read_b32 v249,a81
v_accvgpr_read_b32 v250,a82
v_accvgpr_read_b32 v251,a83
v_pk_mul_f32 v[80:81],v[80:81],v[238:239] op_sel_hi:[1,0]
v_pk_mul_f32 v[82:83],v[82:83],v[238:239] op_sel_hi:[1,0]
v_pk_fma_f32 v[80:81],v[248:249],v[234:235],v[80:81] op_sel_hi:[1,0,1]
v_pk_fma_f32 v[82:83],v[250:251],v[234:235],v[82:83] op_sel_hi:[1,0,1]
v_accvgpr_read_b32 v248,a84
v_accvgpr_read_b32 v249,a85
v_accvgpr_read_b32 v250,a86
v_accvgpr_read_b32 v251,a87
v_pk_mul_f32 v[84:85],v[84:85],v[238:239] op_sel_hi:[1,0]
v_pk_mul_f32 v[86:87],v[86:87],v[238:239] op_sel_hi:[1,0]
v_pk_fma_f32 v[84:85],v[248:249],v[234:235],v[84:85] op_sel_hi:[1,0,1]
v_pk_fma_f32 v[86:87],v[250:251],v[234:235],v[86:87] op_sel_hi:[1,0,1]
v_cvt_pk_bf16_f32 v168,v80,v81
v_cvt_pk_bf16_f32 v169,v82,v83
v_cvt_pk_bf16_f32 v170,v84,v85
v_cvt_pk_bf16_f32 v171,v86,v87
ds_write_b128 v231,v[168:171] offset:6144
buffer_load_dwordx4 v252,s[28:31],s95 offen offset:1024 lds
v_accvgpr_read_b32 v248,a88
v_accvgpr_read_b32 v249,a89
v_accvgpr_read_b32 v250,a90
v_accvgpr_read_b32 v251,a91
v_pk_mul_f32 v[88:89],v[88:89],v[238:239] op_sel_hi:[1,0]
v_pk_mul_f32 v[90:91],v[90:91],v[238:239] op_sel_hi:[1,0]
v_pk_fma_f32 v[88:89],v[248:249],v[234:235],v[88:89] op_sel_hi:[1,0,1]
v_pk_fma_f32 v[90:91],v[250:251],v[234:235],v[90:91] op_sel_hi:[1,0,1]
v_accvgpr_read_b32 v248,a92
v_accvgpr_read_b32 v249,a93
v_accvgpr_read_b32 v250,a94
v_accvgpr_read_b32 v251,a95
v_pk_mul_f32 v[92:93],v[92:93],v[238:239] op_sel_hi:[1,0]
v_pk_mul_f32 v[94:95],v[94:95],v[238:239] op_sel_hi:[1,0]
v_pk_fma_f32 v[92:93],v[248:249],v[234:235],v[92:93] op_sel_hi:[1,0,1]
v_pk_fma_f32 v[94:95],v[250:251],v[234:235],v[94:95] op_sel_hi:[1,0,1]
v_cvt_pk_bf16_f32 v172,v88,v89
v_cvt_pk_bf16_f32 v173,v90,v91
v_cvt_pk_bf16_f32 v174,v92,v93
v_cvt_pk_bf16_f32 v175,v94,v95
ds_write_b128 v231,v[172:175] offset:7168
s_waitcnt vmcnt(6) lgkmcnt(0)
s_barrier
v_mov_b32 v229,1
buffer_store_dword v229,off,s[88:91],s79 sc1
)"
R"(ds_read_b128 a[128:131],v228
ds_read_b128 a[132:135],v228 offset:1024
ds_read_b128 a[136:139],v228 offset:2048
ds_read_b128 a[140:143],v228 offset:3072
ds_read_b128 a[160:163],v228 offset:4096
ds_read_b128 a[164:167],v228 offset:5120
ds_read_b128 a[168:171],v228 offset:6144
ds_read_b128 a[172:175],v228 offset:7168
ds_read_b128 a[192:195],v225
ds_read_b128 a[196:199],v225 offset:1024
ds_read_b128 a[200:203],v225 offset:2048
ds_read_b128 a[204:207],v225 offset:3072
ds_read_b128 a[224:227],v225 offset:4096
ds_read_b128 a[228:231],v225 offset:5120
ds_read_b128 a[232:235],v225 offset:6144
ds_read_b128 a[236:239],v225 offset:7168
ds_read_b128 a[0:3],v224
ds_read_b128 a[4:7],v224 offset:1024
ds_read_b128 a[8:11],v224 offset:2048
ds_read_b128 a[12:15],v224 offset:3072
ds_read_b128 a[64:67],v224 offset:4096
ds_read_b128 a[68:71],v224 offset:5120
ds_read_b128 a[72:75],v224 offset:6144
ds_read_b128 a[76:79],v224 offset:7168
ds_read_b64_tr_b16 v[192:193],v240
ds_read_b64_tr_b16 v[194:195],v240 offset:2048
ds_read_b64_tr_b16 v[196:197],v240 offset:4096
ds_read_b64_tr_b16 v[198:199],v240 offset:6144
ds_read_b64_tr_b16 v[200:201],v240 offset:8192
ds_read_b64_tr_b16 v[202:203],v240 offset:10240
ds_read_b64_tr_b16 v[204:205],v240 offset:12288
ds_read_b64_tr_b16 v[206:207],v240 offset:14336
buffer_load_dwordx4 v252,s[32:35],s95 offen offset:2048 lds
v_accvgpr_read_b32 v248,a32
v_accvgpr_read_b32 v249,a33
v_accvgpr_read_b32 v250,a34
v_accvgpr_read_b32 v251,a35
v_pk_mul_f32 v[32:33],v[32:33],v[236:237] op_sel_hi:[1,0]
v_pk_mul_f32 v[34:35],v[34:35],v[236:237] op_sel_hi:[1,0]
v_pk_fma_f32 v[32:33],v[248:249],v[232:233],v[32:33] op_sel_hi:[1,0,1]
v_pk_fma_f32 v[34:35],v[250:251],v[232:233],v[34:35] op_sel_hi:[1,0,1]
v_accvgpr_read_b32 v248,a36
v_accvgpr_read_b32 v249,a37
buffer_load_dwordx4 v252,s[36:39],s95 offen offset:3072 lds
v_accvgpr_read_b32 v250,a38
v_accvgpr_read_b32 v251,a39
v_pk_mul_f32 v[36:37],v[36:37],v[236:237] op_sel_hi:[1,0]
v_pk_mul_f32 v[38:39],v[38:39],v[236:237] op_sel_hi:[1,0]
v_pk_fma_f32 v[36:37],v[248:249],v[232:233],v[36:37] op_sel_hi:[1,0,1]
v_pk_fma_f32 v[38:39],v[250:251],v[232:233],v[38:39] op_sel_hi:[1,0,1]
v_cvt_pk_bf16_f32 v144,v32,v33
v_cvt_pk_bf16_f32 v145,v34,v35
v_cvt_pk_bf16_f32 v146,v36,v37
v_cvt_pk_bf16_f32 v147,v38,v39
ds_write_b128 v231,v[144:147] offset:32768
s_add_u32 m0,s92,0x10000
s_mov_b32 s95,0x1c000
buffer_load_dwordx4 v252,s[24:27],s95 offen lds
v_accvgpr_read_b32 v248,a40
v_accvgpr_read_b32 v249,a41
v_accvgpr_read_b32 v250,a42
v_accvgpr_read_b32 v251,a43
v_pk_mul_f32 v[40:41],v[40:41],v[236:237] op_sel_hi:[1,0]
v_pk_mul_f32 v[42:43],v[42:43],v[236:237] op_sel_hi:[1,0]
v_pk_fma_f32 v[40:41],v[248:249],v[232:233],v[40:41] op_sel_hi:[1,0,1]
v_pk_fma_f32 v[42:43],v[250:251],v[232:233],v[42:43] op_sel_hi:[1,0,1]
v_accvgpr_read_b32 v248,a44
v_accvgpr_read_b32 v249,a45
buffer_load_dwordx4 v252,s[28:31],s95 offen offset:1024 lds
v_accvgpr_read_b32 v250,a46
v_accvgpr_read_b32 v251,a47
v_pk_mul_f32 v[44:45],v[44:45],v[236:237] op_sel_hi:[1,0]
v_pk_mul_f32 v[46:47],v[46:47],v[236:237] op_sel_hi:[1,0]
v_pk_fma_f32 v[44:45],v[248:249],v[232:233],v[44:45] op_sel_hi:[1,0,1]
v_pk_fma_f32 v[46:47],v[250:251],v[232:233],v[46:47] op_sel_hi:[1,0,1]
v_cvt_pk_bf16_f32 v148,v40,v41
v_cvt_pk_bf16_f32 v149,v42,v43
v_cvt_pk_bf16_f32 v150,v44,v45
v_cvt_pk_bf16_f32 v151,v46,v47
ds_write_b128 v231,v[148:151] offset:33792
buffer_load_dwordx4 v252,s[32:35],s95 offen offset:2048 lds
v_accvgpr_read_b32 v248,a48
v_accvgpr_read_b32 v249,a49
v_accvgpr_read_b32 v250,a50
v_accvgpr_read_b32 v251,a51
v_pk_mul_f32 v[48:49],v[48:49],v[236:237] op_sel_hi:[1,0]
v_pk_mul_f32 v[50:51],v[50:51],v[236:237] op_sel_hi:[1,0]
v_pk_fma_f32 v[48:49],v[248:249],v[232:233],v[48:49] op_sel_hi:[1,0,1]
v_pk_fma_f32 v[50:51],v[250:251],v[232:233],v[50:51] op_sel_hi:[1,0,1]
v_accvgpr_read_b32 v248,a52
v_accvgpr_read_b32 v249,a53
v_accvgpr_read_b32 v250,a54
v_accvgpr_read_b32 v251,a55
v_pk_mul_f32 v[52:53],v[52:53],v[236:237] op_sel_hi:[1,0]
v_pk_mul_f32 v[54:55],v[54:55],v[236:237] op_sel_hi:[1,0]
v_pk_fma_f32 v[52:53],v[248:249],v[232:233],v[52:53] op_sel_hi:[1,0,1]
v_pk_fma_f32 v[54:55],v[250:251],v[232:233],v[54:55] op_sel_hi:[1,0,1]
v_cvt_pk_bf16_f32 v152,v48,v49
v_cvt_pk_bf16_f32 v153,v50,v51
v_cvt_pk_bf16_f32 v154,v52,v53
v_cvt_pk_bf16_f32 v155,v54,v55
ds_write_b128 v231,v[152:155] offset:34816
buffer_load_dwordx4 v252,s[36:39],s95 offen offset:3072 lds
v_accvgpr_read_b32 v248,a56
v_accvgpr_read_b32 v249,a57
v_accvgpr_read_b32 v250,a58
v_accvgpr_read_b32 v251,a59
v_pk_mul_f32 v[56:57],v[56:57],v[236:237] op_sel_hi:[1,0]
v_pk_mul_f32 v[58:59],v[58:59],v[236:237] op_sel_hi:[1,0]
v_pk_fma_f32 v[56:57],v[248:249],v[232:233],v[56:57] op_sel_hi:[1,0,1]
v_pk_fma_f32 v[58:59],v[250:251],v[232:233],v[58:59] op_sel_hi:[1,0,1]
v_accvgpr_read_b32 v248,a60
v_accvgpr_read_b32 v249,a61
v_accvgpr_read_b32 v250,a62
v_accvgpr_read_b32 v251,a63
v_pk_mul_f32 v[60:61],v[60:61],v[236:237] op_sel_hi:[1,0]
v_pk_mul_f32 v[62:63],v[62:63],v[236:237] op_sel_hi:[1,0]
v_pk_fma_f32 v[60:61],v[248:249],v[232:233],v[60:61] op_sel_hi:[1,0,1]
v_pk_fma_f32 v[62:63],v[250:251],v[232:233],v[62:63] op_sel_hi:[1,0,1]
v_cvt_pk_bf16_f32 v156,v56,v57
v_cvt_pk_bf16_f32 v157,v58,v59
v_cvt_pk_bf16_f32 v158,v60,v61
v_cvt_pk_bf16_f32 v159,v62,v63
ds_write_b128 v231,v[156:159] offset:35840
s_add_u32 m0,s92,0x14000
s_mov_b32 s95,0x54000
buffer_load_dwordx4 v252,s[24:27],s95 offen lds
v_accvgpr_read_b32 v248,a96
v_accvgpr_read_b32 v249,a97
v_accvgpr_read_b32 v250,a98
v_accvgpr_read_b32 v251,a99
v_pk_mul_f32 v[96:97],v[96:97],v[238:239] op_sel_hi:[1,0]
v_pk_mul_f32 v[98:99],v[98:99],v[238:239] op_sel_hi:[1,0]
v_pk_fma_f32 v[96:97],v[248:249],v[234:235],v[96:97] op_sel_hi:[1,0,1]
v_pk_fma_f32 v[98:99],v[250:251],v[234:235],v[98:99] op_sel_hi:[1,0,1]
v_accvgpr_read_b32 v248,a100
v_accvgpr_read_b32 v249,a101
v_accvgpr_read_b32 v250,a102
v_accvgpr_read_b32 v251,a103
v_pk_mul_f32 v[100:101],v[100:101],v[238:239] op_sel_hi:[1,0]
v_pk_mul_f32 v[102:103],v[102:103],v[238:239] op_sel_hi:[1,0]
v_pk_fma_f32 v[100:101],v[248:249],v[234:235],v[100:101] op_sel_hi:[1,0,1]
v_pk_fma_f32 v[102:103],v[250:251],v[234:235],v[102:103] op_sel_hi:[1,0,1]
v_cvt_pk_bf16_f32 v176,v96,v97
v_cvt_pk_bf16_f32 v177,v98,v99
v_cvt_pk_bf16_f32 v178,v100,v101
v_cvt_pk_bf16_f32 v179,v102,v103
ds_write_b128 v231,v[176:179] offset:36864
buffer_load_dwordx4 v252,s[28:31],s95 offen offset:1024 lds
v_accvgpr_read_b32 v248,a104
v_accvgpr_read_b32 v249,a105
v_accvgpr_read_b32 v250,a106
v_accvgpr_read_b32 v251,a107
v_pk_mul_f32 v[104:105],v[104:105],v[238:239] op_sel_hi:[1,0]
v_pk_mul_f32 v[106:107],v[106:107],v[238:239] op_sel_hi:[1,0]
v_pk_fma_f32 v[104:105],v[248:249],v[234:235],v[104:105] op_sel_hi:[1,0,1]
v_pk_fma_f32 v[106:107],v[250:251],v[234:235],v[106:107] op_sel_hi:[1,0,1]
v_accvgpr_read_b32 v248,a108
v_accvgpr_read_b32 v249,a109
v_accvgpr_read_b32 v250,a110
v_accvgpr_read_b32 v251,a111
v_pk_mul_f32 v[108:109],v[108:109],v[238:239] op_sel_hi:[1,0]
v_pk_mul_f32 v[110:111],v[110:111],v[238:239] op_sel_hi:[1,0]
v_pk_fma_f32 v[108:109],v[248:249],v[234:235],v[108:109] op_sel_hi:[1,0,1]
v_pk_fma_f32 v[110:111],v[250:251],v[234:235],v[110:111] op_sel_hi:[1,0,1]
v_cvt_pk_bf16_f32 v180,v104,v105
v_cvt_pk_bf16_f32 v181,v106,v107
v_cvt_pk_bf16_f32 v182,v108,v109
v_cvt_pk_bf16_f32 v183,v110,v111
ds_write_b128 v231,v[180:183] offset:37888
buffer_load_dwordx4 v252,s[32:35],s95 offen offset:2048 lds
v_accvgpr_read_b32 v248,a112
v_accvgpr_read_b32 v249,a113
v_accvgpr_read_b32 v250,a114
v_accvgpr_read_b32 v251,a115
v_pk_mul_f32 v[112:113],v[112:113],v[238:239] op_sel_hi:[1,0]
v_pk_mul_f32 v[114:115],v[114:115],v[238:239] op_sel_hi:[1,0]
v_pk_fma_f32 v[112:113],v[248:249],v[234:235],v[112:113] op_sel_hi:[1,0,1]
v_pk_fma_f32 v[114:115],v[250:251],v[234:235],v[114:115] op_sel_hi:[1,0,1]
v_accvgpr_read_b32 v248,a116
v_accvgpr_read_b32 v249,a117
v_accvgpr_read_b32 v250,a118
v_accvgpr_read_b32 v251,a119
v_pk_mul_f32 v[116:117],v[116:117],v[238:239] op_sel_hi:[1,0]
v_pk_mul_f32 v[118:119],v[118:119],v[238:239] op_sel_hi:[1,0]
v_pk_fma_f32 v[116:117],v[248:249],v[234:235],v[116:117] op_sel_hi:[1,0,1]
v_pk_fma_f32 v[118:119],v[250:251],v[234:235],v[118:119] op_sel_hi:[1,0,1]
v_cvt_pk_bf16_f32 v184,v112,v113
v_cvt_pk_bf16_f32 v185,v114,v115
v_cvt_pk_bf16_f32 v186,v116,v117
v_cvt_pk_bf16_f32 v187,v118,v119
ds_write_b128 v231,v[184:187] offset:38912
buffer_load_dwordx4 v252,s[36:39],s95 offen offset:3072 lds
v_accvgpr_read_b32 v248,a120
v_accvgpr_read_b32 v249,a121
v_accvgpr_read_b32 v250,a122
v_accvgpr_read_b32 v251,a123
v_pk_mul_f32 v[120:121],v[120:121],v[238:239] op_sel_hi:[1,0]
v_pk_mul_f32 v[122:123],v[122:123],v[238:239] op_sel_hi:[1,0]
v_pk_fma_f32 v[120:121],v[248:249],v[234:235],v[120:121] op_sel_hi:[1,0,1]
)"
R"(v_pk_fma_f32 v[122:123],v[250:251],v[234:235],v[122:123] op_sel_hi:[1,0,1]
v_accvgpr_read_b32 v248,a124
v_accvgpr_read_b32 v249,a125
v_accvgpr_read_b32 v250,a126
v_accvgpr_read_b32 v251,a127
v_pk_mul_f32 v[124:125],v[124:125],v[238:239] op_sel_hi:[1,0]
v_pk_mul_f32 v[126:127],v[126:127],v[238:239] op_sel_hi:[1,0]
v_pk_fma_f32 v[124:125],v[248:249],v[234:235],v[124:125] op_sel_hi:[1,0,1]
v_pk_fma_f32 v[126:127],v[250:251],v[234:235],v[126:127] op_sel_hi:[1,0,1]
v_cvt_pk_bf16_f32 v188,v120,v121
v_cvt_pk_bf16_f32 v189,v122,v123
v_cvt_pk_bf16_f32 v190,v124,v125
v_cvt_pk_bf16_f32 v191,v126,v127
ds_write_b128 v231,v[188:191] offset:39936
s_waitcnt lgkmcnt(8)
v_mfma_f32_16x16x32_bf16 v[0:3],v[192:195],v[128:131],0
s_add_u32 s99,s96,0x0
ds_read_b64_tr_b16 v[208:209],v241
v_mfma_f32_16x16x32_bf16 v[0:3],v[196:199],v[132:135],v[0:3]
s_mov_b64 s[56:57],s[88:89]
ds_read_b64_tr_b16 v[210:211],v241 offset:2048
v_mfma_f32_16x16x32_bf16 v[0:3],v[200:203],v[136:139],v[0:3]
s_add_u32 s98,s82,14336
ds_read_b64_tr_b16 v[212:213],v241 offset:4096
v_mfma_f32_16x16x32_bf16 v[0:3],v[204:207],v[140:143],v[0:3]
ds_read_b64_tr_b16 v[214:215],v241 offset:6144
v_mfma_f32_16x16x32_bf16 v[4:7],v[192:195],v[160:163],0
ds_read_b64_tr_b16 v[216:217],v241 offset:8192
v_mfma_f32_16x16x32_bf16 v[4:7],v[196:199],v[164:167],v[4:7]
ds_read_b64_tr_b16 v[218:219],v241 offset:10240
v_mfma_f32_16x16x32_bf16 v[4:7],v[200:203],v[168:171],v[4:7]
ds_read_b64_tr_b16 v[220:221],v241 offset:12288
v_mfma_f32_16x16x32_bf16 v[4:7],v[204:207],v[172:175],v[4:7]
ds_read_b64_tr_b16 v[222:223],v241 offset:14336
v_mfma_f32_16x16x32_bf16 v[8:11],v[192:195],a[128:131],0
buffer_load_dwordx4 v[232:235],v227,s[44:47],s82 offen
v_mfma_f32_16x16x32_bf16 v[8:11],v[196:199],a[132:135],v[8:11]
buffer_load_dwordx4 v[236:239],v227,s[44:47],s98 offen
v_mfma_f32_16x16x32_bf16 v[8:11],v[200:203],a[136:139],v[8:11]
v_mfma_f32_16x16x32_bf16 v[8:11],v[204:207],a[140:143],v[8:11]
v_mfma_f32_16x16x32_bf16 v[12:15],v[192:195],a[160:163],0
v_mfma_f32_16x16x32_bf16 v[12:15],v[196:199],a[164:167],v[12:15]
v_mfma_f32_16x16x32_bf16 v[12:15],v[200:203],a[168:171],v[12:15]
v_mfma_f32_16x16x32_bf16 v[12:15],v[204:207],a[172:175],v[12:15]
s_waitcnt vmcnt(10) lgkmcnt(0)
v_mfma_f32_16x16x32_bf16 v[0:3],v[208:211],a[192:195],v[0:3]
s_barrier
ds_read_b64_tr_b16 v[192:193],v240 offset:32768
s_add_u32 m0,s92,0x18000
v_mfma_f32_16x16x32_bf16 v[0:3],v[212:215],a[196:199],v[0:3]
s_mov_b32 s95,0x1c080
ds_read_b64_tr_b16 v[194:195],v240 offset:34816
ds_read_b128 a[144:147],v228 offset:32768
v_mfma_f32_16x16x32_bf16 v[0:3],v[216:219],a[200:203],v[0:3]
buffer_load_dwordx4 v252,s[24:27],s95 offen lds
v_mfma_f32_16x16x32_bf16 v[0:3],v[220:223],a[204:207],v[0:3]
ds_read_b64_tr_b16 v[196:197],v240 offset:36864
ds_read_b64_tr_b16 v[198:199],v240 offset:38912
v_mfma_f32_16x16x32_bf16 v[4:7],v[208:211],a[224:227],v[4:7]
ds_read_b64_tr_b16 v[200:201],v240 offset:40960
ds_read_b128 a[148:151],v228 offset:33792
v_mfma_f32_16x16x32_bf16 v[4:7],v[212:215],a[228:231],v[4:7]
ds_read_b64_tr_b16 v[202:203],v240 offset:43008
ds_read_b128 a[152:155],v228 offset:34816
v_mfma_f32_16x16x32_bf16 v[4:7],v[216:219],a[232:235],v[4:7]
buffer_load_dwordx4 v252,s[28:31],s95 offen offset:1024 lds
v_mfma_f32_16x16x32_bf16 v[4:7],v[220:223],a[236:239],v[4:7]
ds_read_b64_tr_b16 v[204:205],v240 offset:45056
ds_read_b64_tr_b16 v[206:207],v240 offset:47104
v_mfma_f32_16x16x32_bf16 v[8:11],v[208:211],a[0:3],v[8:11]
ds_read_b128 a[156:159],v228 offset:35840
ds_read_b128 a[176:179],v228 offset:36864
v_mfma_f32_16x16x32_bf16 v[8:11],v[212:215],a[4:7],v[8:11]
ds_read_b128 a[180:183],v228 offset:37888
ds_read_b128 a[184:187],v228 offset:38912
v_mfma_f32_16x16x32_bf16 v[8:11],v[216:219],a[8:11],v[8:11]
buffer_load_dwordx4 v252,s[32:35],s95 offen offset:2048 lds
v_mfma_f32_16x16x32_bf16 v[8:11],v[220:223],a[12:15],v[8:11]
ds_read_b128 a[188:191],v228 offset:39936
ds_read_b128 a[208:211],v225 offset:32768
v_mfma_f32_16x16x32_bf16 v[12:15],v[208:211],a[64:67],v[12:15]
ds_read_b128 a[212:215],v225 offset:33792
ds_read_b128 a[216:219],v225 offset:34816
v_mfma_f32_16x16x32_bf16 v[12:15],v[212:215],a[68:71],v[12:15]
ds_read_b128 a[220:223],v225 offset:35840
ds_read_b128 a[240:243],v225 offset:36864
v_mfma_f32_16x16x32_bf16 v[12:15],v[216:219],a[72:75],v[12:15]
buffer_load_dwordx4 v252,s[36:39],s95 offen offset:3072 lds
v_mfma_f32_16x16x32_bf16 v[12:15],v[220:223],a[76:79],v[12:15]
s_waitcnt lgkmcnt(10)
ds_read_b128 a[244:247],v225 offset:37888
ds_read_b128 a[248:251],v225 offset:38912
v_mfma_f32_16x16x32_bf16 v[16:19],v[192:195],v[128:131],0
s_add_u32 m0,s92,0x1c000
ds_read_b64_tr_b16 v[208:209],v241 offset:32768
s_mov_b32 s95,0x54080
v_mfma_f32_16x16x32_bf16 v[16:19],v[196:199],v[132:135],v[16:19]
ds_read_b64_tr_b16 v[210:211],v241 offset:34816
ds_read_b128 a[252:255],v225 offset:39936
v_mfma_f32_16x16x32_bf16 v[16:19],v[200:203],v[136:139],v[16:19]
buffer_load_dwordx4 v252,s[24:27],s95 offen lds
v_mfma_f32_16x16x32_bf16 v[16:19],v[204:207],v[140:143],v[16:19]
ds_read_b64_tr_b16 v[212:213],v241 offset:36864
ds_read_b64_tr_b16 v[214:215],v241 offset:38912
v_mfma_f32_16x16x32_bf16 v[20:23],v[192:195],v[160:163],0
ds_read_b64_tr_b16 v[216:217],v241 offset:40960
ds_read_b128 a[16:19],v224 offset:32768
v_mfma_f32_16x16x32_bf16 v[20:23],v[196:199],v[164:167],v[20:23]
ds_read_b64_tr_b16 v[218:219],v241 offset:43008
ds_read_b128 a[20:23],v224 offset:33792
v_mfma_f32_16x16x32_bf16 v[20:23],v[200:203],v[168:171],v[20:23]
buffer_load_dwordx4 v252,s[28:31],s95 offen offset:1024 lds
v_mfma_f32_16x16x32_bf16 v[20:23],v[204:207],v[172:175],v[20:23]
ds_read_b64_tr_b16 v[220:221],v241 offset:45056
ds_read_b64_tr_b16 v[222:223],v241 offset:47104
v_mfma_f32_16x16x32_bf16 v[24:27],v[192:195],a[128:131],0
ds_read_b128 a[24:27],v224 offset:34816
ds_read_b128 a[28:31],v224 offset:35840
v_mfma_f32_16x16x32_bf16 v[24:27],v[196:199],a[132:135],v[24:27]
ds_read_b128 a[80:83],v224 offset:36864
ds_read_b128 a[84:87],v224 offset:37888
v_mfma_f32_16x16x32_bf16 v[24:27],v[200:203],a[136:139],v[24:27]
buffer_load_dwordx4 v252,s[32:35],s95 offen offset:2048 lds
v_mfma_f32_16x16x32_bf16 v[24:27],v[204:207],a[140:143],v[24:27]
ds_read_b128 a[88:91],v224 offset:38912
ds_read_b128 a[92:95],v224 offset:39936
v_mfma_f32_16x16x32_bf16 v[28:31],v[192:195],a[160:163],0
v_mfma_f32_16x16x32_bf16 v[28:31],v[196:199],a[164:167],v[28:31]
v_mfma_f32_16x16x32_bf16 v[28:31],v[200:203],a[168:171],v[28:31]
buffer_load_dwordx4 v252,s[36:39],s95 offen offset:3072 lds
v_mfma_f32_16x16x32_bf16 v[28:31],v[204:207],a[172:175],v[28:31]
s_waitcnt vmcnt(10) lgkmcnt(0)
v_mfma_f32_16x16x32_bf16 v[16:19],v[208:211],a[192:195],v[16:19]
s_barrier
ds_read_b64_tr_b16 v[192:193],v242
s_add_u32 m0,s92,0x0
v_mfma_f32_16x16x32_bf16 v[16:19],v[212:215],a[196:199],v[16:19]
s_mov_b32 s95,0x100
ds_read_b64_tr_b16 v[194:195],v242 offset:2048
v_mfma_f32_16x16x32_bf16 v[16:19],v[216:219],a[200:203],v[16:19]
buffer_load_dwordx4 v252,s[24:27],s95 offen lds
v_mfma_f32_16x16x32_bf16 v[16:19],v[220:223],a[204:207],v[16:19]
ds_read_b64_tr_b16 v[196:197],v242 offset:4096
ds_read_b64_tr_b16 v[198:199],v242 offset:6144
v_mfma_f32_16x16x32_bf16 v[20:23],v[208:211],a[224:227],v[20:23]
ds_read_b64_tr_b16 v[200:201],v242 offset:8192
v_mfma_f32_16x16x32_bf16 v[20:23],v[212:215],a[228:231],v[20:23]
ds_read_b64_tr_b16 v[202:203],v242 offset:10240
v_mfma_f32_16x16x32_bf16 v[20:23],v[216:219],a[232:235],v[20:23]
buffer_load_dwordx4 v252,s[28:31],s95 offen offset:1024 lds
v_mfma_f32_16x16x32_bf16 v[20:23],v[220:223],a[236:239],v[20:23]
ds_read_b64_tr_b16 v[204:205],v242 offset:12288
ds_read_b64_tr_b16 v[206:207],v242 offset:14336
v_mfma_f32_16x16x32_bf16 v[24:27],v[208:211],a[0:3],v[24:27]
v_mfma_f32_16x16x32_bf16 v[24:27],v[212:215],a[4:7],v[24:27]
v_mfma_f32_16x16x32_bf16 v[24:27],v[216:219],a[8:11],v[24:27]
buffer_load_dwordx4 v252,s[32:35],s95 offen offset:2048 lds
v_mfma_f32_16x16x32_bf16 v[24:27],v[220:223],a[12:15],v[24:27]
v_mfma_f32_16x16x32_bf16 v[28:31],v[208:211],a[64:67],v[28:31]
v_mfma_f32_16x16x32_bf16 v[28:31],v[212:215],a[68:71],v[28:31]
v_mfma_f32_16x16x32_bf16 v[28:31],v[216:219],a[72:75],v[28:31]
buffer_load_dwordx4 v252,s[36:39],s95 offen offset:3072 lds
v_mfma_f32_16x16x32_bf16 v[28:31],v[220:223],a[76:79],v[28:31]
s_waitcnt lgkmcnt(0)
v_mfma_f32_16x16x32_bf16 v[0:3],v[192:195],v[144:147],v[0:3]
s_add_u32 m0,s92,0x4000
ds_read_b64_tr_b16 v[208:209],v243
s_mov_b32 s95,0x38100
v_mfma_f32_16x16x32_bf16 v[0:3],v[196:199],v[148:151],v[0:3]
ds_read_b64_tr_b16 v[210:211],v243 offset:2048
v_mfma_f32_16x16x32_bf16 v[0:3],v[200:203],v[152:155],v[0:3]
buffer_load_dwordx4 v252,s[24:27],s95 offen lds
v_mfma_f32_16x16x32_bf16 v[0:3],v[204:207],v[156:159],v[0:3]
ds_read_b64_tr_b16 v[212:213],v243 offset:4096
ds_read_b64_tr_b16 v[214:215],v243 offset:6144
v_mfma_f32_16x16x32_bf16 v[4:7],v[192:195],v[176:179],v[4:7]
ds_read_b64_tr_b16 v[216:217],v243 offset:8192
v_mfma_f32_16x16x32_bf16 v[4:7],v[196:199],v[180:183],v[4:7]
ds_read_b64_tr_b16 v[218:219],v243 offset:10240
v_mfma_f32_16x16x32_bf16 v[4:7],v[200:203],v[184:187],v[4:7]
buffer_load_dwordx4 v252,s[28:31],s95 offen offset:1024 lds
v_mfma_f32_16x16x32_bf16 v[4:7],v[204:207],v[188:191],v[4:7]
ds_read_b64_tr_b16 v[220:221],v243 offset:12288
ds_read_b64_tr_b16 v[222:223],v243 offset:14336
v_mfma_f32_16x16x32_bf16 v[8:11],v[192:195],a[144:147],v[8:11]
v_mfma_f32_16x16x32_bf16 v[8:11],v[196:199],a[148:151],v[8:11]
v_mfma_f32_16x16x32_bf16 v[8:11],v[200:203],a[152:155],v[8:11]
buffer_load_dwordx4 v252,s[32:35],s95 offen offset:2048 lds
v_mfma_f32_16x16x32_bf16 v[8:11],v[204:207],a[156:159],v[8:11]
v_mfma_f32_16x16x32_bf16 v[12:15],v[192:195],a[176:179],v[12:15]
v_mfma_f32_16x16x32_bf16 v[12:15],v[196:199],a[180:183],v[12:15]
v_mfma_f32_16x16x32_bf16 v[12:15],v[200:203],a[184:187],v[12:15]
buffer_load_dwordx4 v252,s[36:39],s95 offen offset:3072 lds
v_mfma_f32_16x16x32_bf16 v[12:15],v[204:207],a[188:191],v[12:15]
s_waitcnt vmcnt(8) lgkmcnt(0)
v_mfma_f32_16x16x32_bf16 v[0:3],v[208:211],a[208:211],v[0:3]
s_barrier
ds_read_b64_tr_b16 v[192:193],v242 offset:32768
s_add_u32 m0,s92,0x8000
v_mfma_f32_16x16x32_bf16 v[0:3],v[212:215],a[212:215],v[0:3]
)"
R"(s_mov_b32 s95,0x1c100
ds_read_b64_tr_b16 v[194:195],v242 offset:34816
v_mfma_f32_16x16x32_bf16 v[0:3],v[216:219],a[216:219],v[0:3]
buffer_load_dwordx4 v252,s[24:27],s95 offen lds
v_mfma_f32_16x16x32_bf16 v[0:3],v[220:223],a[220:223],v[0:3]
ds_read_b64_tr_b16 v[196:197],v242 offset:36864
ds_read_b64_tr_b16 v[198:199],v242 offset:38912
v_mfma_f32_16x16x32_bf16 v[4:7],v[208:211],a[240:243],v[4:7]
ds_read_b64_tr_b16 v[200:201],v242 offset:40960
v_mfma_f32_16x16x32_bf16 v[4:7],v[212:215],a[244:247],v[4:7]
ds_read_b64_tr_b16 v[202:203],v242 offset:43008
v_mfma_f32_16x16x32_bf16 v[4:7],v[216:219],a[248:251],v[4:7]
buffer_load_dwordx4 v252,s[28:31],s95 offen offset:1024 lds
v_mfma_f32_16x16x32_bf16 v[4:7],v[220:223],a[252:255],v[4:7]
ds_read_b64_tr_b16 v[204:205],v242 offset:45056
ds_read_b64_tr_b16 v[206:207],v242 offset:47104
v_mfma_f32_16x16x32_bf16 v[8:11],v[208:211],a[16:19],v[8:11]
v_cvt_pk_bf16_f32 v32,v0,v1
v_cvt_pk_bf16_f32 v33,v2,v3
v_mfma_f32_16x16x32_bf16 v[8:11],v[212:215],a[20:23],v[8:11]
v_mfma_f32_16x16x32_bf16 v[8:11],v[216:219],a[24:27],v[8:11]
buffer_load_dwordx4 v252,s[32:35],s95 offen offset:2048 lds
v_mfma_f32_16x16x32_bf16 v[8:11],v[220:223],a[28:31],v[8:11]
v_cvt_pk_bf16_f32 v34,v4,v5
v_cvt_pk_bf16_f32 v35,v6,v7
v_mfma_f32_16x16x32_bf16 v[12:15],v[208:211],a[80:83],v[12:15]
buffer_store_dwordx4 v[32:35],v255,s[88:91],s99 offen sc1
v_mfma_f32_16x16x32_bf16 v[12:15],v[212:215],a[84:87],v[12:15]
s_add_u32 s99,s93,0x0
v_mfma_f32_16x16x32_bf16 v[12:15],v[216:219],a[88:91],v[12:15]
buffer_load_dwordx4 v252,s[36:39],s95 offen offset:3072 lds
v_mfma_f32_16x16x32_bf16 v[12:15],v[220:223],a[92:95],v[12:15]
s_waitcnt lgkmcnt(0)
v_cvt_pk_bf16_f32 v36,v8,v9
v_cvt_pk_bf16_f32 v37,v10,v11
v_mfma_f32_16x16x32_bf16 v[16:19],v[192:195],v[144:147],v[16:19]
s_add_u32 m0,s92,0xc000
ds_read_b64_tr_b16 v[208:209],v243 offset:32768
s_mov_b32 s95,0x54100
v_mfma_f32_16x16x32_bf16 v[16:19],v[196:199],v[148:151],v[16:19]
ds_read_b64_tr_b16 v[210:211],v243 offset:34816
v_cvt_pk_bf16_f32 v38,v12,v13
v_mfma_f32_16x16x32_bf16 v[16:19],v[200:203],v[152:155],v[16:19]
buffer_load_dwordx4 v252,s[24:27],s95 offen lds
v_mfma_f32_16x16x32_bf16 v[16:19],v[204:207],v[156:159],v[16:19]
ds_read_b64_tr_b16 v[212:213],v243 offset:36864
ds_read_b64_tr_b16 v[214:215],v243 offset:38912
v_mfma_f32_16x16x32_bf16 v[20:23],v[192:195],v[176:179],v[20:23]
ds_read_b64_tr_b16 v[216:217],v243 offset:40960
v_cvt_pk_bf16_f32 v39,v14,v15
v_mfma_f32_16x16x32_bf16 v[20:23],v[196:199],v[180:183],v[20:23]
ds_read_b64_tr_b16 v[218:219],v243 offset:43008
v_mfma_f32_16x16x32_bf16 v[20:23],v[200:203],v[184:187],v[20:23]
buffer_load_dwordx4 v252,s[28:31],s95 offen offset:1024 lds
v_mfma_f32_16x16x32_bf16 v[20:23],v[204:207],v[188:191],v[20:23]
ds_read_b64_tr_b16 v[220:221],v243 offset:45056
ds_read_b64_tr_b16 v[222:223],v243 offset:47104
v_mfma_f32_16x16x32_bf16 v[24:27],v[192:195],a[144:147],v[24:27]
buffer_store_dwordx4 v[36:39],v255,s[88:91],s99 offen sc1
v_mfma_f32_16x16x32_bf16 v[24:27],v[196:199],a[148:151],v[24:27]
s_add_u32 s99,s96,0x1000
v_mfma_f32_16x16x32_bf16 v[24:27],v[200:203],a[152:155],v[24:27]
buffer_load_dwordx4 v252,s[32:35],s95 offen offset:2048 lds
v_mfma_f32_16x16x32_bf16 v[24:27],v[204:207],a[156:159],v[24:27]
v_mfma_f32_16x16x32_bf16 v[28:31],v[192:195],a[176:179],v[28:31]
v_mfma_f32_16x16x32_bf16 v[28:31],v[196:199],a[180:183],v[28:31]
v_mfma_f32_16x16x32_bf16 v[28:31],v[200:203],a[184:187],v[28:31]
buffer_load_dwordx4 v252,s[36:39],s95 offen offset:3072 lds
v_mfma_f32_16x16x32_bf16 v[28:31],v[204:207],a[188:191],v[28:31]
s_waitcnt vmcnt(10) lgkmcnt(0)
v_mfma_f32_16x16x32_bf16 v[16:19],v[208:211],a[208:211],v[16:19]
s_barrier
ds_read_b64_tr_b16 v[192:193],v240
s_add_u32 m0,s92,0x20000
v_mfma_f32_16x16x32_bf16 v[16:19],v[212:215],a[212:215],v[16:19]
s_mov_b32 s95,0x180
ds_read_b64_tr_b16 v[194:195],v240 offset:2048
v_mfma_f32_16x16x32_bf16 v[16:19],v[216:219],a[216:219],v[16:19]
buffer_load_dwordx4 v252,s[24:27],s95 offen lds
v_mfma_f32_16x16x32_bf16 v[16:19],v[220:223],a[220:223],v[16:19]
ds_read_b64_tr_b16 v[196:197],v240 offset:4096
ds_read_b64_tr_b16 v[198:199],v240 offset:6144
v_mfma_f32_16x16x32_bf16 v[20:23],v[208:211],a[240:243],v[20:23]
ds_read_b64_tr_b16 v[200:201],v240 offset:8192
v_mfma_f32_16x16x32_bf16 v[20:23],v[212:215],a[244:247],v[20:23]
ds_read_b64_tr_b16 v[202:203],v240 offset:10240
v_mfma_f32_16x16x32_bf16 v[20:23],v[216:219],a[248:251],v[20:23]
buffer_load_dwordx4 v252,s[28:31],s95 offen offset:1024 lds
v_mfma_f32_16x16x32_bf16 v[20:23],v[220:223],a[252:255],v[20:23]
ds_read_b64_tr_b16 v[204:205],v240 offset:12288
ds_read_b64_tr_b16 v[206:207],v240 offset:14336
v_mfma_f32_16x16x32_bf16 v[24:27],v[208:211],a[16:19],v[24:27]
v_cvt_pk_bf16_f32 v40,v16,v17
v_cvt_pk_bf16_f32 v41,v18,v19
v_mfma_f32_16x16x32_bf16 v[24:27],v[212:215],a[20:23],v[24:27]
v_mfma_f32_16x16x32_bf16 v[24:27],v[216:219],a[24:27],v[24:27]
buffer_load_dwordx4 v252,s[32:35],s95 offen offset:2048 lds
v_mfma_f32_16x16x32_bf16 v[24:27],v[220:223],a[28:31],v[24:27]
v_cvt_pk_bf16_f32 v42,v20,v21
v_cvt_pk_bf16_f32 v43,v22,v23
v_mfma_f32_16x16x32_bf16 v[28:31],v[208:211],a[80:83],v[28:31]
buffer_store_dwordx4 v[40:43],v255,s[88:91],s99 offen sc1
v_mfma_f32_16x16x32_bf16 v[28:31],v[212:215],a[84:87],v[28:31]
s_add_u32 s99,s93,0x1000
v_mfma_f32_16x16x32_bf16 v[28:31],v[216:219],a[88:91],v[28:31]
buffer_load_dwordx4 v252,s[36:39],s95 offen offset:3072 lds
v_mfma_f32_16x16x32_bf16 v[28:31],v[220:223],a[92:95],v[28:31]
s_waitcnt lgkmcnt(0)
v_cvt_pk_bf16_f32 v44,v24,v25
v_cvt_pk_bf16_f32 v45,v26,v27
v_mfma_f32_16x16x32_bf16 v[0:3],v[192:195],v[128:131],0
s_add_u32 m0,s92,0x24000
ds_read_b64_tr_b16 v[208:209],v241
s_mov_b32 s95,0x38180
v_mfma_f32_16x16x32_bf16 v[0:3],v[196:199],v[132:135],v[0:3]
ds_read_b64_tr_b16 v[210:211],v241 offset:2048
v_cvt_pk_bf16_f32 v46,v28,v29
v_mfma_f32_16x16x32_bf16 v[0:3],v[200:203],v[136:139],v[0:3]
buffer_load_dwordx4 v252,s[24:27],s95 offen lds
v_mfma_f32_16x16x32_bf16 v[0:3],v[204:207],v[140:143],v[0:3]
ds_read_b64_tr_b16 v[212:213],v241 offset:4096
ds_read_b64_tr_b16 v[214:215],v241 offset:6144
v_mfma_f32_16x16x32_bf16 v[4:7],v[192:195],v[160:163],0
ds_read_b64_tr_b16 v[216:217],v241 offset:8192
v_cvt_pk_bf16_f32 v47,v30,v31
v_mfma_f32_16x16x32_bf16 v[4:7],v[196:199],v[164:167],v[4:7]
ds_read_b64_tr_b16 v[218:219],v241 offset:10240
v_mfma_f32_16x16x32_bf16 v[4:7],v[200:203],v[168:171],v[4:7]
buffer_load_dwordx4 v252,s[28:31],s95 offen offset:1024 lds
v_mfma_f32_16x16x32_bf16 v[4:7],v[204:207],v[172:175],v[4:7]
ds_read_b64_tr_b16 v[220:221],v241 offset:12288
ds_read_b64_tr_b16 v[222:223],v241 offset:14336
v_mfma_f32_16x16x32_bf16 v[8:11],v[192:195],a[128:131],0
buffer_store_dwordx4 v[44:47],v255,s[88:91],s99 offen sc1
v_mfma_f32_16x16x32_bf16 v[8:11],v[196:199],a[132:135],v[8:11]
s_add_u32 s99,s96,0x2000
v_mfma_f32_16x16x32_bf16 v[8:11],v[200:203],a[136:139],v[8:11]
buffer_load_dwordx4 v252,s[32:35],s95 offen offset:2048 lds
v_mfma_f32_16x16x32_bf16 v[8:11],v[204:207],a[140:143],v[8:11]
v_mfma_f32_16x16x32_bf16 v[12:15],v[192:195],a[160:163],0
v_mfma_f32_16x16x32_bf16 v[12:15],v[196:199],a[164:167],v[12:15]
v_mfma_f32_16x16x32_bf16 v[12:15],v[200:203],a[168:171],v[12:15]
buffer_load_dwordx4 v252,s[36:39],s95 offen offset:3072 lds
v_mfma_f32_16x16x32_bf16 v[12:15],v[204:207],a[172:175],v[12:15]
s_waitcnt vmcnt(10) lgkmcnt(0)
v_mfma_f32_16x16x32_bf16 v[0:3],v[208:211],a[192:195],v[0:3]
s_barrier
ds_read_b64_tr_b16 v[192:193],v240 offset:32768
s_add_u32 m0,s92,0x10000
v_mfma_f32_16x16x32_bf16 v[0:3],v[212:215],a[196:199],v[0:3]
s_mov_b32 s95,0x1c180
ds_read_b64_tr_b16 v[194:195],v240 offset:34816
v_mfma_f32_16x16x32_bf16 v[0:3],v[216:219],a[200:203],v[0:3]
buffer_load_dwordx4 v252,s[24:27],s95 offen lds
v_mfma_f32_16x16x32_bf16 v[0:3],v[220:223],a[204:207],v[0:3]
ds_read_b64_tr_b16 v[196:197],v240 offset:36864
ds_read_b64_tr_b16 v[198:199],v240 offset:38912
v_mfma_f32_16x16x32_bf16 v[4:7],v[208:211],a[224:227],v[4:7]
ds_read_b64_tr_b16 v[200:201],v240 offset:40960
v_mfma_f32_16x16x32_bf16 v[4:7],v[212:215],a[228:231],v[4:7]
ds_read_b64_tr_b16 v[202:203],v240 offset:43008
v_mfma_f32_16x16x32_bf16 v[4:7],v[216:219],a[232:235],v[4:7]
buffer_load_dwordx4 v252,s[28:31],s95 offen offset:1024 lds
v_mfma_f32_16x16x32_bf16 v[4:7],v[220:223],a[236:239],v[4:7]
ds_read_b64_tr_b16 v[204:205],v240 offset:45056
ds_read_b64_tr_b16 v[206:207],v240 offset:47104
v_mfma_f32_16x16x32_bf16 v[8:11],v[208:211],a[0:3],v[8:11]
v_mfma_f32_16x16x32_bf16 v[8:11],v[212:215],a[4:7],v[8:11]
v_mfma_f32_16x16x32_bf16 v[8:11],v[216:219],a[8:11],v[8:11]
buffer_load_dwordx4 v252,s[32:35],s95 offen offset:2048 lds
v_mfma_f32_16x16x32_bf16 v[8:11],v[220:223],a[12:15],v[8:11]
v_mfma_f32_16x16x32_bf16 v[12:15],v[208:211],a[64:67],v[12:15]
v_mfma_f32_16x16x32_bf16 v[12:15],v[212:215],a[68:71],v[12:15]
v_mfma_f32_16x16x32_bf16 v[12:15],v[216:219],a[72:75],v[12:15]
buffer_load_dwordx4 v252,s[36:39],s95 offen offset:3072 lds
v_mfma_f32_16x16x32_bf16 v[12:15],v[220:223],a[76:79],v[12:15]
s_waitcnt lgkmcnt(0)
v_mfma_f32_16x16x32_bf16 v[0:3],v[192:195],v[144:147],v[0:3]
s_add_u32 m0,s92,0x14000
ds_read_b64_tr_b16 v[208:209],v241 offset:32768
s_mov_b32 s95,0x54180
v_mfma_f32_16x16x32_bf16 v[0:3],v[196:199],v[148:151],v[0:3]
ds_read_b64_tr_b16 v[210:211],v241 offset:34816
v_mfma_f32_16x16x32_bf16 v[0:3],v[200:203],v[152:155],v[0:3]
buffer_load_dwordx4 v252,s[24:27],s95 offen lds
v_mfma_f32_16x16x32_bf16 v[0:3],v[204:207],v[156:159],v[0:3]
ds_read_b64_tr_b16 v[212:213],v241 offset:36864
ds_read_b64_tr_b16 v[214:215],v241 offset:38912
v_mfma_f32_16x16x32_bf16 v[4:7],v[192:195],v[176:179],v[4:7]
ds_read_b64_tr_b16 v[216:217],v241 offset:40960
v_mfma_f32_16x16x32_bf16 v[4:7],v[196:199],v[180:183],v[4:7]
ds_read_b64_tr_b16 v[218:219],v241 offset:43008
v_mfma_f32_16x16x32_bf16 v[4:7],v[200:203],v[184:187],v[4:7]
buffer_load_dwordx4 v252,s[28:31],s95 offen offset:1024 lds
v_mfma_f32_16x16x32_bf16 v[4:7],v[204:207],v[188:191],v[4:7]
ds_read_b64_tr_b16 v[220:221],v241 offset:45056
ds_read_b64_tr_b16 v[222:223],v241 offset:47104
v_mfma_f32_16x16x32_bf16 v[8:11],v[192:195],a[144:147],v[8:11]
v_mfma_f32_16x16x32_bf16 v[8:11],v[196:199],a[148:151],v[8:11]
v_mfma_f32_16x16x32_bf16 v[8:11],v[200:203],a[152:155],v[8:11]
)"
R"(buffer_load_dwordx4 v252,s[32:35],s95 offen offset:2048 lds
v_mfma_f32_16x16x32_bf16 v[8:11],v[204:207],a[156:159],v[8:11]
v_mfma_f32_16x16x32_bf16 v[12:15],v[192:195],a[176:179],v[12:15]
v_mfma_f32_16x16x32_bf16 v[12:15],v[196:199],a[180:183],v[12:15]
v_mfma_f32_16x16x32_bf16 v[12:15],v[200:203],a[184:187],v[12:15]
buffer_load_dwordx4 v252,s[36:39],s95 offen offset:3072 lds
v_mfma_f32_16x16x32_bf16 v[12:15],v[204:207],a[188:191],v[12:15]
s_waitcnt vmcnt(8) lgkmcnt(0)
v_mfma_f32_16x16x32_bf16 v[0:3],v[208:211],a[208:211],v[0:3]
s_barrier
ds_read_b64_tr_b16 v[192:193],v246
s_add_u32 m0,s92,0x18000
v_mfma_f32_16x16x32_bf16 v[0:3],v[212:215],a[212:215],v[0:3]
s_mov_b32 s95,0x200
ds_read_b64_tr_b16 v[194:195],v246 offset:2048
v_mov_b32 v229,2
v_mfma_f32_16x16x32_bf16 v[0:3],v[216:219],a[216:219],v[0:3]
buffer_load_dwordx4 v252,s[24:27],s95 offen lds
v_mfma_f32_16x16x32_bf16 v[0:3],v[220:223],a[220:223],v[0:3]
ds_read_b64_tr_b16 v[196:197],v246 offset:4096
ds_read_b64_tr_b16 v[198:199],v246 offset:6144
v_mfma_f32_16x16x32_bf16 v[4:7],v[208:211],a[240:243],v[4:7]
ds_read_b64_tr_b16 v[200:201],v246 offset:8192
v_mfma_f32_16x16x32_bf16 v[4:7],v[212:215],a[244:247],v[4:7]
ds_read_b64_tr_b16 v[202:203],v246 offset:10240
v_mfma_f32_16x16x32_bf16 v[4:7],v[216:219],a[248:251],v[4:7]
buffer_load_dwordx4 v252,s[28:31],s95 offen offset:1024 lds
v_mfma_f32_16x16x32_bf16 v[4:7],v[220:223],a[252:255],v[4:7]
ds_read_b64_tr_b16 v[204:205],v246 offset:12288
ds_read_b64_tr_b16 v[206:207],v246 offset:14336
v_mfma_f32_16x16x32_bf16 v[8:11],v[208:211],a[16:19],v[8:11]
buffer_store_dword v229,off,s[88:91],s79 sc1
v_mfma_f32_16x16x32_bf16 v[8:11],v[212:215],a[20:23],v[8:11]
v_cvt_pk_bf16_f32 v48,v0,v1
v_cvt_pk_bf16_f32 v49,v2,v3
v_mfma_f32_16x16x32_bf16 v[8:11],v[216:219],a[24:27],v[8:11]
buffer_load_dwordx4 v252,s[32:35],s95 offen offset:2048 lds
v_mfma_f32_16x16x32_bf16 v[8:11],v[220:223],a[28:31],v[8:11]
v_cvt_pk_bf16_f32 v50,v4,v5
v_cvt_pk_bf16_f32 v51,v6,v7
v_mfma_f32_16x16x32_bf16 v[12:15],v[208:211],a[80:83],v[12:15]
buffer_store_dwordx4 v[48:51],v255,s[88:91],s99 offen sc1
v_mfma_f32_16x16x32_bf16 v[12:15],v[212:215],a[84:87],v[12:15]
s_add_u32 s99,s93,0x2000
v_mfma_f32_16x16x32_bf16 v[12:15],v[216:219],a[88:91],v[12:15]
buffer_load_dwordx4 v252,s[36:39],s95 offen offset:3072 lds
v_mfma_f32_16x16x32_bf16 v[12:15],v[220:223],a[92:95],v[12:15]
s_waitcnt lgkmcnt(0)
v_cvt_pk_bf16_f32 v52,v8,v9
v_cvt_pk_bf16_f32 v53,v10,v11
v_mfma_f32_16x16x32_bf16 v[16:19],v[192:195],v[128:131],0
s_add_u32 m0,s92,0x1c000
ds_read_b64_tr_b16 v[208:209],v247
s_mov_b32 s95,0x38200
v_mfma_f32_16x16x32_bf16 v[16:19],v[196:199],v[132:135],v[16:19]
ds_read_b64_tr_b16 v[210:211],v247 offset:2048
v_cvt_pk_bf16_f32 v54,v12,v13
v_mfma_f32_16x16x32_bf16 v[16:19],v[200:203],v[136:139],v[16:19]
buffer_load_dwordx4 v252,s[24:27],s95 offen lds
v_mfma_f32_16x16x32_bf16 v[16:19],v[204:207],v[140:143],v[16:19]
ds_read_b64_tr_b16 v[212:213],v247 offset:4096
ds_read_b64_tr_b16 v[214:215],v247 offset:6144
v_mfma_f32_16x16x32_bf16 v[20:23],v[192:195],v[160:163],0
ds_read_b64_tr_b16 v[216:217],v247 offset:8192
v_cvt_pk_bf16_f32 v55,v14,v15
v_mfma_f32_16x16x32_bf16 v[20:23],v[196:199],v[164:167],v[20:23]
ds_read_b64_tr_b16 v[218:219],v247 offset:10240
v_mfma_f32_16x16x32_bf16 v[20:23],v[200:203],v[168:171],v[20:23]
buffer_load_dwordx4 v252,s[28:31],s95 offen offset:1024 lds
v_mfma_f32_16x16x32_bf16 v[20:23],v[204:207],v[172:175],v[20:23]
ds_read_b64_tr_b16 v[220:221],v247 offset:12288
ds_read_b64_tr_b16 v[222:223],v247 offset:14336
v_mfma_f32_16x16x32_bf16 v[24:27],v[192:195],a[128:131],0
buffer_store_dwordx4 v[52:55],v255,s[88:91],s99 offen sc1
v_mfma_f32_16x16x32_bf16 v[24:27],v[196:199],a[132:135],v[24:27]
s_add_u32 s99,s96,0x3000
v_mfma_f32_16x16x32_bf16 v[24:27],v[200:203],a[136:139],v[24:27]
buffer_load_dwordx4 v252,s[32:35],s95 offen offset:2048 lds
v_mfma_f32_16x16x32_bf16 v[24:27],v[204:207],a[140:143],v[24:27]
v_mfma_f32_16x16x32_bf16 v[28:31],v[192:195],a[160:163],0
v_mfma_f32_16x16x32_bf16 v[28:31],v[196:199],a[164:167],v[28:31]
v_mfma_f32_16x16x32_bf16 v[28:31],v[200:203],a[168:171],v[28:31]
buffer_load_dwordx4 v252,s[36:39],s95 offen offset:3072 lds
v_mfma_f32_16x16x32_bf16 v[28:31],v[204:207],a[172:175],v[28:31]
s_waitcnt vmcnt(11) lgkmcnt(0)
v_mfma_f32_16x16x32_bf16 v[16:19],v[208:211],a[192:195],v[16:19]
s_barrier
ds_read_b64_tr_b16 v[192:193],v242
s_add_u32 m0,s92,0x0
v_mfma_f32_16x16x32_bf16 v[16:19],v[212:215],a[196:199],v[16:19]
s_mov_b32 s95,0x1c200
ds_read_b64_tr_b16 v[194:195],v242 offset:2048
v_mov_b32 v229,3
v_mfma_f32_16x16x32_bf16 v[16:19],v[216:219],a[200:203],v[16:19]
buffer_load_dwordx4 v252,s[24:27],s95 offen lds
v_mfma_f32_16x16x32_bf16 v[16:19],v[220:223],a[204:207],v[16:19]
ds_read_b64_tr_b16 v[196:197],v242 offset:4096
ds_read_b64_tr_b16 v[198:199],v242 offset:6144
v_mfma_f32_16x16x32_bf16 v[20:23],v[208:211],a[224:227],v[20:23]
ds_read_b64_tr_b16 v[200:201],v242 offset:8192
v_mfma_f32_16x16x32_bf16 v[20:23],v[212:215],a[228:231],v[20:23]
ds_read_b64_tr_b16 v[202:203],v242 offset:10240
v_mfma_f32_16x16x32_bf16 v[20:23],v[216:219],a[232:235],v[20:23]
buffer_load_dwordx4 v252,s[28:31],s95 offen offset:1024 lds
v_mfma_f32_16x16x32_bf16 v[20:23],v[220:223],a[236:239],v[20:23]
ds_read_b64_tr_b16 v[204:205],v242 offset:12288
ds_read_b64_tr_b16 v[206:207],v242 offset:14336
v_mfma_f32_16x16x32_bf16 v[24:27],v[208:211],a[0:3],v[24:27]
buffer_store_dword v229,off,s[88:91],s79 sc1
v_mfma_f32_16x16x32_bf16 v[24:27],v[212:215],a[4:7],v[24:27]
v_mfma_f32_16x16x32_bf16 v[24:27],v[216:219],a[8:11],v[24:27]
buffer_load_dwordx4 v252,s[32:35],s95 offen offset:2048 lds
v_mfma_f32_16x16x32_bf16 v[24:27],v[220:223],a[12:15],v[24:27]
v_mfma_f32_16x16x32_bf16 v[28:31],v[208:211],a[64:67],v[28:31]
v_mfma_f32_16x16x32_bf16 v[28:31],v[212:215],a[68:71],v[28:31]
v_mfma_f32_16x16x32_bf16 v[28:31],v[216:219],a[72:75],v[28:31]
buffer_load_dwordx4 v252,s[36:39],s95 offen offset:3072 lds
v_mfma_f32_16x16x32_bf16 v[28:31],v[220:223],a[76:79],v[28:31]
s_waitcnt lgkmcnt(0)
v_mfma_f32_16x16x32_bf16 v[16:19],v[192:195],v[144:147],v[16:19]
s_add_u32 m0,s92,0x4000
ds_read_b64_tr_b16 v[208:209],v243
s_mov_b32 s95,0x54200
v_mfma_f32_16x16x32_bf16 v[16:19],v[196:199],v[148:151],v[16:19]
ds_read_b64_tr_b16 v[210:211],v243 offset:2048
v_mfma_f32_16x16x32_bf16 v[16:19],v[200:203],v[152:155],v[16:19]
buffer_load_dwordx4 v252,s[24:27],s95 offen lds
v_mfma_f32_16x16x32_bf16 v[16:19],v[204:207],v[156:159],v[16:19]
ds_read_b64_tr_b16 v[212:213],v243 offset:4096
ds_read_b64_tr_b16 v[214:215],v243 offset:6144
v_mfma_f32_16x16x32_bf16 v[20:23],v[192:195],v[176:179],v[20:23]
ds_read_b64_tr_b16 v[216:217],v243 offset:8192
v_mfma_f32_16x16x32_bf16 v[20:23],v[196:199],v[180:183],v[20:23]
ds_read_b64_tr_b16 v[218:219],v243 offset:10240
v_mfma_f32_16x16x32_bf16 v[20:23],v[200:203],v[184:187],v[20:23]
buffer_load_dwordx4 v252,s[28:31],s95 offen offset:1024 lds
v_mfma_f32_16x16x32_bf16 v[20:23],v[204:207],v[188:191],v[20:23]
ds_read_b64_tr_b16 v[220:221],v243 offset:12288
ds_read_b64_tr_b16 v[222:223],v243 offset:14336
v_mfma_f32_16x16x32_bf16 v[24:27],v[192:195],a[144:147],v[24:27]
v_mfma_f32_16x16x32_bf16 v[24:27],v[196:199],a[148:151],v[24:27]
v_mfma_f32_16x16x32_bf16 v[24:27],v[200:203],a[152:155],v[24:27]
buffer_load_dwordx4 v252,s[32:35],s95 offen offset:2048 lds
v_mfma_f32_16x16x32_bf16 v[24:27],v[204:207],a[156:159],v[24:27]
v_mfma_f32_16x16x32_bf16 v[28:31],v[192:195],a[176:179],v[28:31]
v_mfma_f32_16x16x32_bf16 v[28:31],v[196:199],a[180:183],v[28:31]
v_mfma_f32_16x16x32_bf16 v[28:31],v[200:203],a[184:187],v[28:31]
buffer_load_dwordx4 v252,s[36:39],s95 offen offset:3072 lds
v_mfma_f32_16x16x32_bf16 v[28:31],v[204:207],a[188:191],v[28:31]
s_waitcnt vmcnt(9) lgkmcnt(0)
v_mfma_f32_16x16x32_bf16 v[16:19],v[208:211],a[208:211],v[16:19]
s_barrier
ds_read_b64_tr_b16 v[192:193],v242 offset:32768
s_add_u32 m0,s92,0x8000
v_mfma_f32_16x16x32_bf16 v[16:19],v[212:215],a[212:215],v[16:19]
s_mov_b32 s95,0x280
ds_read_b64_tr_b16 v[194:195],v242 offset:34816
v_mfma_f32_16x16x32_bf16 v[16:19],v[216:219],a[216:219],v[16:19]
buffer_load_dwordx4 v252,s[24:27],s95 offen lds
v_mfma_f32_16x16x32_bf16 v[16:19],v[220:223],a[220:223],v[16:19]
ds_read_b64_tr_b16 v[196:197],v242 offset:36864
ds_read_b64_tr_b16 v[198:199],v242 offset:38912
v_mfma_f32_16x16x32_bf16 v[20:23],v[208:211],a[240:243],v[20:23]
ds_read_b64_tr_b16 v[200:201],v242 offset:40960
v_mfma_f32_16x16x32_bf16 v[20:23],v[212:215],a[244:247],v[20:23]
ds_read_b64_tr_b16 v[202:203],v242 offset:43008
v_mfma_f32_16x16x32_bf16 v[20:23],v[216:219],a[248:251],v[20:23]
buffer_load_dwordx4 v252,s[28:31],s95 offen offset:1024 lds
v_mfma_f32_16x16x32_bf16 v[20:23],v[220:223],a[252:255],v[20:23]
ds_read_b64_tr_b16 v[204:205],v242 offset:45056
ds_read_b64_tr_b16 v[206:207],v242 offset:47104
v_mfma_f32_16x16x32_bf16 v[24:27],v[208:211],a[16:19],v[24:27]
v_cvt_pk_bf16_f32 v56,v16,v17
v_cvt_pk_bf16_f32 v57,v18,v19
v_mfma_f32_16x16x32_bf16 v[24:27],v[212:215],a[20:23],v[24:27]
v_mfma_f32_16x16x32_bf16 v[24:27],v[216:219],a[24:27],v[24:27]
buffer_load_dwordx4 v252,s[32:35],s95 offen offset:2048 lds
v_mfma_f32_16x16x32_bf16 v[24:27],v[220:223],a[28:31],v[24:27]
v_cvt_pk_bf16_f32 v58,v20,v21
v_cvt_pk_bf16_f32 v59,v22,v23
v_mfma_f32_16x16x32_bf16 v[28:31],v[208:211],a[80:83],v[28:31]
buffer_store_dwordx4 v[56:59],v255,s[88:91],s99 offen sc1
v_mfma_f32_16x16x32_bf16 v[28:31],v[212:215],a[84:87],v[28:31]
s_add_u32 s99,s93,0x3000
v_mfma_f32_16x16x32_bf16 v[28:31],v[216:219],a[88:91],v[28:31]
buffer_load_dwordx4 v252,s[36:39],s95 offen offset:3072 lds
v_mfma_f32_16x16x32_bf16 v[28:31],v[220:223],a[92:95],v[28:31]
s_waitcnt lgkmcnt(0)
v_cvt_pk_bf16_f32 v60,v24,v25
v_cvt_pk_bf16_f32 v61,v26,v27
v_mfma_f32_16x16x32_bf16 v[0:3],v[192:195],v[128:131],0
s_add_u32 m0,s92,0xc000
ds_read_b64_tr_b16 v[208:209],v243 offset:32768
s_mov_b32 s95,0x38280
v_mfma_f32_16x16x32_bf16 v[0:3],v[196:199],v[132:135],v[0:3]
ds_read_b64_tr_b16 v[210:211],v243 offset:34816
v_cvt_pk_bf16_f32 v62,v28,v29
v_mfma_f32_16x16x32_bf16 v[0:3],v[200:203],v[136:139],v[0:3]
buffer_load_dwordx4 v252,s[24:27],s95 offen lds
v_mfma_f32_16x16x32_bf16 v[0:3],v[204:207],v[140:143],v[0:3]
)"
R"(ds_read_b64_tr_b16 v[212:213],v243 offset:36864
ds_read_b64_tr_b16 v[214:215],v243 offset:38912
v_mfma_f32_16x16x32_bf16 v[4:7],v[192:195],v[160:163],0
ds_read_b64_tr_b16 v[216:217],v243 offset:40960
v_cvt_pk_bf16_f32 v63,v30,v31
v_mfma_f32_16x16x32_bf16 v[4:7],v[196:199],v[164:167],v[4:7]
ds_read_b64_tr_b16 v[218:219],v243 offset:43008
v_mfma_f32_16x16x32_bf16 v[4:7],v[200:203],v[168:171],v[4:7]
buffer_load_dwordx4 v252,s[28:31],s95 offen offset:1024 lds
v_mfma_f32_16x16x32_bf16 v[4:7],v[204:207],v[172:175],v[4:7]
ds_read_b64_tr_b16 v[220:221],v243 offset:45056
ds_read_b64_tr_b16 v[222:223],v243 offset:47104
v_mfma_f32_16x16x32_bf16 v[8:11],v[192:195],a[128:131],0
buffer_store_dwordx4 v[60:63],v255,s[88:91],s99 offen sc1
v_mfma_f32_16x16x32_bf16 v[8:11],v[196:199],a[132:135],v[8:11]
s_add_u32 s99,s96,0x4000
v_mfma_f32_16x16x32_bf16 v[8:11],v[200:203],a[136:139],v[8:11]
buffer_load_dwordx4 v252,s[32:35],s95 offen offset:2048 lds
v_mfma_f32_16x16x32_bf16 v[8:11],v[204:207],a[140:143],v[8:11]
v_mfma_f32_16x16x32_bf16 v[12:15],v[192:195],a[160:163],0
v_mfma_f32_16x16x32_bf16 v[12:15],v[196:199],a[164:167],v[12:15]
v_mfma_f32_16x16x32_bf16 v[12:15],v[200:203],a[168:171],v[12:15]
buffer_load_dwordx4 v252,s[36:39],s95 offen offset:3072 lds
v_mfma_f32_16x16x32_bf16 v[12:15],v[204:207],a[172:175],v[12:15]
s_waitcnt vmcnt(10) lgkmcnt(0)
v_mfma_f32_16x16x32_bf16 v[0:3],v[208:211],a[192:195],v[0:3]
s_barrier
ds_read_b64_tr_b16 v[192:193],v240
s_add_u32 m0,s92,0x20000
v_mfma_f32_16x16x32_bf16 v[0:3],v[212:215],a[196:199],v[0:3]
s_mov_b32 s95,0x1c280
ds_read_b64_tr_b16 v[194:195],v240 offset:2048
v_mov_b32 v229,4
v_mfma_f32_16x16x32_bf16 v[0:3],v[216:219],a[200:203],v[0:3]
buffer_load_dwordx4 v252,s[24:27],s95 offen lds
v_mfma_f32_16x16x32_bf16 v[0:3],v[220:223],a[204:207],v[0:3]
ds_read_b64_tr_b16 v[196:197],v240 offset:4096
ds_read_b64_tr_b16 v[198:199],v240 offset:6144
v_mfma_f32_16x16x32_bf16 v[4:7],v[208:211],a[224:227],v[4:7]
ds_read_b64_tr_b16 v[200:201],v240 offset:8192
v_mfma_f32_16x16x32_bf16 v[4:7],v[212:215],a[228:231],v[4:7]
ds_read_b64_tr_b16 v[202:203],v240 offset:10240
v_mfma_f32_16x16x32_bf16 v[4:7],v[216:219],a[232:235],v[4:7]
buffer_load_dwordx4 v252,s[28:31],s95 offen offset:1024 lds
v_mfma_f32_16x16x32_bf16 v[4:7],v[220:223],a[236:239],v[4:7]
ds_read_b64_tr_b16 v[204:205],v240 offset:12288
ds_read_b64_tr_b16 v[206:207],v240 offset:14336
v_mfma_f32_16x16x32_bf16 v[8:11],v[208:211],a[0:3],v[8:11]
buffer_store_dword v229,off,s[88:91],s79 sc1
v_mfma_f32_16x16x32_bf16 v[8:11],v[212:215],a[4:7],v[8:11]
v_mfma_f32_16x16x32_bf16 v[8:11],v[216:219],a[8:11],v[8:11]
buffer_load_dwordx4 v252,s[32:35],s95 offen offset:2048 lds
v_mfma_f32_16x16x32_bf16 v[8:11],v[220:223],a[12:15],v[8:11]
v_mfma_f32_16x16x32_bf16 v[12:15],v[208:211],a[64:67],v[12:15]
v_mfma_f32_16x16x32_bf16 v[12:15],v[212:215],a[68:71],v[12:15]
v_mfma_f32_16x16x32_bf16 v[12:15],v[216:219],a[72:75],v[12:15]
buffer_load_dwordx4 v252,s[36:39],s95 offen offset:3072 lds
v_mfma_f32_16x16x32_bf16 v[12:15],v[220:223],a[76:79],v[12:15]
s_waitcnt lgkmcnt(0)
v_mfma_f32_16x16x32_bf16 v[0:3],v[192:195],v[144:147],v[0:3]
s_add_u32 m0,s92,0x24000
ds_read_b64_tr_b16 v[208:209],v241
s_mov_b32 s95,0x54280
v_mfma_f32_16x16x32_bf16 v[0:3],v[196:199],v[148:151],v[0:3]
ds_read_b64_tr_b16 v[210:211],v241 offset:2048
v_mfma_f32_16x16x32_bf16 v[0:3],v[200:203],v[152:155],v[0:3]
buffer_load_dwordx4 v252,s[24:27],s95 offen lds
v_mfma_f32_16x16x32_bf16 v[0:3],v[204:207],v[156:159],v[0:3]
ds_read_b64_tr_b16 v[212:213],v241 offset:4096
ds_read_b64_tr_b16 v[214:215],v241 offset:6144
v_mfma_f32_16x16x32_bf16 v[4:7],v[192:195],v[176:179],v[4:7]
ds_read_b64_tr_b16 v[216:217],v241 offset:8192
v_mfma_f32_16x16x32_bf16 v[4:7],v[196:199],v[180:183],v[4:7]
ds_read_b64_tr_b16 v[218:219],v241 offset:10240
v_mfma_f32_16x16x32_bf16 v[4:7],v[200:203],v[184:187],v[4:7]
buffer_load_dwordx4 v252,s[28:31],s95 offen offset:1024 lds
v_mfma_f32_16x16x32_bf16 v[4:7],v[204:207],v[188:191],v[4:7]
ds_read_b64_tr_b16 v[220:221],v241 offset:12288
ds_read_b64_tr_b16 v[222:223],v241 offset:14336
v_mfma_f32_16x16x32_bf16 v[8:11],v[192:195],a[144:147],v[8:11]
v_mfma_f32_16x16x32_bf16 v[8:11],v[196:199],a[148:151],v[8:11]
v_mfma_f32_16x16x32_bf16 v[8:11],v[200:203],a[152:155],v[8:11]
buffer_load_dwordx4 v252,s[32:35],s95 offen offset:2048 lds
v_mfma_f32_16x16x32_bf16 v[8:11],v[204:207],a[156:159],v[8:11]
v_mfma_f32_16x16x32_bf16 v[12:15],v[192:195],a[176:179],v[12:15]
v_mfma_f32_16x16x32_bf16 v[12:15],v[196:199],a[180:183],v[12:15]
v_mfma_f32_16x16x32_bf16 v[12:15],v[200:203],a[184:187],v[12:15]
buffer_load_dwordx4 v252,s[36:39],s95 offen offset:3072 lds
v_mfma_f32_16x16x32_bf16 v[12:15],v[204:207],a[188:191],v[12:15]
s_waitcnt vmcnt(9) lgkmcnt(0)
v_mfma_f32_16x16x32_bf16 v[0:3],v[208:211],a[208:211],v[0:3]
s_barrier
ds_read_b64_tr_b16 v[192:193],v240 offset:32768
s_add_u32 m0,s92,0x10000
v_mfma_f32_16x16x32_bf16 v[0:3],v[212:215],a[212:215],v[0:3]
s_mov_b32 s95,0x300
ds_read_b64_tr_b16 v[194:195],v240 offset:34816
v_mfma_f32_16x16x32_bf16 v[0:3],v[216:219],a[216:219],v[0:3]
buffer_load_dwordx4 v252,s[24:27],s95 offen lds
v_mfma_f32_16x16x32_bf16 v[0:3],v[220:223],a[220:223],v[0:3]
ds_read_b64_tr_b16 v[196:197],v240 offset:36864
ds_read_b64_tr_b16 v[198:199],v240 offset:38912
v_mfma_f32_16x16x32_bf16 v[4:7],v[208:211],a[240:243],v[4:7]
ds_read_b64_tr_b16 v[200:201],v240 offset:40960
v_mfma_f32_16x16x32_bf16 v[4:7],v[212:215],a[244:247],v[4:7]
buffer_load_dword v224,v226,s[88:91],s80 offen sc1
v_mfma_f32_16x16x32_bf16 v[4:7],v[216:219],a[248:251],v[4:7]
buffer_load_dwordx4 v252,s[28:31],s95 offen offset:1024 lds
v_mfma_f32_16x16x32_bf16 v[4:7],v[220:223],a[252:255],v[4:7]
ds_read_b64_tr_b16 v[202:203],v240 offset:43008
ds_read_b64_tr_b16 v[204:205],v240 offset:45056
v_mfma_f32_16x16x32_bf16 v[8:11],v[208:211],a[16:19],v[8:11]
ds_read_b64_tr_b16 v[206:207],v240 offset:47104
v_cvt_pk_bf16_f32 v64,v0,v1
v_mfma_f32_16x16x32_bf16 v[8:11],v[212:215],a[20:23],v[8:11]
v_cvt_pk_bf16_f32 v65,v2,v3
v_mfma_f32_16x16x32_bf16 v[8:11],v[216:219],a[24:27],v[8:11]
buffer_load_dwordx4 v252,s[32:35],s95 offen offset:2048 lds
v_mfma_f32_16x16x32_bf16 v[8:11],v[220:223],a[28:31],v[8:11]
v_cvt_pk_bf16_f32 v66,v4,v5
v_cvt_pk_bf16_f32 v67,v6,v7
v_mfma_f32_16x16x32_bf16 v[12:15],v[208:211],a[80:83],v[12:15]
buffer_store_dwordx4 v[64:67],v255,s[88:91],s99 offen sc1
v_mfma_f32_16x16x32_bf16 v[12:15],v[212:215],a[84:87],v[12:15]
s_add_u32 s99,s93,0x4000
v_mfma_f32_16x16x32_bf16 v[12:15],v[216:219],a[88:91],v[12:15]
buffer_load_dwordx4 v252,s[36:39],s95 offen offset:3072 lds
v_mfma_f32_16x16x32_bf16 v[12:15],v[220:223],a[92:95],v[12:15]
s_waitcnt lgkmcnt(0)
v_cvt_pk_bf16_f32 v68,v8,v9
v_cvt_pk_bf16_f32 v69,v10,v11
v_mfma_f32_16x16x32_bf16 v[16:19],v[192:195],v[128:131],0
s_add_u32 m0,s92,0x14000
ds_read_b64_tr_b16 v[208:209],v241 offset:32768
s_mov_b32 s95,0x38300
v_mfma_f32_16x16x32_bf16 v[16:19],v[196:199],v[132:135],v[16:19]
ds_read_b64_tr_b16 v[210:211],v241 offset:34816
v_cvt_pk_bf16_f32 v70,v12,v13
v_mfma_f32_16x16x32_bf16 v[16:19],v[200:203],v[136:139],v[16:19]
buffer_load_dwordx4 v252,s[24:27],s95 offen lds
v_mfma_f32_16x16x32_bf16 v[16:19],v[204:207],v[140:143],v[16:19]
ds_read_b64_tr_b16 v[212:213],v241 offset:36864
ds_read_b64_tr_b16 v[214:215],v241 offset:38912
v_mfma_f32_16x16x32_bf16 v[20:23],v[192:195],v[160:163],0
ds_read_b64_tr_b16 v[216:217],v241 offset:40960
v_cvt_pk_bf16_f32 v71,v14,v15
v_mfma_f32_16x16x32_bf16 v[20:23],v[196:199],v[164:167],v[20:23]
ds_read_b64_tr_b16 v[218:219],v241 offset:43008
v_mfma_f32_16x16x32_bf16 v[20:23],v[200:203],v[168:171],v[20:23]
buffer_load_dwordx4 v252,s[28:31],s95 offen offset:1024 lds
v_mfma_f32_16x16x32_bf16 v[20:23],v[204:207],v[172:175],v[20:23]
ds_read_b64_tr_b16 v[220:221],v241 offset:45056
ds_read_b64_tr_b16 v[222:223],v241 offset:47104
v_mfma_f32_16x16x32_bf16 v[24:27],v[192:195],a[128:131],0
buffer_store_dwordx4 v[68:71],v255,s[88:91],s99 offen sc1
v_mfma_f32_16x16x32_bf16 v[24:27],v[196:199],a[132:135],v[24:27]
s_add_u32 s99,s96,0x5000
v_mfma_f32_16x16x32_bf16 v[24:27],v[200:203],a[136:139],v[24:27]
buffer_load_dwordx4 v252,s[32:35],s95 offen offset:2048 lds
v_mfma_f32_16x16x32_bf16 v[24:27],v[204:207],a[140:143],v[24:27]
v_mfma_f32_16x16x32_bf16 v[28:31],v[192:195],a[160:163],0
v_mfma_f32_16x16x32_bf16 v[28:31],v[196:199],a[164:167],v[28:31]
v_mfma_f32_16x16x32_bf16 v[28:31],v[200:203],a[168:171],v[28:31]
buffer_load_dwordx4 v252,s[36:39],s95 offen offset:3072 lds
v_mfma_f32_16x16x32_bf16 v[28:31],v[204:207],a[172:175],v[28:31]
s_waitcnt vmcnt(11) lgkmcnt(0)
v_mfma_f32_16x16x32_bf16 v[16:19],v[208:211],a[192:195],v[16:19]
s_barrier
ds_read_b64_tr_b16 v[192:193],v246
s_add_u32 m0,s92,0x18000
v_mfma_f32_16x16x32_bf16 v[16:19],v[212:215],a[196:199],v[16:19]
s_mov_b32 s95,0x1c300
ds_read_b64_tr_b16 v[194:195],v246 offset:2048
v_mov_b32 v229,5
v_mfma_f32_16x16x32_bf16 v[16:19],v[216:219],a[200:203],v[16:19]
buffer_load_dwordx4 v252,s[24:27],s95 offen lds
v_mfma_f32_16x16x32_bf16 v[16:19],v[220:223],a[204:207],v[16:19]
ds_read_b64_tr_b16 v[196:197],v246 offset:4096
ds_read_b64_tr_b16 v[198:199],v246 offset:6144
v_mfma_f32_16x16x32_bf16 v[20:23],v[208:211],a[224:227],v[20:23]
ds_read_b64_tr_b16 v[200:201],v246 offset:8192
v_mfma_f32_16x16x32_bf16 v[20:23],v[212:215],a[228:231],v[20:23]
ds_read_b64_tr_b16 v[202:203],v246 offset:10240
v_mfma_f32_16x16x32_bf16 v[20:23],v[216:219],a[232:235],v[20:23]
buffer_load_dwordx4 v252,s[28:31],s95 offen offset:1024 lds
v_mfma_f32_16x16x32_bf16 v[20:23],v[220:223],a[236:239],v[20:23]
ds_read_b64_tr_b16 v[204:205],v246 offset:12288
ds_read_b64_tr_b16 v[206:207],v246 offset:14336
v_mfma_f32_16x16x32_bf16 v[24:27],v[208:211],a[0:3],v[24:27]
buffer_store_dword v229,off,s[88:91],s79 sc1
v_mfma_f32_16x16x32_bf16 v[24:27],v[212:215],a[4:7],v[24:27]
v_mfma_f32_16x16x32_bf16 v[24:27],v[216:219],a[8:11],v[24:27]
buffer_load_dwordx4 v252,s[32:35],s95 offen offset:2048 lds
v_mfma_f32_16x16x32_bf16 v[24:27],v[220:223],a[12:15],v[24:27]
v_mfma_f32_16x16x32_bf16 v[28:31],v[208:211],a[64:67],v[28:31]
v_mfma_f32_16x16x32_bf16 v[28:31],v[212:215],a[68:71],v[28:31]
v_mfma_f32_16x16x32_bf16 v[28:31],v[216:219],a[72:75],v[28:31]
buffer_load_dwordx4 v252,s[36:39],s95 offen offset:3072 lds
v_mfma_f32_16x16x32_bf16 v[28:31],v[220:223],a[76:79],v[28:31]
s_waitcnt lgkmcnt(0)
)"
R"(v_mfma_f32_16x16x32_bf16 v[16:19],v[192:195],v[144:147],v[16:19]
s_add_u32 m0,s92,0x1c000
ds_read_b64_tr_b16 v[208:209],v247
s_mov_b32 s95,0x54300
v_mfma_f32_16x16x32_bf16 v[16:19],v[196:199],v[148:151],v[16:19]
ds_read_b64_tr_b16 v[210:211],v247 offset:2048
v_mfma_f32_16x16x32_bf16 v[16:19],v[200:203],v[152:155],v[16:19]
buffer_load_dwordx4 v252,s[24:27],s95 offen lds
v_mfma_f32_16x16x32_bf16 v[16:19],v[204:207],v[156:159],v[16:19]
ds_read_b64_tr_b16 v[212:213],v247 offset:4096
ds_read_b64_tr_b16 v[214:215],v247 offset:6144
v_mfma_f32_16x16x32_bf16 v[20:23],v[192:195],v[176:179],v[20:23]
ds_read_b64_tr_b16 v[216:217],v247 offset:8192
v_mfma_f32_16x16x32_bf16 v[20:23],v[196:199],v[180:183],v[20:23]
ds_read_b64_tr_b16 v[218:219],v247 offset:10240
v_mfma_f32_16x16x32_bf16 v[20:23],v[200:203],v[184:187],v[20:23]
buffer_load_dwordx4 v252,s[28:31],s95 offen offset:1024 lds
v_mfma_f32_16x16x32_bf16 v[20:23],v[204:207],v[188:191],v[20:23]
ds_read_b64_tr_b16 v[220:221],v247 offset:12288
ds_read_b64_tr_b16 v[222:223],v247 offset:14336
v_mfma_f32_16x16x32_bf16 v[24:27],v[192:195],a[144:147],v[24:27]
v_mfma_f32_16x16x32_bf16 v[24:27],v[196:199],a[148:151],v[24:27]
v_mfma_f32_16x16x32_bf16 v[24:27],v[200:203],a[152:155],v[24:27]
buffer_load_dwordx4 v252,s[32:35],s95 offen offset:2048 lds
v_mfma_f32_16x16x32_bf16 v[24:27],v[204:207],a[156:159],v[24:27]
v_mfma_f32_16x16x32_bf16 v[28:31],v[192:195],a[176:179],v[28:31]
v_mfma_f32_16x16x32_bf16 v[28:31],v[196:199],a[180:183],v[28:31]
v_mfma_f32_16x16x32_bf16 v[28:31],v[200:203],a[184:187],v[28:31]
buffer_load_dwordx4 v252,s[36:39],s95 offen offset:3072 lds
v_mfma_f32_16x16x32_bf16 v[28:31],v[204:207],a[188:191],v[28:31]
s_waitcnt vmcnt(18)
s_waitcnt vmcnt(9) lgkmcnt(0)
v_mfma_f32_16x16x32_bf16 v[16:19],v[208:211],a[208:211],v[16:19]
s_barrier
ds_read_b64_tr_b16 v[192:193],v242
v_mfma_f32_16x16x32_bf16 v[16:19],v[212:215],a[212:215],v[16:19]
ds_read_b64_tr_b16 v[194:195],v242 offset:2048
v_mfma_f32_16x16x32_bf16 v[16:19],v[216:219],a[216:219],v[16:19]
s_add_u32 s60,s83,0
ds_read_b64_tr_b16 v[196:197],v242 offset:4096
v_cmp_gt_u32 vcc,s52,v224
v_mfma_f32_16x16x32_bf16 v[16:19],v[220:223],a[220:223],v[16:19]
s_and_b32 s97,vcc_lo,0xff
s_cselect_b32 s58,0,s42
s_add_u32 s61,s60,4096
ds_read_b64_tr_b16 v[198:199],v242 offset:6144
v_mfma_f32_16x16x32_bf16 v[20:23],v[208:211],a[240:243],v[20:23]
buffer_load_dwordx4 v[96:99],v255,s[56:59],s60 offen sc1
v_mfma_f32_16x16x32_bf16 v[20:23],v[212:215],a[244:247],v[20:23]
ds_read_b64_tr_b16 v[200:201],v242 offset:8192
ds_read_b64_tr_b16 v[202:203],v242 offset:10240
v_mfma_f32_16x16x32_bf16 v[20:23],v[216:219],a[248:251],v[20:23]
ds_read_b64_tr_b16 v[204:205],v242 offset:12288
v_cvt_pk_bf16_f32 v72,v16,v17
v_mfma_f32_16x16x32_bf16 v[20:23],v[220:223],a[252:255],v[20:23]
buffer_load_dwordx4 v[100:103],v255,s[56:59],s61 offen sc1
v_mfma_f32_16x16x32_bf16 v[24:27],v[208:211],a[16:19],v[24:27]
ds_read_b64_tr_b16 v[206:207],v242 offset:14336
v_cvt_pk_bf16_f32 v73,v18,v19
v_mfma_f32_16x16x32_bf16 v[24:27],v[212:215],a[20:23],v[24:27]
v_mfma_f32_16x16x32_bf16 v[24:27],v[216:219],a[24:27],v[24:27]
s_add_u32 s60,s83,114688
s_add_u32 s61,s60,4096
v_cvt_pk_bf16_f32 v74,v20,v21
v_mfma_f32_16x16x32_bf16 v[24:27],v[220:223],a[28:31],v[24:27]
buffer_load_dwordx4 v[104:107],v255,s[56:59],s60 offen sc1
v_mfma_f32_16x16x32_bf16 v[28:31],v[208:211],a[80:83],v[28:31]
v_cvt_pk_bf16_f32 v75,v22,v23
v_mfma_f32_16x16x32_bf16 v[28:31],v[212:215],a[84:87],v[28:31]
buffer_store_dwordx4 v[72:75],v255,s[88:91],s99 offen sc1
v_mfma_f32_16x16x32_bf16 v[28:31],v[216:219],a[88:91],v[28:31]
s_add_u32 s99,s93,0x5000
buffer_load_dwordx4 v[108:111],v255,s[56:59],s61 offen sc1
v_mfma_f32_16x16x32_bf16 v[28:31],v[220:223],a[92:95],v[28:31]
s_waitcnt lgkmcnt(0)
v_cvt_pk_bf16_f32 v76,v24,v25
v_cvt_pk_bf16_f32 v77,v26,v27
v_mfma_f32_16x16x32_bf16 v[0:3],v[192:195],v[128:131],0
ds_read_b64_tr_b16 v[208:209],v243
v_mfma_f32_16x16x32_bf16 v[0:3],v[196:199],v[132:135],v[0:3]
ds_read_b64_tr_b16 v[210:211],v243 offset:2048
v_mfma_f32_16x16x32_bf16 v[0:3],v[200:203],v[136:139],v[0:3]
s_add_u32 s60,s83,229376
ds_read_b64_tr_b16 v[212:213],v243 offset:4096
s_add_u32 s61,s60,4096
v_mfma_f32_16x16x32_bf16 v[0:3],v[204:207],v[140:143],v[0:3]
buffer_load_dwordx4 v[112:115],v255,s[56:59],s60 offen sc1
v_mfma_f32_16x16x32_bf16 v[4:7],v[192:195],v[160:163],0
ds_read_b64_tr_b16 v[214:215],v243 offset:6144
ds_read_b64_tr_b16 v[216:217],v243 offset:8192
v_mfma_f32_16x16x32_bf16 v[4:7],v[196:199],v[164:167],v[4:7]
ds_read_b64_tr_b16 v[218:219],v243 offset:10240
v_cvt_pk_bf16_f32 v78,v28,v29
v_mfma_f32_16x16x32_bf16 v[4:7],v[200:203],v[168:171],v[4:7]
ds_read_b64_tr_b16 v[220:221],v243 offset:12288
v_cvt_pk_bf16_f32 v79,v30,v31
v_mfma_f32_16x16x32_bf16 v[4:7],v[204:207],v[172:175],v[4:7]
buffer_load_dwordx4 v[116:119],v255,s[56:59],s61 offen sc1
v_mfma_f32_16x16x32_bf16 v[8:11],v[192:195],a[128:131],0
ds_read_b64_tr_b16 v[222:223],v243 offset:14336
v_mfma_f32_16x16x32_bf16 v[8:11],v[196:199],a[132:135],v[8:11]
buffer_store_dwordx4 v[76:79],v255,s[88:91],s99 offen sc1
v_mfma_f32_16x16x32_bf16 v[8:11],v[200:203],a[136:139],v[8:11]
s_add_u32 s60,s83,344064
s_add_u32 s61,s60,4096
v_mfma_f32_16x16x32_bf16 v[8:11],v[204:207],a[140:143],v[8:11]
s_add_u32 s99,s96,0x6000
buffer_load_dwordx4 v[120:123],v255,s[56:59],s60 offen sc1
v_mfma_f32_16x16x32_bf16 v[12:15],v[192:195],a[160:163],0
v_mfma_f32_16x16x32_bf16 v[12:15],v[196:199],a[164:167],v[12:15]
v_mfma_f32_16x16x32_bf16 v[12:15],v[200:203],a[168:171],v[12:15]
buffer_load_dwordx4 v[124:127],v255,s[56:59],s61 offen sc1
v_mfma_f32_16x16x32_bf16 v[12:15],v[204:207],a[172:175],v[12:15]
s_waitcnt vmcnt(10) lgkmcnt(0)
v_mfma_f32_16x16x32_bf16 v[0:3],v[208:211],a[192:195],v[0:3]
s_barrier
ds_read_b64_tr_b16 v[192:193],v242 offset:32768
v_mov_b32 v229,6
v_mfma_f32_16x16x32_bf16 v[0:3],v[212:215],a[196:199],v[0:3]
ds_read_b64_tr_b16 v[194:195],v242 offset:34816
v_mfma_f32_16x16x32_bf16 v[0:3],v[216:219],a[200:203],v[0:3]
ds_read_b64_tr_b16 v[196:197],v242 offset:36864
v_mfma_f32_16x16x32_bf16 v[0:3],v[220:223],a[204:207],v[0:3]
ds_read_b64_tr_b16 v[198:199],v242 offset:38912
v_mfma_f32_16x16x32_bf16 v[4:7],v[208:211],a[224:227],v[4:7]
ds_read_b64_tr_b16 v[200:201],v242 offset:40960
v_mfma_f32_16x16x32_bf16 v[4:7],v[212:215],a[228:231],v[4:7]
ds_read_b64_tr_b16 v[202:203],v242 offset:43008
v_mfma_f32_16x16x32_bf16 v[4:7],v[216:219],a[232:235],v[4:7]
ds_read_b64_tr_b16 v[204:205],v242 offset:45056
v_mfma_f32_16x16x32_bf16 v[4:7],v[220:223],a[236:239],v[4:7]
ds_read_b64_tr_b16 v[206:207],v242 offset:47104
v_mfma_f32_16x16x32_bf16 v[8:11],v[208:211],a[0:3],v[8:11]
buffer_store_dword v229,off,s[88:91],s79 sc1
v_mfma_f32_16x16x32_bf16 v[8:11],v[212:215],a[4:7],v[8:11]
v_mfma_f32_16x16x32_bf16 v[8:11],v[216:219],a[8:11],v[8:11]
v_mfma_f32_16x16x32_bf16 v[8:11],v[220:223],a[12:15],v[8:11]
v_mfma_f32_16x16x32_bf16 v[12:15],v[208:211],a[64:67],v[12:15]
v_mfma_f32_16x16x32_bf16 v[12:15],v[212:215],a[68:71],v[12:15]
v_mfma_f32_16x16x32_bf16 v[12:15],v[216:219],a[72:75],v[12:15]
v_mfma_f32_16x16x32_bf16 v[12:15],v[220:223],a[76:79],v[12:15]
s_waitcnt lgkmcnt(0)
v_mfma_f32_16x16x32_bf16 v[0:3],v[192:195],v[144:147],v[0:3]
ds_read_b64_tr_b16 v[208:209],v243 offset:32768
v_mfma_f32_16x16x32_bf16 v[0:3],v[196:199],v[148:151],v[0:3]
ds_read_b64_tr_b16 v[210:211],v243 offset:34816
v_mfma_f32_16x16x32_bf16 v[0:3],v[200:203],v[152:155],v[0:3]
ds_read_b64_tr_b16 v[212:213],v243 offset:36864
v_mfma_f32_16x16x32_bf16 v[0:3],v[204:207],v[156:159],v[0:3]
ds_read_b64_tr_b16 v[214:215],v243 offset:38912
v_mfma_f32_16x16x32_bf16 v[4:7],v[192:195],v[176:179],v[4:7]
ds_read_b64_tr_b16 v[216:217],v243 offset:40960
v_mfma_f32_16x16x32_bf16 v[4:7],v[196:199],v[180:183],v[4:7]
ds_read_b64_tr_b16 v[218:219],v243 offset:43008
v_mfma_f32_16x16x32_bf16 v[4:7],v[200:203],v[184:187],v[4:7]
ds_read_b64_tr_b16 v[220:221],v243 offset:45056
v_mfma_f32_16x16x32_bf16 v[4:7],v[204:207],v[188:191],v[4:7]
ds_read_b64_tr_b16 v[222:223],v243 offset:47104
v_mfma_f32_16x16x32_bf16 v[8:11],v[192:195],a[144:147],v[8:11]
v_mfma_f32_16x16x32_bf16 v[8:11],v[196:199],a[148:151],v[8:11]
v_mfma_f32_16x16x32_bf16 v[8:11],v[200:203],a[152:155],v[8:11]
v_mfma_f32_16x16x32_bf16 v[8:11],v[204:207],a[156:159],v[8:11]
v_mfma_f32_16x16x32_bf16 v[12:15],v[192:195],a[176:179],v[12:15]
v_mfma_f32_16x16x32_bf16 v[12:15],v[196:199],a[180:183],v[12:15]
v_mfma_f32_16x16x32_bf16 v[12:15],v[200:203],a[184:187],v[12:15]
v_mfma_f32_16x16x32_bf16 v[12:15],v[204:207],a[188:191],v[12:15]
s_waitcnt lgkmcnt(0)
v_mfma_f32_16x16x32_bf16 v[0:3],v[208:211],a[208:211],v[0:3]
v_mfma_f32_16x16x32_bf16 v[0:3],v[212:215],a[212:215],v[0:3]
v_mfma_f32_16x16x32_bf16 v[0:3],v[216:219],a[216:219],v[0:3]
v_mfma_f32_16x16x32_bf16 v[0:3],v[220:223],a[220:223],v[0:3]
v_mfma_f32_16x16x32_bf16 v[4:7],v[208:211],a[240:243],v[4:7]
v_mfma_f32_16x16x32_bf16 v[4:7],v[212:215],a[244:247],v[4:7]
v_mfma_f32_16x16x32_bf16 v[4:7],v[216:219],a[248:251],v[4:7]
v_mfma_f32_16x16x32_bf16 v[4:7],v[220:223],a[252:255],v[4:7]
v_mfma_f32_16x16x32_bf16 v[8:11],v[208:211],a[16:19],v[8:11]
v_mfma_f32_16x16x32_bf16 v[8:11],v[212:215],a[20:23],v[8:11]
v_mfma_f32_16x16x32_bf16 v[8:11],v[216:219],a[24:27],v[8:11]
v_mfma_f32_16x16x32_bf16 v[8:11],v[220:223],a[28:31],v[8:11]
v_cvt_pk_bf16_f32 v80,v0,v1
v_cvt_pk_bf16_f32 v81,v2,v3
v_mfma_f32_16x16x32_bf16 v[12:15],v[208:211],a[80:83],v[12:15]
v_mfma_f32_16x16x32_bf16 v[12:15],v[212:215],a[84:87],v[12:15]
v_cvt_pk_bf16_f32 v82,v4,v5
v_cvt_pk_bf16_f32 v83,v6,v7
v_mfma_f32_16x16x32_bf16 v[12:15],v[216:219],a[88:91],v[12:15]
buffer_store_dwordx4 v[80:83],v255,s[88:91],s99 offen sc1
v_mfma_f32_16x16x32_bf16 v[12:15],v[220:223],a[92:95],v[12:15]
s_add_u32 s99,s93,0x6000
v_cvt_pk_bf16_f32 v84,v8,v9
v_cvt_pk_bf16_f32 v85,v10,v11
s_nop 4
v_cvt_pk_bf16_f32 v86,v12,v13
v_cvt_pk_bf16_f32 v87,v14,v15
buffer_store_dwordx4 v[84:87],v255,s[88:91],s99 offen sc1
v_mov_b32 v192,0
v_mov_b32 v193,0
v_mov_b32 v194,0
v_mov_b32 v195,0
v_mov_b32 v196,0
)"
R"(v_mov_b32 v197,0
v_mov_b32 v198,0
v_mov_b32 v199,0
v_mov_b32 v200,0
v_mov_b32 v201,0
v_mov_b32 v202,0
v_mov_b32 v203,0
v_mov_b32 v204,0
v_mov_b32 v205,0
v_mov_b32 v206,0
v_mov_b32 v207,0
v_mbcnt_lo_u32_b32 v253,-1,0
v_mbcnt_hi_u32_b32 v253,-1,v253
v_and_b32 v230,15,v253
v_lshlrev_b32 v228,6,v230
v_bfe_u32 v230,v253,1,2
v_lshrrev_b32 v225,5,v253
v_xor_b32 v230,v230,v225
v_lshl_add_u32 v228,v230,4,v228
v_bfe_u32 v230,v253,4,1
v_lshl_add_u32 v228,v230,3,v228
s_add_u32 s101,s92,131072
v_add_u32 v228,s101,v228
v_xor_b32 v230,32,v228
v_add_u32 v225,s101,v255
s_mov_b32 s98,0x00003f80
s_mov_b32 s100,0x3f800000
s_mov_b64 s[24:25],s[88:89]
s_mov_b32 s74,1000000
s_lshr_b32 m0,%[h],1
s_waitcnt vmcnt(3)
v_mov_b32 v208,0
v_mov_b32 v209,0
v_mov_b32 v210,0
v_mov_b32 v211,0
v_mov_b32 v212,0
v_mov_b32 v213,0
v_mov_b32 v214,0
v_mov_b32 v215,0
v_mov_b32 v216,0
v_mov_b32 v217,0
v_mov_b32 v218,0
v_mov_b32 v219,0
v_mov_b32 v220,0
v_mov_b32 v221,0
v_mov_b32 v222,0
v_mov_b32 v223,0
v_dot2_f32_bf16 v208,v96,s98,v208
v_dot2_f32_bf16 v209,v96,s100,v209
v_dot2_f32_bf16 v210,v97,s98,v210
v_dot2_f32_bf16 v211,v97,s100,v211
v_dot2_f32_bf16 v216,v98,s98,v216
v_dot2_f32_bf16 v217,v98,s100,v217
v_dot2_f32_bf16 v218,v99,s98,v218
v_dot2_f32_bf16 v219,v99,s100,v219
v_dot2_f32_bf16 v212,v100,s98,v212
v_dot2_f32_bf16 v213,v100,s100,v213
v_dot2_f32_bf16 v214,v101,s98,v214
v_dot2_f32_bf16 v215,v101,s100,v215
v_dot2_f32_bf16 v220,v102,s98,v220
v_dot2_f32_bf16 v221,v102,s100,v221
v_dot2_f32_bf16 v222,v103,s98,v222
v_dot2_f32_bf16 v223,v103,s100,v223
v_dot2_f32_bf16 v208,v104,s98,v208
v_dot2_f32_bf16 v209,v104,s100,v209
v_dot2_f32_bf16 v210,v105,s98,v210
v_dot2_f32_bf16 v211,v105,s100,v211
v_dot2_f32_bf16 v216,v106,s98,v216
v_dot2_f32_bf16 v217,v106,s100,v217
v_dot2_f32_bf16 v218,v107,s98,v218
v_dot2_f32_bf16 v219,v107,s100,v219
v_dot2_f32_bf16 v212,v108,s98,v212
v_dot2_f32_bf16 v213,v108,s100,v213
v_dot2_f32_bf16 v214,v109,s98,v214
v_dot2_f32_bf16 v215,v109,s100,v215
v_dot2_f32_bf16 v220,v110,s98,v220
v_dot2_f32_bf16 v221,v110,s100,v221
v_dot2_f32_bf16 v222,v111,s98,v222
v_dot2_f32_bf16 v223,v111,s100,v223
v_dot2_f32_bf16 v208,v112,s98,v208
v_dot2_f32_bf16 v209,v112,s100,v209
buffer_load_dword v224,v226,s[88:91],s80 offen sc1
v_dot2_f32_bf16 v210,v113,s98,v210
v_dot2_f32_bf16 v211,v113,s100,v211
v_dot2_f32_bf16 v216,v114,s98,v216
v_dot2_f32_bf16 v217,v114,s100,v217
v_dot2_f32_bf16 v218,v115,s98,v218
v_dot2_f32_bf16 v219,v115,s100,v219
v_dot2_f32_bf16 v212,v116,s98,v212
v_dot2_f32_bf16 v213,v116,s100,v213
v_dot2_f32_bf16 v214,v117,s98,v214
v_dot2_f32_bf16 v215,v117,s100,v215
v_dot2_f32_bf16 v220,v118,s98,v220
v_dot2_f32_bf16 v221,v118,s100,v221
v_dot2_f32_bf16 v222,v119,s98,v222
v_dot2_f32_bf16 v223,v119,s100,v223
v_dot2_f32_bf16 v208,v120,s98,v208
v_dot2_f32_bf16 v209,v120,s100,v209
v_dot2_f32_bf16 v210,v121,s98,v210
v_dot2_f32_bf16 v211,v121,s100,v211
v_dot2_f32_bf16 v216,v122,s98,v216
v_dot2_f32_bf16 v217,v122,s100,v217
v_dot2_f32_bf16 v218,v123,s98,v218
v_dot2_f32_bf16 v219,v123,s100,v219
v_dot2_f32_bf16 v212,v124,s98,v212
v_dot2_f32_bf16 v213,v124,s100,v213
v_dot2_f32_bf16 v214,v125,s98,v214
v_dot2_f32_bf16 v215,v125,s100,v215
v_dot2_f32_bf16 v220,v126,s98,v220
v_dot2_f32_bf16 v221,v126,s100,v221
v_dot2_f32_bf16 v222,v127,s98,v222
v_dot2_f32_bf16 v223,v127,s100,v223
v_cvt_pk_bf16_f32 v208,0,v208
v_cvt_pk_bf16_f32 v209,0,v209
v_cvt_pk_bf16_f32 v210,0,v210
v_cvt_pk_bf16_f32 v211,0,v211
v_cvt_pk_bf16_f32 v212,0,v212
v_cvt_pk_bf16_f32 v213,0,v213
v_cvt_pk_bf16_f32 v214,0,v214
v_cvt_pk_bf16_f32 v215,0,v215
v_cvt_pk_bf16_f32 v216,0,v216
v_cvt_pk_bf16_f32 v217,0,v217
v_cvt_pk_bf16_f32 v218,0,v218
v_cvt_pk_bf16_f32 v219,0,v219
v_cvt_pk_bf16_f32 v220,0,v220
v_cvt_pk_bf16_f32 v221,0,v221
v_cvt_pk_bf16_f32 v222,0,v222
v_cvt_pk_bf16_f32 v223,0,v223
v_mul_f32 v144,0xbfb8aa3b,v208
v_mul_f32 v145,0xbfb8aa3b,v209
v_mul_f32 v146,0xbfb8aa3b,v210
v_mul_f32 v147,0xbfb8aa3b,v211
s_waitcnt vmcnt(1)
s_barrier
v_mov_b32 v229,8
buffer_store_dword v229,off,s[88:91],s79 sc1
s_waitcnt vmcnt(1)
v_writelane_b32 v224,8,m0
v_cmp_le_u32 vcc,s81,v224
s_nop 1
s_andn2_b32 s85,vcc_lo,s84
s_and_b32 s85,s85,255
s_or_b32 s84,s84,s85
s_mov_b32 s99,s55
s_add_u32 s101,s55,1024
s_bitcmp1_b32 s85,0
s_cselect_b32 s26,s42,0
buffer_load_dwordx4 v[0:3],v255,s[24:27],s99 offen sc1
buffer_load_dwordx4 v[64:67],v255,s[24:27],s101 offen sc1
s_add_u32 s99,s99,1835008
s_add_u32 s101,s101,1835008
s_bitcmp1_b32 s85,1
s_cselect_b32 s26,s42,0
buffer_load_dwordx4 v[4:7],v255,s[24:27],s99 offen sc1
buffer_load_dwordx4 v[68:71],v255,s[24:27],s101 offen sc1
s_add_u32 s99,s99,1835008
s_add_u32 s101,s101,1835008
s_bitcmp1_b32 s85,2
s_cselect_b32 s26,s42,0
buffer_load_dwordx4 v[8:11],v255,s[24:27],s99 offen sc1
buffer_load_dwordx4 v[72:75],v255,s[24:27],s101 offen sc1
s_add_u32 s99,s99,1835008
s_add_u32 s101,s101,1835008
s_bitcmp1_b32 s85,3
s_cselect_b32 s26,s42,0
buffer_load_dwordx4 v[12:15],v255,s[24:27],s99 offen sc1
buffer_load_dwordx4 v[76:79],v255,s[24:27],s101 offen sc1
s_add_u32 s99,s99,1835008
s_add_u32 s101,s101,1835008
s_bitcmp1_b32 s85,4
s_cselect_b32 s26,s42,0
buffer_load_dwordx4 v[16:19],v255,s[24:27],s99 offen sc1
buffer_load_dwordx4 v[80:83],v255,s[24:27],s101 offen sc1
s_add_u32 s99,s99,1835008
s_add_u32 s101,s101,1835008
s_bitcmp1_b32 s85,5
s_cselect_b32 s26,s42,0
buffer_load_dwordx4 v[20:23],v255,s[24:27],s99 offen sc1
buffer_load_dwordx4 v[84:87],v255,s[24:27],s101 offen sc1
s_add_u32 s99,s99,1835008
s_add_u32 s101,s101,1835008
s_bitcmp1_b32 s85,6
s_cselect_b32 s26,s42,0
buffer_load_dwordx4 v[24:27],v255,s[24:27],s99 offen sc1
buffer_load_dwordx4 v[88:91],v255,s[24:27],s101 offen sc1
s_add_u32 s99,s99,1835008
s_add_u32 s101,s101,1835008
s_bitcmp1_b32 s85,7
s_cselect_b32 s26,s42,0
buffer_load_dwordx4 v[28:31],v255,s[24:27],s99 offen sc1
buffer_load_dwordx4 v[92:95],v255,s[24:27],s101 offen sc1
s_mov_b64 exec,-1
v_mul_f32 v148,0xbfb8aa3b,v212
v_mul_f32 v149,0xbfb8aa3b,v213
v_mul_f32 v150,0xbfb8aa3b,v214
v_mul_f32 v151,0xbfb8aa3b,v215
v_mul_f32 v152,0xbfb8aa3b,v216
v_mul_f32 v153,0xbfb8aa3b,v217
v_mul_f32 v154,0xbfb8aa3b,v218
v_mul_f32 v155,0xbfb8aa3b,v219
)"
R"(v_mul_f32 v156,0xbfb8aa3b,v220
v_mul_f32 v157,0xbfb8aa3b,v221
v_mul_f32 v158,0xbfb8aa3b,v222
v_mul_f32 v159,0xbfb8aa3b,v223
v_exp_f32 v144,v144
v_exp_f32 v145,v145
v_exp_f32 v146,v146
v_exp_f32 v147,v147
v_exp_f32 v148,v148
v_exp_f32 v149,v149
v_exp_f32 v150,v150
v_exp_f32 v151,v151
v_exp_f32 v152,v152
v_exp_f32 v153,v153
v_exp_f32 v154,v154
v_exp_f32 v155,v155
v_exp_f32 v156,v156
v_exp_f32 v157,v157
v_exp_f32 v158,v158
v_exp_f32 v159,v159
v_add_f32 v144,1.0,v144
v_add_f32 v145,1.0,v145
v_add_f32 v146,1.0,v146
v_add_f32 v147,1.0,v147
v_add_f32 v148,1.0,v148
v_add_f32 v149,1.0,v149
v_add_f32 v150,1.0,v150
v_add_f32 v151,1.0,v151
v_add_f32 v152,1.0,v152
v_add_f32 v153,1.0,v153
v_add_f32 v154,1.0,v154
v_add_f32 v155,1.0,v155
v_add_f32 v156,1.0,v156
v_add_f32 v157,1.0,v157
v_add_f32 v158,1.0,v158
v_add_f32 v159,1.0,v159
v_rcp_f32 v144,v144
v_rcp_f32 v145,v145
v_rcp_f32 v146,v146
v_rcp_f32 v147,v147
v_rcp_f32 v148,v148
v_rcp_f32 v149,v149
v_rcp_f32 v150,v150
v_rcp_f32 v151,v151
v_rcp_f32 v152,v152
v_rcp_f32 v153,v153
v_rcp_f32 v154,v154
v_rcp_f32 v155,v155
v_rcp_f32 v156,v156
v_rcp_f32 v157,v157
v_rcp_f32 v158,v158
v_rcp_f32 v159,v159
v_mul_f32 v208,v208,v144
v_mul_f32 v209,v209,v145
v_mul_f32 v210,v210,v146
v_mul_f32 v211,v211,v147
v_mul_f32 v212,v212,v148
v_mul_f32 v213,v213,v149
v_mul_f32 v214,v214,v150
v_mul_f32 v215,v215,v151
v_mul_f32 v216,v216,v152
v_mul_f32 v217,v217,v153
v_mul_f32 v218,v218,v154
v_mul_f32 v219,v219,v155
v_mul_f32 v220,v220,v156
v_mul_f32 v221,v221,v157
v_mul_f32 v222,v222,v158
v_mul_f32 v223,v223,v159
v_cvt_pk_bf16_f32 v208,0,v208
v_cvt_pk_bf16_f32 v209,0,v209
v_cvt_pk_bf16_f32 v210,0,v210
v_cvt_pk_bf16_f32 v211,0,v211
v_cvt_pk_bf16_f32 v212,0,v212
v_cvt_pk_bf16_f32 v213,0,v213
v_cvt_pk_bf16_f32 v214,0,v214
v_cvt_pk_bf16_f32 v215,0,v215
v_cvt_pk_bf16_f32 v216,0,v216
v_cvt_pk_bf16_f32 v217,0,v217
v_cvt_pk_bf16_f32 v218,0,v218
v_cvt_pk_bf16_f32 v219,0,v219
v_cvt_pk_bf16_f32 v220,0,v220
v_cvt_pk_bf16_f32 v221,0,v221
v_cvt_pk_bf16_f32 v222,0,v222
v_cvt_pk_bf16_f32 v223,0,v223
v_lshlrev_b32 v128,16,v232
v_and_b32 v129,0xffff0000,v232
v_lshlrev_b32 v130,16,v233
v_and_b32 v131,0xffff0000,v233
v_lshlrev_b32 v132,16,v234
v_and_b32 v133,0xffff0000,v234
v_lshlrev_b32 v134,16,v235
v_and_b32 v135,0xffff0000,v235
v_lshlrev_b32 v136,16,v236
v_and_b32 v137,0xffff0000,v236
v_lshlrev_b32 v138,16,v237
v_and_b32 v139,0xffff0000,v237
v_lshlrev_b32 v140,16,v238
v_and_b32 v141,0xffff0000,v238
v_lshlrev_b32 v142,16,v239
v_and_b32 v143,0xffff0000,v239
s_cmp_lg_u32 s97,0
s_cbranch_scc1 Lg4a_%=
Lg2a_%=:
s_cmp_lg_u32 s84,255
s_cbranch_scc1 Lf0a_%=
s_cmp_lg_u32 s97,0
s_cbranch_scc1 Lf3a_%=
Lf1a_%=:
s_waitcnt vmcnt(14)
v_dot2_f32_bf16 v192,v0,s98,v192
v_dot2_f32_bf16 v193,v0,s100,v193
v_dot2_f32_bf16 v194,v1,s98,v194
v_dot2_f32_bf16 v195,v1,s100,v195
v_dot2_f32_bf16 v200,v2,s98,v200
v_dot2_f32_bf16 v201,v2,s100,v201
v_dot2_f32_bf16 v202,v3,s98,v202
v_dot2_f32_bf16 v203,v3,s100,v203
v_dot2_f32_bf16 v196,v64,s98,v196
v_dot2_f32_bf16 v197,v64,s100,v197
v_dot2_f32_bf16 v198,v65,s98,v198
v_dot2_f32_bf16 v199,v65,s100,v199
v_dot2_f32_bf16 v204,v66,s98,v204
v_dot2_f32_bf16 v205,v66,s100,v205
v_dot2_f32_bf16 v206,v67,s98,v206
v_dot2_f32_bf16 v207,v67,s100,v207
s_waitcnt vmcnt(12)
v_dot2_f32_bf16 v192,v4,s98,v192
v_dot2_f32_bf16 v193,v4,s100,v193
v_dot2_f32_bf16 v194,v5,s98,v194
v_dot2_f32_bf16 v195,v5,s100,v195
v_dot2_f32_bf16 v200,v6,s98,v200
v_dot2_f32_bf16 v201,v6,s100,v201
v_dot2_f32_bf16 v202,v7,s98,v202
v_dot2_f32_bf16 v203,v7,s100,v203
v_dot2_f32_bf16 v196,v68,s98,v196
v_dot2_f32_bf16 v197,v68,s100,v197
v_dot2_f32_bf16 v198,v69,s98,v198
v_dot2_f32_bf16 v199,v69,s100,v199
v_dot2_f32_bf16 v204,v70,s98,v204
v_dot2_f32_bf16 v205,v70,s100,v205
v_dot2_f32_bf16 v206,v71,s98,v206
v_dot2_f32_bf16 v207,v71,s100,v207
s_waitcnt vmcnt(10)
v_dot2_f32_bf16 v192,v8,s98,v192
v_dot2_f32_bf16 v193,v8,s100,v193
v_dot2_f32_bf16 v194,v9,s98,v194
v_dot2_f32_bf16 v195,v9,s100,v195
v_dot2_f32_bf16 v200,v10,s98,v200
v_dot2_f32_bf16 v201,v10,s100,v201
v_dot2_f32_bf16 v202,v11,s98,v202
v_dot2_f32_bf16 v203,v11,s100,v203
v_dot2_f32_bf16 v196,v72,s98,v196
v_dot2_f32_bf16 v197,v72,s100,v197
v_dot2_f32_bf16 v198,v73,s98,v198
v_dot2_f32_bf16 v199,v73,s100,v199
v_dot2_f32_bf16 v204,v74,s98,v204
v_dot2_f32_bf16 v205,v74,s100,v205
v_dot2_f32_bf16 v206,v75,s98,v206
v_dot2_f32_bf16 v207,v75,s100,v207
s_waitcnt vmcnt(8)
v_dot2_f32_bf16 v192,v12,s98,v192
v_dot2_f32_bf16 v193,v12,s100,v193
v_dot2_f32_bf16 v194,v13,s98,v194
v_dot2_f32_bf16 v195,v13,s100,v195
v_dot2_f32_bf16 v200,v14,s98,v200
v_dot2_f32_bf16 v201,v14,s100,v201
v_dot2_f32_bf16 v202,v15,s98,v202
v_dot2_f32_bf16 v203,v15,s100,v203
v_dot2_f32_bf16 v196,v76,s98,v196
v_dot2_f32_bf16 v197,v76,s100,v197
v_dot2_f32_bf16 v198,v77,s98,v198
v_dot2_f32_bf16 v199,v77,s100,v199
v_dot2_f32_bf16 v204,v78,s98,v204
v_dot2_f32_bf16 v205,v78,s100,v205
v_dot2_f32_bf16 v206,v79,s98,v206
v_dot2_f32_bf16 v207,v79,s100,v207
s_waitcnt vmcnt(6)
v_dot2_f32_bf16 v192,v16,s98,v192
v_dot2_f32_bf16 v193,v16,s100,v193
v_dot2_f32_bf16 v194,v17,s98,v194
v_dot2_f32_bf16 v195,v17,s100,v195
v_dot2_f32_bf16 v200,v18,s98,v200
v_dot2_f32_bf16 v201,v18,s100,v201
v_dot2_f32_bf16 v202,v19,s98,v202
v_dot2_f32_bf16 v203,v19,s100,v203
v_dot2_f32_bf16 v196,v80,s98,v196
v_dot2_f32_bf16 v197,v80,s100,v197
v_dot2_f32_bf16 v198,v81,s98,v198
v_dot2_f32_bf16 v199,v81,s100,v199
v_dot2_f32_bf16 v204,v82,s98,v204
v_dot2_f32_bf16 v205,v82,s100,v205
v_dot2_f32_bf16 v206,v83,s98,v206
v_dot2_f32_bf16 v207,v83,s100,v207
s_waitcnt vmcnt(4)
v_dot2_f32_bf16 v192,v20,s98,v192
v_dot2_f32_bf16 v193,v20,s100,v193
v_dot2_f32_bf16 v194,v21,s98,v194
v_dot2_f32_bf16 v195,v21,s100,v195
v_dot2_f32_bf16 v200,v22,s98,v200
v_dot2_f32_bf16 v201,v22,s100,v201
)"
R"(v_dot2_f32_bf16 v202,v23,s98,v202
v_dot2_f32_bf16 v203,v23,s100,v203
v_dot2_f32_bf16 v196,v84,s98,v196
v_dot2_f32_bf16 v197,v84,s100,v197
v_dot2_f32_bf16 v198,v85,s98,v198
v_dot2_f32_bf16 v199,v85,s100,v199
v_dot2_f32_bf16 v204,v86,s98,v204
v_dot2_f32_bf16 v205,v86,s100,v205
v_dot2_f32_bf16 v206,v87,s98,v206
v_dot2_f32_bf16 v207,v87,s100,v207
s_waitcnt vmcnt(2)
v_dot2_f32_bf16 v192,v24,s98,v192
v_dot2_f32_bf16 v193,v24,s100,v193
v_dot2_f32_bf16 v194,v25,s98,v194
v_dot2_f32_bf16 v195,v25,s100,v195
v_dot2_f32_bf16 v200,v26,s98,v200
v_dot2_f32_bf16 v201,v26,s100,v201
v_dot2_f32_bf16 v202,v27,s98,v202
v_dot2_f32_bf16 v203,v27,s100,v203
v_dot2_f32_bf16 v196,v88,s98,v196
v_dot2_f32_bf16 v197,v88,s100,v197
v_dot2_f32_bf16 v198,v89,s98,v198
v_dot2_f32_bf16 v199,v89,s100,v199
v_dot2_f32_bf16 v204,v90,s98,v204
v_dot2_f32_bf16 v205,v90,s100,v205
v_dot2_f32_bf16 v206,v91,s98,v206
v_dot2_f32_bf16 v207,v91,s100,v207
s_waitcnt vmcnt(0)
v_dot2_f32_bf16 v192,v28,s98,v192
v_dot2_f32_bf16 v193,v28,s100,v193
v_dot2_f32_bf16 v194,v29,s98,v194
v_dot2_f32_bf16 v195,v29,s100,v195
v_dot2_f32_bf16 v200,v30,s98,v200
v_dot2_f32_bf16 v201,v30,s100,v201
v_dot2_f32_bf16 v202,v31,s98,v202
v_dot2_f32_bf16 v203,v31,s100,v203
v_dot2_f32_bf16 v196,v92,s98,v196
v_dot2_f32_bf16 v197,v92,s100,v197
v_dot2_f32_bf16 v198,v93,s98,v198
v_dot2_f32_bf16 v199,v93,s100,v199
v_dot2_f32_bf16 v204,v94,s98,v204
v_dot2_f32_bf16 v205,v94,s100,v205
v_dot2_f32_bf16 v206,v95,s98,v206
v_dot2_f32_bf16 v207,v95,s100,v207
v_cvt_pk_bf16_f32 v192,0,v192
v_cvt_pk_bf16_f32 v193,0,v193
v_cvt_pk_bf16_f32 v194,0,v194
v_cvt_pk_bf16_f32 v195,0,v195
v_cvt_pk_bf16_f32 v196,0,v196
v_cvt_pk_bf16_f32 v197,0,v197
v_cvt_pk_bf16_f32 v198,0,v198
v_cvt_pk_bf16_f32 v199,0,v199
v_cvt_pk_bf16_f32 v200,0,v200
v_cvt_pk_bf16_f32 v201,0,v201
v_cvt_pk_bf16_f32 v202,0,v202
v_cvt_pk_bf16_f32 v203,0,v203
v_cvt_pk_bf16_f32 v204,0,v204
v_cvt_pk_bf16_f32 v205,0,v205
v_cvt_pk_bf16_f32 v206,0,v206
v_cvt_pk_bf16_f32 v207,0,v207
v_mul_f32 v192,v192,v208
v_mul_f32 v193,v193,v209
v_mul_f32 v194,v194,v210
v_mul_f32 v195,v195,v211
v_mul_f32 v196,v196,v212
v_mul_f32 v197,v197,v213
v_mul_f32 v198,v198,v214
v_mul_f32 v199,v199,v215
v_mul_f32 v200,v200,v216
v_mul_f32 v201,v201,v217
v_mul_f32 v202,v202,v218
v_mul_f32 v203,v203,v219
v_mul_f32 v204,v204,v220
v_mul_f32 v205,v205,v221
v_mul_f32 v206,v206,v222
v_mul_f32 v207,v207,v223
v_cvt_pk_bf16_f32 v192,v192,v193
v_cvt_pk_bf16_f32 v193,v194,v195
v_cvt_pk_bf16_f32 v194,v196,v197
v_cvt_pk_bf16_f32 v195,v198,v199
v_cvt_pk_bf16_f32 v196,v200,v201
v_cvt_pk_bf16_f32 v197,v202,v203
v_cvt_pk_bf16_f32 v198,v204,v205
v_cvt_pk_bf16_f32 v199,v206,v207
ds_write_b64 v228,v[192:193]
ds_write_b64 v230,v[194:195]
ds_write_b64 v228,v[196:197] offset:1024
ds_write_b64 v230,v[198:199] offset:1024
s_waitcnt lgkmcnt(0)
ds_read_b128 v[160:163],v225
ds_read_b128 v[164:167],v225 offset:1024
s_waitcnt lgkmcnt(0)
v_lshlrev_b32 v144,16,v160
v_and_b32 v145,0xffff0000,v160
v_lshlrev_b32 v146,16,v161
v_and_b32 v147,0xffff0000,v161
v_lshlrev_b32 v148,16,v162
v_and_b32 v149,0xffff0000,v162
v_lshlrev_b32 v150,16,v163
v_and_b32 v151,0xffff0000,v163
v_lshlrev_b32 v152,16,v164
v_and_b32 v153,0xffff0000,v164
v_lshlrev_b32 v154,16,v165
v_and_b32 v155,0xffff0000,v165
v_lshlrev_b32 v156,16,v166
v_and_b32 v157,0xffff0000,v166
v_lshlrev_b32 v158,16,v167
v_and_b32 v159,0xffff0000,v167
v_add_f32 v128,v128,v144
v_add_f32 v129,v129,v145
v_add_f32 v130,v130,v146
v_add_f32 v131,v131,v147
v_add_f32 v132,v132,v148
v_add_f32 v133,v133,v149
v_add_f32 v134,v134,v150
v_add_f32 v135,v135,v151
v_add_f32 v136,v136,v152
v_add_f32 v137,v137,v153
v_add_f32 v138,v138,v154
v_add_f32 v139,v139,v155
v_add_f32 v140,v140,v156
v_add_f32 v141,v141,v157
v_add_f32 v142,v142,v158
v_add_f32 v143,v143,v159
v_cvt_pk_bf16_f32 v128,v128,v129
v_cvt_pk_bf16_f32 v129,v130,v131
v_cvt_pk_bf16_f32 v130,v132,v133
v_cvt_pk_bf16_f32 v131,v134,v135
v_cvt_pk_bf16_f32 v132,v136,v137
v_cvt_pk_bf16_f32 v133,v138,v139
v_cvt_pk_bf16_f32 v134,v140,v141
v_cvt_pk_bf16_f32 v135,v142,v143
s_add_u32 s101,s82,14336
buffer_store_dwordx4 v[128:131],v227,s[48:51],s82 offen sc1
buffer_store_dwordx4 v[132:135],v227,s[48:51],s101 offen sc1
s_branch Lenda_%=
Lstub0a_%=:
s_getpc_b64 s[76:77]
s_add_u32 s76,s76,12
s_addc_u32 s77,s77,0
s_branch Lrare0a_%=
s_branch Lback0a_%=
Lstub1a_%=:
s_getpc_b64 s[76:77]
s_add_u32 s76,s76,12
s_addc_u32 s77,s77,0
s_branch Lrare1a_%=
s_branch Lback1a_%=
Lf1la_%=:
s_lshr_b32 s97,%[tb],2
s_lshl_b32 s97,s97,10
s_add_u32 s97,s97,0x2b00000
v_mbcnt_lo_u32_b32 v254,-1,0
v_mbcnt_hi_u32_b32 v254,-1,v254
v_and_b32 v254,7,v254
v_lshlrev_b32 v254,7,v254
buffer_load_dword v254,v254,s[88:91],s97 offen sc1
s_branch Lloopa_%=
Lstub2a_%=:
s_getpc_b64 s[76:77]
s_add_u32 s76,s76,12
s_addc_u32 s77,s77,0
s_branch Lrare0a_%=
s_branch Lback2a_%=
Lrolea_%=:
s_lshr_b32 s99,%[h],1
s_and_b32 s101,s99,6
s_lshr_b32 s101,s84,s101
s_andn2_b32 s101,s99,s101
s_and_b32 s101,s101,1
s_cbranch_scc0 Lrwa_%=
s_waitcnt vmcnt(0) lgkmcnt(0)
s_barrier
s_load_dwordx2 s[76:77],%[ka],0x10
s_load_dwordx2 s[56:57],%[ka],0x20
s_load_dwordx2 s[60:61],%[ka],0x28
s_mov_b64 s[72:73],%[x]
s_lshl_b32 s101,s99,10
s_lshr_b32 s99,s99,1
s_mul_i32 s99,s99,896
s_waitcnt lgkmcnt(0)
s_add_u32 s76,s76,s101
s_addc_u32 s77,s77,0
s_add_u32 s56,s56,s99
s_addc_u32 s57,s57,0
s_add_u32 s60,s60,s99
s_addc_u32 s61,s61,0
s_mov_b32 s100,0
v_mov_b32 v63,3
s_branch Lre_%=
Lrwa_%=:
s_mov_b32 s74,1000000
Lrwba_%=:
s_sleep 8
v_mbcnt_lo_u32_b32 v255,-1,0
v_mbcnt_hi_u32_b32 v255,-1,v255
v_and_b32 v255,7,v255
v_lshlrev_b32 v255,7,v255
buffer_load_dword v255,v255,s[88:91],s97 offen offset:4 sc1
s_waitcnt vmcnt(0)
)"
R"(v_cmp_ne_u32 s[84:85],0,v255
s_nop 0
s_lshr_b32 s85,s84,1
s_or_b32 s84,s84,s85
s_and_b32 s84,s84,85
s_cmp_lg_u32 s84,85
s_cbranch_scc0 Lok3a_%=
s_sub_u32 s74,s74,1
s_cmp_lg_u32 s74,0
s_cbranch_scc1 Lrwba_%=
s_branch Lok3a_%=
Lspin1a_%=:
s_mov_b32 s74,1000000
Lspin1ba_%=:
s_sleep 4
v_mbcnt_lo_u32_b32 v254,-1,0
v_mbcnt_hi_u32_b32 v254,-1,v254
v_and_b32 v254,7,v254
v_lshlrev_b32 v254,7,v254
buffer_load_dword v254,v254,s[88:91],s97 offen sc1
s_waitcnt vmcnt(0)
v_cmp_eq_u32 vcc,v254,v255
s_nop 1
s_cbranch_vccz Lok1a_%=
s_sub_u32 s74,s74,1
s_cmp_lg_u32 s74,0
s_cbranch_scc1 Lspin1ba_%=
s_branch Lok1a_%=
Lstub3a_%=:
s_getpc_b64 s[76:77]
s_add_u32 s76,s76,12
s_addc_u32 s77,s77,0
s_branch Lrare1a_%=
s_branch Lback3a_%=
Lg1a_%=:
buffer_load_dword v224,v226,s[88:91],s80 offen sc1
s_waitcnt vmcnt(0)
Lg4a_%=:
v_cmp_gt_u32 vcc,s52,v224
s_nop 0
s_and_b32 s97,vcc_lo,0xff
s_cbranch_scc0 Lg3a_%=
v_cmp_le_u32 vcc,s81,v224
s_nop 1
s_andn2_b32 s85,vcc_lo,s84
s_and_b32 s85,s85,255
s_or_b32 s84,s84,s85
s_mov_b32 s99,s55
s_add_u32 s101,s55,1024
s_bitcmp1_b32 s85,0
s_cselect_b64 exec,-1,0
buffer_load_dwordx4 v[0:3],v255,s[40:43],s99 offen sc1
buffer_load_dwordx4 v[64:67],v255,s[40:43],s101 offen sc1
s_add_u32 s99,s99,1835008
s_add_u32 s101,s101,1835008
s_bitcmp1_b32 s85,1
s_cselect_b64 exec,-1,0
buffer_load_dwordx4 v[4:7],v255,s[40:43],s99 offen sc1
buffer_load_dwordx4 v[68:71],v255,s[40:43],s101 offen sc1
s_add_u32 s99,s99,1835008
s_add_u32 s101,s101,1835008
s_bitcmp1_b32 s85,2
s_cselect_b64 exec,-1,0
buffer_load_dwordx4 v[8:11],v255,s[40:43],s99 offen sc1
buffer_load_dwordx4 v[72:75],v255,s[40:43],s101 offen sc1
s_add_u32 s99,s99,1835008
s_add_u32 s101,s101,1835008
s_bitcmp1_b32 s85,3
s_cselect_b64 exec,-1,0
buffer_load_dwordx4 v[12:15],v255,s[40:43],s99 offen sc1
buffer_load_dwordx4 v[76:79],v255,s[40:43],s101 offen sc1
s_add_u32 s99,s99,1835008
s_add_u32 s101,s101,1835008
s_bitcmp1_b32 s85,4
s_cselect_b64 exec,-1,0
buffer_load_dwordx4 v[16:19],v255,s[40:43],s99 offen sc1
buffer_load_dwordx4 v[80:83],v255,s[40:43],s101 offen sc1
s_add_u32 s99,s99,1835008
s_add_u32 s101,s101,1835008
s_bitcmp1_b32 s85,5
s_cselect_b64 exec,-1,0
buffer_load_dwordx4 v[20:23],v255,s[40:43],s99 offen sc1
buffer_load_dwordx4 v[84:87],v255,s[40:43],s101 offen sc1
s_add_u32 s99,s99,1835008
s_add_u32 s101,s101,1835008
s_bitcmp1_b32 s85,6
s_cselect_b64 exec,-1,0
buffer_load_dwordx4 v[24:27],v255,s[40:43],s99 offen sc1
buffer_load_dwordx4 v[88:91],v255,s[40:43],s101 offen sc1
s_add_u32 s99,s99,1835008
s_add_u32 s101,s101,1835008
s_bitcmp1_b32 s85,7
s_cselect_b64 exec,-1,0
buffer_load_dwordx4 v[28:31],v255,s[40:43],s99 offen sc1
buffer_load_dwordx4 v[92:95],v255,s[40:43],s101 offen sc1
s_mov_b64 exec,-1
s_sub_u32 s74,s74,1
s_cmp_lg_u32 s74,0
s_cbranch_scc1 Lg1a_%=
Lg3a_%=:
s_add_u32 s60,s83,0
s_add_u32 s61,s60,4096
buffer_load_dwordx4 v[96:99],v255,s[40:43],s60 offen sc1
buffer_load_dwordx4 v[100:103],v255,s[40:43],s61 offen sc1
s_add_u32 s60,s83,114688
s_add_u32 s61,s60,4096
buffer_load_dwordx4 v[104:107],v255,s[40:43],s60 offen sc1
buffer_load_dwordx4 v[108:111],v255,s[40:43],s61 offen sc1
s_add_u32 s60,s83,229376
s_add_u32 s61,s60,4096
buffer_load_dwordx4 v[112:115],v255,s[40:43],s60 offen sc1
buffer_load_dwordx4 v[116:119],v255,s[40:43],s61 offen sc1
s_add_u32 s60,s83,344064
s_add_u32 s61,s60,4096
buffer_load_dwordx4 v[120:123],v255,s[40:43],s60 offen sc1
buffer_load_dwordx4 v[124:127],v255,s[40:43],s61 offen sc1
s_waitcnt vmcnt(0)
v_cmp_le_u32 vcc,s81,v224
s_nop 1
s_andn2_b32 s85,vcc_lo,s84
s_and_b32 s85,s85,255
s_or_b32 s84,s84,s85
s_mov_b32 s99,s55
s_add_u32 s101,s55,1024
s_bitcmp1_b32 s85,0
s_cselect_b64 exec,-1,0
buffer_load_dwordx4 v[0:3],v255,s[40:43],s99 offen sc1
buffer_load_dwordx4 v[64:67],v255,s[40:43],s101 offen sc1
s_add_u32 s99,s99,1835008
s_add_u32 s101,s101,1835008
s_bitcmp1_b32 s85,1
s_cselect_b64 exec,-1,0
buffer_load_dwordx4 v[4:7],v255,s[40:43],s99 offen sc1
buffer_load_dwordx4 v[68:71],v255,s[40:43],s101 offen sc1
s_add_u32 s99,s99,1835008
s_add_u32 s101,s101,1835008
s_bitcmp1_b32 s85,2
s_cselect_b64 exec,-1,0
buffer_load_dwordx4 v[8:11],v255,s[40:43],s99 offen sc1
buffer_load_dwordx4 v[72:75],v255,s[40:43],s101 offen sc1
s_add_u32 s99,s99,1835008
s_add_u32 s101,s101,1835008
s_bitcmp1_b32 s85,3
s_cselect_b64 exec,-1,0
buffer_load_dwordx4 v[12:15],v255,s[40:43],s99 offen sc1
buffer_load_dwordx4 v[76:79],v255,s[40:43],s101 offen sc1
s_add_u32 s99,s99,1835008
s_add_u32 s101,s101,1835008
s_bitcmp1_b32 s85,4
s_cselect_b64 exec,-1,0
buffer_load_dwordx4 v[16:19],v255,s[40:43],s99 offen sc1
buffer_load_dwordx4 v[80:83],v255,s[40:43],s101 offen sc1
s_add_u32 s99,s99,1835008
s_add_u32 s101,s101,1835008
s_bitcmp1_b32 s85,5
s_cselect_b64 exec,-1,0
buffer_load_dwordx4 v[20:23],v255,s[40:43],s99 offen sc1
buffer_load_dwordx4 v[84:87],v255,s[40:43],s101 offen sc1
s_add_u32 s99,s99,1835008
s_add_u32 s101,s101,1835008
s_bitcmp1_b32 s85,6
s_cselect_b64 exec,-1,0
buffer_load_dwordx4 v[24:27],v255,s[40:43],s99 offen sc1
buffer_load_dwordx4 v[88:91],v255,s[40:43],s101 offen sc1
s_add_u32 s99,s99,1835008
s_add_u32 s101,s101,1835008
s_bitcmp1_b32 s85,7
s_cselect_b64 exec,-1,0
buffer_load_dwordx4 v[28:31],v255,s[40:43],s99 offen sc1
buffer_load_dwordx4 v[92:95],v255,s[40:43],s101 offen sc1
s_mov_b64 exec,-1
v_mov_b32 v208,0
v_mov_b32 v209,0
v_mov_b32 v210,0
v_mov_b32 v211,0
v_mov_b32 v212,0
v_mov_b32 v213,0
v_mov_b32 v214,0
v_mov_b32 v215,0
v_mov_b32 v216,0
v_mov_b32 v217,0
v_mov_b32 v218,0
v_mov_b32 v219,0
v_mov_b32 v220,0
v_mov_b32 v221,0
v_mov_b32 v222,0
v_mov_b32 v223,0
v_dot2_f32_bf16 v208,v96,s98,v208
v_dot2_f32_bf16 v209,v96,s100,v209
v_dot2_f32_bf16 v210,v97,s98,v210
v_dot2_f32_bf16 v211,v97,s100,v211
v_dot2_f32_bf16 v216,v98,s98,v216
v_dot2_f32_bf16 v217,v98,s100,v217
v_dot2_f32_bf16 v218,v99,s98,v218
v_dot2_f32_bf16 v219,v99,s100,v219
v_dot2_f32_bf16 v212,v100,s98,v212
v_dot2_f32_bf16 v213,v100,s100,v213
v_dot2_f32_bf16 v214,v101,s98,v214
v_dot2_f32_bf16 v215,v101,s100,v215
v_dot2_f32_bf16 v220,v102,s98,v220
)"
R"(v_dot2_f32_bf16 v221,v102,s100,v221
v_dot2_f32_bf16 v222,v103,s98,v222
v_dot2_f32_bf16 v223,v103,s100,v223
v_dot2_f32_bf16 v208,v104,s98,v208
v_dot2_f32_bf16 v209,v104,s100,v209
v_dot2_f32_bf16 v210,v105,s98,v210
v_dot2_f32_bf16 v211,v105,s100,v211
v_dot2_f32_bf16 v216,v106,s98,v216
v_dot2_f32_bf16 v217,v106,s100,v217
v_dot2_f32_bf16 v218,v107,s98,v218
v_dot2_f32_bf16 v219,v107,s100,v219
v_dot2_f32_bf16 v212,v108,s98,v212
v_dot2_f32_bf16 v213,v108,s100,v213
v_dot2_f32_bf16 v214,v109,s98,v214
v_dot2_f32_bf16 v215,v109,s100,v215
v_dot2_f32_bf16 v220,v110,s98,v220
v_dot2_f32_bf16 v221,v110,s100,v221
v_dot2_f32_bf16 v222,v111,s98,v222
v_dot2_f32_bf16 v223,v111,s100,v223
v_dot2_f32_bf16 v208,v112,s98,v208
v_dot2_f32_bf16 v209,v112,s100,v209
v_dot2_f32_bf16 v210,v113,s98,v210
v_dot2_f32_bf16 v211,v113,s100,v211
v_dot2_f32_bf16 v216,v114,s98,v216
v_dot2_f32_bf16 v217,v114,s100,v217
v_dot2_f32_bf16 v218,v115,s98,v218
v_dot2_f32_bf16 v219,v115,s100,v219
v_dot2_f32_bf16 v212,v116,s98,v212
v_dot2_f32_bf16 v213,v116,s100,v213
v_dot2_f32_bf16 v214,v117,s98,v214
v_dot2_f32_bf16 v215,v117,s100,v215
v_dot2_f32_bf16 v220,v118,s98,v220
v_dot2_f32_bf16 v221,v118,s100,v221
v_dot2_f32_bf16 v222,v119,s98,v222
v_dot2_f32_bf16 v223,v119,s100,v223
v_dot2_f32_bf16 v208,v120,s98,v208
v_dot2_f32_bf16 v209,v120,s100,v209
v_dot2_f32_bf16 v210,v121,s98,v210
v_dot2_f32_bf16 v211,v121,s100,v211
v_dot2_f32_bf16 v216,v122,s98,v216
v_dot2_f32_bf16 v217,v122,s100,v217
v_dot2_f32_bf16 v218,v123,s98,v218
v_dot2_f32_bf16 v219,v123,s100,v219
v_dot2_f32_bf16 v212,v124,s98,v212
v_dot2_f32_bf16 v213,v124,s100,v213
v_dot2_f32_bf16 v214,v125,s98,v214
v_dot2_f32_bf16 v215,v125,s100,v215
v_dot2_f32_bf16 v220,v126,s98,v220
v_dot2_f32_bf16 v221,v126,s100,v221
v_dot2_f32_bf16 v222,v127,s98,v222
v_dot2_f32_bf16 v223,v127,s100,v223
v_cvt_pk_bf16_f32 v208,0,v208
v_cvt_pk_bf16_f32 v209,0,v209
v_cvt_pk_bf16_f32 v210,0,v210
v_cvt_pk_bf16_f32 v211,0,v211
v_cvt_pk_bf16_f32 v212,0,v212
v_cvt_pk_bf16_f32 v213,0,v213
v_cvt_pk_bf16_f32 v214,0,v214
v_cvt_pk_bf16_f32 v215,0,v215
v_cvt_pk_bf16_f32 v216,0,v216
v_cvt_pk_bf16_f32 v217,0,v217
v_cvt_pk_bf16_f32 v218,0,v218
v_cvt_pk_bf16_f32 v219,0,v219
v_cvt_pk_bf16_f32 v220,0,v220
v_cvt_pk_bf16_f32 v221,0,v221
v_cvt_pk_bf16_f32 v222,0,v222
v_cvt_pk_bf16_f32 v223,0,v223
v_mul_f32 v144,0xbfb8aa3b,v208
v_mul_f32 v145,0xbfb8aa3b,v209
v_mul_f32 v146,0xbfb8aa3b,v210
v_mul_f32 v147,0xbfb8aa3b,v211
v_mul_f32 v148,0xbfb8aa3b,v212
v_mul_f32 v149,0xbfb8aa3b,v213
v_mul_f32 v150,0xbfb8aa3b,v214
v_mul_f32 v151,0xbfb8aa3b,v215
v_mul_f32 v152,0xbfb8aa3b,v216
v_mul_f32 v153,0xbfb8aa3b,v217
v_mul_f32 v154,0xbfb8aa3b,v218
v_mul_f32 v155,0xbfb8aa3b,v219
v_mul_f32 v156,0xbfb8aa3b,v220
v_mul_f32 v157,0xbfb8aa3b,v221
v_mul_f32 v158,0xbfb8aa3b,v222
v_mul_f32 v159,0xbfb8aa3b,v223
v_exp_f32 v144,v144
v_exp_f32 v145,v145
v_exp_f32 v146,v146
v_exp_f32 v147,v147
v_exp_f32 v148,v148
v_exp_f32 v149,v149
v_exp_f32 v150,v150
v_exp_f32 v151,v151
v_exp_f32 v152,v152
v_exp_f32 v153,v153
v_exp_f32 v154,v154
v_exp_f32 v155,v155
v_exp_f32 v156,v156
v_exp_f32 v157,v157
v_exp_f32 v158,v158
v_exp_f32 v159,v159
v_add_f32 v144,1.0,v144
v_add_f32 v145,1.0,v145
v_add_f32 v146,1.0,v146
v_add_f32 v147,1.0,v147
v_add_f32 v148,1.0,v148
v_add_f32 v149,1.0,v149
v_add_f32 v150,1.0,v150
v_add_f32 v151,1.0,v151
v_add_f32 v152,1.0,v152
v_add_f32 v153,1.0,v153
v_add_f32 v154,1.0,v154
v_add_f32 v155,1.0,v155
v_add_f32 v156,1.0,v156
v_add_f32 v157,1.0,v157
v_add_f32 v158,1.0,v158
v_add_f32 v159,1.0,v159
v_rcp_f32 v144,v144
v_rcp_f32 v145,v145
v_rcp_f32 v146,v146
v_rcp_f32 v147,v147
v_rcp_f32 v148,v148
v_rcp_f32 v149,v149
v_rcp_f32 v150,v150
v_rcp_f32 v151,v151
v_rcp_f32 v152,v152
v_rcp_f32 v153,v153
v_rcp_f32 v154,v154
v_rcp_f32 v155,v155
v_rcp_f32 v156,v156
v_rcp_f32 v157,v157
v_rcp_f32 v158,v158
v_rcp_f32 v159,v159
v_mul_f32 v208,v208,v144
v_mul_f32 v209,v209,v145
v_mul_f32 v210,v210,v146
v_mul_f32 v211,v211,v147
v_mul_f32 v212,v212,v148
v_mul_f32 v213,v213,v149
v_mul_f32 v214,v214,v150
v_mul_f32 v215,v215,v151
v_mul_f32 v216,v216,v152
v_mul_f32 v217,v217,v153
v_mul_f32 v218,v218,v154
v_mul_f32 v219,v219,v155
v_mul_f32 v220,v220,v156
v_mul_f32 v221,v221,v157
v_mul_f32 v222,v222,v158
v_mul_f32 v223,v223,v159
v_cvt_pk_bf16_f32 v208,0,v208
v_cvt_pk_bf16_f32 v209,0,v209
v_cvt_pk_bf16_f32 v210,0,v210
v_cvt_pk_bf16_f32 v211,0,v211
v_cvt_pk_bf16_f32 v212,0,v212
v_cvt_pk_bf16_f32 v213,0,v213
v_cvt_pk_bf16_f32 v214,0,v214
v_cvt_pk_bf16_f32 v215,0,v215
v_cvt_pk_bf16_f32 v216,0,v216
v_cvt_pk_bf16_f32 v217,0,v217
v_cvt_pk_bf16_f32 v218,0,v218
v_cvt_pk_bf16_f32 v219,0,v219
v_cvt_pk_bf16_f32 v220,0,v220
v_cvt_pk_bf16_f32 v221,0,v221
v_cvt_pk_bf16_f32 v222,0,v222
v_cvt_pk_bf16_f32 v223,0,v223
s_mov_b32 s97,1
s_branch Lg2a_%=
Lf0a_%=:
buffer_load_dword v224,v226,s[88:91],s80 offen sc1
s_waitcnt vmcnt(0)
v_cmp_le_u32 vcc,s81,v224
s_nop 1
s_andn2_b32 s85,vcc_lo,s84
s_and_b32 s85,s85,255
s_or_b32 s84,s84,s85
s_mov_b32 s99,s55
s_add_u32 s101,s55,1024
s_bitcmp1_b32 s85,0
s_cselect_b64 exec,-1,0
buffer_load_dwordx4 v[0:3],v255,s[40:43],s99 offen sc1
buffer_load_dwordx4 v[64:67],v255,s[40:43],s101 offen sc1
s_add_u32 s99,s99,1835008
s_add_u32 s101,s101,1835008
s_bitcmp1_b32 s85,1
s_cselect_b64 exec,-1,0
buffer_load_dwordx4 v[4:7],v255,s[40:43],s99 offen sc1
buffer_load_dwordx4 v[68:71],v255,s[40:43],s101 offen sc1
s_add_u32 s99,s99,1835008
s_add_u32 s101,s101,1835008
s_bitcmp1_b32 s85,2
s_cselect_b64 exec,-1,0
buffer_load_dwordx4 v[8:11],v255,s[40:43],s99 offen sc1
buffer_load_dwordx4 v[72:75],v255,s[40:43],s101 offen sc1
s_add_u32 s99,s99,1835008
s_add_u32 s101,s101,1835008
s_bitcmp1_b32 s85,3
s_cselect_b64 exec,-1,0
buffer_load_dwordx4 v[12:15],v255,s[40:43],s99 offen sc1
buffer_load_dwordx4 v[76:79],v255,s[40:43],s101 offen sc1
s_add_u32 s99,s99,1835008
s_add_u32 s101,s101,1835008
s_bitcmp1_b32 s85,4
)"
R"(s_cselect_b64 exec,-1,0
buffer_load_dwordx4 v[16:19],v255,s[40:43],s99 offen sc1
buffer_load_dwordx4 v[80:83],v255,s[40:43],s101 offen sc1
s_add_u32 s99,s99,1835008
s_add_u32 s101,s101,1835008
s_bitcmp1_b32 s85,5
s_cselect_b64 exec,-1,0
buffer_load_dwordx4 v[20:23],v255,s[40:43],s99 offen sc1
buffer_load_dwordx4 v[84:87],v255,s[40:43],s101 offen sc1
s_add_u32 s99,s99,1835008
s_add_u32 s101,s101,1835008
s_bitcmp1_b32 s85,6
s_cselect_b64 exec,-1,0
buffer_load_dwordx4 v[24:27],v255,s[40:43],s99 offen sc1
buffer_load_dwordx4 v[88:91],v255,s[40:43],s101 offen sc1
s_add_u32 s99,s99,1835008
s_add_u32 s101,s101,1835008
s_bitcmp1_b32 s85,7
s_cselect_b64 exec,-1,0
buffer_load_dwordx4 v[28:31],v255,s[40:43],s99 offen sc1
buffer_load_dwordx4 v[92:95],v255,s[40:43],s101 offen sc1
s_mov_b64 exec,-1
s_sub_u32 s74,s74,1
s_cmp_lt_i32 s74,1
s_cbranch_scc1 Lf3a_%=
s_cmp_lg_u32 s84,255
s_cbranch_scc1 Lf0a_%=
Lf3a_%=:
s_waitcnt vmcnt(0)
s_branch Lf1a_%=
Lrare0a_%=:
s_nop 7
v_max3_f32 v160,v128,v129,v130
v_max3_f32 v161,v136,v137,v138
v_max3_f32 v162,v144,v145,v146
v_max3_f32 v163,v152,v153,v154
v_max3_f32 v160,v131,v132,v160
v_max3_f32 v161,v139,v140,v161
v_max3_f32 v162,v147,v148,v162
v_max3_f32 v163,v155,v156,v163
v_max3_f32 v160,v133,v134,v160
v_max3_f32 v161,v141,v142,v161
v_max3_f32 v162,v149,v150,v162
v_max3_f32 v163,v157,v158,v163
v_max_f32 v160,v135,v160
v_max_f32 v161,v143,v161
v_max_f32 v162,v151,v162
v_max_f32 v163,v159,v163
v_mov_b32 v164,v160
v_mov_b32 v165,v161
v_mov_b32 v166,v162
v_mov_b32 v167,v163
v_permlane16_swap_b32 v160,v164
v_permlane16_swap_b32 v161,v165
v_permlane16_swap_b32 v162,v166
v_permlane16_swap_b32 v163,v167
v_max_f32 v160,v160,v164
v_max_f32 v161,v161,v165
v_max_f32 v162,v162,v166
v_max_f32 v163,v163,v167
v_mov_b32 v164,v160
v_mov_b32 v165,v161
v_mov_b32 v166,v162
v_mov_b32 v167,v163
v_permlane32_swap_b32 v160,v164
v_permlane32_swap_b32 v161,v165
v_permlane32_swap_b32 v162,v166
v_permlane32_swap_b32 v163,v167
v_max_f32 v160,v160,v164
v_max_f32 v161,v161,v165
v_max_f32 v162,v162,v166
v_max_f32 v163,v163,v167
v_add_f32 v160,0x42c00000,v160
v_max_f32 v160,0,v160
v_sub_f32 v244,v244,v160
v_exp_f32 v164,-v160
v_sub_f32 v128,v128,v160
v_sub_f32 v129,v129,v160
v_sub_f32 v130,v130,v160
v_sub_f32 v131,v131,v160
v_sub_f32 v132,v132,v160
v_sub_f32 v133,v133,v160
v_sub_f32 v134,v134,v160
v_sub_f32 v135,v135,v160
v_mul_f32 v224,v224,v164
v_mul_f32 v225,v225,v164
v_mul_f32 v226,v226,v164
v_mul_f32 v227,v227,v164
v_accvgpr_read_b32 v168,a0
v_accvgpr_read_b32 v169,a1
v_accvgpr_read_b32 v170,a2
v_accvgpr_read_b32 v171,a3
v_accvgpr_read_b32 v172,a4
v_accvgpr_read_b32 v173,a5
v_accvgpr_read_b32 v174,a6
v_accvgpr_read_b32 v175,a7
v_mul_f32 v168,v168,v164
v_mul_f32 v169,v169,v164
v_mul_f32 v170,v170,v164
v_mul_f32 v171,v171,v164
v_mul_f32 v172,v172,v164
v_mul_f32 v173,v173,v164
v_mul_f32 v174,v174,v164
v_mul_f32 v175,v175,v164
v_accvgpr_write_b32 a0,v168
v_accvgpr_write_b32 a1,v169
v_accvgpr_write_b32 a2,v170
v_accvgpr_write_b32 a3,v171
v_accvgpr_write_b32 a4,v172
v_accvgpr_write_b32 a5,v173
v_accvgpr_write_b32 a6,v174
v_accvgpr_write_b32 a7,v175
v_accvgpr_read_b32 v168,a8
v_accvgpr_read_b32 v169,a9
v_accvgpr_read_b32 v170,a10
v_accvgpr_read_b32 v171,a11
v_accvgpr_read_b32 v172,a12
v_accvgpr_read_b32 v173,a13
v_accvgpr_read_b32 v174,a14
v_accvgpr_read_b32 v175,a15
v_mul_f32 v168,v168,v164
v_mul_f32 v169,v169,v164
v_mul_f32 v170,v170,v164
v_mul_f32 v171,v171,v164
v_mul_f32 v172,v172,v164
v_mul_f32 v173,v173,v164
v_mul_f32 v174,v174,v164
v_mul_f32 v175,v175,v164
v_accvgpr_write_b32 a8,v168
v_accvgpr_write_b32 a9,v169
v_accvgpr_write_b32 a10,v170
v_accvgpr_write_b32 a11,v171
v_accvgpr_write_b32 a12,v172
v_accvgpr_write_b32 a13,v173
v_accvgpr_write_b32 a14,v174
v_accvgpr_write_b32 a15,v175
v_accvgpr_read_b32 v168,a16
v_accvgpr_read_b32 v169,a17
v_accvgpr_read_b32 v170,a18
v_accvgpr_read_b32 v171,a19
v_accvgpr_read_b32 v172,a20
v_accvgpr_read_b32 v173,a21
v_accvgpr_read_b32 v174,a22
v_accvgpr_read_b32 v175,a23
v_mul_f32 v168,v168,v164
v_mul_f32 v169,v169,v164
v_mul_f32 v170,v170,v164
v_mul_f32 v171,v171,v164
v_mul_f32 v172,v172,v164
v_mul_f32 v173,v173,v164
v_mul_f32 v174,v174,v164
v_mul_f32 v175,v175,v164
v_accvgpr_write_b32 a16,v168
v_accvgpr_write_b32 a17,v169
v_accvgpr_write_b32 a18,v170
v_accvgpr_write_b32 a19,v171
v_accvgpr_write_b32 a20,v172
v_accvgpr_write_b32 a21,v173
v_accvgpr_write_b32 a22,v174
v_accvgpr_write_b32 a23,v175
v_accvgpr_read_b32 v168,a24
v_accvgpr_read_b32 v169,a25
v_accvgpr_read_b32 v170,a26
v_accvgpr_read_b32 v171,a27
v_accvgpr_read_b32 v172,a28
v_accvgpr_read_b32 v173,a29
v_accvgpr_read_b32 v174,a30
v_accvgpr_read_b32 v175,a31
v_mul_f32 v168,v168,v164
v_mul_f32 v169,v169,v164
v_mul_f32 v170,v170,v164
v_mul_f32 v171,v171,v164
v_mul_f32 v172,v172,v164
v_mul_f32 v173,v173,v164
v_mul_f32 v174,v174,v164
v_mul_f32 v175,v175,v164
v_accvgpr_write_b32 a24,v168
v_accvgpr_write_b32 a25,v169
v_accvgpr_write_b32 a26,v170
v_accvgpr_write_b32 a27,v171
v_accvgpr_write_b32 a28,v172
v_accvgpr_write_b32 a29,v173
v_accvgpr_write_b32 a30,v174
v_accvgpr_write_b32 a31,v175
v_accvgpr_read_b32 v168,a32
v_accvgpr_read_b32 v169,a33
v_accvgpr_read_b32 v170,a34
v_accvgpr_read_b32 v171,a35
v_accvgpr_read_b32 v172,a36
v_accvgpr_read_b32 v173,a37
v_accvgpr_read_b32 v174,a38
v_accvgpr_read_b32 v175,a39
v_mul_f32 v168,v168,v164
v_mul_f32 v169,v169,v164
v_mul_f32 v170,v170,v164
v_mul_f32 v171,v171,v164
v_mul_f32 v172,v172,v164
v_mul_f32 v173,v173,v164
v_mul_f32 v174,v174,v164
v_mul_f32 v175,v175,v164
)"
R"(v_accvgpr_write_b32 a32,v168
v_accvgpr_write_b32 a33,v169
v_accvgpr_write_b32 a34,v170
v_accvgpr_write_b32 a35,v171
v_accvgpr_write_b32 a36,v172
v_accvgpr_write_b32 a37,v173
v_accvgpr_write_b32 a38,v174
v_accvgpr_write_b32 a39,v175
v_accvgpr_read_b32 v168,a40
v_accvgpr_read_b32 v169,a41
v_accvgpr_read_b32 v170,a42
v_accvgpr_read_b32 v171,a43
v_accvgpr_read_b32 v172,a44
v_accvgpr_read_b32 v173,a45
v_accvgpr_read_b32 v174,a46
v_accvgpr_read_b32 v175,a47
v_mul_f32 v168,v168,v164
v_mul_f32 v169,v169,v164
v_mul_f32 v170,v170,v164
v_mul_f32 v171,v171,v164
v_mul_f32 v172,v172,v164
v_mul_f32 v173,v173,v164
v_mul_f32 v174,v174,v164
v_mul_f32 v175,v175,v164
v_accvgpr_write_b32 a40,v168
v_accvgpr_write_b32 a41,v169
v_accvgpr_write_b32 a42,v170
v_accvgpr_write_b32 a43,v171
v_accvgpr_write_b32 a44,v172
v_accvgpr_write_b32 a45,v173
v_accvgpr_write_b32 a46,v174
v_accvgpr_write_b32 a47,v175
v_accvgpr_read_b32 v168,a48
v_accvgpr_read_b32 v169,a49
v_accvgpr_read_b32 v170,a50
v_accvgpr_read_b32 v171,a51
v_accvgpr_read_b32 v172,a52
v_accvgpr_read_b32 v173,a53
v_accvgpr_read_b32 v174,a54
v_accvgpr_read_b32 v175,a55
v_mul_f32 v168,v168,v164
v_mul_f32 v169,v169,v164
v_mul_f32 v170,v170,v164
v_mul_f32 v171,v171,v164
v_mul_f32 v172,v172,v164
v_mul_f32 v173,v173,v164
v_mul_f32 v174,v174,v164
v_mul_f32 v175,v175,v164
v_accvgpr_write_b32 a48,v168
v_accvgpr_write_b32 a49,v169
v_accvgpr_write_b32 a50,v170
v_accvgpr_write_b32 a51,v171
v_accvgpr_write_b32 a52,v172
v_accvgpr_write_b32 a53,v173
v_accvgpr_write_b32 a54,v174
v_accvgpr_write_b32 a55,v175
v_accvgpr_read_b32 v168,a56
v_accvgpr_read_b32 v169,a57
v_accvgpr_read_b32 v170,a58
v_accvgpr_read_b32 v171,a59
v_accvgpr_read_b32 v172,a60
v_accvgpr_read_b32 v173,a61
v_accvgpr_read_b32 v174,a62
v_accvgpr_read_b32 v175,a63
v_mul_f32 v168,v168,v164
v_mul_f32 v169,v169,v164
v_mul_f32 v170,v170,v164
v_mul_f32 v171,v171,v164
v_mul_f32 v172,v172,v164
v_mul_f32 v173,v173,v164
v_mul_f32 v174,v174,v164
v_mul_f32 v175,v175,v164
v_accvgpr_write_b32 a56,v168
v_accvgpr_write_b32 a57,v169
v_accvgpr_write_b32 a58,v170
v_accvgpr_write_b32 a59,v171
v_accvgpr_write_b32 a60,v172
v_accvgpr_write_b32 a61,v173
v_accvgpr_write_b32 a62,v174
v_accvgpr_write_b32 a63,v175
v_add_f32 v161,0x42c00000,v161
v_max_f32 v161,0,v161
v_sub_f32 v245,v245,v161
v_exp_f32 v165,-v161
v_sub_f32 v136,v136,v161
v_sub_f32 v137,v137,v161
v_sub_f32 v138,v138,v161
v_sub_f32 v139,v139,v161
v_sub_f32 v140,v140,v161
v_sub_f32 v141,v141,v161
v_sub_f32 v142,v142,v161
v_sub_f32 v143,v143,v161
v_mul_f32 v228,v228,v165
v_mul_f32 v229,v229,v165
v_mul_f32 v230,v230,v165
v_mul_f32 v231,v231,v165
v_accvgpr_read_b32 v168,a64
v_accvgpr_read_b32 v169,a65
v_accvgpr_read_b32 v170,a66
v_accvgpr_read_b32 v171,a67
v_accvgpr_read_b32 v172,a68
v_accvgpr_read_b32 v173,a69
v_accvgpr_read_b32 v174,a70
v_accvgpr_read_b32 v175,a71
v_mul_f32 v168,v168,v165
v_mul_f32 v169,v169,v165
v_mul_f32 v170,v170,v165
v_mul_f32 v171,v171,v165
v_mul_f32 v172,v172,v165
v_mul_f32 v173,v173,v165
v_mul_f32 v174,v174,v165
v_mul_f32 v175,v175,v165
v_accvgpr_write_b32 a64,v168
v_accvgpr_write_b32 a65,v169
v_accvgpr_write_b32 a66,v170
v_accvgpr_write_b32 a67,v171
v_accvgpr_write_b32 a68,v172
v_accvgpr_write_b32 a69,v173
v_accvgpr_write_b32 a70,v174
v_accvgpr_write_b32 a71,v175
v_accvgpr_read_b32 v168,a72
v_accvgpr_read_b32 v169,a73
v_accvgpr_read_b32 v170,a74
v_accvgpr_read_b32 v171,a75
v_accvgpr_read_b32 v172,a76
v_accvgpr_read_b32 v173,a77
v_accvgpr_read_b32 v174,a78
v_accvgpr_read_b32 v175,a79
v_mul_f32 v168,v168,v165
v_mul_f32 v169,v169,v165
v_mul_f32 v170,v170,v165
v_mul_f32 v171,v171,v165
v_mul_f32 v172,v172,v165
v_mul_f32 v173,v173,v165
v_mul_f32 v174,v174,v165
v_mul_f32 v175,v175,v165
v_accvgpr_write_b32 a72,v168
v_accvgpr_write_b32 a73,v169
v_accvgpr_write_b32 a74,v170
v_accvgpr_write_b32 a75,v171
v_accvgpr_write_b32 a76,v172
v_accvgpr_write_b32 a77,v173
v_accvgpr_write_b32 a78,v174
v_accvgpr_write_b32 a79,v175
v_accvgpr_read_b32 v168,a80
v_accvgpr_read_b32 v169,a81
v_accvgpr_read_b32 v170,a82
v_accvgpr_read_b32 v171,a83
v_accvgpr_read_b32 v172,a84
v_accvgpr_read_b32 v173,a85
v_accvgpr_read_b32 v174,a86
v_accvgpr_read_b32 v175,a87
v_mul_f32 v168,v168,v165
v_mul_f32 v169,v169,v165
v_mul_f32 v170,v170,v165
v_mul_f32 v171,v171,v165
v_mul_f32 v172,v172,v165
v_mul_f32 v173,v173,v165
v_mul_f32 v174,v174,v165
v_mul_f32 v175,v175,v165
v_accvgpr_write_b32 a80,v168
v_accvgpr_write_b32 a81,v169
v_accvgpr_write_b32 a82,v170
v_accvgpr_write_b32 a83,v171
v_accvgpr_write_b32 a84,v172
v_accvgpr_write_b32 a85,v173
v_accvgpr_write_b32 a86,v174
v_accvgpr_write_b32 a87,v175
v_accvgpr_read_b32 v168,a88
v_accvgpr_read_b32 v169,a89
v_accvgpr_read_b32 v170,a90
v_accvgpr_read_b32 v171,a91
v_accvgpr_read_b32 v172,a92
v_accvgpr_read_b32 v173,a93
v_accvgpr_read_b32 v174,a94
v_accvgpr_read_b32 v175,a95
v_mul_f32 v168,v168,v165
v_mul_f32 v169,v169,v165
v_mul_f32 v170,v170,v165
v_mul_f32 v171,v171,v165
v_mul_f32 v172,v172,v165
v_mul_f32 v173,v173,v165
v_mul_f32 v174,v174,v165
v_mul_f32 v175,v175,v165
v_accvgpr_write_b32 a88,v168
v_accvgpr_write_b32 a89,v169
v_accvgpr_write_b32 a90,v170
v_accvgpr_write_b32 a91,v171
v_accvgpr_write_b32 a92,v172
v_accvgpr_write_b32 a93,v173
v_accvgpr_write_b32 a94,v174
v_accvgpr_write_b32 a95,v175
v_accvgpr_read_b32 v168,a96
v_accvgpr_read_b32 v169,a97
v_accvgpr_read_b32 v170,a98
v_accvgpr_read_b32 v171,a99
v_accvgpr_read_b32 v172,a100
v_accvgpr_read_b32 v173,a101
v_accvgpr_read_b32 v174,a102
v_accvgpr_read_b32 v175,a103
)"
R"(v_mul_f32 v168,v168,v165
v_mul_f32 v169,v169,v165
v_mul_f32 v170,v170,v165
v_mul_f32 v171,v171,v165
v_mul_f32 v172,v172,v165
v_mul_f32 v173,v173,v165
v_mul_f32 v174,v174,v165
v_mul_f32 v175,v175,v165
v_accvgpr_write_b32 a96,v168
v_accvgpr_write_b32 a97,v169
v_accvgpr_write_b32 a98,v170
v_accvgpr_write_b32 a99,v171
v_accvgpr_write_b32 a100,v172
v_accvgpr_write_b32 a101,v173
v_accvgpr_write_b32 a102,v174
v_accvgpr_write_b32 a103,v175
v_accvgpr_read_b32 v168,a104
v_accvgpr_read_b32 v169,a105
v_accvgpr_read_b32 v170,a106
v_accvgpr_read_b32 v171,a107
v_accvgpr_read_b32 v172,a108
v_accvgpr_read_b32 v173,a109
v_accvgpr_read_b32 v174,a110
v_accvgpr_read_b32 v175,a111
v_mul_f32 v168,v168,v165
v_mul_f32 v169,v169,v165
v_mul_f32 v170,v170,v165
v_mul_f32 v171,v171,v165
v_mul_f32 v172,v172,v165
v_mul_f32 v173,v173,v165
v_mul_f32 v174,v174,v165
v_mul_f32 v175,v175,v165
v_accvgpr_write_b32 a104,v168
v_accvgpr_write_b32 a105,v169
v_accvgpr_write_b32 a106,v170
v_accvgpr_write_b32 a107,v171
v_accvgpr_write_b32 a108,v172
v_accvgpr_write_b32 a109,v173
v_accvgpr_write_b32 a110,v174
v_accvgpr_write_b32 a111,v175
v_accvgpr_read_b32 v168,a112
v_accvgpr_read_b32 v169,a113
v_accvgpr_read_b32 v170,a114
v_accvgpr_read_b32 v171,a115
v_accvgpr_read_b32 v172,a116
v_accvgpr_read_b32 v173,a117
v_accvgpr_read_b32 v174,a118
v_accvgpr_read_b32 v175,a119
v_mul_f32 v168,v168,v165
v_mul_f32 v169,v169,v165
v_mul_f32 v170,v170,v165
v_mul_f32 v171,v171,v165
v_mul_f32 v172,v172,v165
v_mul_f32 v173,v173,v165
v_mul_f32 v174,v174,v165
v_mul_f32 v175,v175,v165
v_accvgpr_write_b32 a112,v168
v_accvgpr_write_b32 a113,v169
v_accvgpr_write_b32 a114,v170
v_accvgpr_write_b32 a115,v171
v_accvgpr_write_b32 a116,v172
v_accvgpr_write_b32 a117,v173
v_accvgpr_write_b32 a118,v174
v_accvgpr_write_b32 a119,v175
v_accvgpr_read_b32 v168,a120
v_accvgpr_read_b32 v169,a121
v_accvgpr_read_b32 v170,a122
v_accvgpr_read_b32 v171,a123
v_accvgpr_read_b32 v172,a124
v_accvgpr_read_b32 v173,a125
v_accvgpr_read_b32 v174,a126
v_accvgpr_read_b32 v175,a127
v_mul_f32 v168,v168,v165
v_mul_f32 v169,v169,v165
v_mul_f32 v170,v170,v165
v_mul_f32 v171,v171,v165
v_mul_f32 v172,v172,v165
v_mul_f32 v173,v173,v165
v_mul_f32 v174,v174,v165
v_mul_f32 v175,v175,v165
v_accvgpr_write_b32 a120,v168
v_accvgpr_write_b32 a121,v169
v_accvgpr_write_b32 a122,v170
v_accvgpr_write_b32 a123,v171
v_accvgpr_write_b32 a124,v172
v_accvgpr_write_b32 a125,v173
v_accvgpr_write_b32 a126,v174
v_accvgpr_write_b32 a127,v175
v_add_f32 v162,0x42c00000,v162
v_max_f32 v162,0,v162
v_sub_f32 v246,v246,v162
v_exp_f32 v166,-v162
v_sub_f32 v144,v144,v162
v_sub_f32 v145,v145,v162
v_sub_f32 v146,v146,v162
v_sub_f32 v147,v147,v162
v_sub_f32 v148,v148,v162
v_sub_f32 v149,v149,v162
v_sub_f32 v150,v150,v162
v_sub_f32 v151,v151,v162
v_mul_f32 v232,v232,v166
v_mul_f32 v233,v233,v166
v_mul_f32 v234,v234,v166
v_mul_f32 v235,v235,v166
v_accvgpr_read_b32 v168,a128
v_accvgpr_read_b32 v169,a129
v_accvgpr_read_b32 v170,a130
v_accvgpr_read_b32 v171,a131
v_accvgpr_read_b32 v172,a132
v_accvgpr_read_b32 v173,a133
v_accvgpr_read_b32 v174,a134
v_accvgpr_read_b32 v175,a135
v_mul_f32 v168,v168,v166
v_mul_f32 v169,v169,v166
v_mul_f32 v170,v170,v166
v_mul_f32 v171,v171,v166
v_mul_f32 v172,v172,v166
v_mul_f32 v173,v173,v166
v_mul_f32 v174,v174,v166
v_mul_f32 v175,v175,v166
v_accvgpr_write_b32 a128,v168
v_accvgpr_write_b32 a129,v169
v_accvgpr_write_b32 a130,v170
v_accvgpr_write_b32 a131,v171
v_accvgpr_write_b32 a132,v172
v_accvgpr_write_b32 a133,v173
v_accvgpr_write_b32 a134,v174
v_accvgpr_write_b32 a135,v175
v_accvgpr_read_b32 v168,a136
v_accvgpr_read_b32 v169,a137
v_accvgpr_read_b32 v170,a138
v_accvgpr_read_b32 v171,a139
v_accvgpr_read_b32 v172,a140
v_accvgpr_read_b32 v173,a141
v_accvgpr_read_b32 v174,a142
v_accvgpr_read_b32 v175,a143
v_mul_f32 v168,v168,v166
v_mul_f32 v169,v169,v166
v_mul_f32 v170,v170,v166
v_mul_f32 v171,v171,v166
v_mul_f32 v172,v172,v166
v_mul_f32 v173,v173,v166
v_mul_f32 v174,v174,v166
v_mul_f32 v175,v175,v166
v_accvgpr_write_b32 a136,v168
v_accvgpr_write_b32 a137,v169
v_accvgpr_write_b32 a138,v170
v_accvgpr_write_b32 a139,v171
v_accvgpr_write_b32 a140,v172
v_accvgpr_write_b32 a141,v173
v_accvgpr_write_b32 a142,v174
v_accvgpr_write_b32 a143,v175
v_accvgpr_read_b32 v168,a144
v_accvgpr_read_b32 v169,a145
v_accvgpr_read_b32 v170,a146
v_accvgpr_read_b32 v171,a147
v_accvgpr_read_b32 v172,a148
v_accvgpr_read_b32 v173,a149
v_accvgpr_read_b32 v174,a150
v_accvgpr_read_b32 v175,a151
v_mul_f32 v168,v168,v166
v_mul_f32 v169,v169,v166
v_mul_f32 v170,v170,v166
v_mul_f32 v171,v171,v166
v_mul_f32 v172,v172,v166
v_mul_f32 v173,v173,v166
v_mul_f32 v174,v174,v166
v_mul_f32 v175,v175,v166
v_accvgpr_write_b32 a144,v168
v_accvgpr_write_b32 a145,v169
v_accvgpr_write_b32 a146,v170
v_accvgpr_write_b32 a147,v171
v_accvgpr_write_b32 a148,v172
v_accvgpr_write_b32 a149,v173
v_accvgpr_write_b32 a150,v174
v_accvgpr_write_b32 a151,v175
v_accvgpr_read_b32 v168,a152
v_accvgpr_read_b32 v169,a153
v_accvgpr_read_b32 v170,a154
v_accvgpr_read_b32 v171,a155
v_accvgpr_read_b32 v172,a156
v_accvgpr_read_b32 v173,a157
v_accvgpr_read_b32 v174,a158
v_accvgpr_read_b32 v175,a159
v_mul_f32 v168,v168,v166
v_mul_f32 v169,v169,v166
v_mul_f32 v170,v170,v166
v_mul_f32 v171,v171,v166
v_mul_f32 v172,v172,v166
v_mul_f32 v173,v173,v166
v_mul_f32 v174,v174,v166
v_mul_f32 v175,v175,v166
v_accvgpr_write_b32 a152,v168
v_accvgpr_write_b32 a153,v169
v_accvgpr_write_b32 a154,v170
v_accvgpr_write_b32 a155,v171
v_accvgpr_write_b32 a156,v172
v_accvgpr_write_b32 a157,v173
v_accvgpr_write_b32 a158,v174
v_accvgpr_write_b32 a159,v175
)"
R"(v_accvgpr_read_b32 v168,a160
v_accvgpr_read_b32 v169,a161
v_accvgpr_read_b32 v170,a162
v_accvgpr_read_b32 v171,a163
v_accvgpr_read_b32 v172,a164
v_accvgpr_read_b32 v173,a165
v_accvgpr_read_b32 v174,a166
v_accvgpr_read_b32 v175,a167
v_mul_f32 v168,v168,v166
v_mul_f32 v169,v169,v166
v_mul_f32 v170,v170,v166
v_mul_f32 v171,v171,v166
v_mul_f32 v172,v172,v166
v_mul_f32 v173,v173,v166
v_mul_f32 v174,v174,v166
v_mul_f32 v175,v175,v166
v_accvgpr_write_b32 a160,v168
v_accvgpr_write_b32 a161,v169
v_accvgpr_write_b32 a162,v170
v_accvgpr_write_b32 a163,v171
v_accvgpr_write_b32 a164,v172
v_accvgpr_write_b32 a165,v173
v_accvgpr_write_b32 a166,v174
v_accvgpr_write_b32 a167,v175
v_accvgpr_read_b32 v168,a168
v_accvgpr_read_b32 v169,a169
v_accvgpr_read_b32 v170,a170
v_accvgpr_read_b32 v171,a171
v_accvgpr_read_b32 v172,a172
v_accvgpr_read_b32 v173,a173
v_accvgpr_read_b32 v174,a174
v_accvgpr_read_b32 v175,a175
v_mul_f32 v168,v168,v166
v_mul_f32 v169,v169,v166
v_mul_f32 v170,v170,v166
v_mul_f32 v171,v171,v166
v_mul_f32 v172,v172,v166
v_mul_f32 v173,v173,v166
v_mul_f32 v174,v174,v166
v_mul_f32 v175,v175,v166
v_accvgpr_write_b32 a168,v168
v_accvgpr_write_b32 a169,v169
v_accvgpr_write_b32 a170,v170
v_accvgpr_write_b32 a171,v171
v_accvgpr_write_b32 a172,v172
v_accvgpr_write_b32 a173,v173
v_accvgpr_write_b32 a174,v174
v_accvgpr_write_b32 a175,v175
v_accvgpr_read_b32 v168,a176
v_accvgpr_read_b32 v169,a177
v_accvgpr_read_b32 v170,a178
v_accvgpr_read_b32 v171,a179
v_accvgpr_read_b32 v172,a180
v_accvgpr_read_b32 v173,a181
v_accvgpr_read_b32 v174,a182
v_accvgpr_read_b32 v175,a183
v_mul_f32 v168,v168,v166
v_mul_f32 v169,v169,v166
v_mul_f32 v170,v170,v166
v_mul_f32 v171,v171,v166
v_mul_f32 v172,v172,v166
v_mul_f32 v173,v173,v166
v_mul_f32 v174,v174,v166
v_mul_f32 v175,v175,v166
v_accvgpr_write_b32 a176,v168
v_accvgpr_write_b32 a177,v169
v_accvgpr_write_b32 a178,v170
v_accvgpr_write_b32 a179,v171
v_accvgpr_write_b32 a180,v172
v_accvgpr_write_b32 a181,v173
v_accvgpr_write_b32 a182,v174
v_accvgpr_write_b32 a183,v175
v_accvgpr_read_b32 v168,a184
v_accvgpr_read_b32 v169,a185
v_accvgpr_read_b32 v170,a186
v_accvgpr_read_b32 v171,a187
v_accvgpr_read_b32 v172,a188
v_accvgpr_read_b32 v173,a189
v_accvgpr_read_b32 v174,a190
v_accvgpr_read_b32 v175,a191
v_mul_f32 v168,v168,v166
v_mul_f32 v169,v169,v166
v_mul_f32 v170,v170,v166
v_mul_f32 v171,v171,v166
v_mul_f32 v172,v172,v166
v_mul_f32 v173,v173,v166
v_mul_f32 v174,v174,v166
v_mul_f32 v175,v175,v166
v_accvgpr_write_b32 a184,v168
v_accvgpr_write_b32 a185,v169
v_accvgpr_write_b32 a186,v170
v_accvgpr_write_b32 a187,v171
v_accvgpr_write_b32 a188,v172
v_accvgpr_write_b32 a189,v173
v_accvgpr_write_b32 a190,v174
v_accvgpr_write_b32 a191,v175
v_add_f32 v163,0x42c00000,v163
v_max_f32 v163,0,v163
v_sub_f32 v247,v247,v163
v_exp_f32 v167,-v163
v_sub_f32 v152,v152,v163
v_sub_f32 v153,v153,v163
v_sub_f32 v154,v154,v163
v_sub_f32 v155,v155,v163
v_sub_f32 v156,v156,v163
v_sub_f32 v157,v157,v163
v_sub_f32 v158,v158,v163
v_sub_f32 v159,v159,v163
v_mul_f32 v236,v236,v167
v_mul_f32 v237,v237,v167
v_mul_f32 v238,v238,v167
v_mul_f32 v239,v239,v167
v_accvgpr_read_b32 v168,a192
v_accvgpr_read_b32 v169,a193
v_accvgpr_read_b32 v170,a194
v_accvgpr_read_b32 v171,a195
v_accvgpr_read_b32 v172,a196
v_accvgpr_read_b32 v173,a197
v_accvgpr_read_b32 v174,a198
v_accvgpr_read_b32 v175,a199
v_mul_f32 v168,v168,v167
v_mul_f32 v169,v169,v167
v_mul_f32 v170,v170,v167
v_mul_f32 v171,v171,v167
v_mul_f32 v172,v172,v167
v_mul_f32 v173,v173,v167
v_mul_f32 v174,v174,v167
v_mul_f32 v175,v175,v167
v_accvgpr_write_b32 a192,v168
v_accvgpr_write_b32 a193,v169
v_accvgpr_write_b32 a194,v170
v_accvgpr_write_b32 a195,v171
v_accvgpr_write_b32 a196,v172
v_accvgpr_write_b32 a197,v173
v_accvgpr_write_b32 a198,v174
v_accvgpr_write_b32 a199,v175
v_accvgpr_read_b32 v168,a200
v_accvgpr_read_b32 v169,a201
v_accvgpr_read_b32 v170,a202
v_accvgpr_read_b32 v171,a203
v_accvgpr_read_b32 v172,a204
v_accvgpr_read_b32 v173,a205
v_accvgpr_read_b32 v174,a206
v_accvgpr_read_b32 v175,a207
v_mul_f32 v168,v168,v167
v_mul_f32 v169,v169,v167
v_mul_f32 v170,v170,v167
v_mul_f32 v171,v171,v167
v_mul_f32 v172,v172,v167
v_mul_f32 v173,v173,v167
v_mul_f32 v174,v174,v167
v_mul_f32 v175,v175,v167
v_accvgpr_write_b32 a200,v168
v_accvgpr_write_b32 a201,v169
v_accvgpr_write_b32 a202,v170
v_accvgpr_write_b32 a203,v171
v_accvgpr_write_b32 a204,v172
v_accvgpr_write_b32 a205,v173
v_accvgpr_write_b32 a206,v174
v_accvgpr_write_b32 a207,v175
v_accvgpr_read_b32 v168,a208
v_accvgpr_read_b32 v169,a209
v_accvgpr_read_b32 v170,a210
v_accvgpr_read_b32 v171,a211
v_accvgpr_read_b32 v172,a212
v_accvgpr_read_b32 v173,a213
v_accvgpr_read_b32 v174,a214
v_accvgpr_read_b32 v175,a215
v_mul_f32 v168,v168,v167
v_mul_f32 v169,v169,v167
v_mul_f32 v170,v170,v167
v_mul_f32 v171,v171,v167
v_mul_f32 v172,v172,v167
v_mul_f32 v173,v173,v167
v_mul_f32 v174,v174,v167
v_mul_f32 v175,v175,v167
v_accvgpr_write_b32 a208,v168
v_accvgpr_write_b32 a209,v169
v_accvgpr_write_b32 a210,v170
v_accvgpr_write_b32 a211,v171
v_accvgpr_write_b32 a212,v172
v_accvgpr_write_b32 a213,v173
v_accvgpr_write_b32 a214,v174
v_accvgpr_write_b32 a215,v175
v_accvgpr_read_b32 v168,a216
v_accvgpr_read_b32 v169,a217
v_accvgpr_read_b32 v170,a218
v_accvgpr_read_b32 v171,a219
v_accvgpr_read_b32 v172,a220
v_accvgpr_read_b32 v173,a221
v_accvgpr_read_b32 v174,a222
v_accvgpr_read_b32 v175,a223
v_mul_f32 v168,v168,v167
v_mul_f32 v169,v169,v167
v_mul_f32 v170,v170,v167
v_mul_f32 v171,v171,v167
v_mul_f32 v172,v172,v167
v_mul_f32 v173,v173,v167
v_mul_f32 v174,v174,v167
v_mul_f32 v175,v175,v167
)"
R"(v_accvgpr_write_b32 a216,v168
v_accvgpr_write_b32 a217,v169
v_accvgpr_write_b32 a218,v170
v_accvgpr_write_b32 a219,v171
v_accvgpr_write_b32 a220,v172
v_accvgpr_write_b32 a221,v173
v_accvgpr_write_b32 a222,v174
v_accvgpr_write_b32 a223,v175
v_accvgpr_read_b32 v168,a224
v_accvgpr_read_b32 v169,a225
v_accvgpr_read_b32 v170,a226
v_accvgpr_read_b32 v171,a227
v_accvgpr_read_b32 v172,a228
v_accvgpr_read_b32 v173,a229
v_accvgpr_read_b32 v174,a230
v_accvgpr_read_b32 v175,a231
v_mul_f32 v168,v168,v167
v_mul_f32 v169,v169,v167
v_mul_f32 v170,v170,v167
v_mul_f32 v171,v171,v167
v_mul_f32 v172,v172,v167
v_mul_f32 v173,v173,v167
v_mul_f32 v174,v174,v167
v_mul_f32 v175,v175,v167
v_accvgpr_write_b32 a224,v168
v_accvgpr_write_b32 a225,v169
v_accvgpr_write_b32 a226,v170
v_accvgpr_write_b32 a227,v171
v_accvgpr_write_b32 a228,v172
v_accvgpr_write_b32 a229,v173
v_accvgpr_write_b32 a230,v174
v_accvgpr_write_b32 a231,v175
v_accvgpr_read_b32 v168,a232
v_accvgpr_read_b32 v169,a233
v_accvgpr_read_b32 v170,a234
v_accvgpr_read_b32 v171,a235
v_accvgpr_read_b32 v172,a236
v_accvgpr_read_b32 v173,a237
v_accvgpr_read_b32 v174,a238
v_accvgpr_read_b32 v175,a239
v_mul_f32 v168,v168,v167
v_mul_f32 v169,v169,v167
v_mul_f32 v170,v170,v167
v_mul_f32 v171,v171,v167
v_mul_f32 v172,v172,v167
v_mul_f32 v173,v173,v167
v_mul_f32 v174,v174,v167
v_mul_f32 v175,v175,v167
v_accvgpr_write_b32 a232,v168
v_accvgpr_write_b32 a233,v169
v_accvgpr_write_b32 a234,v170
v_accvgpr_write_b32 a235,v171
v_accvgpr_write_b32 a236,v172
v_accvgpr_write_b32 a237,v173
v_accvgpr_write_b32 a238,v174
v_accvgpr_write_b32 a239,v175
v_accvgpr_read_b32 v168,a240
v_accvgpr_read_b32 v169,a241
v_accvgpr_read_b32 v170,a242
v_accvgpr_read_b32 v171,a243
v_accvgpr_read_b32 v172,a244
v_accvgpr_read_b32 v173,a245
v_accvgpr_read_b32 v174,a246
v_accvgpr_read_b32 v175,a247
v_mul_f32 v168,v168,v167
v_mul_f32 v169,v169,v167
v_mul_f32 v170,v170,v167
v_mul_f32 v171,v171,v167
v_mul_f32 v172,v172,v167
v_mul_f32 v173,v173,v167
v_mul_f32 v174,v174,v167
v_mul_f32 v175,v175,v167
v_accvgpr_write_b32 a240,v168
v_accvgpr_write_b32 a241,v169
v_accvgpr_write_b32 a242,v170
v_accvgpr_write_b32 a243,v171
v_accvgpr_write_b32 a244,v172
v_accvgpr_write_b32 a245,v173
v_accvgpr_write_b32 a246,v174
v_accvgpr_write_b32 a247,v175
v_accvgpr_read_b32 v168,a248
v_accvgpr_read_b32 v169,a249
v_accvgpr_read_b32 v170,a250
v_accvgpr_read_b32 v171,a251
v_accvgpr_read_b32 v172,a252
v_accvgpr_read_b32 v173,a253
v_accvgpr_read_b32 v174,a254
v_accvgpr_read_b32 v175,a255
v_mul_f32 v168,v168,v167
v_mul_f32 v169,v169,v167
v_mul_f32 v170,v170,v167
v_mul_f32 v171,v171,v167
v_mul_f32 v172,v172,v167
v_mul_f32 v173,v173,v167
v_mul_f32 v174,v174,v167
v_mul_f32 v175,v175,v167
v_accvgpr_write_b32 a248,v168
v_accvgpr_write_b32 a249,v169
v_accvgpr_write_b32 a250,v170
v_accvgpr_write_b32 a251,v171
v_accvgpr_write_b32 a252,v172
v_accvgpr_write_b32 a253,v173
v_accvgpr_write_b32 a254,v174
v_accvgpr_write_b32 a255,v175
s_nop 3
s_setpc_b64 s[76:77]
Lrare1a_%=:
s_nop 7
v_max3_f32 v128,v160,v161,v162
v_max3_f32 v129,v168,v169,v170
v_max3_f32 v130,v176,v177,v178
v_max3_f32 v131,v184,v185,v186
v_max3_f32 v128,v163,v164,v128
v_max3_f32 v129,v171,v172,v129
v_max3_f32 v130,v179,v180,v130
v_max3_f32 v131,v187,v188,v131
v_max3_f32 v128,v165,v166,v128
v_max3_f32 v129,v173,v174,v129
v_max3_f32 v130,v181,v182,v130
v_max3_f32 v131,v189,v190,v131
v_max_f32 v128,v167,v128
v_max_f32 v129,v175,v129
v_max_f32 v130,v183,v130
v_max_f32 v131,v191,v131
v_mov_b32 v132,v128
v_mov_b32 v133,v129
v_mov_b32 v134,v130
v_mov_b32 v135,v131
v_permlane16_swap_b32 v128,v132
v_permlane16_swap_b32 v129,v133
v_permlane16_swap_b32 v130,v134
v_permlane16_swap_b32 v131,v135
v_max_f32 v128,v128,v132
v_max_f32 v129,v129,v133
v_max_f32 v130,v130,v134
v_max_f32 v131,v131,v135
v_mov_b32 v132,v128
v_mov_b32 v133,v129
v_mov_b32 v134,v130
v_mov_b32 v135,v131
v_permlane32_swap_b32 v128,v132
v_permlane32_swap_b32 v129,v133
v_permlane32_swap_b32 v130,v134
v_permlane32_swap_b32 v131,v135
v_max_f32 v128,v128,v132
v_max_f32 v129,v129,v133
v_max_f32 v130,v130,v134
v_max_f32 v131,v131,v135
v_add_f32 v128,0x42c00000,v128
v_max_f32 v128,0,v128
v_sub_f32 v244,v244,v128
v_exp_f32 v132,-v128
v_sub_f32 v160,v160,v128
v_sub_f32 v161,v161,v128
v_sub_f32 v162,v162,v128
v_sub_f32 v163,v163,v128
v_sub_f32 v164,v164,v128
v_sub_f32 v165,v165,v128
v_sub_f32 v166,v166,v128
v_sub_f32 v167,v167,v128
v_mul_f32 v224,v224,v132
v_mul_f32 v225,v225,v132
v_mul_f32 v226,v226,v132
v_mul_f32 v227,v227,v132
v_accvgpr_read_b32 v136,a0
v_accvgpr_read_b32 v137,a1
v_accvgpr_read_b32 v138,a2
v_accvgpr_read_b32 v139,a3
v_accvgpr_read_b32 v140,a4
v_accvgpr_read_b32 v141,a5
v_accvgpr_read_b32 v142,a6
v_accvgpr_read_b32 v143,a7
v_mul_f32 v136,v136,v132
v_mul_f32 v137,v137,v132
v_mul_f32 v138,v138,v132
v_mul_f32 v139,v139,v132
v_mul_f32 v140,v140,v132
v_mul_f32 v141,v141,v132
v_mul_f32 v142,v142,v132
v_mul_f32 v143,v143,v132
v_accvgpr_write_b32 a0,v136
v_accvgpr_write_b32 a1,v137
v_accvgpr_write_b32 a2,v138
v_accvgpr_write_b32 a3,v139
v_accvgpr_write_b32 a4,v140
v_accvgpr_write_b32 a5,v141
v_accvgpr_write_b32 a6,v142
v_accvgpr_write_b32 a7,v143
v_accvgpr_read_b32 v136,a8
v_accvgpr_read_b32 v137,a9
v_accvgpr_read_b32 v138,a10
v_accvgpr_read_b32 v139,a11
v_accvgpr_read_b32 v140,a12
v_accvgpr_read_b32 v141,a13
v_accvgpr_read_b32 v142,a14
v_accvgpr_read_b32 v143,a15
v_mul_f32 v136,v136,v132
v_mul_f32 v137,v137,v132
v_mul_f32 v138,v138,v132
v_mul_f32 v139,v139,v132
)"
R"(v_mul_f32 v140,v140,v132
v_mul_f32 v141,v141,v132
v_mul_f32 v142,v142,v132
v_mul_f32 v143,v143,v132
v_accvgpr_write_b32 a8,v136
v_accvgpr_write_b32 a9,v137
v_accvgpr_write_b32 a10,v138
v_accvgpr_write_b32 a11,v139
v_accvgpr_write_b32 a12,v140
v_accvgpr_write_b32 a13,v141
v_accvgpr_write_b32 a14,v142
v_accvgpr_write_b32 a15,v143
v_accvgpr_read_b32 v136,a16
v_accvgpr_read_b32 v137,a17
v_accvgpr_read_b32 v138,a18
v_accvgpr_read_b32 v139,a19
v_accvgpr_read_b32 v140,a20
v_accvgpr_read_b32 v141,a21
v_accvgpr_read_b32 v142,a22
v_accvgpr_read_b32 v143,a23
v_mul_f32 v136,v136,v132
v_mul_f32 v137,v137,v132
v_mul_f32 v138,v138,v132
v_mul_f32 v139,v139,v132
v_mul_f32 v140,v140,v132
v_mul_f32 v141,v141,v132
v_mul_f32 v142,v142,v132
v_mul_f32 v143,v143,v132
v_accvgpr_write_b32 a16,v136
v_accvgpr_write_b32 a17,v137
v_accvgpr_write_b32 a18,v138
v_accvgpr_write_b32 a19,v139
v_accvgpr_write_b32 a20,v140
v_accvgpr_write_b32 a21,v141
v_accvgpr_write_b32 a22,v142
v_accvgpr_write_b32 a23,v143
v_accvgpr_read_b32 v136,a24
v_accvgpr_read_b32 v137,a25
v_accvgpr_read_b32 v138,a26
v_accvgpr_read_b32 v139,a27
v_accvgpr_read_b32 v140,a28
v_accvgpr_read_b32 v141,a29
v_accvgpr_read_b32 v142,a30
v_accvgpr_read_b32 v143,a31
v_mul_f32 v136,v136,v132
v_mul_f32 v137,v137,v132
v_mul_f32 v138,v138,v132
v_mul_f32 v139,v139,v132
v_mul_f32 v140,v140,v132
v_mul_f32 v141,v141,v132
v_mul_f32 v142,v142,v132
v_mul_f32 v143,v143,v132
v_accvgpr_write_b32 a24,v136
v_accvgpr_write_b32 a25,v137
v_accvgpr_write_b32 a26,v138
v_accvgpr_write_b32 a27,v139
v_accvgpr_write_b32 a28,v140
v_accvgpr_write_b32 a29,v141
v_accvgpr_write_b32 a30,v142
v_accvgpr_write_b32 a31,v143
v_accvgpr_read_b32 v136,a32
v_accvgpr_read_b32 v137,a33
v_accvgpr_read_b32 v138,a34
v_accvgpr_read_b32 v139,a35
v_accvgpr_read_b32 v140,a36
v_accvgpr_read_b32 v141,a37
v_accvgpr_read_b32 v142,a38
v_accvgpr_read_b32 v143,a39
v_mul_f32 v136,v136,v132
v_mul_f32 v137,v137,v132
v_mul_f32 v138,v138,v132
v_mul_f32 v139,v139,v132
v_mul_f32 v140,v140,v132
v_mul_f32 v141,v141,v132
v_mul_f32 v142,v142,v132
v_mul_f32 v143,v143,v132
v_accvgpr_write_b32 a32,v136
v_accvgpr_write_b32 a33,v137
v_accvgpr_write_b32 a34,v138
v_accvgpr_write_b32 a35,v139
v_accvgpr_write_b32 a36,v140
v_accvgpr_write_b32 a37,v141
v_accvgpr_write_b32 a38,v142
v_accvgpr_write_b32 a39,v143
v_accvgpr_read_b32 v136,a40
v_accvgpr_read_b32 v137,a41
v_accvgpr_read_b32 v138,a42
v_accvgpr_read_b32 v139,a43
v_accvgpr_read_b32 v140,a44
v_accvgpr_read_b32 v141,a45
v_accvgpr_read_b32 v142,a46
v_accvgpr_read_b32 v143,a47
v_mul_f32 v136,v136,v132
v_mul_f32 v137,v137,v132
v_mul_f32 v138,v138,v132
v_mul_f32 v139,v139,v132
v_mul_f32 v140,v140,v132
v_mul_f32 v141,v141,v132
v_mul_f32 v142,v142,v132
v_mul_f32 v143,v143,v132
v_accvgpr_write_b32 a40,v136
v_accvgpr_write_b32 a41,v137
v_accvgpr_write_b32 a42,v138
v_accvgpr_write_b32 a43,v139
v_accvgpr_write_b32 a44,v140
v_accvgpr_write_b32 a45,v141
v_accvgpr_write_b32 a46,v142
v_accvgpr_write_b32 a47,v143
v_accvgpr_read_b32 v136,a48
v_accvgpr_read_b32 v137,a49
v_accvgpr_read_b32 v138,a50
v_accvgpr_read_b32 v139,a51
v_accvgpr_read_b32 v140,a52
v_accvgpr_read_b32 v141,a53
v_accvgpr_read_b32 v142,a54
v_accvgpr_read_b32 v143,a55
v_mul_f32 v136,v136,v132
v_mul_f32 v137,v137,v132
v_mul_f32 v138,v138,v132
v_mul_f32 v139,v139,v132
v_mul_f32 v140,v140,v132
v_mul_f32 v141,v141,v132
v_mul_f32 v142,v142,v132
v_mul_f32 v143,v143,v132
v_accvgpr_write_b32 a48,v136
v_accvgpr_write_b32 a49,v137
v_accvgpr_write_b32 a50,v138
v_accvgpr_write_b32 a51,v139
v_accvgpr_write_b32 a52,v140
v_accvgpr_write_b32 a53,v141
v_accvgpr_write_b32 a54,v142
v_accvgpr_write_b32 a55,v143
v_accvgpr_read_b32 v136,a56
v_accvgpr_read_b32 v137,a57
v_accvgpr_read_b32 v138,a58
v_accvgpr_read_b32 v139,a59
v_accvgpr_read_b32 v140,a60
v_accvgpr_read_b32 v141,a61
v_accvgpr_read_b32 v142,a62
v_accvgpr_read_b32 v143,a63
v_mul_f32 v136,v136,v132
v_mul_f32 v137,v137,v132
v_mul_f32 v138,v138,v132
v_mul_f32 v139,v139,v132
v_mul_f32 v140,v140,v132
v_mul_f32 v141,v141,v132
v_mul_f32 v142,v142,v132
v_mul_f32 v143,v143,v132
v_accvgpr_write_b32 a56,v136
v_accvgpr_write_b32 a57,v137
v_accvgpr_write_b32 a58,v138
v_accvgpr_write_b32 a59,v139
v_accvgpr_write_b32 a60,v140
v_accvgpr_write_b32 a61,v141
v_accvgpr_write_b32 a62,v142
v_accvgpr_write_b32 a63,v143
v_add_f32 v129,0x42c00000,v129
v_max_f32 v129,0,v129
v_sub_f32 v245,v245,v129
v_exp_f32 v133,-v129
v_sub_f32 v168,v168,v129
v_sub_f32 v169,v169,v129
v_sub_f32 v170,v170,v129
v_sub_f32 v171,v171,v129
v_sub_f32 v172,v172,v129
v_sub_f32 v173,v173,v129
v_sub_f32 v174,v174,v129
v_sub_f32 v175,v175,v129
v_mul_f32 v228,v228,v133
v_mul_f32 v229,v229,v133
v_mul_f32 v230,v230,v133
v_mul_f32 v231,v231,v133
v_accvgpr_read_b32 v136,a64
v_accvgpr_read_b32 v137,a65
v_accvgpr_read_b32 v138,a66
v_accvgpr_read_b32 v139,a67
v_accvgpr_read_b32 v140,a68
v_accvgpr_read_b32 v141,a69
v_accvgpr_read_b32 v142,a70
v_accvgpr_read_b32 v143,a71
v_mul_f32 v136,v136,v133
v_mul_f32 v137,v137,v133
v_mul_f32 v138,v138,v133
v_mul_f32 v139,v139,v133
v_mul_f32 v140,v140,v133
v_mul_f32 v141,v141,v133
v_mul_f32 v142,v142,v133
v_mul_f32 v143,v143,v133
v_accvgpr_write_b32 a64,v136
v_accvgpr_write_b32 a65,v137
v_accvgpr_write_b32 a66,v138
v_accvgpr_write_b32 a67,v139
v_accvgpr_write_b32 a68,v140
v_accvgpr_write_b32 a69,v141
v_accvgpr_write_b32 a70,v142
v_accvgpr_write_b32 a71,v143
v_accvgpr_read_b32 v136,a72
v_accvgpr_read_b32 v137,a73
v_accvgpr_read_b32 v138,a74
v_accvgpr_read_b32 v139,a75
)"
R"(v_accvgpr_read_b32 v140,a76
v_accvgpr_read_b32 v141,a77
v_accvgpr_read_b32 v142,a78
v_accvgpr_read_b32 v143,a79
v_mul_f32 v136,v136,v133
v_mul_f32 v137,v137,v133
v_mul_f32 v138,v138,v133
v_mul_f32 v139,v139,v133
v_mul_f32 v140,v140,v133
v_mul_f32 v141,v141,v133
v_mul_f32 v142,v142,v133
v_mul_f32 v143,v143,v133
v_accvgpr_write_b32 a72,v136
v_accvgpr_write_b32 a73,v137
v_accvgpr_write_b32 a74,v138
v_accvgpr_write_b32 a75,v139
v_accvgpr_write_b32 a76,v140
v_accvgpr_write_b32 a77,v141
v_accvgpr_write_b32 a78,v142
v_accvgpr_write_b32 a79,v143
v_accvgpr_read_b32 v136,a80
v_accvgpr_read_b32 v137,a81
v_accvgpr_read_b32 v138,a82
v_accvgpr_read_b32 v139,a83
v_accvgpr_read_b32 v140,a84
v_accvgpr_read_b32 v141,a85
v_accvgpr_read_b32 v142,a86
v_accvgpr_read_b32 v143,a87
v_mul_f32 v136,v136,v133
v_mul_f32 v137,v137,v133
v_mul_f32 v138,v138,v133
v_mul_f32 v139,v139,v133
v_mul_f32 v140,v140,v133
v_mul_f32 v141,v141,v133
v_mul_f32 v142,v142,v133
v_mul_f32 v143,v143,v133
v_accvgpr_write_b32 a80,v136
v_accvgpr_write_b32 a81,v137
v_accvgpr_write_b32 a82,v138
v_accvgpr_write_b32 a83,v139
v_accvgpr_write_b32 a84,v140
v_accvgpr_write_b32 a85,v141
v_accvgpr_write_b32 a86,v142
v_accvgpr_write_b32 a87,v143
v_accvgpr_read_b32 v136,a88
v_accvgpr_read_b32 v137,a89
v_accvgpr_read_b32 v138,a90
v_accvgpr_read_b32 v139,a91
v_accvgpr_read_b32 v140,a92
v_accvgpr_read_b32 v141,a93
v_accvgpr_read_b32 v142,a94
v_accvgpr_read_b32 v143,a95
v_mul_f32 v136,v136,v133
v_mul_f32 v137,v137,v133
v_mul_f32 v138,v138,v133
v_mul_f32 v139,v139,v133
v_mul_f32 v140,v140,v133
v_mul_f32 v141,v141,v133
v_mul_f32 v142,v142,v133
v_mul_f32 v143,v143,v133
v_accvgpr_write_b32 a88,v136
v_accvgpr_write_b32 a89,v137
v_accvgpr_write_b32 a90,v138
v_accvgpr_write_b32 a91,v139
v_accvgpr_write_b32 a92,v140
v_accvgpr_write_b32 a93,v141
v_accvgpr_write_b32 a94,v142
v_accvgpr_write_b32 a95,v143
v_accvgpr_read_b32 v136,a96
v_accvgpr_read_b32 v137,a97
v_accvgpr_read_b32 v138,a98
v_accvgpr_read_b32 v139,a99
v_accvgpr_read_b32 v140,a100
v_accvgpr_read_b32 v141,a101
v_accvgpr_read_b32 v142,a102
v_accvgpr_read_b32 v143,a103
v_mul_f32 v136,v136,v133
v_mul_f32 v137,v137,v133
v_mul_f32 v138,v138,v133
v_mul_f32 v139,v139,v133
v_mul_f32 v140,v140,v133
v_mul_f32 v141,v141,v133
v_mul_f32 v142,v142,v133
v_mul_f32 v143,v143,v133
v_accvgpr_write_b32 a96,v136
v_accvgpr_write_b32 a97,v137
v_accvgpr_write_b32 a98,v138
v_accvgpr_write_b32 a99,v139
v_accvgpr_write_b32 a100,v140
v_accvgpr_write_b32 a101,v141
v_accvgpr_write_b32 a102,v142
v_accvgpr_write_b32 a103,v143
v_accvgpr_read_b32 v136,a104
v_accvgpr_read_b32 v137,a105
v_accvgpr_read_b32 v138,a106
v_accvgpr_read_b32 v139,a107
v_accvgpr_read_b32 v140,a108
v_accvgpr_read_b32 v141,a109
v_accvgpr_read_b32 v142,a110
v_accvgpr_read_b32 v143,a111
v_mul_f32 v136,v136,v133
v_mul_f32 v137,v137,v133
v_mul_f32 v138,v138,v133
v_mul_f32 v139,v139,v133
v_mul_f32 v140,v140,v133
v_mul_f32 v141,v141,v133
v_mul_f32 v142,v142,v133
v_mul_f32 v143,v143,v133
v_accvgpr_write_b32 a104,v136
v_accvgpr_write_b32 a105,v137
v_accvgpr_write_b32 a106,v138
v_accvgpr_write_b32 a107,v139
v_accvgpr_write_b32 a108,v140
v_accvgpr_write_b32 a109,v141
v_accvgpr_write_b32 a110,v142
v_accvgpr_write_b32 a111,v143
v_accvgpr_read_b32 v136,a112
v_accvgpr_read_b32 v137,a113
v_accvgpr_read_b32 v138,a114
v_accvgpr_read_b32 v139,a115
v_accvgpr_read_b32 v140,a116
v_accvgpr_read_b32 v141,a117
v_accvgpr_read_b32 v142,a118
v_accvgpr_read_b32 v143,a119
v_mul_f32 v136,v136,v133
v_mul_f32 v137,v137,v133
v_mul_f32 v138,v138,v133
v_mul_f32 v139,v139,v133
v_mul_f32 v140,v140,v133
v_mul_f32 v141,v141,v133
v_mul_f32 v142,v142,v133
v_mul_f32 v143,v143,v133
v_accvgpr_write_b32 a112,v136
v_accvgpr_write_b32 a113,v137
v_accvgpr_write_b32 a114,v138
v_accvgpr_write_b32 a115,v139
v_accvgpr_write_b32 a116,v140
v_accvgpr_write_b32 a117,v141
v_accvgpr_write_b32 a118,v142
v_accvgpr_write_b32 a119,v143
v_accvgpr_read_b32 v136,a120
v_accvgpr_read_b32 v137,a121
v_accvgpr_read_b32 v138,a122
v_accvgpr_read_b32 v139,a123
v_accvgpr_read_b32 v140,a124
v_accvgpr_read_b32 v141,a125
v_accvgpr_read_b32 v142,a126
v_accvgpr_read_b32 v143,a127
v_mul_f32 v136,v136,v133
v_mul_f32 v137,v137,v133
v_mul_f32 v138,v138,v133
v_mul_f32 v139,v139,v133
v_mul_f32 v140,v140,v133
v_mul_f32 v141,v141,v133
v_mul_f32 v142,v142,v133
v_mul_f32 v143,v143,v133
v_accvgpr_write_b32 a120,v136
v_accvgpr_write_b32 a121,v137
v_accvgpr_write_b32 a122,v138
v_accvgpr_write_b32 a123,v139
v_accvgpr_write_b32 a124,v140
v_accvgpr_write_b32 a125,v141
v_accvgpr_write_b32 a126,v142
v_accvgpr_write_b32 a127,v143
v_add_f32 v130,0x42c00000,v130
v_max_f32 v130,0,v130
v_sub_f32 v246,v246,v130
v_exp_f32 v134,-v130
v_sub_f32 v176,v176,v130
v_sub_f32 v177,v177,v130
v_sub_f32 v178,v178,v130
v_sub_f32 v179,v179,v130
v_sub_f32 v180,v180,v130
v_sub_f32 v181,v181,v130
v_sub_f32 v182,v182,v130
v_sub_f32 v183,v183,v130
v_mul_f32 v232,v232,v134
v_mul_f32 v233,v233,v134
v_mul_f32 v234,v234,v134
v_mul_f32 v235,v235,v134
v_accvgpr_read_b32 v136,a128
v_accvgpr_read_b32 v137,a129
v_accvgpr_read_b32 v138,a130
v_accvgpr_read_b32 v139,a131
v_accvgpr_read_b32 v140,a132
v_accvgpr_read_b32 v141,a133
v_accvgpr_read_b32 v142,a134
v_accvgpr_read_b32 v143,a135
v_mul_f32 v136,v136,v134
v_mul_f32 v137,v137,v134
v_mul_f32 v138,v138,v134
v_mul_f32 v139,v139,v134
v_mul_f32 v140,v140,v134
v_mul_f32 v141,v141,v134
v_mul_f32 v142,v142,v134
v_mul_f32 v143,v143,v134
v_accvgpr_write_b32 a128,v136
v_accvgpr_write_b32 a129,v137
v_accvgpr_write_b32 a130,v138
v_accvgpr_write_b32 a131,v139
)"
R"(v_accvgpr_write_b32 a132,v140
v_accvgpr_write_b32 a133,v141
v_accvgpr_write_b32 a134,v142
v_accvgpr_write_b32 a135,v143
v_accvgpr_read_b32 v136,a136
v_accvgpr_read_b32 v137,a137
v_accvgpr_read_b32 v138,a138
v_accvgpr_read_b32 v139,a139
v_accvgpr_read_b32 v140,a140
v_accvgpr_read_b32 v141,a141
v_accvgpr_read_b32 v142,a142
v_accvgpr_read_b32 v143,a143
v_mul_f32 v136,v136,v134
v_mul_f32 v137,v137,v134
v_mul_f32 v138,v138,v134
v_mul_f32 v139,v139,v134
v_mul_f32 v140,v140,v134
v_mul_f32 v141,v141,v134
v_mul_f32 v142,v142,v134
v_mul_f32 v143,v143,v134
v_accvgpr_write_b32 a136,v136
v_accvgpr_write_b32 a137,v137
v_accvgpr_write_b32 a138,v138
v_accvgpr_write_b32 a139,v139
v_accvgpr_write_b32 a140,v140
v_accvgpr_write_b32 a141,v141
v_accvgpr_write_b32 a142,v142
v_accvgpr_write_b32 a143,v143
v_accvgpr_read_b32 v136,a144
v_accvgpr_read_b32 v137,a145
v_accvgpr_read_b32 v138,a146
v_accvgpr_read_b32 v139,a147
v_accvgpr_read_b32 v140,a148
v_accvgpr_read_b32 v141,a149
v_accvgpr_read_b32 v142,a150
v_accvgpr_read_b32 v143,a151
v_mul_f32 v136,v136,v134
v_mul_f32 v137,v137,v134
v_mul_f32 v138,v138,v134
v_mul_f32 v139,v139,v134
v_mul_f32 v140,v140,v134
v_mul_f32 v141,v141,v134
v_mul_f32 v142,v142,v134
v_mul_f32 v143,v143,v134
v_accvgpr_write_b32 a144,v136
v_accvgpr_write_b32 a145,v137
v_accvgpr_write_b32 a146,v138
v_accvgpr_write_b32 a147,v139
v_accvgpr_write_b32 a148,v140
v_accvgpr_write_b32 a149,v141
v_accvgpr_write_b32 a150,v142
v_accvgpr_write_b32 a151,v143
v_accvgpr_read_b32 v136,a152
v_accvgpr_read_b32 v137,a153
v_accvgpr_read_b32 v138,a154
v_accvgpr_read_b32 v139,a155
v_accvgpr_read_b32 v140,a156
v_accvgpr_read_b32 v141,a157
v_accvgpr_read_b32 v142,a158
v_accvgpr_read_b32 v143,a159
v_mul_f32 v136,v136,v134
v_mul_f32 v137,v137,v134
v_mul_f32 v138,v138,v134
v_mul_f32 v139,v139,v134
v_mul_f32 v140,v140,v134
v_mul_f32 v141,v141,v134
v_mul_f32 v142,v142,v134
v_mul_f32 v143,v143,v134
v_accvgpr_write_b32 a152,v136
v_accvgpr_write_b32 a153,v137
v_accvgpr_write_b32 a154,v138
v_accvgpr_write_b32 a155,v139
v_accvgpr_write_b32 a156,v140
v_accvgpr_write_b32 a157,v141
v_accvgpr_write_b32 a158,v142
v_accvgpr_write_b32 a159,v143
v_accvgpr_read_b32 v136,a160
v_accvgpr_read_b32 v137,a161
v_accvgpr_read_b32 v138,a162
v_accvgpr_read_b32 v139,a163
v_accvgpr_read_b32 v140,a164
v_accvgpr_read_b32 v141,a165
v_accvgpr_read_b32 v142,a166
v_accvgpr_read_b32 v143,a167
v_mul_f32 v136,v136,v134
v_mul_f32 v137,v137,v134
v_mul_f32 v138,v138,v134
v_mul_f32 v139,v139,v134
v_mul_f32 v140,v140,v134
v_mul_f32 v141,v141,v134
v_mul_f32 v142,v142,v134
v_mul_f32 v143,v143,v134
v_accvgpr_write_b32 a160,v136
v_accvgpr_write_b32 a161,v137
v_accvgpr_write_b32 a162,v138
v_accvgpr_write_b32 a163,v139
v_accvgpr_write_b32 a164,v140
v_accvgpr_write_b32 a165,v141
v_accvgpr_write_b32 a166,v142
v_accvgpr_write_b32 a167,v143
v_accvgpr_read_b32 v136,a168
v_accvgpr_read_b32 v137,a169
v_accvgpr_read_b32 v138,a170
v_accvgpr_read_b32 v139,a171
v_accvgpr_read_b32 v140,a172
v_accvgpr_read_b32 v141,a173
v_accvgpr_read_b32 v142,a174
v_accvgpr_read_b32 v143,a175
v_mul_f32 v136,v136,v134
v_mul_f32 v137,v137,v134
v_mul_f32 v138,v138,v134
v_mul_f32 v139,v139,v134
v_mul_f32 v140,v140,v134
v_mul_f32 v141,v141,v134
v_mul_f32 v142,v142,v134
v_mul_f32 v143,v143,v134
v_accvgpr_write_b32 a168,v136
v_accvgpr_write_b32 a169,v137
v_accvgpr_write_b32 a170,v138
v_accvgpr_write_b32 a171,v139
v_accvgpr_write_b32 a172,v140
v_accvgpr_write_b32 a173,v141
v_accvgpr_write_b32 a174,v142
v_accvgpr_write_b32 a175,v143
v_accvgpr_read_b32 v136,a176
v_accvgpr_read_b32 v137,a177
v_accvgpr_read_b32 v138,a178
v_accvgpr_read_b32 v139,a179
v_accvgpr_read_b32 v140,a180
v_accvgpr_read_b32 v141,a181
v_accvgpr_read_b32 v142,a182
v_accvgpr_read_b32 v143,a183
v_mul_f32 v136,v136,v134
v_mul_f32 v137,v137,v134
v_mul_f32 v138,v138,v134
v_mul_f32 v139,v139,v134
v_mul_f32 v140,v140,v134
v_mul_f32 v141,v141,v134
v_mul_f32 v142,v142,v134
v_mul_f32 v143,v143,v134
v_accvgpr_write_b32 a176,v136
v_accvgpr_write_b32 a177,v137
v_accvgpr_write_b32 a178,v138
v_accvgpr_write_b32 a179,v139
v_accvgpr_write_b32 a180,v140
v_accvgpr_write_b32 a181,v141
v_accvgpr_write_b32 a182,v142
v_accvgpr_write_b32 a183,v143
v_accvgpr_read_b32 v136,a184
v_accvgpr_read_b32 v137,a185
v_accvgpr_read_b32 v138,a186
v_accvgpr_read_b32 v139,a187
v_accvgpr_read_b32 v140,a188
v_accvgpr_read_b32 v141,a189
v_accvgpr_read_b32 v142,a190
v_accvgpr_read_b32 v143,a191
v_mul_f32 v136,v136,v134
v_mul_f32 v137,v137,v134
v_mul_f32 v138,v138,v134
v_mul_f32 v139,v139,v134
v_mul_f32 v140,v140,v134
v_mul_f32 v141,v141,v134
v_mul_f32 v142,v142,v134
v_mul_f32 v143,v143,v134
v_accvgpr_write_b32 a184,v136
v_accvgpr_write_b32 a185,v137
v_accvgpr_write_b32 a186,v138
v_accvgpr_write_b32 a187,v139
v_accvgpr_write_b32 a188,v140
v_accvgpr_write_b32 a189,v141
v_accvgpr_write_b32 a190,v142
v_accvgpr_write_b32 a191,v143
v_add_f32 v131,0x42c00000,v131
v_max_f32 v131,0,v131
v_sub_f32 v247,v247,v131
v_exp_f32 v135,-v131
v_sub_f32 v184,v184,v131
v_sub_f32 v185,v185,v131
v_sub_f32 v186,v186,v131
v_sub_f32 v187,v187,v131
v_sub_f32 v188,v188,v131
v_sub_f32 v189,v189,v131
v_sub_f32 v190,v190,v131
v_sub_f32 v191,v191,v131
v_mul_f32 v236,v236,v135
v_mul_f32 v237,v237,v135
v_mul_f32 v238,v238,v135
v_mul_f32 v239,v239,v135
v_accvgpr_read_b32 v136,a192
v_accvgpr_read_b32 v137,a193
v_accvgpr_read_b32 v138,a194
v_accvgpr_read_b32 v139,a195
v_accvgpr_read_b32 v140,a196
v_accvgpr_read_b32 v141,a197
v_accvgpr_read_b32 v142,a198
v_accvgpr_read_b32 v143,a199
v_mul_f32 v136,v136,v135
v_mul_f32 v137,v137,v135
v_mul_f32 v138,v138,v135
v_mul_f32 v139,v139,v135
)"
R"(v_mul_f32 v140,v140,v135
v_mul_f32 v141,v141,v135
v_mul_f32 v142,v142,v135
v_mul_f32 v143,v143,v135
v_accvgpr_write_b32 a192,v136
v_accvgpr_write_b32 a193,v137
v_accvgpr_write_b32 a194,v138
v_accvgpr_write_b32 a195,v139
v_accvgpr_write_b32 a196,v140
v_accvgpr_write_b32 a197,v141
v_accvgpr_write_b32 a198,v142
v_accvgpr_write_b32 a199,v143
v_accvgpr_read_b32 v136,a200
v_accvgpr_read_b32 v137,a201
v_accvgpr_read_b32 v138,a202
v_accvgpr_read_b32 v139,a203
v_accvgpr_read_b32 v140,a204
v_accvgpr_read_b32 v141,a205
v_accvgpr_read_b32 v142,a206
v_accvgpr_read_b32 v143,a207
v_mul_f32 v136,v136,v135
v_mul_f32 v137,v137,v135
v_mul_f32 v138,v138,v135
v_mul_f32 v139,v139,v135
v_mul_f32 v140,v140,v135
v_mul_f32 v141,v141,v135
v_mul_f32 v142,v142,v135
v_mul_f32 v143,v143,v135
v_accvgpr_write_b32 a200,v136
v_accvgpr_write_b32 a201,v137
v_accvgpr_write_b32 a202,v138
v_accvgpr_write_b32 a203,v139
v_accvgpr_write_b32 a204,v140
v_accvgpr_write_b32 a205,v141
v_accvgpr_write_b32 a206,v142
v_accvgpr_write_b32 a207,v143
v_accvgpr_read_b32 v136,a208
v_accvgpr_read_b32 v137,a209
v_accvgpr_read_b32 v138,a210
v_accvgpr_read_b32 v139,a211
v_accvgpr_read_b32 v140,a212
v_accvgpr_read_b32 v141,a213
v_accvgpr_read_b32 v142,a214
v_accvgpr_read_b32 v143,a215
v_mul_f32 v136,v136,v135
v_mul_f32 v137,v137,v135
v_mul_f32 v138,v138,v135
v_mul_f32 v139,v139,v135
v_mul_f32 v140,v140,v135
v_mul_f32 v141,v141,v135
v_mul_f32 v142,v142,v135
v_mul_f32 v143,v143,v135
v_accvgpr_write_b32 a208,v136
v_accvgpr_write_b32 a209,v137
v_accvgpr_write_b32 a210,v138
v_accvgpr_write_b32 a211,v139
v_accvgpr_write_b32 a212,v140
v_accvgpr_write_b32 a213,v141
v_accvgpr_write_b32 a214,v142
v_accvgpr_write_b32 a215,v143
v_accvgpr_read_b32 v136,a216
v_accvgpr_read_b32 v137,a217
v_accvgpr_read_b32 v138,a218
v_accvgpr_read_b32 v139,a219
v_accvgpr_read_b32 v140,a220
v_accvgpr_read_b32 v141,a221
v_accvgpr_read_b32 v142,a222
v_accvgpr_read_b32 v143,a223
v_mul_f32 v136,v136,v135
v_mul_f32 v137,v137,v135
v_mul_f32 v138,v138,v135
v_mul_f32 v139,v139,v135
v_mul_f32 v140,v140,v135
v_mul_f32 v141,v141,v135
v_mul_f32 v142,v142,v135
v_mul_f32 v143,v143,v135
v_accvgpr_write_b32 a216,v136
v_accvgpr_write_b32 a217,v137
v_accvgpr_write_b32 a218,v138
v_accvgpr_write_b32 a219,v139
v_accvgpr_write_b32 a220,v140
v_accvgpr_write_b32 a221,v141
v_accvgpr_write_b32 a222,v142
v_accvgpr_write_b32 a223,v143
v_accvgpr_read_b32 v136,a224
v_accvgpr_read_b32 v137,a225
v_accvgpr_read_b32 v138,a226
v_accvgpr_read_b32 v139,a227
v_accvgpr_read_b32 v140,a228
v_accvgpr_read_b32 v141,a229
v_accvgpr_read_b32 v142,a230
v_accvgpr_read_b32 v143,a231
v_mul_f32 v136,v136,v135
v_mul_f32 v137,v137,v135
v_mul_f32 v138,v138,v135
v_mul_f32 v139,v139,v135
v_mul_f32 v140,v140,v135
v_mul_f32 v141,v141,v135
v_mul_f32 v142,v142,v135
v_mul_f32 v143,v143,v135
v_accvgpr_write_b32 a224,v136
v_accvgpr_write_b32 a225,v137
v_accvgpr_write_b32 a226,v138
v_accvgpr_write_b32 a227,v139
v_accvgpr_write_b32 a228,v140
v_accvgpr_write_b32 a229,v141
v_accvgpr_write_b32 a230,v142
v_accvgpr_write_b32 a231,v143
v_accvgpr_read_b32 v136,a232
v_accvgpr_read_b32 v137,a233
v_accvgpr_read_b32 v138,a234
v_accvgpr_read_b32 v139,a235
v_accvgpr_read_b32 v140,a236
v_accvgpr_read_b32 v141,a237
v_accvgpr_read_b32 v142,a238
v_accvgpr_read_b32 v143,a239
v_mul_f32 v136,v136,v135
v_mul_f32 v137,v137,v135
v_mul_f32 v138,v138,v135
v_mul_f32 v139,v139,v135
v_mul_f32 v140,v140,v135
v_mul_f32 v141,v141,v135
v_mul_f32 v142,v142,v135
v_mul_f32 v143,v143,v135
v_accvgpr_write_b32 a232,v136
v_accvgpr_write_b32 a233,v137
v_accvgpr_write_b32 a234,v138
v_accvgpr_write_b32 a235,v139
v_accvgpr_write_b32 a236,v140
v_accvgpr_write_b32 a237,v141
v_accvgpr_write_b32 a238,v142
v_accvgpr_write_b32 a239,v143
v_accvgpr_read_b32 v136,a240
v_accvgpr_read_b32 v137,a241
v_accvgpr_read_b32 v138,a242
v_accvgpr_read_b32 v139,a243
v_accvgpr_read_b32 v140,a244
v_accvgpr_read_b32 v141,a245
v_accvgpr_read_b32 v142,a246
v_accvgpr_read_b32 v143,a247
v_mul_f32 v136,v136,v135
v_mul_f32 v137,v137,v135
v_mul_f32 v138,v138,v135
v_mul_f32 v139,v139,v135
v_mul_f32 v140,v140,v135
v_mul_f32 v141,v141,v135
v_mul_f32 v142,v142,v135
v_mul_f32 v143,v143,v135
v_accvgpr_write_b32 a240,v136
v_accvgpr_write_b32 a241,v137
v_accvgpr_write_b32 a242,v138
v_accvgpr_write_b32 a243,v139
v_accvgpr_write_b32 a244,v140
v_accvgpr_write_b32 a245,v141
v_accvgpr_write_b32 a246,v142
v_accvgpr_write_b32 a247,v143
v_accvgpr_read_b32 v136,a248
v_accvgpr_read_b32 v137,a249
v_accvgpr_read_b32 v138,a250
v_accvgpr_read_b32 v139,a251
v_accvgpr_read_b32 v140,a252
v_accvgpr_read_b32 v141,a253
v_accvgpr_read_b32 v142,a254
v_accvgpr_read_b32 v143,a255
v_mul_f32 v136,v136,v135
v_mul_f32 v137,v137,v135
v_mul_f32 v138,v138,v135
v_mul_f32 v139,v139,v135
v_mul_f32 v140,v140,v135
v_mul_f32 v141,v141,v135
v_mul_f32 v142,v142,v135
v_mul_f32 v143,v143,v135
v_accvgpr_write_b32 a248,v136
v_accvgpr_write_b32 a249,v137
v_accvgpr_write_b32 a250,v138
v_accvgpr_write_b32 a251,v139
v_accvgpr_write_b32 a252,v140
v_accvgpr_write_b32 a253,v141
v_accvgpr_write_b32 a254,v142
v_accvgpr_write_b32 a255,v143
s_nop 3
s_setpc_b64 s[76:77]
Lenda_%=:
)"
"s_endpgm\nLodd_%=:\n"
R"(s_mov_b64 s[68:69],s[60:61]
s_add_u32 s68,s68,0x6400
s_addc_u32 s69,s69,0
s_mov_b32 s70,0x181180
s_mov_b32 s71,0x20000
s_mov_b64 s[64:65],s[56:57]
s_add_u32 s64,s64,0x6800
s_addc_u32 s65,s65,0
s_mov_b32 s66,0x180d80
s_mov_b32 s67,0x20000
s_add_u32 s60,s60,0xfffffc00
s_addc_u32 s61,s61,0xffffffff
s_mov_b32 s62,0x187980
s_mov_b32 s63,0x20000
s_mov_b32 s58,0x187580
s_mov_b32 s59,0x20000
v_mbcnt_lo_u32_b32 v237,-1,0
v_mbcnt_hi_u32_b32 v237,-1,v237
s_lshl_b32 s99,%[w],4
v_bfe_u32 v238,v237,2,3
v_add_u32 v238,s99,v238
v_mul_u32_u24 v230,0xe00,v238
v_lshrrev_b32 v238,5,v237
v_lshl_add_u32 v230,v238,6,v230
v_and_b32 v238,3,v237
v_lshl_add_u32 v230,v238,4,v230
v_xor_b32 v231,32,v230
s_and_b32 s97,%[h],2
s_mul_i32 s97,s97,192
v_add_u32 v230,s97,v230
v_add_u32 v231,s97,v231
v_accvgpr_write_b32 a255,v63
v_mbcnt_lo_u32_b32 v254,-1,0
v_mbcnt_hi_u32_b32 v254,-1,v254
v_lshlrev_b32 v254,4,v254
s_and_b32 s99,%[tb],124
s_add_u32 s99,s99,%[w]
s_lshl_b32 s99,s99,10
s_mul_i32 s99,s99,7
s_lshr_b32 s99,s99,11
v_mbcnt_lo_u32_b32 v237,-1,0
v_mbcnt_hi_u32_b32 v237,-1,v237
v_bfe_u32 v238,v237,3,2
v_add_u32 v238,s99,v238
v_and_b32 v237,7,v237
v_mul_u32_u24 v176,3584,v238
v_min_u32 v175,3,v237
v_lshl_add_u32 v176,v175,7,v176
v_add_u32 v176,s97,v176
v_lshlrev_b32 v177,13,v238
v_lshl_add_u32 v177,v237,7,v177
s_mov_b32 s26,0x100000
s_mov_b32 s27,0x20000
s_mov_b64 s[28:29],s[24:25]
s_add_u32 s28,s28,0xfffffc80
s_addc_u32 s29,s29,0xffffffff
s_mov_b32 s30,0x100380
s_mov_b32 s31,0x20000
s_mov_b64 s[32:33],s[24:25]
s_add_u32 s32,s32,0xfffff900
s_addc_u32 s33,s33,0xffffffff
s_mov_b32 s34,0x100700
s_mov_b32 s35,0x20000
s_mov_b64 s[36:37],s[24:25]
s_add_u32 s36,s36,0xfffff580
s_addc_u32 s37,s37,0xffffffff
s_mov_b32 s38,0x100a80
s_mov_b32 s39,0x20000
s_mov_b32 s42,0x100000
s_mov_b32 s43,0x20000
s_mov_b64 s[44:45],s[40:41]
s_add_u32 s44,s44,0xfffffc80
s_addc_u32 s45,s45,0xffffffff
s_mov_b32 s46,0x100380
s_mov_b32 s47,0x20000
s_mov_b64 s[48:49],s[40:41]
s_add_u32 s48,s48,0xfffff900
s_addc_u32 s49,s49,0xffffffff
s_mov_b32 s50,0x100700
s_mov_b32 s51,0x20000
s_mov_b64 s[52:53],s[40:41]
s_add_u32 s52,s52,0xfffff580
s_addc_u32 s53,s53,0xffffffff
s_mov_b32 s54,0x100a80
s_mov_b32 s55,0x20000
v_mbcnt_lo_u32_b32 v237,-1,0
v_mbcnt_hi_u32_b32 v237,-1,v237
s_lshl_b32 s99,%[w],4
v_bfe_u32 v238,v237,2,3
v_add_u32 v238,s99,v238
v_mul_u32_u24 v232,0x2000,v238
v_lshrrev_b32 v238,5,v237
v_lshl_add_u32 v232,v238,6,v232
v_and_b32 v238,3,v237
v_lshl_add_u32 v232,v238,4,v232
v_xor_b32 v233,32,v232
v_mbcnt_lo_u32_b32 v237,-1,0
v_mbcnt_hi_u32_b32 v237,-1,v237
s_mul_i32 s99,%[w],8
v_bfe_u32 v238,v237,2,3
v_add_u32 v238,s99,v238
v_mul_u32_u24 v253,0x200,v238
v_lshrrev_b32 v238,5,v237
v_lshl_add_u32 v253,v238,6,v253
v_lshrrev_b32 v238,3,v237
v_and_b32 v238,2,v238
v_and_b32 v237,3,v237
v_xor_b32 v238,v238,v237
v_lshl_add_u32 v253,v238,4,v253
v_mbcnt_lo_u32_b32 v237,-1,0
v_mbcnt_hi_u32_b32 v237,-1,v237
v_bfe_u32 v238,v237,2,2
v_lshlrev_b32 v226,6,v238
v_and_b32 v238,3,v237
v_lshl_add_u32 v226,v238,3,v226
v_bfe_u32 v238,v237,4,1
v_lshl_add_u32 v226,v238,5,v226
v_lshrrev_b32 v238,4,v237
v_lshl_add_u32 v226,v238,11,v226
s_lshr_b32 s99,%[w],1
s_lshl_b32 s101,s99,9
s_and_b32 s98,%[w],1
s_lshl_b32 s100,s98,5
s_lshl_b32 s98,s98,14
v_add_u32 v226,s101,v226
v_add_u32 v224,s98,v226
v_xor_b32 v225,32,v224
v_xor_b32 v226,s100,v226
s_mov_b32 s93,0
s_mov_b32 s94,0
v_add_u32 v227,65536,v224
v_add_u32 v228,65536,v225
v_add_u32 v229,65536,v226
v_mbcnt_lo_u32_b32 v237,-1,0
v_mbcnt_hi_u32_b32 v237,-1,v237
v_bfe_u32 v238,v237,2,3
v_lshlrev_b32 v250,6,v238
v_and_b32 v238,3,v237
v_lshl_add_u32 v250,v238,3,v250
v_bfe_u32 v238,v237,4,1
v_lshl_add_u32 v250,v238,5,v250
v_lshrrev_b32 v238,5,v237
v_lshl_add_u32 v250,v238,12,v250
s_lshl_b32 s101,s99,14
s_add_u32 s98,s101,65536
v_add_u32 v250,s98,v250
v_xor_b32 v251,32,v250
v_mbcnt_lo_u32_b32 v237,-1,0
v_mbcnt_hi_u32_b32 v237,-1,v237
v_lshrrev_b32 v238,4,v237
v_bfe_u32 v239,v237,1,3
v_xor_b32 v238,v238,v239
v_and_b32 v248,4,v238
v_and_b32 v239,1,v238
v_lshl_add_u32 v248,v239,1,v248
v_bfe_u32 v239,v238,1,1
v_add_u32 v248,v248,v239
s_lshl_b32 s98,s99,2
v_xor_b32 v248,s98,v248
v_lshlrev_b32 v248,4,v248
v_and_b32 v238,7,v237
v_lshl_add_u32 v248,v238,7,v248
v_bfe_u32 v238,v237,3,1
v_lshl_add_u32 v248,v238,12,v248
v_add_u32 v248,s101,v248
v_xor_b32 v249,64,v248
v_lshrrev_b32 v238,4,v237
v_and_b32 v252,4,v238
v_and_b32 v239,1,v238
v_lshl_add_u32 v252,v239,1,v252
v_bfe_u32 v239,v238,1,1
v_add_u32 v252,v252,v239
s_and_b32 s98,%[w],1
s_lshl_b32 s98,s98,2
v_or_b32 v252,s98,v252
v_and_b32 v238,7,v237
v_xor_b32 v252,v252,v238
v_lshlrev_b32 v252,4,v252
v_lshrrev_b32 v238,3,v237
v_lshl_add_u32 v252,v238,9,v252
v_add_u32 v252,s92,v252
s_lshl_b32 s98,%[w],13
s_add_u32 s98,s98,98304
v_add_u32 v235,s98,v254
v_xor_b32 v236,16384,v235
s_mul_i32 s96,%[tb],57344
s_xor_b32 s97,%[tb],2
s_mul_i32 s97,s97,57344
s_add_u32 s96,s96,0x700000
s_add_u32 s97,s97,0x700000
s_mov_b32 s100,0xbdd2d3e8
v_mov_b32 v234,0xc0135761
v_mbcnt_lo_u32_b32 v237,-1,0
v_mbcnt_hi_u32_b32 v237,-1,v237
v_lshrrev_b32 v238,4,v237
v_bfe_u32 v239,v237,1,3
v_xor_b32 v238,v238,v239
v_lshlrev_b32 v146,4,v238
v_and_b32 v238,7,v237
v_lshl_add_u32 v146,v238,7,v146
)"
R"(v_bfe_u32 v238,v237,3,1
v_lshl_add_u32 v146,v238,10,v146
s_lshr_b32 s98,%[w],1
s_mul_i32 s98,s98,65536
s_sub_u32 s101,131072,s98
s_add_u32 s98,s98,65536
v_add_u32 v148,s101,v146
v_add_u32 v146,s98,v146
v_xor_b32 v147,64,v146
v_xor_b32 v149,64,v148
s_and_b32 s101,%[w],1
s_lshl_b32 s101,s101,14
s_add_u32 s98,s98,s101
v_add_u32 v150,s98,v254
s_mov_b32 s99,0x3f803f80
s_mov_b32 s98,0x00003f80
s_mov_b32 s101,0x3f800000
s_add_u32 m0,s92,0x0
s_mov_b32 s95,0x100
buffer_load_dwordx4 v230,s[56:59],s95 offen lds
buffer_load_dwordx4 v230,s[60:63],s95 offen offset:1024 lds
buffer_load_dwordx4 v231,s[64:67],s95 offen offset:2048 lds
buffer_load_dwordx4 v231,s[68:71],s95 offen offset:3072 lds
s_add_u32 m0,s92,0x4000
s_mov_b32 s95,0x180
buffer_load_dwordx4 v230,s[56:59],s95 offen lds
buffer_load_dwordx4 v230,s[60:63],s95 offen offset:1024 lds
buffer_load_dwordx4 v231,s[64:67],s95 offen offset:2048 lds
buffer_load_dwordx4 v231,s[68:71],s95 offen offset:3072 lds
v_mov_b32 v128,0
v_mov_b32 v129,0
v_mov_b32 v130,0
v_mov_b32 v131,0
s_waitcnt vmcnt(20)
v_dot2_f32_bf16 v129,v0,s99,v129
v_dot2_f32_bf16 v128,v0,v0,v128
v_dot2_f32_bf16 v131,v4,s99,v131
v_dot2_f32_bf16 v130,v4,v4,v130
v_dot2_f32_bf16 v129,v1,s99,v129
v_dot2_f32_bf16 v128,v1,v1,v128
v_dot2_f32_bf16 v131,v5,s99,v131
v_dot2_f32_bf16 v130,v5,v5,v130
v_dot2_f32_bf16 v129,v2,s99,v129
v_dot2_f32_bf16 v128,v2,v2,v128
v_dot2_f32_bf16 v131,v6,s99,v131
v_dot2_f32_bf16 v130,v6,v6,v130
v_dot2_f32_bf16 v129,v3,s99,v129
v_dot2_f32_bf16 v128,v3,v3,v128
v_dot2_f32_bf16 v131,v7,s99,v131
v_dot2_f32_bf16 v130,v7,v7,v130
s_waitcnt vmcnt(18)
v_dot2_f32_bf16 v129,v8,s99,v129
v_dot2_f32_bf16 v128,v8,v8,v128
v_dot2_f32_bf16 v131,v12,s99,v131
v_dot2_f32_bf16 v130,v12,v12,v130
v_dot2_f32_bf16 v129,v9,s99,v129
v_dot2_f32_bf16 v128,v9,v9,v128
v_dot2_f32_bf16 v131,v13,s99,v131
v_dot2_f32_bf16 v130,v13,v13,v130
v_dot2_f32_bf16 v129,v10,s99,v129
v_dot2_f32_bf16 v128,v10,v10,v128
v_dot2_f32_bf16 v131,v14,s99,v131
v_dot2_f32_bf16 v130,v14,v14,v130
v_dot2_f32_bf16 v129,v11,s99,v129
v_dot2_f32_bf16 v128,v11,v11,v128
v_dot2_f32_bf16 v131,v15,s99,v131
v_dot2_f32_bf16 v130,v15,v15,v130
s_waitcnt vmcnt(16)
v_dot2_f32_bf16 v129,v16,s99,v129
v_dot2_f32_bf16 v128,v16,v16,v128
v_dot2_f32_bf16 v131,v20,s99,v131
v_dot2_f32_bf16 v130,v20,v20,v130
v_dot2_f32_bf16 v129,v17,s99,v129
v_dot2_f32_bf16 v128,v17,v17,v128
v_dot2_f32_bf16 v131,v21,s99,v131
v_dot2_f32_bf16 v130,v21,v21,v130
v_dot2_f32_bf16 v129,v18,s99,v129
v_dot2_f32_bf16 v128,v18,v18,v128
v_dot2_f32_bf16 v131,v22,s99,v131
v_dot2_f32_bf16 v130,v22,v22,v130
v_dot2_f32_bf16 v129,v19,s99,v129
v_dot2_f32_bf16 v128,v19,v19,v128
v_dot2_f32_bf16 v131,v23,s99,v131
v_dot2_f32_bf16 v130,v23,v23,v130
s_waitcnt vmcnt(14)
v_dot2_f32_bf16 v129,v24,s99,v129
v_dot2_f32_bf16 v128,v24,v24,v128
v_dot2_f32_bf16 v131,v28,s99,v131
v_dot2_f32_bf16 v130,v28,v28,v130
v_dot2_f32_bf16 v129,v25,s99,v129
v_dot2_f32_bf16 v128,v25,v25,v128
v_dot2_f32_bf16 v131,v29,s99,v131
v_dot2_f32_bf16 v130,v29,v29,v130
v_dot2_f32_bf16 v129,v26,s99,v129
v_dot2_f32_bf16 v128,v26,v26,v128
v_dot2_f32_bf16 v131,v30,s99,v131
v_dot2_f32_bf16 v130,v30,v30,v130
v_dot2_f32_bf16 v129,v27,s99,v129
v_dot2_f32_bf16 v128,v27,v27,v128
v_dot2_f32_bf16 v131,v31,s99,v131
v_dot2_f32_bf16 v130,v31,v31,v130
s_waitcnt vmcnt(12)
v_dot2_f32_bf16 v129,v32,s99,v129
v_dot2_f32_bf16 v128,v32,v32,v128
v_dot2_f32_bf16 v131,v36,s99,v131
v_dot2_f32_bf16 v130,v36,v36,v130
v_dot2_f32_bf16 v129,v33,s99,v129
v_dot2_f32_bf16 v128,v33,v33,v128
v_dot2_f32_bf16 v131,v37,s99,v131
v_dot2_f32_bf16 v130,v37,v37,v130
v_dot2_f32_bf16 v129,v34,s99,v129
v_dot2_f32_bf16 v128,v34,v34,v128
v_dot2_f32_bf16 v131,v38,s99,v131
v_dot2_f32_bf16 v130,v38,v38,v130
v_dot2_f32_bf16 v129,v35,s99,v129
v_dot2_f32_bf16 v128,v35,v35,v128
v_dot2_f32_bf16 v131,v39,s99,v131
v_dot2_f32_bf16 v130,v39,v39,v130
s_waitcnt vmcnt(10)
v_dot2_f32_bf16 v129,v40,s99,v129
v_dot2_f32_bf16 v128,v40,v40,v128
v_dot2_f32_bf16 v131,v44,s99,v131
v_dot2_f32_bf16 v130,v44,v44,v130
v_dot2_f32_bf16 v129,v41,s99,v129
v_dot2_f32_bf16 v128,v41,v41,v128
v_dot2_f32_bf16 v131,v45,s99,v131
v_dot2_f32_bf16 v130,v45,v45,v130
v_dot2_f32_bf16 v129,v42,s99,v129
v_dot2_f32_bf16 v128,v42,v42,v128
v_dot2_f32_bf16 v131,v46,s99,v131
v_dot2_f32_bf16 v130,v46,v46,v130
v_dot2_f32_bf16 v129,v43,s99,v129
v_dot2_f32_bf16 v128,v43,v43,v128
v_dot2_f32_bf16 v131,v47,s99,v131
v_dot2_f32_bf16 v130,v47,v47,v130
s_waitcnt vmcnt(8)
v_dot2_f32_bf16 v129,v48,s99,v129
v_dot2_f32_bf16 v128,v48,v48,v128
v_dot2_f32_bf16 v131,v52,s99,v131
v_dot2_f32_bf16 v130,v52,v52,v130
v_dot2_f32_bf16 v129,v49,s99,v129
v_dot2_f32_bf16 v128,v49,v49,v128
v_dot2_f32_bf16 v131,v53,s99,v131
v_dot2_f32_bf16 v130,v53,v53,v130
v_dot2_f32_bf16 v129,v50,s99,v129
v_dot2_f32_bf16 v128,v50,v50,v128
v_dot2_f32_bf16 v131,v54,s99,v131
v_dot2_f32_bf16 v130,v54,v54,v130
v_dot2_f32_bf16 v129,v51,s99,v129
v_dot2_f32_bf16 v128,v51,v51,v128
v_dot2_f32_bf16 v131,v55,s99,v131
v_dot2_f32_bf16 v130,v55,v55,v130
s_nop 0
v_add_f32_dpp v128,v128,v128 quad_perm:[1,0,3,2] row_mask:0xf bank_mask:0xf
v_add_f32_dpp v129,v129,v129 quad_perm:[1,0,3,2] row_mask:0xf bank_mask:0xf
v_add_f32_dpp v130,v130,v130 quad_perm:[1,0,3,2] row_mask:0xf bank_mask:0xf
v_add_f32_dpp v131,v131,v131 quad_perm:[1,0,3,2] row_mask:0xf bank_mask:0xf
v_add_f32_dpp v128,v128,v128 quad_perm:[2,3,0,1] row_mask:0xf bank_mask:0xf
v_add_f32_dpp v129,v129,v129 quad_perm:[2,3,0,1] row_mask:0xf bank_mask:0xf
v_add_f32_dpp v130,v130,v130 quad_perm:[2,3,0,1] row_mask:0xf bank_mask:0xf
v_add_f32_dpp v131,v131,v131 quad_perm:[2,3,0,1] row_mask:0xf bank_mask:0xf
v_add_f32_dpp v128,v128,v128 row_half_mirror row_mask:0xf bank_mask:0xf
v_add_f32_dpp v129,v129,v129 row_half_mirror row_mask:0xf bank_mask:0xf
v_add_f32_dpp v130,v130,v130 row_half_mirror row_mask:0xf bank_mask:0xf
v_add_f32_dpp v131,v131,v131 row_half_mirror row_mask:0xf bank_mask:0xf
s_mov_b64 s[72:73],s[76:77]
s_mov_b32 s74,0x37e400
s_mov_b32 s75,0x20000
s_mov_b64 s[80:81],s[76:77]
s_add_u32 s80,s80,0xf800
s_addc_u32 s81,s81,0
s_mov_b32 s82,0x36ec00
s_mov_b32 s83,0x20000
s_mov_b64 s[84:85],s[76:77]
s_add_u32 s84,s84,0xf480
s_addc_u32 s85,s85,0
s_mov_b32 s86,0x36ef80
s_mov_b32 s87,0x20000
s_add_u32 s76,s76,0xfffffc80
s_addc_u32 s77,s77,0xffffffff
s_mov_b32 s78,0x37e780
s_mov_b32 s79,0x20000
v_mul_f32 v129,0x3b124925,v129
v_mul_f32 v131,0x3b124925,v131
v_mul_f32 v128,0x3b124925,v128
v_mul_f32 v130,0x3b124925,v130
v_fma_f32 v128,-v129,v129,v128
v_fma_f32 v130,-v131,v131,v130
v_max_f32 v128,0,v128
v_max_f32 v130,0,v130
v_cvt_pk_bf16_f32 v128,0,v128
v_cvt_pk_bf16_f32 v130,0,v130
v_add_f32 v128,0x3727c5ac,v128
v_add_f32 v130,0x3727c5ac,v130
v_cvt_pk_bf16_f32 v128,0,v128
v_cvt_pk_bf16_f32 v130,0,v130
v_sqrt_f32 v128,v128
v_sqrt_f32 v130,v130
v_cvt_pk_bf16_f32 v129,0,-v129
v_cvt_pk_bf16_f32 v131,0,-v131
)"
R"(v_cvt_pk_bf16_f32 v128,0,v128
v_cvt_pk_bf16_f32 v130,0,v130
v_rcp_f32 v128,v128
v_rcp_f32 v130,v130
s_add_u32 m0,s92,0x8000
s_mov_b32 s95,0x38100
buffer_load_dwordx4 v230,s[56:59],s95 offen lds
v_dot2_f32_bf16 v136,v0,s98,v129
v_dot2_f32_bf16 v137,v0,s101,v129
v_dot2_f32_bf16 v138,v1,s98,v129
v_dot2_f32_bf16 v139,v1,s101,v129
v_dot2_f32_bf16 v140,v2,s98,v129
v_dot2_f32_bf16 v141,v2,s101,v129
v_dot2_f32_bf16 v142,v3,s98,v129
v_dot2_f32_bf16 v143,v3,s101,v129
v_cvt_pk_bf16_f32 v136,0,v136
v_cvt_pk_bf16_f32 v137,0,v137
v_cvt_pk_bf16_f32 v138,0,v138
v_cvt_pk_bf16_f32 v139,0,v139
buffer_load_dwordx4 v230,s[60:63],s95 offen offset:1024 lds
v_cvt_pk_bf16_f32 v140,0,v140
v_cvt_pk_bf16_f32 v141,0,v141
v_cvt_pk_bf16_f32 v142,0,v142
v_cvt_pk_bf16_f32 v143,0,v143
v_pk_mul_f32 v[136:137],v[136:137],v[128:129] op_sel_hi:[1,0]
v_pk_mul_f32 v[138:139],v[138:139],v[128:129] op_sel_hi:[1,0]
v_pk_mul_f32 v[140:141],v[140:141],v[128:129] op_sel_hi:[1,0]
v_pk_mul_f32 v[142:143],v[142:143],v[128:129] op_sel_hi:[1,0]
v_cvt_pk_bf16_f32 v0,v136,v137
v_cvt_pk_bf16_f32 v1,v138,v139
v_cvt_pk_bf16_f32 v2,v140,v141
v_cvt_pk_bf16_f32 v3,v142,v143
ds_write_b128 v150,v[0:3]
buffer_load_dwordx4 v231,s[64:67],s95 offen offset:2048 lds
v_dot2_f32_bf16 v136,v4,s98,v131
v_dot2_f32_bf16 v137,v4,s101,v131
v_dot2_f32_bf16 v138,v5,s98,v131
v_dot2_f32_bf16 v139,v5,s101,v131
v_dot2_f32_bf16 v140,v6,s98,v131
v_dot2_f32_bf16 v141,v6,s101,v131
v_dot2_f32_bf16 v142,v7,s98,v131
v_dot2_f32_bf16 v143,v7,s101,v131
v_cvt_pk_bf16_f32 v136,0,v136
v_cvt_pk_bf16_f32 v137,0,v137
v_cvt_pk_bf16_f32 v138,0,v138
v_cvt_pk_bf16_f32 v139,0,v139
buffer_load_dwordx4 v231,s[68:71],s95 offen offset:3072 lds
v_cvt_pk_bf16_f32 v140,0,v140
v_cvt_pk_bf16_f32 v141,0,v141
v_cvt_pk_bf16_f32 v142,0,v142
v_cvt_pk_bf16_f32 v143,0,v143
v_pk_mul_f32 v[136:137],v[136:137],v[130:131] op_sel_hi:[1,0]
v_pk_mul_f32 v[138:139],v[138:139],v[130:131] op_sel_hi:[1,0]
v_pk_mul_f32 v[140:141],v[140:141],v[130:131] op_sel_hi:[1,0]
v_pk_mul_f32 v[142:143],v[142:143],v[130:131] op_sel_hi:[1,0]
v_cvt_pk_bf16_f32 v4,v136,v137
v_cvt_pk_bf16_f32 v5,v138,v139
v_cvt_pk_bf16_f32 v6,v140,v141
v_cvt_pk_bf16_f32 v7,v142,v143
ds_write_b128 v150,v[4:7] offset:1024
s_add_u32 m0,s92,0xc000
s_mov_b32 s95,0x38180
buffer_load_dwordx4 v230,s[56:59],s95 offen lds
v_dot2_f32_bf16 v136,v8,s98,v129
v_dot2_f32_bf16 v137,v8,s101,v129
v_dot2_f32_bf16 v138,v9,s98,v129
v_dot2_f32_bf16 v139,v9,s101,v129
v_dot2_f32_bf16 v140,v10,s98,v129
v_dot2_f32_bf16 v141,v10,s101,v129
v_dot2_f32_bf16 v142,v11,s98,v129
v_dot2_f32_bf16 v143,v11,s101,v129
v_cvt_pk_bf16_f32 v136,0,v136
v_cvt_pk_bf16_f32 v137,0,v137
v_cvt_pk_bf16_f32 v138,0,v138
v_cvt_pk_bf16_f32 v139,0,v139
buffer_load_dwordx4 v230,s[60:63],s95 offen offset:1024 lds
v_cvt_pk_bf16_f32 v140,0,v140
v_cvt_pk_bf16_f32 v141,0,v141
v_cvt_pk_bf16_f32 v142,0,v142
v_cvt_pk_bf16_f32 v143,0,v143
v_pk_mul_f32 v[136:137],v[136:137],v[128:129] op_sel_hi:[1,0]
v_pk_mul_f32 v[138:139],v[138:139],v[128:129] op_sel_hi:[1,0]
v_pk_mul_f32 v[140:141],v[140:141],v[128:129] op_sel_hi:[1,0]
v_pk_mul_f32 v[142:143],v[142:143],v[128:129] op_sel_hi:[1,0]
v_cvt_pk_bf16_f32 v8,v136,v137
v_cvt_pk_bf16_f32 v9,v138,v139
v_cvt_pk_bf16_f32 v10,v140,v141
v_cvt_pk_bf16_f32 v11,v142,v143
ds_write_b128 v150,v[8:11] offset:2048
buffer_load_dwordx4 v231,s[64:67],s95 offen offset:2048 lds
v_dot2_f32_bf16 v136,v12,s98,v131
v_dot2_f32_bf16 v137,v12,s101,v131
v_dot2_f32_bf16 v138,v13,s98,v131
v_dot2_f32_bf16 v139,v13,s101,v131
v_dot2_f32_bf16 v140,v14,s98,v131
v_dot2_f32_bf16 v141,v14,s101,v131
v_dot2_f32_bf16 v142,v15,s98,v131
v_dot2_f32_bf16 v143,v15,s101,v131
v_cvt_pk_bf16_f32 v136,0,v136
v_cvt_pk_bf16_f32 v137,0,v137
v_cvt_pk_bf16_f32 v138,0,v138
v_cvt_pk_bf16_f32 v139,0,v139
buffer_load_dwordx4 v231,s[68:71],s95 offen offset:3072 lds
v_cvt_pk_bf16_f32 v140,0,v140
v_cvt_pk_bf16_f32 v141,0,v141
v_cvt_pk_bf16_f32 v142,0,v142
v_cvt_pk_bf16_f32 v143,0,v143
v_pk_mul_f32 v[136:137],v[136:137],v[130:131] op_sel_hi:[1,0]
v_pk_mul_f32 v[138:139],v[138:139],v[130:131] op_sel_hi:[1,0]
v_pk_mul_f32 v[140:141],v[140:141],v[130:131] op_sel_hi:[1,0]
v_pk_mul_f32 v[142:143],v[142:143],v[130:131] op_sel_hi:[1,0]
v_cvt_pk_bf16_f32 v12,v136,v137
v_cvt_pk_bf16_f32 v13,v138,v139
v_cvt_pk_bf16_f32 v14,v140,v141
v_cvt_pk_bf16_f32 v15,v142,v143
ds_write_b128 v150,v[12:15] offset:3072
s_add_u32 m0,s92,0x18000
s_mov_b32 s95,0x70100
buffer_load_dwordx4 v230,s[56:59],s95 offen lds
v_dot2_f32_bf16 v136,v16,s98,v129
v_dot2_f32_bf16 v137,v16,s101,v129
v_dot2_f32_bf16 v138,v17,s98,v129
v_dot2_f32_bf16 v139,v17,s101,v129
v_dot2_f32_bf16 v140,v18,s98,v129
v_dot2_f32_bf16 v141,v18,s101,v129
v_dot2_f32_bf16 v142,v19,s98,v129
v_dot2_f32_bf16 v143,v19,s101,v129
v_cvt_pk_bf16_f32 v136,0,v136
v_cvt_pk_bf16_f32 v137,0,v137
v_cvt_pk_bf16_f32 v138,0,v138
v_cvt_pk_bf16_f32 v139,0,v139
buffer_load_dwordx4 v230,s[60:63],s95 offen offset:1024 lds
v_cvt_pk_bf16_f32 v140,0,v140
v_cvt_pk_bf16_f32 v141,0,v141
v_cvt_pk_bf16_f32 v142,0,v142
v_cvt_pk_bf16_f32 v143,0,v143
v_pk_mul_f32 v[136:137],v[136:137],v[128:129] op_sel_hi:[1,0]
v_pk_mul_f32 v[138:139],v[138:139],v[128:129] op_sel_hi:[1,0]
v_pk_mul_f32 v[140:141],v[140:141],v[128:129] op_sel_hi:[1,0]
v_pk_mul_f32 v[142:143],v[142:143],v[128:129] op_sel_hi:[1,0]
v_cvt_pk_bf16_f32 v16,v136,v137
v_cvt_pk_bf16_f32 v17,v138,v139
v_cvt_pk_bf16_f32 v18,v140,v141
v_cvt_pk_bf16_f32 v19,v142,v143
ds_write_b128 v150,v[16:19] offset:4096
buffer_load_dwordx4 v231,s[64:67],s95 offen offset:2048 lds
v_dot2_f32_bf16 v136,v20,s98,v131
v_dot2_f32_bf16 v137,v20,s101,v131
v_dot2_f32_bf16 v138,v21,s98,v131
v_dot2_f32_bf16 v139,v21,s101,v131
v_dot2_f32_bf16 v140,v22,s98,v131
v_dot2_f32_bf16 v141,v22,s101,v131
v_dot2_f32_bf16 v142,v23,s98,v131
v_dot2_f32_bf16 v143,v23,s101,v131
v_cvt_pk_bf16_f32 v136,0,v136
v_cvt_pk_bf16_f32 v137,0,v137
v_cvt_pk_bf16_f32 v138,0,v138
v_cvt_pk_bf16_f32 v139,0,v139
buffer_load_dwordx4 v231,s[68:71],s95 offen offset:3072 lds
v_cvt_pk_bf16_f32 v140,0,v140
v_cvt_pk_bf16_f32 v141,0,v141
v_cvt_pk_bf16_f32 v142,0,v142
v_cvt_pk_bf16_f32 v143,0,v143
v_pk_mul_f32 v[136:137],v[136:137],v[130:131] op_sel_hi:[1,0]
v_pk_mul_f32 v[138:139],v[138:139],v[130:131] op_sel_hi:[1,0]
v_pk_mul_f32 v[140:141],v[140:141],v[130:131] op_sel_hi:[1,0]
v_pk_mul_f32 v[142:143],v[142:143],v[130:131] op_sel_hi:[1,0]
v_cvt_pk_bf16_f32 v20,v136,v137
v_cvt_pk_bf16_f32 v21,v138,v139
v_cvt_pk_bf16_f32 v22,v140,v141
v_cvt_pk_bf16_f32 v23,v142,v143
ds_write_b128 v150,v[20:23] offset:5120
s_add_u32 m0,s92,0x1c000
s_mov_b32 s95,0x70180
buffer_load_dwordx4 v230,s[56:59],s95 offen lds
v_dot2_f32_bf16 v136,v24,s98,v129
v_dot2_f32_bf16 v137,v24,s101,v129
v_dot2_f32_bf16 v138,v25,s98,v129
v_dot2_f32_bf16 v139,v25,s101,v129
v_dot2_f32_bf16 v140,v26,s98,v129
v_dot2_f32_bf16 v141,v26,s101,v129
v_dot2_f32_bf16 v142,v27,s98,v129
v_dot2_f32_bf16 v143,v27,s101,v129
v_cvt_pk_bf16_f32 v136,0,v136
v_cvt_pk_bf16_f32 v137,0,v137
v_cvt_pk_bf16_f32 v138,0,v138
v_cvt_pk_bf16_f32 v139,0,v139
buffer_load_dwordx4 v230,s[60:63],s95 offen offset:1024 lds
v_cvt_pk_bf16_f32 v140,0,v140
v_cvt_pk_bf16_f32 v141,0,v141
v_cvt_pk_bf16_f32 v142,0,v142
v_cvt_pk_bf16_f32 v143,0,v143
v_pk_mul_f32 v[136:137],v[136:137],v[128:129] op_sel_hi:[1,0]
v_pk_mul_f32 v[138:139],v[138:139],v[128:129] op_sel_hi:[1,0]
v_pk_mul_f32 v[140:141],v[140:141],v[128:129] op_sel_hi:[1,0]
v_pk_mul_f32 v[142:143],v[142:143],v[128:129] op_sel_hi:[1,0]
v_cvt_pk_bf16_f32 v24,v136,v137
v_cvt_pk_bf16_f32 v25,v138,v139
v_cvt_pk_bf16_f32 v26,v140,v141
v_cvt_pk_bf16_f32 v27,v142,v143
)"
R"(ds_write_b128 v150,v[24:27] offset:6144
buffer_load_dwordx4 v231,s[64:67],s95 offen offset:2048 lds
v_dot2_f32_bf16 v136,v28,s98,v131
v_dot2_f32_bf16 v137,v28,s101,v131
v_dot2_f32_bf16 v138,v29,s98,v131
v_dot2_f32_bf16 v139,v29,s101,v131
v_dot2_f32_bf16 v140,v30,s98,v131
v_dot2_f32_bf16 v141,v30,s101,v131
v_dot2_f32_bf16 v142,v31,s98,v131
v_dot2_f32_bf16 v143,v31,s101,v131
v_cvt_pk_bf16_f32 v136,0,v136
v_cvt_pk_bf16_f32 v137,0,v137
v_cvt_pk_bf16_f32 v138,0,v138
v_cvt_pk_bf16_f32 v139,0,v139
buffer_load_dwordx4 v231,s[68:71],s95 offen offset:3072 lds
v_cvt_pk_bf16_f32 v140,0,v140
v_cvt_pk_bf16_f32 v141,0,v141
v_cvt_pk_bf16_f32 v142,0,v142
v_cvt_pk_bf16_f32 v143,0,v143
v_pk_mul_f32 v[136:137],v[136:137],v[130:131] op_sel_hi:[1,0]
v_pk_mul_f32 v[138:139],v[138:139],v[130:131] op_sel_hi:[1,0]
v_pk_mul_f32 v[140:141],v[140:141],v[130:131] op_sel_hi:[1,0]
v_pk_mul_f32 v[142:143],v[142:143],v[130:131] op_sel_hi:[1,0]
v_cvt_pk_bf16_f32 v28,v136,v137
v_cvt_pk_bf16_f32 v29,v138,v139
v_cvt_pk_bf16_f32 v30,v140,v141
v_cvt_pk_bf16_f32 v31,v142,v143
ds_write_b128 v150,v[28:31] offset:7168
buffer_load_dword v60,v176,s[56:59],0 offen
v_dot2_f32_bf16 v136,v32,s98,v129
v_dot2_f32_bf16 v137,v32,s101,v129
v_dot2_f32_bf16 v138,v33,s98,v129
v_dot2_f32_bf16 v139,v33,s101,v129
v_dot2_f32_bf16 v140,v34,s98,v129
v_dot2_f32_bf16 v141,v34,s101,v129
v_dot2_f32_bf16 v142,v35,s98,v129
v_dot2_f32_bf16 v143,v35,s101,v129
v_cvt_pk_bf16_f32 v136,0,v136
v_cvt_pk_bf16_f32 v137,0,v137
v_cvt_pk_bf16_f32 v138,0,v138
v_cvt_pk_bf16_f32 v139,0,v139
buffer_load_dword v61,v176,s[60:63],0 offen offset:1024
v_cvt_pk_bf16_f32 v140,0,v140
v_cvt_pk_bf16_f32 v141,0,v141
v_cvt_pk_bf16_f32 v142,0,v142
v_cvt_pk_bf16_f32 v143,0,v143
v_pk_mul_f32 v[136:137],v[136:137],v[128:129] op_sel_hi:[1,0]
v_pk_mul_f32 v[138:139],v[138:139],v[128:129] op_sel_hi:[1,0]
v_pk_mul_f32 v[140:141],v[140:141],v[128:129] op_sel_hi:[1,0]
v_pk_mul_f32 v[142:143],v[142:143],v[128:129] op_sel_hi:[1,0]
v_cvt_pk_bf16_f32 v32,v136,v137
v_cvt_pk_bf16_f32 v33,v138,v139
v_cvt_pk_bf16_f32 v34,v140,v141
v_cvt_pk_bf16_f32 v35,v142,v143
ds_write_b128 v150,v[32:35] offset:8192
buffer_load_dword v62,v177,s[72:75],0 offen
v_dot2_f32_bf16 v136,v36,s98,v131
v_dot2_f32_bf16 v137,v36,s101,v131
v_dot2_f32_bf16 v138,v37,s98,v131
v_dot2_f32_bf16 v139,v37,s101,v131
v_dot2_f32_bf16 v140,v38,s98,v131
v_dot2_f32_bf16 v141,v38,s101,v131
v_dot2_f32_bf16 v142,v39,s98,v131
v_dot2_f32_bf16 v143,v39,s101,v131
v_cvt_pk_bf16_f32 v136,0,v136
v_cvt_pk_bf16_f32 v137,0,v137
v_cvt_pk_bf16_f32 v138,0,v138
v_cvt_pk_bf16_f32 v139,0,v139
v_cvt_pk_bf16_f32 v140,0,v140
v_cvt_pk_bf16_f32 v141,0,v141
v_cvt_pk_bf16_f32 v142,0,v142
v_cvt_pk_bf16_f32 v143,0,v143
v_pk_mul_f32 v[136:137],v[136:137],v[130:131] op_sel_hi:[1,0]
v_pk_mul_f32 v[138:139],v[138:139],v[130:131] op_sel_hi:[1,0]
v_pk_mul_f32 v[140:141],v[140:141],v[130:131] op_sel_hi:[1,0]
v_pk_mul_f32 v[142:143],v[142:143],v[130:131] op_sel_hi:[1,0]
v_cvt_pk_bf16_f32 v36,v136,v137
v_cvt_pk_bf16_f32 v37,v138,v139
v_cvt_pk_bf16_f32 v38,v140,v141
v_cvt_pk_bf16_f32 v39,v142,v143
ds_write_b128 v150,v[36:39] offset:9216
v_dot2_f32_bf16 v136,v40,s98,v129
v_dot2_f32_bf16 v137,v40,s101,v129
v_dot2_f32_bf16 v138,v41,s98,v129
v_dot2_f32_bf16 v139,v41,s101,v129
v_dot2_f32_bf16 v140,v42,s98,v129
v_dot2_f32_bf16 v141,v42,s101,v129
v_dot2_f32_bf16 v142,v43,s98,v129
v_dot2_f32_bf16 v143,v43,s101,v129
v_cvt_pk_bf16_f32 v136,0,v136
v_cvt_pk_bf16_f32 v137,0,v137
v_cvt_pk_bf16_f32 v138,0,v138
v_cvt_pk_bf16_f32 v139,0,v139
v_cvt_pk_bf16_f32 v140,0,v140
v_cvt_pk_bf16_f32 v141,0,v141
v_cvt_pk_bf16_f32 v142,0,v142
v_cvt_pk_bf16_f32 v143,0,v143
v_pk_mul_f32 v[136:137],v[136:137],v[128:129] op_sel_hi:[1,0]
v_pk_mul_f32 v[138:139],v[138:139],v[128:129] op_sel_hi:[1,0]
v_pk_mul_f32 v[140:141],v[140:141],v[128:129] op_sel_hi:[1,0]
v_pk_mul_f32 v[142:143],v[142:143],v[128:129] op_sel_hi:[1,0]
v_cvt_pk_bf16_f32 v40,v136,v137
v_cvt_pk_bf16_f32 v41,v138,v139
v_cvt_pk_bf16_f32 v42,v140,v141
v_cvt_pk_bf16_f32 v43,v142,v143
ds_write_b128 v150,v[40:43] offset:10240
v_dot2_f32_bf16 v136,v44,s98,v131
v_dot2_f32_bf16 v137,v44,s101,v131
v_dot2_f32_bf16 v138,v45,s98,v131
v_dot2_f32_bf16 v139,v45,s101,v131
v_dot2_f32_bf16 v140,v46,s98,v131
v_dot2_f32_bf16 v141,v46,s101,v131
v_dot2_f32_bf16 v142,v47,s98,v131
v_dot2_f32_bf16 v143,v47,s101,v131
v_cvt_pk_bf16_f32 v136,0,v136
v_cvt_pk_bf16_f32 v137,0,v137
v_cvt_pk_bf16_f32 v138,0,v138
v_cvt_pk_bf16_f32 v139,0,v139
v_cvt_pk_bf16_f32 v140,0,v140
v_cvt_pk_bf16_f32 v141,0,v141
v_cvt_pk_bf16_f32 v142,0,v142
v_cvt_pk_bf16_f32 v143,0,v143
v_pk_mul_f32 v[136:137],v[136:137],v[130:131] op_sel_hi:[1,0]
v_pk_mul_f32 v[138:139],v[138:139],v[130:131] op_sel_hi:[1,0]
v_pk_mul_f32 v[140:141],v[140:141],v[130:131] op_sel_hi:[1,0]
v_pk_mul_f32 v[142:143],v[142:143],v[130:131] op_sel_hi:[1,0]
v_cvt_pk_bf16_f32 v44,v136,v137
v_cvt_pk_bf16_f32 v45,v138,v139
v_cvt_pk_bf16_f32 v46,v140,v141
v_cvt_pk_bf16_f32 v47,v142,v143
ds_write_b128 v150,v[44:47] offset:11264
v_dot2_f32_bf16 v136,v48,s98,v129
v_dot2_f32_bf16 v137,v48,s101,v129
v_dot2_f32_bf16 v138,v49,s98,v129
v_dot2_f32_bf16 v139,v49,s101,v129
v_dot2_f32_bf16 v140,v50,s98,v129
v_dot2_f32_bf16 v141,v50,s101,v129
v_dot2_f32_bf16 v142,v51,s98,v129
v_dot2_f32_bf16 v143,v51,s101,v129
v_cvt_pk_bf16_f32 v136,0,v136
v_cvt_pk_bf16_f32 v137,0,v137
v_cvt_pk_bf16_f32 v138,0,v138
v_cvt_pk_bf16_f32 v139,0,v139
v_cvt_pk_bf16_f32 v140,0,v140
v_cvt_pk_bf16_f32 v141,0,v141
v_cvt_pk_bf16_f32 v142,0,v142
v_cvt_pk_bf16_f32 v143,0,v143
v_pk_mul_f32 v[136:137],v[136:137],v[128:129] op_sel_hi:[1,0]
v_pk_mul_f32 v[138:139],v[138:139],v[128:129] op_sel_hi:[1,0]
v_pk_mul_f32 v[140:141],v[140:141],v[128:129] op_sel_hi:[1,0]
v_pk_mul_f32 v[142:143],v[142:143],v[128:129] op_sel_hi:[1,0]
v_cvt_pk_bf16_f32 v48,v136,v137
v_cvt_pk_bf16_f32 v49,v138,v139
v_cvt_pk_bf16_f32 v50,v140,v141
v_cvt_pk_bf16_f32 v51,v142,v143
ds_write_b128 v150,v[48:51] offset:12288
v_dot2_f32_bf16 v136,v52,s98,v131
v_dot2_f32_bf16 v137,v52,s101,v131
v_dot2_f32_bf16 v138,v53,s98,v131
v_dot2_f32_bf16 v139,v53,s101,v131
v_dot2_f32_bf16 v140,v54,s98,v131
v_dot2_f32_bf16 v141,v54,s101,v131
v_dot2_f32_bf16 v142,v55,s98,v131
v_dot2_f32_bf16 v143,v55,s101,v131
v_cvt_pk_bf16_f32 v136,0,v136
v_cvt_pk_bf16_f32 v137,0,v137
v_cvt_pk_bf16_f32 v138,0,v138
v_cvt_pk_bf16_f32 v139,0,v139
v_cvt_pk_bf16_f32 v140,0,v140
v_cvt_pk_bf16_f32 v141,0,v141
v_cvt_pk_bf16_f32 v142,0,v142
v_cvt_pk_bf16_f32 v143,0,v143
v_pk_mul_f32 v[136:137],v[136:137],v[130:131] op_sel_hi:[1,0]
v_pk_mul_f32 v[138:139],v[138:139],v[130:131] op_sel_hi:[1,0]
v_pk_mul_f32 v[140:141],v[140:141],v[130:131] op_sel_hi:[1,0]
v_pk_mul_f32 v[142:143],v[142:143],v[130:131] op_sel_hi:[1,0]
v_cvt_pk_bf16_f32 v52,v136,v137
v_cvt_pk_bf16_f32 v53,v138,v139
v_cvt_pk_bf16_f32 v54,v140,v141
v_cvt_pk_bf16_f32 v55,v142,v143
ds_write_b128 v150,v[52:55] offset:13312
s_lshr_b32 s99,%[tb],2
s_lshl_b32 s99,s99,10
s_add_u32 s99,s99,0x2b00000
v_lshlrev_b32 v255,3,v254
v_and_b32 v255,896,v255
buffer_load_dword v255,v255,s[88:91],s99 offen sc1
s_lshr_b32 s99,%[tb],2
s_lshl_b32 s99,s99,3
s_lshr_b32 s101,%[h],1
s_add_u32 s99,s99,s101
s_lshl_b32 s99,s99,7
s_lshl_b32 s101,%[w],15
s_add_u32 s99,s99,s101
s_add_u32 s99,s99,0x2b20000
v_mov_b32 v237,0
buffer_store_dword v237,off,s[88:91],s99 sc1
buffer_store_dword v237,off,s[88:91],s99 offset:64 sc1
s_sub_u32 s99,s99,131072
v_sub_u32 v63,3,v63
)"
R"(buffer_store_dword v63,off,s[88:91],s99 offset:4 sc1
s_waitcnt vmcnt(23) lgkmcnt(0)
s_barrier
ds_read_b64_tr_b16 v[192:193],v224
ds_read_b64_tr_b16 v[194:195],v224 offset:256
ds_read_b64_tr_b16 v[196:197],v224 offset:8192
ds_read_b64_tr_b16 v[198:199],v224 offset:8448
ds_read_b64_tr_b16 v[200:201],v225
ds_read_b64_tr_b16 v[202:203],v225 offset:256
ds_read_b64_tr_b16 v[204:205],v225 offset:8192
ds_read_b64_tr_b16 v[206:207],v225 offset:8448
ds_read_b128 a[0:3],v146
ds_read_b128 a[56:59],v146 offset:16384
ds_read_b128 a[112:115],v148
ds_read_b128 a[168:171],v148 offset:16384
ds_read_b128 a[4:7],v147
ds_read_b128 a[60:63],v147 offset:16384
ds_read_b128 a[116:119],v149
ds_read_b128 a[172:175],v149 offset:16384
ds_read_b128 a[8:11],v146 offset:2048
ds_read_b128 a[64:67],v146 offset:18432
ds_read_b128 a[120:123],v148 offset:2048
ds_read_b128 a[176:179],v148 offset:18432
ds_read_b128 a[12:15],v147 offset:2048
ds_read_b128 a[68:71],v147 offset:18432
ds_read_b128 a[124:127],v149 offset:2048
ds_read_b128 a[180:183],v149 offset:18432
ds_read_b128 a[16:19],v146 offset:4096
ds_read_b128 a[72:75],v146 offset:20480
ds_read_b128 a[128:131],v148 offset:4096
ds_read_b128 a[184:187],v148 offset:20480
ds_read_b128 a[20:23],v147 offset:4096
ds_read_b128 a[76:79],v147 offset:20480
ds_read_b128 a[132:135],v149 offset:4096
ds_read_b128 a[188:191],v149 offset:20480
ds_read_b128 a[24:27],v146 offset:6144
ds_read_b128 a[80:83],v146 offset:22528
ds_read_b128 a[136:139],v148 offset:6144
ds_read_b128 a[192:195],v148 offset:22528
ds_read_b128 a[28:31],v147 offset:6144
ds_read_b128 a[84:87],v147 offset:22528
ds_read_b128 a[140:143],v149 offset:6144
ds_read_b128 a[196:199],v149 offset:22528
ds_read_b128 a[32:35],v146 offset:8192
ds_read_b128 a[88:91],v146 offset:24576
ds_read_b128 a[144:147],v148 offset:8192
ds_read_b128 a[200:203],v148 offset:24576
ds_read_b128 a[36:39],v147 offset:8192
ds_read_b128 a[92:95],v147 offset:24576
ds_read_b128 a[148:151],v149 offset:8192
ds_read_b128 a[204:207],v149 offset:24576
ds_read_b128 a[40:43],v146 offset:10240
ds_read_b128 a[96:99],v146 offset:26624
ds_read_b128 a[152:155],v148 offset:10240
ds_read_b128 a[208:211],v148 offset:26624
ds_read_b128 a[44:47],v147 offset:10240
ds_read_b128 a[100:103],v147 offset:26624
ds_read_b128 a[156:159],v149 offset:10240
ds_read_b128 a[212:215],v149 offset:26624
ds_read_b128 a[48:51],v146 offset:12288
ds_read_b128 a[104:107],v146 offset:28672
ds_read_b128 a[160:163],v148 offset:12288
ds_read_b128 a[216:219],v148 offset:28672
ds_read_b128 a[52:55],v147 offset:12288
ds_read_b128 a[108:111],v147 offset:28672
ds_read_b128 a[164:167],v149 offset:12288
ds_read_b128 a[220:223],v149 offset:28672
s_waitcnt lgkmcnt(0)
s_barrier
v_mfma_f32_16x16x32_bf16 v[64:67],v[192:195],a[0:3],0
s_add_u32 m0,s92,0x10000
ds_read_b64_tr_b16 v[208:209],v224 offset:1024
s_mov_b32 s95,0xa8100
v_mfma_f32_16x16x32_bf16 v[64:67],v[196:199],a[4:7],v[64:67]
s_lshr_b32 s98,%[h],2
ds_read_b64_tr_b16 v[210:211],v224 offset:1280
v_and_b32 v226,48,v254
v_mfma_f32_16x16x32_bf16 v[68:71],v[192:195],a[56:59],0
s_mul_i32 s98,s98,28
buffer_load_dwordx4 v230,s[56:59],s95 offen lds
v_mfma_f32_16x16x32_bf16 v[68:71],v[196:199],a[60:63],v[68:71]
s_and_b32 s101,%[w],1
ds_read_b64_tr_b16 v[212:213],v224 offset:9216
ds_read_b64_tr_b16 v[214:215],v224 offset:9472
v_mfma_f32_16x16x32_bf16 v[72:75],v[192:195],a[112:115],0
s_lshl_b32 s101,s101,2
buffer_load_dwordx4 v230,s[60:63],s95 offen offset:1024 lds
v_mfma_f32_16x16x32_bf16 v[72:75],v[196:199],a[116:119],v[72:75]
s_add_u32 s98,s98,s101
ds_read_b64_tr_b16 v[216:217],v225 offset:1024
ds_read_b64_tr_b16 v[218:219],v225 offset:1280
v_mfma_f32_16x16x32_bf16 v[76:79],v[192:195],a[168:171],0
s_and_b32 s101,%[w],2
buffer_load_dwordx4 v231,s[64:67],s95 offen offset:2048 lds
v_mfma_f32_16x16x32_bf16 v[76:79],v[196:199],a[172:175],v[76:79]
s_add_u32 s98,s98,s101
ds_read_b64_tr_b16 v[220:221],v225 offset:9216
ds_read_b64_tr_b16 v[222:223],v225 offset:9472
v_mfma_f32_16x16x32_bf16 v[80:83],v[200:203],a[0:3],0
s_and_b32 s101,%[h],2
buffer_load_dwordx4 v231,s[68:71],s95 offen offset:3072 lds
v_mfma_f32_16x16x32_bf16 v[80:83],v[204:207],a[4:7],v[80:83]
s_add_u32 m0,s92,0x14000
s_mov_b32 s95,0xa8180
v_mfma_f32_16x16x32_bf16 v[84:87],v[200:203],a[56:59],0
s_mul_i32 s101,s101,6
buffer_load_dwordx4 v230,s[56:59],s95 offen lds
v_mfma_f32_16x16x32_bf16 v[84:87],v[204:207],a[60:63],v[84:87]
s_add_u32 s98,s98,s101
v_mfma_f32_16x16x32_bf16 v[88:91],v[200:203],a[112:115],0
s_add_u32 s98,s98,8
buffer_load_dwordx4 v230,s[60:63],s95 offen offset:1024 lds
v_mfma_f32_16x16x32_bf16 v[88:91],v[204:207],a[116:119],v[88:91]
s_lshl_b32 s98,s98,10
v_mfma_f32_16x16x32_bf16 v[92:95],v[200:203],a[168:171],0
s_add_u32 s99,s96,s98
buffer_load_dwordx4 v231,s[64:67],s95 offen offset:2048 lds
buffer_load_dwordx4 v231,s[68:71],s95 offen offset:3072 lds
v_mfma_f32_16x16x32_bf16 v[92:95],v[204:207],a[172:175],v[92:95]
s_waitcnt vmcnt(23) lgkmcnt(0)
v_mfma_f32_16x16x32_bf16 v[96:99],v[208:211],a[0:3],0
s_barrier
ds_read_b64_tr_b16 v[192:193],v224 offset:32768
s_add_u32 m0,s92,0x0
v_mfma_f32_16x16x32_bf16 v[96:99],v[212:215],a[4:7],v[96:99]
s_mov_b32 s95,0xe0100
ds_read_b64_tr_b16 v[194:195],v224 offset:33024
v_mfma_f32_16x16x32_bf16 v[100:103],v[208:211],a[56:59],0
s_and_b32 s101,%[w],1
buffer_load_dwordx4 v230,s[56:59],s95 offen lds
v_mfma_f32_16x16x32_bf16 v[100:103],v[212:215],a[60:63],v[100:103]
s_lshl_b32 s101,s101,2
ds_read_b64_tr_b16 v[196:197],v224 offset:40960
ds_read_b64_tr_b16 v[198:199],v224 offset:41216
v_mfma_f32_16x16x32_bf16 v[104:107],v[208:211],a[112:115],0
ds_read_b64_tr_b16 v[200:201],v225 offset:32768
v_mfma_f32_16x16x32_bf16 v[104:107],v[212:215],a[116:119],v[104:107]
ds_read_b64_tr_b16 v[202:203],v225 offset:33024
v_mfma_f32_16x16x32_bf16 v[108:111],v[208:211],a[168:171],0
buffer_load_dwordx4 v230,s[60:63],s95 offen offset:1024 lds
v_mfma_f32_16x16x32_bf16 v[108:111],v[212:215],a[172:175],v[108:111]
ds_read_b64_tr_b16 v[204:205],v225 offset:40960
ds_read_b64_tr_b16 v[206:207],v225 offset:41216
v_mfma_f32_16x16x32_bf16 v[112:115],v[216:219],a[0:3],0
v_mfma_f32_16x16x32_bf16 v[112:115],v[220:223],a[4:7],v[112:115]
v_mfma_f32_16x16x32_bf16 v[116:119],v[216:219],a[56:59],0
buffer_load_dwordx4 v231,s[64:67],s95 offen offset:2048 lds
v_mfma_f32_16x16x32_bf16 v[116:119],v[220:223],a[60:63],v[116:119]
v_mfma_f32_16x16x32_bf16 v[120:123],v[216:219],a[112:115],0
v_mfma_f32_16x16x32_bf16 v[120:123],v[220:223],a[116:119],v[120:123]
v_mfma_f32_16x16x32_bf16 v[124:127],v[216:219],a[168:171],0
buffer_load_dwordx4 v231,s[68:71],s95 offen offset:3072 lds
v_mfma_f32_16x16x32_bf16 v[124:127],v[220:223],a[172:175],v[124:127]
s_waitcnt lgkmcnt(0)
v_mfma_f32_16x16x32_bf16 v[64:67],v[192:195],a[8:11],v[64:67]
s_add_u32 m0,s92,0x4000
ds_read_b64_tr_b16 v[208:209],v224 offset:33792
s_mov_b32 s95,0xe0180
v_mfma_f32_16x16x32_bf16 v[64:67],v[196:199],a[12:15],v[64:67]
ds_read_b64_tr_b16 v[210:211],v224 offset:34048
v_mfma_f32_16x16x32_bf16 v[68:71],v[192:195],a[64:67],v[68:71]
buffer_load_dwordx4 v230,s[56:59],s95 offen lds
v_mfma_f32_16x16x32_bf16 v[68:71],v[196:199],a[68:71],v[68:71]
ds_read_b64_tr_b16 v[212:213],v224 offset:41984
ds_read_b64_tr_b16 v[214:215],v224 offset:42240
v_mfma_f32_16x16x32_bf16 v[72:75],v[192:195],a[120:123],v[72:75]
ds_read_b64_tr_b16 v[216:217],v225 offset:33792
v_mfma_f32_16x16x32_bf16 v[72:75],v[196:199],a[124:127],v[72:75]
ds_read_b64_tr_b16 v[218:219],v225 offset:34048
v_mfma_f32_16x16x32_bf16 v[76:79],v[192:195],a[176:179],v[76:79]
buffer_load_dwordx4 v230,s[60:63],s95 offen offset:1024 lds
v_mfma_f32_16x16x32_bf16 v[76:79],v[196:199],a[180:183],v[76:79]
ds_read_b64_tr_b16 v[220:221],v225 offset:41984
ds_read_b64_tr_b16 v[222:223],v225 offset:42240
v_mfma_f32_16x16x32_bf16 v[80:83],v[200:203],a[8:11],v[80:83]
v_mfma_f32_16x16x32_bf16 v[80:83],v[204:207],a[12:15],v[80:83]
v_mfma_f32_16x16x32_bf16 v[84:87],v[200:203],a[64:67],v[84:87]
buffer_load_dwordx4 v231,s[64:67],s95 offen offset:2048 lds
v_mfma_f32_16x16x32_bf16 v[84:87],v[204:207],a[68:71],v[84:87]
v_mfma_f32_16x16x32_bf16 v[88:91],v[200:203],a[120:123],v[88:91]
v_mfma_f32_16x16x32_bf16 v[88:91],v[204:207],a[124:127],v[88:91]
v_mfma_f32_16x16x32_bf16 v[92:95],v[200:203],a[176:179],v[92:95]
buffer_load_dwordx4 v231,s[68:71],s95 offen offset:3072 lds
v_mfma_f32_16x16x32_bf16 v[92:95],v[204:207],a[180:183],v[92:95]
s_waitcnt vmcnt(23) lgkmcnt(0)
v_mfma_f32_16x16x32_bf16 v[96:99],v[208:211],a[8:11],v[96:99]
s_barrier
ds_read_b64_tr_b16 v[192:193],v227 offset:32768
s_add_u32 m0,s92,0x8000
v_mfma_f32_16x16x32_bf16 v[96:99],v[212:215],a[12:15],v[96:99]
s_mov_b32 s95,0x118100
ds_read_b64_tr_b16 v[194:195],v227 offset:33024
v_mfma_f32_16x16x32_bf16 v[100:103],v[208:211],a[64:67],v[100:103]
buffer_load_dwordx4 v230,s[56:59],s95 offen lds
v_mfma_f32_16x16x32_bf16 v[100:103],v[212:215],a[68:71],v[100:103]
ds_read_b64_tr_b16 v[196:197],v227 offset:40960
ds_read_b64_tr_b16 v[198:199],v227 offset:41216
v_mfma_f32_16x16x32_bf16 v[104:107],v[208:211],a[120:123],v[104:107]
ds_read_b64_tr_b16 v[200:201],v228 offset:32768
v_mfma_f32_16x16x32_bf16 v[104:107],v[212:215],a[124:127],v[104:107]
)"
R"(ds_read_b64_tr_b16 v[202:203],v228 offset:33024
v_mfma_f32_16x16x32_bf16 v[108:111],v[208:211],a[176:179],v[108:111]
buffer_load_dwordx4 v230,s[60:63],s95 offen offset:1024 lds
v_mfma_f32_16x16x32_bf16 v[108:111],v[212:215],a[180:183],v[108:111]
ds_read_b64_tr_b16 v[204:205],v228 offset:40960
ds_read_b64_tr_b16 v[206:207],v228 offset:41216
v_mfma_f32_16x16x32_bf16 v[112:115],v[216:219],a[8:11],v[112:115]
v_mfma_f32_16x16x32_bf16 v[112:115],v[220:223],a[12:15],v[112:115]
v_mfma_f32_16x16x32_bf16 v[116:119],v[216:219],a[64:67],v[116:119]
buffer_load_dwordx4 v231,s[64:67],s95 offen offset:2048 lds
v_mfma_f32_16x16x32_bf16 v[116:119],v[220:223],a[68:71],v[116:119]
v_mfma_f32_16x16x32_bf16 v[120:123],v[216:219],a[120:123],v[120:123]
v_mfma_f32_16x16x32_bf16 v[120:123],v[220:223],a[124:127],v[120:123]
v_mfma_f32_16x16x32_bf16 v[124:127],v[216:219],a[176:179],v[124:127]
buffer_load_dwordx4 v231,s[68:71],s95 offen offset:3072 lds
v_mfma_f32_16x16x32_bf16 v[124:127],v[220:223],a[180:183],v[124:127]
s_waitcnt lgkmcnt(0)
v_mfma_f32_16x16x32_bf16 v[64:67],v[192:195],a[16:19],v[64:67]
s_add_u32 m0,s92,0xc000
ds_read_b64_tr_b16 v[208:209],v227 offset:33792
s_mov_b32 s95,0x118180
v_mfma_f32_16x16x32_bf16 v[64:67],v[196:199],a[20:23],v[64:67]
ds_read_b64_tr_b16 v[210:211],v227 offset:34048
v_mfma_f32_16x16x32_bf16 v[68:71],v[192:195],a[72:75],v[68:71]
buffer_load_dwordx4 v230,s[56:59],s95 offen lds
v_mfma_f32_16x16x32_bf16 v[68:71],v[196:199],a[76:79],v[68:71]
ds_read_b64_tr_b16 v[212:213],v227 offset:41984
ds_read_b64_tr_b16 v[214:215],v227 offset:42240
v_mfma_f32_16x16x32_bf16 v[72:75],v[192:195],a[128:131],v[72:75]
ds_read_b64_tr_b16 v[216:217],v228 offset:33792
v_mfma_f32_16x16x32_bf16 v[72:75],v[196:199],a[132:135],v[72:75]
ds_read_b64_tr_b16 v[218:219],v228 offset:34048
v_mfma_f32_16x16x32_bf16 v[76:79],v[192:195],a[184:187],v[76:79]
buffer_load_dwordx4 v230,s[60:63],s95 offen offset:1024 lds
v_mfma_f32_16x16x32_bf16 v[76:79],v[196:199],a[188:191],v[76:79]
ds_read_b64_tr_b16 v[220:221],v228 offset:41984
ds_read_b64_tr_b16 v[222:223],v228 offset:42240
v_mfma_f32_16x16x32_bf16 v[80:83],v[200:203],a[16:19],v[80:83]
v_mfma_f32_16x16x32_bf16 v[80:83],v[204:207],a[20:23],v[80:83]
v_mfma_f32_16x16x32_bf16 v[84:87],v[200:203],a[72:75],v[84:87]
buffer_load_dwordx4 v231,s[64:67],s95 offen offset:2048 lds
v_mfma_f32_16x16x32_bf16 v[84:87],v[204:207],a[76:79],v[84:87]
v_mfma_f32_16x16x32_bf16 v[88:91],v[200:203],a[128:131],v[88:91]
v_mfma_f32_16x16x32_bf16 v[88:91],v[204:207],a[132:135],v[88:91]
v_mfma_f32_16x16x32_bf16 v[92:95],v[200:203],a[184:187],v[92:95]
buffer_load_dwordx4 v231,s[68:71],s95 offen offset:3072 lds
v_mfma_f32_16x16x32_bf16 v[92:95],v[204:207],a[188:191],v[92:95]
s_waitcnt vmcnt(16) lgkmcnt(0)
v_mfma_f32_16x16x32_bf16 v[96:99],v[208:211],a[16:19],v[96:99]
s_barrier
ds_read_b64_tr_b16 v[192:193],v227
s_add_u32 m0,s92,0x18000
v_mfma_f32_16x16x32_bf16 v[96:99],v[212:215],a[20:23],v[96:99]
s_mov_b32 s95,0x150100
ds_read_b64_tr_b16 v[194:195],v227 offset:256
v_mfma_f32_16x16x32_bf16 v[100:103],v[208:211],a[72:75],v[100:103]
buffer_load_dwordx4 v230,s[56:59],s95 offen lds
v_mfma_f32_16x16x32_bf16 v[100:103],v[212:215],a[76:79],v[100:103]
ds_read_b64_tr_b16 v[196:197],v227 offset:8192
ds_read_b64_tr_b16 v[198:199],v227 offset:8448
v_mfma_f32_16x16x32_bf16 v[104:107],v[208:211],a[128:131],v[104:107]
ds_read_b64_tr_b16 v[200:201],v228
v_mfma_f32_16x16x32_bf16 v[104:107],v[212:215],a[132:135],v[104:107]
ds_read_b64_tr_b16 v[202:203],v228 offset:256
v_mfma_f32_16x16x32_bf16 v[108:111],v[208:211],a[184:187],v[108:111]
buffer_load_dwordx4 v230,s[60:63],s95 offen offset:1024 lds
v_mfma_f32_16x16x32_bf16 v[108:111],v[212:215],a[188:191],v[108:111]
ds_read_b64_tr_b16 v[204:205],v228 offset:8192
ds_read_b64_tr_b16 v[206:207],v228 offset:8448
v_mfma_f32_16x16x32_bf16 v[112:115],v[216:219],a[16:19],v[112:115]
v_mfma_f32_16x16x32_bf16 v[112:115],v[220:223],a[20:23],v[112:115]
v_mfma_f32_16x16x32_bf16 v[116:119],v[216:219],a[72:75],v[116:119]
buffer_load_dwordx4 v231,s[64:67],s95 offen offset:2048 lds
v_mfma_f32_16x16x32_bf16 v[116:119],v[220:223],a[76:79],v[116:119]
v_mfma_f32_16x16x32_bf16 v[120:123],v[216:219],a[128:131],v[120:123]
v_mfma_f32_16x16x32_bf16 v[120:123],v[220:223],a[132:135],v[120:123]
v_mfma_f32_16x16x32_bf16 v[124:127],v[216:219],a[184:187],v[124:127]
buffer_load_dwordx4 v231,s[68:71],s95 offen offset:3072 lds
v_mfma_f32_16x16x32_bf16 v[124:127],v[220:223],a[188:191],v[124:127]
s_waitcnt lgkmcnt(0)
v_mfma_f32_16x16x32_bf16 v[64:67],v[192:195],a[24:27],v[64:67]
s_add_u32 m0,s92,0x1c000
ds_read_b64_tr_b16 v[208:209],v227 offset:1024
s_mov_b32 s95,0x150180
v_mfma_f32_16x16x32_bf16 v[64:67],v[196:199],a[28:31],v[64:67]
ds_read_b64_tr_b16 v[210:211],v227 offset:1280
v_mfma_f32_16x16x32_bf16 v[68:71],v[192:195],a[80:83],v[68:71]
buffer_load_dwordx4 v230,s[56:59],s95 offen lds
v_mfma_f32_16x16x32_bf16 v[68:71],v[196:199],a[84:87],v[68:71]
ds_read_b64_tr_b16 v[212:213],v227 offset:9216
ds_read_b64_tr_b16 v[214:215],v227 offset:9472
v_mfma_f32_16x16x32_bf16 v[72:75],v[192:195],a[136:139],v[72:75]
ds_read_b64_tr_b16 v[216:217],v228 offset:1024
v_mfma_f32_16x16x32_bf16 v[72:75],v[196:199],a[140:143],v[72:75]
ds_read_b64_tr_b16 v[218:219],v228 offset:1280
v_mfma_f32_16x16x32_bf16 v[76:79],v[192:195],a[192:195],v[76:79]
buffer_load_dwordx4 v230,s[60:63],s95 offen offset:1024 lds
v_mfma_f32_16x16x32_bf16 v[76:79],v[196:199],a[196:199],v[76:79]
ds_read_b64_tr_b16 v[220:221],v228 offset:9216
ds_read_b64_tr_b16 v[222:223],v228 offset:9472
v_mfma_f32_16x16x32_bf16 v[80:83],v[200:203],a[24:27],v[80:83]
v_mfma_f32_16x16x32_bf16 v[80:83],v[204:207],a[28:31],v[80:83]
v_mfma_f32_16x16x32_bf16 v[84:87],v[200:203],a[80:83],v[84:87]
buffer_load_dwordx4 v231,s[64:67],s95 offen offset:2048 lds
v_mfma_f32_16x16x32_bf16 v[84:87],v[204:207],a[84:87],v[84:87]
v_mfma_f32_16x16x32_bf16 v[88:91],v[200:203],a[136:139],v[88:91]
v_mfma_f32_16x16x32_bf16 v[88:91],v[204:207],a[140:143],v[88:91]
v_mfma_f32_16x16x32_bf16 v[92:95],v[200:203],a[192:195],v[92:95]
buffer_load_dwordx4 v231,s[68:71],s95 offen offset:3072 lds
v_mfma_f32_16x16x32_bf16 v[92:95],v[204:207],a[196:199],v[92:95]
s_waitcnt vmcnt(16) lgkmcnt(0)
v_mfma_f32_16x16x32_bf16 v[96:99],v[208:211],a[24:27],v[96:99]
s_barrier
ds_read_b64_tr_b16 v[192:193],v224
s_add_u32 m0,s92,0x10000
v_mfma_f32_16x16x32_bf16 v[96:99],v[212:215],a[28:31],v[96:99]
s_mov_b32 s95,0x0
ds_read_b64_tr_b16 v[194:195],v224 offset:256
v_mfma_f32_16x16x32_bf16 v[100:103],v[208:211],a[80:83],v[100:103]
buffer_load_dwordx4 v230,s[56:59],s95 offen lds
v_mfma_f32_16x16x32_bf16 v[100:103],v[212:215],a[84:87],v[100:103]
ds_read_b64_tr_b16 v[196:197],v224 offset:8192
ds_read_b64_tr_b16 v[198:199],v224 offset:8448
v_mfma_f32_16x16x32_bf16 v[104:107],v[208:211],a[136:139],v[104:107]
ds_read_b64_tr_b16 v[200:201],v225
v_mfma_f32_16x16x32_bf16 v[104:107],v[212:215],a[140:143],v[104:107]
ds_read_b64_tr_b16 v[202:203],v225 offset:256
v_mfma_f32_16x16x32_bf16 v[108:111],v[208:211],a[192:195],v[108:111]
buffer_load_dwordx4 v230,s[60:63],s95 offen offset:1024 lds
v_mfma_f32_16x16x32_bf16 v[108:111],v[212:215],a[196:199],v[108:111]
ds_read_b64_tr_b16 v[204:205],v225 offset:8192
ds_read_b64_tr_b16 v[206:207],v225 offset:8448
v_mfma_f32_16x16x32_bf16 v[112:115],v[216:219],a[24:27],v[112:115]
v_mfma_f32_16x16x32_bf16 v[112:115],v[220:223],a[28:31],v[112:115]
v_mfma_f32_16x16x32_bf16 v[116:119],v[216:219],a[80:83],v[116:119]
buffer_load_dwordx4 v231,s[64:67],s95 offen offset:2048 lds
v_mfma_f32_16x16x32_bf16 v[116:119],v[220:223],a[84:87],v[116:119]
v_mfma_f32_16x16x32_bf16 v[120:123],v[216:219],a[136:139],v[120:123]
v_mfma_f32_16x16x32_bf16 v[120:123],v[220:223],a[140:143],v[120:123]
v_mfma_f32_16x16x32_bf16 v[124:127],v[216:219],a[192:195],v[124:127]
buffer_load_dwordx4 v231,s[68:71],s95 offen offset:3072 lds
v_mfma_f32_16x16x32_bf16 v[124:127],v[220:223],a[196:199],v[124:127]
s_waitcnt lgkmcnt(0)
v_mfma_f32_16x16x32_bf16 v[64:67],v[192:195],a[32:35],v[64:67]
s_add_u32 m0,s92,0x14000
ds_read_b64_tr_b16 v[208:209],v224 offset:1024
s_mov_b32 s95,0x80
v_mfma_f32_16x16x32_bf16 v[64:67],v[196:199],a[36:39],v[64:67]
ds_read_b64_tr_b16 v[210:211],v224 offset:1280
v_mfma_f32_16x16x32_bf16 v[68:71],v[192:195],a[88:91],v[68:71]
buffer_load_dwordx4 v230,s[56:59],s95 offen lds
v_mfma_f32_16x16x32_bf16 v[68:71],v[196:199],a[92:95],v[68:71]
ds_read_b64_tr_b16 v[212:213],v224 offset:9216
ds_read_b64_tr_b16 v[214:215],v224 offset:9472
v_mfma_f32_16x16x32_bf16 v[72:75],v[192:195],a[144:147],v[72:75]
ds_read_b64_tr_b16 v[216:217],v225 offset:1024
v_mfma_f32_16x16x32_bf16 v[72:75],v[196:199],a[148:151],v[72:75]
ds_read_b64_tr_b16 v[218:219],v225 offset:1280
v_mfma_f32_16x16x32_bf16 v[76:79],v[192:195],a[200:203],v[76:79]
buffer_load_dwordx4 v230,s[60:63],s95 offen offset:1024 lds
v_mfma_f32_16x16x32_bf16 v[76:79],v[196:199],a[204:207],v[76:79]
ds_read_b64_tr_b16 v[220:221],v225 offset:9216
ds_read_b64_tr_b16 v[222:223],v225 offset:9472
v_mfma_f32_16x16x32_bf16 v[80:83],v[200:203],a[32:35],v[80:83]
v_mfma_f32_16x16x32_bf16 v[80:83],v[204:207],a[36:39],v[80:83]
v_mfma_f32_16x16x32_bf16 v[84:87],v[200:203],a[88:91],v[84:87]
buffer_load_dwordx4 v231,s[64:67],s95 offen offset:2048 lds
v_mfma_f32_16x16x32_bf16 v[84:87],v[204:207],a[92:95],v[84:87]
v_mfma_f32_16x16x32_bf16 v[88:91],v[200:203],a[144:147],v[88:91]
v_mfma_f32_16x16x32_bf16 v[88:91],v[204:207],a[148:151],v[88:91]
v_mfma_f32_16x16x32_bf16 v[92:95],v[200:203],a[200:203],v[92:95]
buffer_load_dwordx4 v231,s[68:71],s95 offen offset:3072 lds
v_mfma_f32_16x16x32_bf16 v[92:95],v[204:207],a[204:207],v[92:95]
s_waitcnt vmcnt(16) lgkmcnt(0)
v_mfma_f32_16x16x32_bf16 v[96:99],v[208:211],a[32:35],v[96:99]
s_barrier
ds_read_b64_tr_b16 v[192:193],v224 offset:32768
s_add_u32 m0,s92,0x0
v_mfma_f32_16x16x32_bf16 v[96:99],v[212:215],a[36:39],v[96:99]
s_mov_b32 s95,0x38000
ds_read_b64_tr_b16 v[194:195],v224 offset:33024
v_mfma_f32_16x16x32_bf16 v[100:103],v[208:211],a[88:91],v[100:103]
buffer_load_dwordx4 v230,s[56:59],s95 offen lds
v_mfma_f32_16x16x32_bf16 v[100:103],v[212:215],a[92:95],v[100:103]
ds_read_b64_tr_b16 v[196:197],v224 offset:40960
ds_read_b64_tr_b16 v[198:199],v224 offset:41216
v_mfma_f32_16x16x32_bf16 v[104:107],v[208:211],a[144:147],v[104:107]
ds_read_b64_tr_b16 v[200:201],v225 offset:32768
v_mfma_f32_16x16x32_bf16 v[104:107],v[212:215],a[148:151],v[104:107]
ds_read_b64_tr_b16 v[202:203],v225 offset:33024
v_mfma_f32_16x16x32_bf16 v[108:111],v[208:211],a[200:203],v[108:111]
buffer_load_dwordx4 v230,s[60:63],s95 offen offset:1024 lds
v_mfma_f32_16x16x32_bf16 v[108:111],v[212:215],a[204:207],v[108:111]
ds_read_b64_tr_b16 v[204:205],v225 offset:40960
ds_read_b64_tr_b16 v[206:207],v225 offset:41216
v_mfma_f32_16x16x32_bf16 v[112:115],v[216:219],a[32:35],v[112:115]
v_mfma_f32_16x16x32_bf16 v[112:115],v[220:223],a[36:39],v[112:115]
v_mfma_f32_16x16x32_bf16 v[116:119],v[216:219],a[88:91],v[116:119]
buffer_load_dwordx4 v231,s[64:67],s95 offen offset:2048 lds
v_mfma_f32_16x16x32_bf16 v[116:119],v[220:223],a[92:95],v[116:119]
)"
R"(v_mfma_f32_16x16x32_bf16 v[120:123],v[216:219],a[144:147],v[120:123]
v_mfma_f32_16x16x32_bf16 v[120:123],v[220:223],a[148:151],v[120:123]
v_mfma_f32_16x16x32_bf16 v[124:127],v[216:219],a[200:203],v[124:127]
buffer_load_dwordx4 v231,s[68:71],s95 offen offset:3072 lds
v_mfma_f32_16x16x32_bf16 v[124:127],v[220:223],a[204:207],v[124:127]
s_waitcnt lgkmcnt(0)
v_mfma_f32_16x16x32_bf16 v[64:67],v[192:195],a[40:43],v[64:67]
s_add_u32 m0,s92,0x4000
ds_read_b64_tr_b16 v[208:209],v224 offset:33792
s_mov_b32 s95,0x38080
v_mfma_f32_16x16x32_bf16 v[64:67],v[196:199],a[44:47],v[64:67]
ds_read_b64_tr_b16 v[210:211],v224 offset:34048
v_mfma_f32_16x16x32_bf16 v[68:71],v[192:195],a[96:99],v[68:71]
buffer_load_dwordx4 v230,s[56:59],s95 offen lds
v_mfma_f32_16x16x32_bf16 v[68:71],v[196:199],a[100:103],v[68:71]
ds_read_b64_tr_b16 v[212:213],v224 offset:41984
ds_read_b64_tr_b16 v[214:215],v224 offset:42240
v_mfma_f32_16x16x32_bf16 v[72:75],v[192:195],a[152:155],v[72:75]
ds_read_b64_tr_b16 v[216:217],v225 offset:33792
v_mfma_f32_16x16x32_bf16 v[72:75],v[196:199],a[156:159],v[72:75]
ds_read_b64_tr_b16 v[218:219],v225 offset:34048
v_mfma_f32_16x16x32_bf16 v[76:79],v[192:195],a[208:211],v[76:79]
buffer_load_dwordx4 v230,s[60:63],s95 offen offset:1024 lds
v_mfma_f32_16x16x32_bf16 v[76:79],v[196:199],a[212:215],v[76:79]
ds_read_b64_tr_b16 v[220:221],v225 offset:41984
ds_read_b64_tr_b16 v[222:223],v225 offset:42240
v_mfma_f32_16x16x32_bf16 v[80:83],v[200:203],a[40:43],v[80:83]
v_mfma_f32_16x16x32_bf16 v[80:83],v[204:207],a[44:47],v[80:83]
v_mfma_f32_16x16x32_bf16 v[84:87],v[200:203],a[96:99],v[84:87]
buffer_load_dwordx4 v231,s[64:67],s95 offen offset:2048 lds
v_mfma_f32_16x16x32_bf16 v[84:87],v[204:207],a[100:103],v[84:87]
v_mfma_f32_16x16x32_bf16 v[88:91],v[200:203],a[152:155],v[88:91]
v_mfma_f32_16x16x32_bf16 v[88:91],v[204:207],a[156:159],v[88:91]
v_mfma_f32_16x16x32_bf16 v[92:95],v[200:203],a[208:211],v[92:95]
buffer_load_dwordx4 v231,s[68:71],s95 offen offset:3072 lds
v_mfma_f32_16x16x32_bf16 v[92:95],v[204:207],a[212:215],v[92:95]
s_waitcnt vmcnt(16) lgkmcnt(0)
v_mfma_f32_16x16x32_bf16 v[96:99],v[208:211],a[40:43],v[96:99]
s_barrier
ds_read_b64_tr_b16 v[192:193],v227 offset:32768
s_add_u32 m0,s92,0x8000
v_mfma_f32_16x16x32_bf16 v[96:99],v[212:215],a[44:47],v[96:99]
s_mov_b32 s95,0x70000
ds_read_b64_tr_b16 v[194:195],v227 offset:33024
v_mfma_f32_16x16x32_bf16 v[100:103],v[208:211],a[96:99],v[100:103]
buffer_load_dwordx4 v230,s[56:59],s95 offen lds
v_mfma_f32_16x16x32_bf16 v[100:103],v[212:215],a[100:103],v[100:103]
ds_read_b64_tr_b16 v[196:197],v227 offset:40960
ds_read_b64_tr_b16 v[198:199],v227 offset:41216
v_mfma_f32_16x16x32_bf16 v[104:107],v[208:211],a[152:155],v[104:107]
ds_read_b64_tr_b16 v[200:201],v228 offset:32768
v_mfma_f32_16x16x32_bf16 v[104:107],v[212:215],a[156:159],v[104:107]
ds_read_b64_tr_b16 v[202:203],v228 offset:33024
v_mfma_f32_16x16x32_bf16 v[108:111],v[208:211],a[208:211],v[108:111]
buffer_load_dwordx4 v230,s[60:63],s95 offen offset:1024 lds
v_mfma_f32_16x16x32_bf16 v[108:111],v[212:215],a[212:215],v[108:111]
ds_read_b64_tr_b16 v[204:205],v228 offset:40960
ds_read_b64_tr_b16 v[206:207],v228 offset:41216
v_mfma_f32_16x16x32_bf16 v[112:115],v[216:219],a[40:43],v[112:115]
v_mfma_f32_16x16x32_bf16 v[112:115],v[220:223],a[44:47],v[112:115]
v_mfma_f32_16x16x32_bf16 v[116:119],v[216:219],a[96:99],v[116:119]
buffer_load_dwordx4 v231,s[64:67],s95 offen offset:2048 lds
v_mfma_f32_16x16x32_bf16 v[116:119],v[220:223],a[100:103],v[116:119]
v_mfma_f32_16x16x32_bf16 v[120:123],v[216:219],a[152:155],v[120:123]
v_mfma_f32_16x16x32_bf16 v[120:123],v[220:223],a[156:159],v[120:123]
v_mfma_f32_16x16x32_bf16 v[124:127],v[216:219],a[208:211],v[124:127]
buffer_load_dwordx4 v231,s[68:71],s95 offen offset:3072 lds
v_mfma_f32_16x16x32_bf16 v[124:127],v[220:223],a[212:215],v[124:127]
s_waitcnt lgkmcnt(0)
v_mfma_f32_16x16x32_bf16 v[64:67],v[192:195],a[48:51],v[64:67]
s_add_u32 m0,s92,0xc000
ds_read_b64_tr_b16 v[208:209],v227 offset:33792
s_mov_b32 s95,0x70080
v_mfma_f32_16x16x32_bf16 v[64:67],v[196:199],a[52:55],v[64:67]
ds_read_b64_tr_b16 v[210:211],v227 offset:34048
v_mfma_f32_16x16x32_bf16 v[68:71],v[192:195],a[104:107],v[68:71]
buffer_load_dwordx4 v230,s[56:59],s95 offen lds
v_mfma_f32_16x16x32_bf16 v[68:71],v[196:199],a[108:111],v[68:71]
ds_read_b64_tr_b16 v[212:213],v227 offset:41984
ds_read_b64_tr_b16 v[214:215],v227 offset:42240
v_mfma_f32_16x16x32_bf16 v[72:75],v[192:195],a[160:163],v[72:75]
ds_read_b64_tr_b16 v[216:217],v228 offset:33792
v_mul_f32 v237,v64,v64
v_mfma_f32_16x16x32_bf16 v[72:75],v[196:199],a[164:167],v[72:75]
ds_read_b64_tr_b16 v[218:219],v228 offset:34048
v_fma_f32 v237,v237,s100,v234
v_mfma_f32_16x16x32_bf16 v[76:79],v[192:195],a[216:219],v[76:79]
buffer_load_dwordx4 v230,s[60:63],s95 offen offset:1024 lds
v_mfma_f32_16x16x32_bf16 v[76:79],v[196:199],a[220:223],v[76:79]
ds_read_b64_tr_b16 v[220:221],v228 offset:41984
ds_read_b64_tr_b16 v[222:223],v228 offset:42240
v_mfma_f32_16x16x32_bf16 v[80:83],v[200:203],a[48:51],v[80:83]
v_mul_f32 v237,v237,v64
v_mul_f32 v238,v65,v65
v_mfma_f32_16x16x32_bf16 v[80:83],v[204:207],a[52:55],v[80:83]
v_exp_f32 v237,v237
v_fma_f32 v238,v238,s100,v234
v_mfma_f32_16x16x32_bf16 v[84:87],v[200:203],a[104:107],v[84:87]
buffer_load_dwordx4 v231,s[64:67],s95 offen offset:2048 lds
v_mfma_f32_16x16x32_bf16 v[84:87],v[204:207],a[108:111],v[84:87]
v_add_f32 v237,1.0,v237
v_mul_f32 v238,v238,v65
v_mfma_f32_16x16x32_bf16 v[88:91],v[200:203],a[160:163],v[88:91]
v_rcp_f32 v237,v237
v_mfma_f32_16x16x32_bf16 v[88:91],v[204:207],a[164:167],v[88:91]
v_exp_f32 v238,v238
v_mfma_f32_16x16x32_bf16 v[92:95],v[200:203],a[216:219],v[92:95]
buffer_load_dwordx4 v231,s[68:71],s95 offen offset:3072 lds
v_mfma_f32_16x16x32_bf16 v[92:95],v[204:207],a[220:223],v[92:95]
s_waitcnt vmcnt(16) lgkmcnt(0)
v_add_f32 v238,1.0,v238
v_mul_f32 v239,v66,v66
v_mfma_f32_16x16x32_bf16 v[96:99],v[208:211],a[48:51],v[96:99]
s_barrier
ds_read_b64_tr_b16 v[192:193],v227
s_add_u32 m0,s92,0x18000
v_mfma_f32_16x16x32_bf16 v[96:99],v[212:215],a[52:55],v[96:99]
s_mov_b32 s95,0xa8000
ds_read_b64_tr_b16 v[194:195],v227 offset:256
v_rcp_f32 v238,v238
v_mfma_f32_16x16x32_bf16 v[100:103],v[208:211],a[104:107],v[100:103]
buffer_load_dwordx4 v230,s[56:59],s95 offen lds
v_mfma_f32_16x16x32_bf16 v[100:103],v[212:215],a[108:111],v[100:103]
ds_read_b64_tr_b16 v[196:197],v227 offset:8192
ds_read_b64_tr_b16 v[198:199],v227 offset:8448
v_mfma_f32_16x16x32_bf16 v[104:107],v[208:211],a[160:163],v[104:107]
ds_read_b64_tr_b16 v[200:201],v228
v_mul_f32 v96,v64,v96
v_mfma_f32_16x16x32_bf16 v[104:107],v[212:215],a[164:167],v[104:107]
ds_read_b64_tr_b16 v[202:203],v228 offset:256
v_mul_f32 v96,v96,v237
v_mfma_f32_16x16x32_bf16 v[108:111],v[208:211],a[216:219],v[108:111]
buffer_load_dwordx4 v230,s[60:63],s95 offen offset:1024 lds
v_mfma_f32_16x16x32_bf16 v[108:111],v[212:215],a[220:223],v[108:111]
ds_read_b64_tr_b16 v[204:205],v228 offset:8192
ds_read_b64_tr_b16 v[206:207],v228 offset:8448
v_mfma_f32_16x16x32_bf16 v[112:115],v[216:219],a[48:51],v[112:115]
v_mul_f32 v97,v65,v97
v_mul_f32 v97,v97,v238
v_mfma_f32_16x16x32_bf16 v[112:115],v[220:223],a[52:55],v[112:115]
v_fma_f32 v239,v239,s100,v234
v_mul_f32 v239,v239,v66
v_mul_f32 v98,v66,v98
v_mfma_f32_16x16x32_bf16 v[116:119],v[216:219],a[104:107],v[116:119]
buffer_load_dwordx4 v231,s[64:67],s95 offen offset:2048 lds
v_mfma_f32_16x16x32_bf16 v[116:119],v[220:223],a[108:111],v[116:119]
v_exp_f32 v239,v239
v_mfma_f32_16x16x32_bf16 v[120:123],v[216:219],a[160:163],v[120:123]
v_add_f32 v239,1.0,v239
v_mul_f32 v240,v67,v67
v_mfma_f32_16x16x32_bf16 v[120:123],v[220:223],a[164:167],v[120:123]
v_rcp_f32 v239,v239
v_mfma_f32_16x16x32_bf16 v[124:127],v[216:219],a[216:219],v[124:127]
buffer_load_dwordx4 v231,s[68:71],s95 offen offset:3072 lds
v_mfma_f32_16x16x32_bf16 v[124:127],v[220:223],a[220:223],v[124:127]
s_waitcnt lgkmcnt(0)
v_mul_f32 v98,v98,v239
v_fma_f32 v240,v240,s100,v234
v_mfma_f32_16x16x32_bf16 v[128:131],v[192:195],a[0:3],0
s_add_u32 m0,s92,0x1c000
ds_read_b64_tr_b16 v[208:209],v227 offset:1024
s_mov_b32 s95,0xa8080
v_mfma_f32_16x16x32_bf16 v[128:131],v[196:199],a[4:7],v[128:131]
ds_read_b64_tr_b16 v[210:211],v227 offset:1280
v_mul_f32 v240,v240,v67
v_mul_f32 v99,v67,v99
v_mfma_f32_16x16x32_bf16 v[132:135],v[192:195],a[56:59],0
buffer_load_dwordx4 v230,s[56:59],s95 offen lds
v_mfma_f32_16x16x32_bf16 v[132:135],v[196:199],a[60:63],v[132:135]
ds_read_b64_tr_b16 v[212:213],v227 offset:9216
ds_read_b64_tr_b16 v[214:215],v227 offset:9472
v_mfma_f32_16x16x32_bf16 v[136:139],v[192:195],a[112:115],0
ds_read_b64_tr_b16 v[216:217],v228 offset:1024
v_cvt_pk_bf16_f32 v96,v96,v97
v_mfma_f32_16x16x32_bf16 v[136:139],v[196:199],a[116:119],v[136:139]
ds_read_b64_tr_b16 v[218:219],v228 offset:1280
v_mul_f32 v241,v68,v68
v_mfma_f32_16x16x32_bf16 v[140:143],v[192:195],a[168:171],0
buffer_load_dwordx4 v230,s[60:63],s95 offen offset:1024 lds
v_mfma_f32_16x16x32_bf16 v[140:143],v[196:199],a[172:175],v[140:143]
ds_read_b64_tr_b16 v[220:221],v228 offset:9216
ds_read_b64_tr_b16 v[222:223],v228 offset:9472
v_mfma_f32_16x16x32_bf16 v[144:147],v[200:203],a[0:3],0
v_exp_f32 v240,v240
v_mfma_f32_16x16x32_bf16 v[144:147],v[204:207],a[4:7],v[144:147]
v_add_f32 v240,1.0,v240
v_rcp_f32 v240,v240
v_mfma_f32_16x16x32_bf16 v[148:151],v[200:203],a[56:59],0
buffer_load_dwordx4 v231,s[64:67],s95 offen offset:2048 lds
v_mfma_f32_16x16x32_bf16 v[148:151],v[204:207],a[60:63],v[148:151]
v_mul_f32 v99,v99,v240
v_cvt_pk_bf16_f32 v97,v98,v99
v_mfma_f32_16x16x32_bf16 v[152:155],v[200:203],a[112:115],0
v_fma_f32 v241,v241,s100,v234
v_mul_f32 v241,v241,v68
v_mfma_f32_16x16x32_bf16 v[152:155],v[204:207],a[116:119],v[152:155]
v_exp_f32 v241,v241
v_mfma_f32_16x16x32_bf16 v[156:159],v[200:203],a[168:171],0
buffer_load_dwordx4 v231,s[68:71],s95 offen offset:3072 lds
v_mfma_f32_16x16x32_bf16 v[156:159],v[204:207],a[172:175],v[156:159]
)"
R"(s_waitcnt vmcnt(16) lgkmcnt(0)
v_mul_f32 v100,v68,v100
v_add_f32 v241,1.0,v241
v_mfma_f32_16x16x32_bf16 v[160:163],v[208:211],a[0:3],0
s_barrier
ds_read_b64_tr_b16 v[192:193],v224
s_add_u32 m0,s92,0x10000
v_mfma_f32_16x16x32_bf16 v[160:163],v[212:215],a[4:7],v[160:163]
s_mov_b32 s95,0xe0000
ds_read_b64_tr_b16 v[194:195],v224 offset:256
v_rcp_f32 v241,v241
v_mfma_f32_16x16x32_bf16 v[164:167],v[208:211],a[56:59],0
buffer_load_dwordx4 v230,s[56:59],s95 offen lds
v_mfma_f32_16x16x32_bf16 v[164:167],v[212:215],a[60:63],v[164:167]
ds_read_b64_tr_b16 v[196:197],v224 offset:8192
ds_read_b64_tr_b16 v[198:199],v224 offset:8448
v_mfma_f32_16x16x32_bf16 v[168:171],v[208:211],a[112:115],0
ds_read_b64_tr_b16 v[200:201],v225
v_mul_f32 v100,v100,v241
v_mfma_f32_16x16x32_bf16 v[168:171],v[212:215],a[116:119],v[168:171]
ds_read_b64_tr_b16 v[202:203],v225 offset:256
v_mul_f32 v242,v69,v69
v_mfma_f32_16x16x32_bf16 v[172:175],v[208:211],a[168:171],0
buffer_load_dwordx4 v230,s[60:63],s95 offen offset:1024 lds
v_mfma_f32_16x16x32_bf16 v[172:175],v[212:215],a[172:175],v[172:175]
ds_read_b64_tr_b16 v[204:205],v225 offset:8192
ds_read_b64_tr_b16 v[206:207],v225 offset:8448
v_mfma_f32_16x16x32_bf16 v[176:179],v[216:219],a[0:3],0
v_fma_f32 v242,v242,s100,v234
v_mul_f32 v242,v242,v69
v_mfma_f32_16x16x32_bf16 v[176:179],v[220:223],a[4:7],v[176:179]
v_exp_f32 v242,v242
v_mul_f32 v101,v69,v101
v_mfma_f32_16x16x32_bf16 v[180:183],v[216:219],a[56:59],0
buffer_load_dwordx4 v231,s[64:67],s95 offen offset:2048 lds
v_mfma_f32_16x16x32_bf16 v[180:183],v[220:223],a[60:63],v[180:183]
v_add_f32 v242,1.0,v242
v_mul_f32 v243,v70,v70
v_mfma_f32_16x16x32_bf16 v[184:187],v[216:219],a[112:115],0
v_rcp_f32 v242,v242
v_mfma_f32_16x16x32_bf16 v[184:187],v[220:223],a[116:119],v[184:187]
v_mul_f32 v101,v101,v242
v_fma_f32 v243,v243,s100,v234
v_mfma_f32_16x16x32_bf16 v[188:191],v[216:219],a[168:171],0
buffer_load_dwordx4 v231,s[68:71],s95 offen offset:3072 lds
v_mfma_f32_16x16x32_bf16 v[188:191],v[220:223],a[172:175],v[188:191]
s_waitcnt lgkmcnt(0)
v_mul_f32 v243,v243,v70
v_mul_f32 v102,v70,v102
v_mfma_f32_16x16x32_bf16 v[128:131],v[192:195],a[8:11],v[128:131]
s_add_u32 m0,s92,0x14000
ds_read_b64_tr_b16 v[208:209],v224 offset:1024
s_mov_b32 s95,0xe0080
v_mfma_f32_16x16x32_bf16 v[128:131],v[196:199],a[12:15],v[128:131]
ds_read_b64_tr_b16 v[210:211],v224 offset:1280
v_exp_f32 v243,v243
v_mfma_f32_16x16x32_bf16 v[132:135],v[192:195],a[64:67],v[132:135]
buffer_load_dwordx4 v230,s[56:59],s95 offen lds
v_mfma_f32_16x16x32_bf16 v[132:135],v[196:199],a[68:71],v[132:135]
ds_read_b64_tr_b16 v[212:213],v224 offset:9216
ds_read_b64_tr_b16 v[214:215],v224 offset:9472
v_mfma_f32_16x16x32_bf16 v[136:139],v[192:195],a[120:123],v[136:139]
ds_read_b64_tr_b16 v[216:217],v225 offset:1024
v_add_f32 v243,1.0,v243
v_mfma_f32_16x16x32_bf16 v[136:139],v[196:199],a[124:127],v[136:139]
ds_read_b64_tr_b16 v[218:219],v225 offset:1280
v_mul_f32 v244,v71,v71
v_mfma_f32_16x16x32_bf16 v[140:143],v[192:195],a[176:179],v[140:143]
buffer_load_dwordx4 v230,s[60:63],s95 offen offset:1024 lds
v_mfma_f32_16x16x32_bf16 v[140:143],v[196:199],a[180:183],v[140:143]
ds_read_b64_tr_b16 v[220:221],v225 offset:9216
ds_read_b64_tr_b16 v[222:223],v225 offset:9472
v_mfma_f32_16x16x32_bf16 v[144:147],v[200:203],a[8:11],v[144:147]
v_rcp_f32 v243,v243
v_mfma_f32_16x16x32_bf16 v[144:147],v[204:207],a[12:15],v[144:147]
v_mul_f32 v102,v102,v243
v_fma_f32 v244,v244,s100,v234
v_mul_f32 v244,v244,v71
v_mfma_f32_16x16x32_bf16 v[148:151],v[200:203],a[64:67],v[148:151]
buffer_load_dwordx4 v231,s[64:67],s95 offen offset:2048 lds
v_mfma_f32_16x16x32_bf16 v[148:151],v[204:207],a[68:71],v[148:151]
v_exp_f32 v244,v244
v_mfma_f32_16x16x32_bf16 v[152:155],v[200:203],a[120:123],v[152:155]
v_mul_f32 v103,v71,v103
v_add_f32 v244,1.0,v244
v_mfma_f32_16x16x32_bf16 v[152:155],v[204:207],a[124:127],v[152:155]
v_rcp_f32 v244,v244
v_mfma_f32_16x16x32_bf16 v[156:159],v[200:203],a[176:179],v[156:159]
buffer_load_dwordx4 v231,s[68:71],s95 offen offset:3072 lds
v_mfma_f32_16x16x32_bf16 v[156:159],v[204:207],a[180:183],v[156:159]
s_waitcnt vmcnt(16) lgkmcnt(0)
v_mul_f32 v103,v103,v244
v_cvt_pk_bf16_f32 v98,v100,v101
v_mfma_f32_16x16x32_bf16 v[160:163],v[208:211],a[8:11],v[160:163]
s_barrier
ds_read_b64_tr_b16 v[192:193],v224 offset:32768
s_add_u32 m0,s92,0x0
v_mfma_f32_16x16x32_bf16 v[160:163],v[212:215],a[12:15],v[160:163]
s_mov_b32 s95,0x118000
ds_read_b64_tr_b16 v[194:195],v224 offset:33024
v_cvt_pk_bf16_f32 v99,v102,v103
v_mul_f32 v245,v72,v72
v_mfma_f32_16x16x32_bf16 v[164:167],v[208:211],a[64:67],v[164:167]
buffer_load_dwordx4 v230,s[56:59],s95 offen lds
v_mfma_f32_16x16x32_bf16 v[164:167],v[212:215],a[68:71],v[164:167]
ds_read_b64_tr_b16 v[196:197],v224 offset:40960
ds_read_b64_tr_b16 v[198:199],v224 offset:41216
v_mfma_f32_16x16x32_bf16 v[168:171],v[208:211],a[120:123],v[168:171]
ds_read_b64_tr_b16 v[200:201],v225 offset:32768
v_fma_f32 v245,v245,s100,v234
v_mfma_f32_16x16x32_bf16 v[168:171],v[212:215],a[124:127],v[168:171]
ds_read_b64_tr_b16 v[202:203],v225 offset:33024
v_mul_f32 v245,v245,v72
v_mfma_f32_16x16x32_bf16 v[172:175],v[208:211],a[176:179],v[172:175]
buffer_load_dwordx4 v230,s[60:63],s95 offen offset:1024 lds
v_mfma_f32_16x16x32_bf16 v[172:175],v[212:215],a[180:183],v[172:175]
ds_read_b64_tr_b16 v[204:205],v225 offset:40960
ds_read_b64_tr_b16 v[206:207],v225 offset:41216
v_mfma_f32_16x16x32_bf16 v[176:179],v[216:219],a[8:11],v[176:179]
v_exp_f32 v245,v245
v_mfma_f32_16x16x32_bf16 v[176:179],v[220:223],a[12:15],v[176:179]
v_mul_f32 v104,v72,v104
v_add_f32 v245,1.0,v245
v_mul_f32 v246,v73,v73
v_mfma_f32_16x16x32_bf16 v[180:183],v[216:219],a[64:67],v[180:183]
buffer_load_dwordx4 v231,s[64:67],s95 offen offset:2048 lds
v_mfma_f32_16x16x32_bf16 v[180:183],v[220:223],a[68:71],v[180:183]
v_rcp_f32 v245,v245
v_mfma_f32_16x16x32_bf16 v[184:187],v[216:219],a[120:123],v[184:187]
v_mul_f32 v104,v104,v245
v_fma_f32 v246,v246,s100,v234
v_mfma_f32_16x16x32_bf16 v[184:187],v[220:223],a[124:127],v[184:187]
v_mul_f32 v246,v246,v73
v_mul_f32 v105,v73,v105
v_mfma_f32_16x16x32_bf16 v[188:191],v[216:219],a[176:179],v[188:191]
buffer_load_dwordx4 v231,s[68:71],s95 offen offset:3072 lds
v_mfma_f32_16x16x32_bf16 v[188:191],v[220:223],a[180:183],v[188:191]
s_waitcnt lgkmcnt(0)
v_exp_f32 v246,v246
v_mfma_f32_16x16x32_bf16 v[128:131],v[192:195],a[16:19],v[128:131]
s_add_u32 m0,s92,0x4000
ds_read_b64_tr_b16 v[208:209],v224 offset:33792
s_mov_b32 s95,0x118080
v_mfma_f32_16x16x32_bf16 v[128:131],v[196:199],a[20:23],v[128:131]
ds_read_b64_tr_b16 v[210:211],v224 offset:34048
v_add_f32 v246,1.0,v246
v_mul_f32 v247,v74,v74
v_mfma_f32_16x16x32_bf16 v[132:135],v[192:195],a[72:75],v[132:135]
buffer_load_dwordx4 v230,s[56:59],s95 offen lds
v_mfma_f32_16x16x32_bf16 v[132:135],v[196:199],a[76:79],v[132:135]
ds_read_b64_tr_b16 v[212:213],v224 offset:41984
ds_read_b64_tr_b16 v[214:215],v224 offset:42240
v_mfma_f32_16x16x32_bf16 v[136:139],v[192:195],a[128:131],v[136:139]
ds_read_b64_tr_b16 v[216:217],v225 offset:33792
v_fma_f32 v247,v247,s100,v234
v_mfma_f32_16x16x32_bf16 v[136:139],v[196:199],a[132:135],v[136:139]
ds_read_b64_tr_b16 v[218:219],v225 offset:34048
v_mul_f32 v247,v247,v74
v_mfma_f32_16x16x32_bf16 v[140:143],v[192:195],a[184:187],v[140:143]
buffer_load_dwordx4 v230,s[60:63],s95 offen offset:1024 lds
v_mfma_f32_16x16x32_bf16 v[140:143],v[196:199],a[188:191],v[140:143]
ds_read_b64_tr_b16 v[220:221],v225 offset:41984
ds_read_b64_tr_b16 v[222:223],v225 offset:42240
v_mfma_f32_16x16x32_bf16 v[144:147],v[200:203],a[16:19],v[144:147]
v_rcp_f32 v246,v246
v_mfma_f32_16x16x32_bf16 v[144:147],v[204:207],a[20:23],v[144:147]
v_mul_f32 v105,v105,v246
v_exp_f32 v247,v247
v_mfma_f32_16x16x32_bf16 v[148:151],v[200:203],a[72:75],v[148:151]
buffer_load_dwordx4 v231,s[64:67],s95 offen offset:2048 lds
v_mfma_f32_16x16x32_bf16 v[148:151],v[204:207],a[76:79],v[148:151]
v_mul_f32 v106,v74,v106
v_add_f32 v247,1.0,v247
v_mfma_f32_16x16x32_bf16 v[152:155],v[200:203],a[128:131],v[152:155]
v_rcp_f32 v247,v247
v_mfma_f32_16x16x32_bf16 v[152:155],v[204:207],a[132:135],v[152:155]
v_mul_f32 v106,v106,v247
v_mul_f32 v237,v75,v75
v_mfma_f32_16x16x32_bf16 v[156:159],v[200:203],a[184:187],v[156:159]
buffer_load_dwordx4 v231,s[68:71],s95 offen offset:3072 lds
v_mfma_f32_16x16x32_bf16 v[156:159],v[204:207],a[188:191],v[156:159]
s_waitcnt vmcnt(16) lgkmcnt(0)
v_fma_f32 v237,v237,s100,v234
v_mul_f32 v237,v237,v75
v_mfma_f32_16x16x32_bf16 v[160:163],v[208:211],a[16:19],v[160:163]
s_barrier
ds_read_b64_tr_b16 v[192:193],v227 offset:32768
s_add_u32 m0,s92,0x8000
v_mfma_f32_16x16x32_bf16 v[160:163],v[212:215],a[20:23],v[160:163]
s_mov_b32 s95,0x150000
ds_read_b64_tr_b16 v[194:195],v227 offset:33024
v_exp_f32 v237,v237
v_mfma_f32_16x16x32_bf16 v[164:167],v[208:211],a[72:75],v[164:167]
buffer_load_dwordx4 v230,s[56:59],s95 offen lds
v_mfma_f32_16x16x32_bf16 v[164:167],v[212:215],a[76:79],v[164:167]
ds_read_b64_tr_b16 v[196:197],v227 offset:40960
ds_read_b64_tr_b16 v[198:199],v227 offset:41216
v_mfma_f32_16x16x32_bf16 v[168:171],v[208:211],a[128:131],v[168:171]
ds_read_b64_tr_b16 v[200:201],v228 offset:32768
v_mul_f32 v107,v75,v107
)"
R"(v_mfma_f32_16x16x32_bf16 v[168:171],v[212:215],a[132:135],v[168:171]
ds_read_b64_tr_b16 v[202:203],v228 offset:33024
v_add_f32 v237,1.0,v237
v_mfma_f32_16x16x32_bf16 v[172:175],v[208:211],a[184:187],v[172:175]
buffer_load_dwordx4 v230,s[60:63],s95 offen offset:1024 lds
v_mfma_f32_16x16x32_bf16 v[172:175],v[212:215],a[188:191],v[172:175]
ds_read_b64_tr_b16 v[204:205],v228 offset:40960
ds_read_b64_tr_b16 v[206:207],v228 offset:41216
v_mfma_f32_16x16x32_bf16 v[176:179],v[216:219],a[16:19],v[176:179]
v_rcp_f32 v237,v237
v_mfma_f32_16x16x32_bf16 v[176:179],v[220:223],a[20:23],v[176:179]
v_mul_f32 v107,v107,v237
v_cvt_pk_bf16_f32 v104,v104,v105
v_cvt_pk_bf16_f32 v105,v106,v107
v_mfma_f32_16x16x32_bf16 v[180:183],v[216:219],a[72:75],v[180:183]
buffer_load_dwordx4 v231,s[64:67],s95 offen offset:2048 lds
v_mfma_f32_16x16x32_bf16 v[180:183],v[220:223],a[76:79],v[180:183]
v_mul_f32 v238,v76,v76
v_fma_f32 v238,v238,s100,v234
v_mfma_f32_16x16x32_bf16 v[184:187],v[216:219],a[128:131],v[184:187]
v_mul_f32 v238,v238,v76
v_mul_f32 v108,v76,v108
v_mfma_f32_16x16x32_bf16 v[184:187],v[220:223],a[132:135],v[184:187]
v_exp_f32 v238,v238
v_mfma_f32_16x16x32_bf16 v[188:191],v[216:219],a[184:187],v[188:191]
buffer_load_dwordx4 v231,s[68:71],s95 offen offset:3072 lds
v_mfma_f32_16x16x32_bf16 v[188:191],v[220:223],a[188:191],v[188:191]
s_waitcnt lgkmcnt(0)
v_add_f32 v238,1.0,v238
v_mul_f32 v239,v77,v77
v_mfma_f32_16x16x32_bf16 v[128:131],v[192:195],a[24:27],v[128:131]
s_add_u32 m0,s92,0xc000
ds_read_b64_tr_b16 v[208:209],v227 offset:33792
s_mov_b32 s95,0x150080
v_mfma_f32_16x16x32_bf16 v[128:131],v[196:199],a[28:31],v[128:131]
ds_read_b64_tr_b16 v[210:211],v227 offset:34048
v_rcp_f32 v238,v238
v_mfma_f32_16x16x32_bf16 v[132:135],v[192:195],a[80:83],v[132:135]
buffer_load_dwordx4 v230,s[56:59],s95 offen lds
v_mfma_f32_16x16x32_bf16 v[132:135],v[196:199],a[84:87],v[132:135]
s_mov_b64 s[56:57],%[wo]
ds_read_b64_tr_b16 v[212:213],v227 offset:41984
ds_read_b64_tr_b16 v[214:215],v227 offset:42240
v_mfma_f32_16x16x32_bf16 v[136:139],v[192:195],a[136:139],v[136:139]
s_mov_b32 s58,458752
ds_read_b64_tr_b16 v[216:217],v228 offset:33792
v_mul_f32 v108,v108,v238
v_mfma_f32_16x16x32_bf16 v[136:139],v[196:199],a[140:143],v[136:139]
ds_read_b64_tr_b16 v[218:219],v228 offset:34048
v_fma_f32 v239,v239,s100,v234
v_mfma_f32_16x16x32_bf16 v[140:143],v[192:195],a[192:195],v[140:143]
buffer_load_dwordx4 v230,s[60:63],s95 offen offset:1024 lds
v_mfma_f32_16x16x32_bf16 v[140:143],v[196:199],a[196:199],v[140:143]
s_mov_b64 s[60:61],%[wd]
ds_read_b64_tr_b16 v[220:221],v228 offset:41984
ds_read_b64_tr_b16 v[222:223],v228 offset:42240
v_mfma_f32_16x16x32_bf16 v[144:147],v[200:203],a[24:27],v[144:147]
s_mov_b32 s62,401408
v_mul_f32 v239,v239,v77
v_mul_f32 v109,v77,v109
v_mfma_f32_16x16x32_bf16 v[144:147],v[204:207],a[28:31],v[144:147]
v_exp_f32 v239,v239
v_mul_f32 v240,v78,v78
v_mfma_f32_16x16x32_bf16 v[148:151],v[200:203],a[80:83],v[148:151]
buffer_load_dwordx4 v231,s[64:67],s95 offen offset:2048 lds
v_mfma_f32_16x16x32_bf16 v[148:151],v[204:207],a[84:87],v[148:151]
s_and_b32 s64,%[tb],124
v_add_f32 v239,1.0,v239
v_fma_f32 v240,v240,s100,v234
v_mfma_f32_16x16x32_bf16 v[152:155],v[200:203],a[136:139],v[152:155]
s_add_u32 s64,s64,%[w]
v_rcp_f32 v239,v239
v_mfma_f32_16x16x32_bf16 v[152:155],v[204:207],a[140:143],v[152:155]
s_lshl_b32 s64,s64,10
v_mul_f32 v109,v109,v239
v_mul_f32 v240,v240,v78
v_mfma_f32_16x16x32_bf16 v[156:159],v[200:203],a[192:195],v[156:159]
s_lshr_b32 s66,s64,1
buffer_load_dwordx4 v231,s[68:71],s95 offen offset:3072 lds
v_mfma_f32_16x16x32_bf16 v[156:159],v[204:207],a[196:199],v[156:159]
s_waitcnt vmcnt(16) lgkmcnt(0)
v_lshl_add_u32 v226,v226,3,s66
v_mul_f32 v110,v78,v110
v_mfma_f32_16x16x32_bf16 v[160:163],v[208:211],a[24:27],v[160:163]
s_barrier
ds_read_b64_tr_b16 v[192:193],v227
s_add_u32 m0,s92,0x18000
v_mfma_f32_16x16x32_bf16 v[160:163],v[212:215],a[28:31],v[160:163]
s_mov_b32 s95,0x0
ds_read_b64_tr_b16 v[194:195],v227 offset:256
v_exp_f32 v240,v240
v_mfma_f32_16x16x32_bf16 v[164:167],v[208:211],a[80:83],v[164:167]
s_mul_i32 s64,s64,7
buffer_load_dwordx4 v232,s[72:75],s95 offen lds
v_mfma_f32_16x16x32_bf16 v[164:167],v[212:215],a[84:87],v[164:167]
s_lshr_b32 s65,s64,1
ds_read_b64_tr_b16 v[196:197],v227 offset:8192
ds_read_b64_tr_b16 v[198:199],v227 offset:8448
v_mfma_f32_16x16x32_bf16 v[168:171],v[208:211],a[136:139],v[168:171]
s_lshr_b32 s64,s64,11
ds_read_b64_tr_b16 v[200:201],v228
v_add_f32 v240,1.0,v240
v_mfma_f32_16x16x32_bf16 v[168:171],v[212:215],a[140:143],v[168:171]
s_mul_i32 s64,s64,896
ds_read_b64_tr_b16 v[202:203],v228 offset:256
v_mul_f32 v241,v79,v79
v_mfma_f32_16x16x32_bf16 v[172:175],v[208:211],a[192:195],v[172:175]
buffer_load_dwordx4 v232,s[76:79],s95 offen offset:1024 lds
v_mfma_f32_16x16x32_bf16 v[172:175],v[212:215],a[196:199],v[172:175]
ds_read_b64_tr_b16 v[204:205],v228 offset:8192
ds_read_b64_tr_b16 v[206:207],v228 offset:8448
v_mfma_f32_16x16x32_bf16 v[176:179],v[216:219],a[24:27],v[176:179]
v_rcp_f32 v240,v240
v_mfma_f32_16x16x32_bf16 v[176:179],v[220:223],a[28:31],v[176:179]
v_mul_f32 v110,v110,v240
v_fma_f32 v241,v241,s100,v234
v_mul_f32 v241,v241,v79
v_mfma_f32_16x16x32_bf16 v[180:183],v[216:219],a[80:83],v[180:183]
buffer_load_dwordx4 v233,s[80:83],s95 offen offset:2048 lds
v_mfma_f32_16x16x32_bf16 v[180:183],v[220:223],a[84:87],v[180:183]
v_exp_f32 v241,v241
v_mfma_f32_16x16x32_bf16 v[184:187],v[216:219],a[136:139],v[184:187]
v_mul_f32 v111,v79,v111
v_add_f32 v241,1.0,v241
v_mfma_f32_16x16x32_bf16 v[184:187],v[220:223],a[140:143],v[184:187]
v_rcp_f32 v241,v241
v_mfma_f32_16x16x32_bf16 v[188:191],v[216:219],a[192:195],v[188:191]
buffer_load_dwordx4 v233,s[84:87],s95 offen offset:3072 lds
v_mfma_f32_16x16x32_bf16 v[188:191],v[220:223],a[196:199],v[188:191]
s_waitcnt lgkmcnt(0)
v_mul_f32 v111,v111,v241
v_cvt_pk_bf16_f32 v106,v108,v109
v_mfma_f32_16x16x32_bf16 v[128:131],v[192:195],a[32:35],v[128:131]
s_add_u32 m0,s92,0x1c000
ds_read_b64_tr_b16 v[208:209],v227 offset:1024
s_mov_b32 s95,0x200
v_mfma_f32_16x16x32_bf16 v[128:131],v[196:199],a[36:39],v[128:131]
ds_read_b64_tr_b16 v[210:211],v227 offset:1280
v_cvt_pk_bf16_f32 v107,v110,v111
v_mul_f32 v242,v80,v80
v_mfma_f32_16x16x32_bf16 v[132:135],v[192:195],a[88:91],v[132:135]
buffer_load_dwordx4 v232,s[72:75],s95 offen lds
v_mfma_f32_16x16x32_bf16 v[132:135],v[196:199],a[92:95],v[132:135]
ds_read_b64_tr_b16 v[212:213],v227 offset:9216
ds_read_b64_tr_b16 v[214:215],v227 offset:9472
v_mfma_f32_16x16x32_bf16 v[136:139],v[192:195],a[144:147],v[136:139]
ds_read_b64_tr_b16 v[216:217],v228 offset:1024
v_fma_f32 v242,v242,s100,v234
v_mfma_f32_16x16x32_bf16 v[136:139],v[196:199],a[148:151],v[136:139]
ds_read_b64_tr_b16 v[218:219],v228 offset:1280
v_mul_f32 v242,v242,v80
v_mfma_f32_16x16x32_bf16 v[140:143],v[192:195],a[200:203],v[140:143]
buffer_load_dwordx4 v232,s[76:79],s95 offen offset:1024 lds
v_mfma_f32_16x16x32_bf16 v[140:143],v[196:199],a[204:207],v[140:143]
ds_read_b64_tr_b16 v[220:221],v228 offset:9216
ds_read_b64_tr_b16 v[222:223],v228 offset:9472
v_mfma_f32_16x16x32_bf16 v[144:147],v[200:203],a[32:35],v[144:147]
v_exp_f32 v242,v242
v_mfma_f32_16x16x32_bf16 v[144:147],v[204:207],a[36:39],v[144:147]
v_mul_f32 v112,v80,v112
v_add_f32 v242,1.0,v242
v_mul_f32 v243,v81,v81
v_mfma_f32_16x16x32_bf16 v[148:151],v[200:203],a[88:91],v[148:151]
buffer_load_dwordx4 v233,s[80:83],s95 offen offset:2048 lds
v_mfma_f32_16x16x32_bf16 v[148:151],v[204:207],a[92:95],v[148:151]
v_rcp_f32 v242,v242
v_mfma_f32_16x16x32_bf16 v[152:155],v[200:203],a[144:147],v[152:155]
v_mul_f32 v112,v112,v242
v_fma_f32 v243,v243,s100,v234
v_mfma_f32_16x16x32_bf16 v[152:155],v[204:207],a[148:151],v[152:155]
v_mul_f32 v243,v243,v81
v_mul_f32 v113,v81,v113
v_mfma_f32_16x16x32_bf16 v[156:159],v[200:203],a[200:203],v[156:159]
buffer_load_dwordx4 v233,s[84:87],s95 offen offset:3072 lds
v_mfma_f32_16x16x32_bf16 v[156:159],v[204:207],a[204:207],v[156:159]
s_waitcnt vmcnt(16) lgkmcnt(0)
v_exp_f32 v243,v243
v_mfma_f32_16x16x32_bf16 v[160:163],v[208:211],a[32:35],v[160:163]
s_barrier
ds_read_b64_tr_b16 v[192:193],v224
s_add_u32 m0,s92,0x10000
v_mfma_f32_16x16x32_bf16 v[160:163],v[212:215],a[36:39],v[160:163]
s_mov_b32 s95,0x80000
ds_read_b64_tr_b16 v[194:195],v224 offset:256
v_add_f32 v243,1.0,v243
v_mul_f32 v244,v82,v82
v_mfma_f32_16x16x32_bf16 v[164:167],v[208:211],a[88:91],v[164:167]
buffer_load_dwordx4 v232,s[72:75],s95 offen lds
v_mfma_f32_16x16x32_bf16 v[164:167],v[212:215],a[92:95],v[164:167]
ds_read_b64_tr_b16 v[196:197],v224 offset:8192
ds_read_b64_tr_b16 v[198:199],v224 offset:8448
v_mfma_f32_16x16x32_bf16 v[168:171],v[208:211],a[144:147],v[168:171]
ds_read_b64_tr_b16 v[200:201],v225
v_fma_f32 v244,v244,s100,v234
v_mfma_f32_16x16x32_bf16 v[168:171],v[212:215],a[148:151],v[168:171]
ds_read_b64_tr_b16 v[202:203],v225 offset:256
v_mul_f32 v244,v244,v82
v_mfma_f32_16x16x32_bf16 v[172:175],v[208:211],a[200:203],v[172:175]
buffer_load_dwordx4 v232,s[76:79],s95 offen offset:1024 lds
v_mfma_f32_16x16x32_bf16 v[172:175],v[212:215],a[204:207],v[172:175]
)"
R"(ds_read_b64_tr_b16 v[204:205],v225 offset:8192
ds_read_b64_tr_b16 v[206:207],v225 offset:8448
v_mfma_f32_16x16x32_bf16 v[176:179],v[216:219],a[32:35],v[176:179]
v_rcp_f32 v243,v243
v_mfma_f32_16x16x32_bf16 v[176:179],v[220:223],a[36:39],v[176:179]
v_mul_f32 v113,v113,v243
v_exp_f32 v244,v244
v_mfma_f32_16x16x32_bf16 v[180:183],v[216:219],a[88:91],v[180:183]
buffer_load_dwordx4 v233,s[80:83],s95 offen offset:2048 lds
v_mfma_f32_16x16x32_bf16 v[180:183],v[220:223],a[92:95],v[180:183]
v_mul_f32 v114,v82,v114
v_add_f32 v244,1.0,v244
v_mfma_f32_16x16x32_bf16 v[184:187],v[216:219],a[144:147],v[184:187]
v_rcp_f32 v244,v244
v_mfma_f32_16x16x32_bf16 v[184:187],v[220:223],a[148:151],v[184:187]
v_mul_f32 v114,v114,v244
v_mul_f32 v245,v83,v83
v_mfma_f32_16x16x32_bf16 v[188:191],v[216:219],a[200:203],v[188:191]
buffer_load_dwordx4 v233,s[84:87],s95 offen offset:3072 lds
v_mfma_f32_16x16x32_bf16 v[188:191],v[220:223],a[204:207],v[188:191]
s_waitcnt lgkmcnt(0)
v_fma_f32 v245,v245,s100,v234
v_mul_f32 v245,v245,v83
v_mfma_f32_16x16x32_bf16 v[128:131],v[192:195],a[40:43],v[128:131]
s_add_u32 m0,s92,0x14000
ds_read_b64_tr_b16 v[208:209],v224 offset:1024
s_mov_b32 s95,0x80200
v_mfma_f32_16x16x32_bf16 v[128:131],v[196:199],a[44:47],v[128:131]
ds_read_b64_tr_b16 v[210:211],v224 offset:1280
v_exp_f32 v245,v245
v_mfma_f32_16x16x32_bf16 v[132:135],v[192:195],a[96:99],v[132:135]
buffer_load_dwordx4 v232,s[72:75],s95 offen lds
v_mfma_f32_16x16x32_bf16 v[132:135],v[196:199],a[100:103],v[132:135]
ds_read_b64_tr_b16 v[212:213],v224 offset:9216
ds_read_b64_tr_b16 v[214:215],v224 offset:9472
v_mfma_f32_16x16x32_bf16 v[136:139],v[192:195],a[152:155],v[136:139]
ds_read_b64_tr_b16 v[216:217],v225 offset:1024
v_mul_f32 v115,v83,v115
v_mfma_f32_16x16x32_bf16 v[136:139],v[196:199],a[156:159],v[136:139]
ds_read_b64_tr_b16 v[218:219],v225 offset:1280
v_add_f32 v245,1.0,v245
v_mfma_f32_16x16x32_bf16 v[140:143],v[192:195],a[208:211],v[140:143]
buffer_load_dwordx4 v232,s[76:79],s95 offen offset:1024 lds
v_mfma_f32_16x16x32_bf16 v[140:143],v[196:199],a[212:215],v[140:143]
ds_read_b64_tr_b16 v[220:221],v225 offset:9216
ds_read_b64_tr_b16 v[222:223],v225 offset:9472
v_mfma_f32_16x16x32_bf16 v[144:147],v[200:203],a[40:43],v[144:147]
v_rcp_f32 v245,v245
v_mfma_f32_16x16x32_bf16 v[144:147],v[204:207],a[44:47],v[144:147]
v_mul_f32 v115,v115,v245
v_cvt_pk_bf16_f32 v112,v112,v113
v_cvt_pk_bf16_f32 v113,v114,v115
v_mfma_f32_16x16x32_bf16 v[148:151],v[200:203],a[96:99],v[148:151]
buffer_load_dwordx4 v233,s[80:83],s95 offen offset:2048 lds
v_mfma_f32_16x16x32_bf16 v[148:151],v[204:207],a[100:103],v[148:151]
v_mul_f32 v246,v84,v84
v_fma_f32 v246,v246,s100,v234
v_mfma_f32_16x16x32_bf16 v[152:155],v[200:203],a[152:155],v[152:155]
v_mul_f32 v246,v246,v84
v_mul_f32 v116,v84,v116
v_mfma_f32_16x16x32_bf16 v[152:155],v[204:207],a[156:159],v[152:155]
v_exp_f32 v246,v246
v_mfma_f32_16x16x32_bf16 v[156:159],v[200:203],a[208:211],v[156:159]
buffer_load_dwordx4 v233,s[84:87],s95 offen offset:3072 lds
v_mfma_f32_16x16x32_bf16 v[156:159],v[204:207],a[212:215],v[156:159]
s_waitcnt vmcnt(16) lgkmcnt(0)
v_add_f32 v246,1.0,v246
v_mul_f32 v247,v85,v85
v_mfma_f32_16x16x32_bf16 v[160:163],v[208:211],a[40:43],v[160:163]
s_barrier
ds_read_b64_tr_b16 v[192:193],v224 offset:32768
s_add_u32 m0,s92,0x0
v_mfma_f32_16x16x32_bf16 v[160:163],v[212:215],a[44:47],v[160:163]
s_mov_b32 s95,0x100000
ds_read_b64_tr_b16 v[194:195],v224 offset:33024
v_rcp_f32 v246,v246
v_mfma_f32_16x16x32_bf16 v[164:167],v[208:211],a[96:99],v[164:167]
buffer_load_dwordx4 v232,s[72:75],s95 offen lds
v_mfma_f32_16x16x32_bf16 v[164:167],v[212:215],a[100:103],v[164:167]
ds_read_b64_tr_b16 v[196:197],v224 offset:40960
ds_read_b64_tr_b16 v[198:199],v224 offset:41216
v_mfma_f32_16x16x32_bf16 v[168:171],v[208:211],a[152:155],v[168:171]
ds_read_b64_tr_b16 v[200:201],v225 offset:32768
v_mul_f32 v116,v116,v246
v_mfma_f32_16x16x32_bf16 v[168:171],v[212:215],a[156:159],v[168:171]
ds_read_b64_tr_b16 v[202:203],v225 offset:33024
v_fma_f32 v247,v247,s100,v234
v_mfma_f32_16x16x32_bf16 v[172:175],v[208:211],a[208:211],v[172:175]
buffer_load_dwordx4 v232,s[76:79],s95 offen offset:1024 lds
v_mfma_f32_16x16x32_bf16 v[172:175],v[212:215],a[212:215],v[172:175]
ds_read_b64_tr_b16 v[204:205],v225 offset:40960
ds_read_b64_tr_b16 v[206:207],v225 offset:41216
v_mfma_f32_16x16x32_bf16 v[176:179],v[216:219],a[40:43],v[176:179]
v_mul_f32 v247,v247,v85
v_mul_f32 v117,v85,v117
v_mfma_f32_16x16x32_bf16 v[176:179],v[220:223],a[44:47],v[176:179]
v_exp_f32 v247,v247
v_mul_f32 v237,v86,v86
v_mfma_f32_16x16x32_bf16 v[180:183],v[216:219],a[96:99],v[180:183]
buffer_load_dwordx4 v233,s[80:83],s95 offen offset:2048 lds
v_mfma_f32_16x16x32_bf16 v[180:183],v[220:223],a[100:103],v[180:183]
v_add_f32 v247,1.0,v247
v_fma_f32 v237,v237,s100,v234
v_mfma_f32_16x16x32_bf16 v[184:187],v[216:219],a[152:155],v[184:187]
v_rcp_f32 v247,v247
v_mfma_f32_16x16x32_bf16 v[184:187],v[220:223],a[156:159],v[184:187]
v_mul_f32 v117,v117,v247
v_mul_f32 v237,v237,v86
v_mfma_f32_16x16x32_bf16 v[188:191],v[216:219],a[208:211],v[188:191]
buffer_load_dwordx4 v233,s[84:87],s95 offen offset:3072 lds
v_mfma_f32_16x16x32_bf16 v[188:191],v[220:223],a[212:215],v[188:191]
s_waitcnt lgkmcnt(0)
v_exp_f32 v237,v237
v_mfma_f32_16x16x32_bf16 v[128:131],v[192:195],a[48:51],v[128:131]
s_add_u32 m0,s92,0x4000
ds_read_b64_tr_b16 v[208:209],v224 offset:33792
s_mov_b32 s95,0x100200
v_mfma_f32_16x16x32_bf16 v[128:131],v[196:199],a[52:55],v[128:131]
ds_read_b64_tr_b16 v[210:211],v224 offset:34048
v_mul_f32 v118,v86,v118
v_add_f32 v237,1.0,v237
v_mfma_f32_16x16x32_bf16 v[132:135],v[192:195],a[104:107],v[132:135]
buffer_load_dwordx4 v232,s[72:75],s95 offen lds
v_mfma_f32_16x16x32_bf16 v[132:135],v[196:199],a[108:111],v[132:135]
ds_read_b64_tr_b16 v[212:213],v224 offset:41984
ds_read_b64_tr_b16 v[214:215],v224 offset:42240
v_mfma_f32_16x16x32_bf16 v[136:139],v[192:195],a[160:163],v[136:139]
buffer_load_dwordx4 v232,s[76:79],s95 offen offset:1024 lds
v_mfma_f32_16x16x32_bf16 v[136:139],v[196:199],a[164:167],v[136:139]
ds_read_b64_tr_b16 v[216:217],v225 offset:33792
ds_read_b64_tr_b16 v[218:219],v225 offset:34048
v_mfma_f32_16x16x32_bf16 v[140:143],v[192:195],a[216:219],v[140:143]
ds_read_b64_tr_b16 v[220:221],v225 offset:41984
v_mul_f32 v238,v87,v87
v_mfma_f32_16x16x32_bf16 v[140:143],v[196:199],a[220:223],v[140:143]
buffer_load_dwordx4 v233,s[80:83],s95 offen offset:2048 lds
v_mfma_f32_16x16x32_bf16 v[144:147],v[200:203],a[48:51],v[144:147]
ds_read_b64_tr_b16 v[222:223],v225 offset:42240
v_fma_f32 v238,v238,s100,v234
v_mfma_f32_16x16x32_bf16 v[144:147],v[204:207],a[52:55],v[144:147]
v_rcp_f32 v237,v237
v_mul_f32 v238,v238,v87
v_mfma_f32_16x16x32_bf16 v[148:151],v[200:203],a[104:107],v[148:151]
buffer_load_dwordx4 v233,s[84:87],s95 offen offset:3072 lds
v_mfma_f32_16x16x32_bf16 v[148:151],v[204:207],a[108:111],v[148:151]
v_mul_f32 v118,v118,v237
v_mul_f32 v119,v87,v119
v_mfma_f32_16x16x32_bf16 v[152:155],v[200:203],a[160:163],v[152:155]
buffer_load_dword v229,v226,s[24:27],0 offen
v_mfma_f32_16x16x32_bf16 v[152:155],v[204:207],a[164:167],v[152:155]
v_exp_f32 v238,v238
v_mfma_f32_16x16x32_bf16 v[156:159],v[200:203],a[216:219],v[156:159]
buffer_load_dword v226,v226,s[40:43],0 offen
v_mfma_f32_16x16x32_bf16 v[156:159],v[204:207],a[220:223],v[156:159]
s_waitcnt vmcnt(18) lgkmcnt(0)
v_add_f32 v238,1.0,v238
v_cvt_pk_bf16_f32 v114,v116,v117
v_mfma_f32_16x16x32_bf16 v[160:163],v[208:211],a[48:51],v[160:163]
s_barrier
ds_read_b64_tr_b16 v[192:193],v227 offset:32768
s_add_u32 m0,s92,0x8000
v_mfma_f32_16x16x32_bf16 v[160:163],v[212:215],a[52:55],v[160:163]
s_mov_b32 s95,0x180000
ds_read_b64_tr_b16 v[194:195],v227 offset:33024
v_rcp_f32 v238,v238
v_mfma_f32_16x16x32_bf16 v[164:167],v[208:211],a[104:107],v[164:167]
buffer_load_dwordx4 v232,s[72:75],s95 offen lds
v_mfma_f32_16x16x32_bf16 v[164:167],v[212:215],a[108:111],v[164:167]
ds_read_b64_tr_b16 v[196:197],v227 offset:40960
ds_read_b64_tr_b16 v[198:199],v227 offset:41216
v_mfma_f32_16x16x32_bf16 v[168:171],v[208:211],a[160:163],v[168:171]
ds_read_b64_tr_b16 v[200:201],v228 offset:32768
v_mul_f32 v119,v119,v238
v_mfma_f32_16x16x32_bf16 v[168:171],v[212:215],a[164:167],v[168:171]
ds_read_b64_tr_b16 v[202:203],v228 offset:33024
v_cvt_pk_bf16_f32 v115,v118,v119
v_mfma_f32_16x16x32_bf16 v[172:175],v[208:211],a[216:219],v[172:175]
buffer_load_dwordx4 v232,s[76:79],s95 offen offset:1024 lds
v_mfma_f32_16x16x32_bf16 v[172:175],v[212:215],a[220:223],v[172:175]
ds_read_b64_tr_b16 v[204:205],v228 offset:40960
ds_read_b64_tr_b16 v[206:207],v228 offset:41216
v_mfma_f32_16x16x32_bf16 v[176:179],v[216:219],a[48:51],v[176:179]
v_mul_f32 v239,v88,v88
v_fma_f32 v239,v239,s100,v234
v_mfma_f32_16x16x32_bf16 v[176:179],v[220:223],a[52:55],v[176:179]
v_mul_f32 v239,v239,v88
v_exp_f32 v239,v239
v_mfma_f32_16x16x32_bf16 v[180:183],v[216:219],a[104:107],v[180:183]
buffer_load_dwordx4 v233,s[80:83],s95 offen offset:2048 lds
v_mfma_f32_16x16x32_bf16 v[180:183],v[220:223],a[108:111],v[180:183]
v_mul_f32 v120,v88,v120
v_add_f32 v239,1.0,v239
v_mfma_f32_16x16x32_bf16 v[184:187],v[216:219],a[160:163],v[184:187]
v_rcp_f32 v239,v239
v_mfma_f32_16x16x32_bf16 v[184:187],v[220:223],a[164:167],v[184:187]
v_mul_f32 v120,v120,v239
v_mul_f32 v240,v89,v89
v_mfma_f32_16x16x32_bf16 v[188:191],v[216:219],a[216:219],v[188:191]
buffer_load_dwordx4 v233,s[84:87],s95 offen offset:3072 lds
v_mfma_f32_16x16x32_bf16 v[188:191],v[220:223],a[220:223],v[188:191]
)"
R"(s_waitcnt lgkmcnt(0)
v_fma_f32 v240,v240,s100,v234
v_mul_f32 v240,v240,v89
v_mfma_f32_16x16x32_bf16 v[0:3],v[192:195],a[0:3],0
s_add_u32 m0,s92,0xc000
ds_read_b64_tr_b16 v[208:209],v227 offset:33792
s_mov_b32 s95,0x180200
v_mfma_f32_16x16x32_bf16 v[0:3],v[196:199],a[4:7],v[0:3]
ds_read_b64_tr_b16 v[210:211],v227 offset:34048
v_exp_f32 v240,v240
v_mfma_f32_16x16x32_bf16 v[8:11],v[192:195],a[56:59],0
buffer_load_dwordx4 v232,s[72:75],s95 offen lds
v_mfma_f32_16x16x32_bf16 v[8:11],v[196:199],a[60:63],v[8:11]
ds_read_b64_tr_b16 v[212:213],v227 offset:41984
ds_read_b64_tr_b16 v[214:215],v227 offset:42240
v_mfma_f32_16x16x32_bf16 v[16:19],v[192:195],a[112:115],0
ds_read_b64_tr_b16 v[216:217],v228 offset:33792
v_mul_f32 v121,v89,v121
v_mfma_f32_16x16x32_bf16 v[16:19],v[196:199],a[116:119],v[16:19]
ds_read_b64_tr_b16 v[218:219],v228 offset:34048
v_add_f32 v240,1.0,v240
v_mfma_f32_16x16x32_bf16 v[24:27],v[192:195],a[168:171],0
buffer_load_dwordx4 v232,s[76:79],s95 offen offset:1024 lds
v_mfma_f32_16x16x32_bf16 v[24:27],v[196:199],a[172:175],v[24:27]
ds_read_b64_tr_b16 v[220:221],v228 offset:41984
ds_read_b64_tr_b16 v[222:223],v228 offset:42240
v_mfma_f32_16x16x32_bf16 v[4:7],v[200:203],a[0:3],0
v_rcp_f32 v240,v240
v_mfma_f32_16x16x32_bf16 v[4:7],v[204:207],a[4:7],v[4:7]
v_mul_f32 v121,v121,v240
v_mul_f32 v241,v90,v90
v_fma_f32 v241,v241,s100,v234
v_mfma_f32_16x16x32_bf16 v[12:15],v[200:203],a[56:59],0
buffer_load_dwordx4 v233,s[80:83],s95 offen offset:2048 lds
v_mfma_f32_16x16x32_bf16 v[12:15],v[204:207],a[60:63],v[12:15]
v_mul_f32 v241,v241,v90
v_mul_f32 v122,v90,v122
v_mfma_f32_16x16x32_bf16 v[20:23],v[200:203],a[112:115],0
v_exp_f32 v241,v241
v_mfma_f32_16x16x32_bf16 v[20:23],v[204:207],a[116:119],v[20:23]
v_add_f32 v241,1.0,v241
v_mul_f32 v242,v91,v91
v_mfma_f32_16x16x32_bf16 v[28:31],v[200:203],a[168:171],0
buffer_load_dwordx4 v233,s[84:87],s95 offen offset:3072 lds
v_mfma_f32_16x16x32_bf16 v[28:31],v[204:207],a[172:175],v[28:31]
s_waitcnt vmcnt(18) lgkmcnt(0)
v_rcp_f32 v241,v241
v_mfma_f32_16x16x32_bf16 v[32:35],v[208:211],a[0:3],0
s_barrier
ds_read_b64_tr_b16 v[192:193],v227
s_add_u32 m0,s92,0x18000
v_mfma_f32_16x16x32_bf16 v[32:35],v[212:215],a[4:7],v[32:35]
s_mov_b32 s95,0x200000
ds_read_b64_tr_b16 v[194:195],v227 offset:256
v_mul_f32 v122,v122,v241
v_fma_f32 v242,v242,s100,v234
v_mfma_f32_16x16x32_bf16 v[40:43],v[208:211],a[56:59],0
buffer_load_dwordx4 v232,s[72:75],s95 offen lds
v_mfma_f32_16x16x32_bf16 v[40:43],v[212:215],a[60:63],v[40:43]
ds_read_b64_tr_b16 v[196:197],v227 offset:8192
ds_read_b64_tr_b16 v[198:199],v227 offset:8448
v_mfma_f32_16x16x32_bf16 v[48:51],v[208:211],a[112:115],0
ds_read_b64_tr_b16 v[200:201],v228
v_mul_f32 v242,v242,v91
v_mfma_f32_16x16x32_bf16 v[48:51],v[212:215],a[116:119],v[48:51]
ds_read_b64_tr_b16 v[202:203],v228 offset:256
v_mul_f32 v123,v91,v123
v_mfma_f32_16x16x32_bf16 v[56:59],v[208:211],a[168:171],0
buffer_load_dwordx4 v232,s[76:79],s95 offen offset:1024 lds
v_mfma_f32_16x16x32_bf16 v[56:59],v[212:215],a[172:175],v[56:59]
ds_read_b64_tr_b16 v[204:205],v228 offset:8192
ds_read_b64_tr_b16 v[206:207],v228 offset:8448
v_mfma_f32_16x16x32_bf16 v[36:39],v[216:219],a[0:3],0
v_exp_f32 v242,v242
v_mfma_f32_16x16x32_bf16 v[36:39],v[220:223],a[4:7],v[36:39]
v_add_f32 v242,1.0,v242
v_rcp_f32 v242,v242
v_mfma_f32_16x16x32_bf16 v[44:47],v[216:219],a[56:59],0
buffer_load_dwordx4 v233,s[80:83],s95 offen offset:2048 lds
v_mfma_f32_16x16x32_bf16 v[44:47],v[220:223],a[60:63],v[44:47]
v_mul_f32 v123,v123,v242
v_cvt_pk_bf16_f32 v120,v120,v121
v_mfma_f32_16x16x32_bf16 v[52:55],v[216:219],a[112:115],0
v_cvt_pk_bf16_f32 v121,v122,v123
v_mul_f32 v243,v92,v92
v_mfma_f32_16x16x32_bf16 v[52:55],v[220:223],a[116:119],v[52:55]
v_fma_f32 v243,v243,s100,v234
v_mul_f32 v243,v243,v92
v_mfma_f32_16x16x32_bf16 v[60:63],v[216:219],a[168:171],0
buffer_load_dwordx4 v233,s[84:87],s95 offen offset:3072 lds
v_mfma_f32_16x16x32_bf16 v[60:63],v[220:223],a[172:175],v[60:63]
s_waitcnt lgkmcnt(0)
v_exp_f32 v243,v243
v_mfma_f32_16x16x32_bf16 v[0:3],v[192:195],a[8:11],v[0:3]
s_add_u32 m0,s92,0x1c000
ds_read_b64_tr_b16 v[208:209],v227 offset:1024
s_mov_b32 s95,0x200200
v_mfma_f32_16x16x32_bf16 v[0:3],v[196:199],a[12:15],v[0:3]
ds_read_b64_tr_b16 v[210:211],v227 offset:1280
v_mul_f32 v124,v92,v124
v_add_f32 v243,1.0,v243
v_mfma_f32_16x16x32_bf16 v[8:11],v[192:195],a[64:67],v[8:11]
buffer_load_dwordx4 v232,s[72:75],s95 offen lds
v_mfma_f32_16x16x32_bf16 v[8:11],v[196:199],a[68:71],v[8:11]
ds_read_b64_tr_b16 v[212:213],v227 offset:9216
ds_read_b64_tr_b16 v[214:215],v227 offset:9472
v_mfma_f32_16x16x32_bf16 v[16:19],v[192:195],a[120:123],v[16:19]
ds_read_b64_tr_b16 v[216:217],v228 offset:1024
v_mul_f32 v244,v93,v93
v_mfma_f32_16x16x32_bf16 v[16:19],v[196:199],a[124:127],v[16:19]
ds_read_b64_tr_b16 v[218:219],v228 offset:1280
v_fma_f32 v244,v244,s100,v234
v_mfma_f32_16x16x32_bf16 v[24:27],v[192:195],a[176:179],v[24:27]
buffer_load_dwordx4 v232,s[76:79],s95 offen offset:1024 lds
v_mfma_f32_16x16x32_bf16 v[24:27],v[196:199],a[180:183],v[24:27]
ds_read_b64_tr_b16 v[220:221],v228 offset:9216
ds_read_b64_tr_b16 v[222:223],v228 offset:9472
v_mfma_f32_16x16x32_bf16 v[4:7],v[200:203],a[8:11],v[4:7]
v_rcp_f32 v243,v243
v_mfma_f32_16x16x32_bf16 v[4:7],v[204:207],a[12:15],v[4:7]
v_mul_f32 v124,v124,v243
v_mul_f32 v244,v244,v93
v_mul_f32 v125,v93,v125
v_mfma_f32_16x16x32_bf16 v[12:15],v[200:203],a[64:67],v[12:15]
buffer_load_dwordx4 v233,s[80:83],s95 offen offset:2048 lds
v_mfma_f32_16x16x32_bf16 v[12:15],v[204:207],a[68:71],v[12:15]
v_exp_f32 v244,v244
v_mfma_f32_16x16x32_bf16 v[20:23],v[200:203],a[120:123],v[20:23]
v_add_f32 v244,1.0,v244
v_mul_f32 v245,v94,v94
v_mfma_f32_16x16x32_bf16 v[20:23],v[204:207],a[124:127],v[20:23]
v_rcp_f32 v244,v244
v_mfma_f32_16x16x32_bf16 v[28:31],v[200:203],a[176:179],v[28:31]
buffer_load_dwordx4 v233,s[84:87],s95 offen offset:3072 lds
v_mfma_f32_16x16x32_bf16 v[28:31],v[204:207],a[180:183],v[28:31]
s_waitcnt vmcnt(18) lgkmcnt(0)
v_mul_f32 v125,v125,v244
v_fma_f32 v245,v245,s100,v234
v_mfma_f32_16x16x32_bf16 v[32:35],v[208:211],a[8:11],v[32:35]
s_barrier
ds_read_b64_tr_b16 v[192:193],v224
s_add_u32 m0,s92,0x10000
v_mfma_f32_16x16x32_bf16 v[32:35],v[212:215],a[12:15],v[32:35]
s_mov_b32 s95,0x280000
ds_read_b64_tr_b16 v[194:195],v224 offset:256
v_mul_f32 v245,v245,v94
v_mul_f32 v126,v94,v126
v_mfma_f32_16x16x32_bf16 v[40:43],v[208:211],a[64:67],v[40:43]
buffer_load_dwordx4 v232,s[72:75],s95 offen lds
v_mfma_f32_16x16x32_bf16 v[40:43],v[212:215],a[68:71],v[40:43]
ds_read_b64_tr_b16 v[196:197],v224 offset:8192
ds_read_b64_tr_b16 v[198:199],v224 offset:8448
v_mfma_f32_16x16x32_bf16 v[48:51],v[208:211],a[120:123],v[48:51]
ds_read_b64_tr_b16 v[200:201],v225
v_mul_f32 v246,v95,v95
v_mfma_f32_16x16x32_bf16 v[48:51],v[212:215],a[124:127],v[48:51]
ds_read_b64_tr_b16 v[202:203],v225 offset:256
v_fma_f32 v246,v246,s100,v234
v_mfma_f32_16x16x32_bf16 v[56:59],v[208:211],a[176:179],v[56:59]
buffer_load_dwordx4 v232,s[76:79],s95 offen offset:1024 lds
v_mfma_f32_16x16x32_bf16 v[56:59],v[212:215],a[180:183],v[56:59]
ds_read_b64_tr_b16 v[204:205],v225 offset:8192
ds_read_b64_tr_b16 v[206:207],v225 offset:8448
v_mfma_f32_16x16x32_bf16 v[36:39],v[216:219],a[8:11],v[36:39]
v_exp_f32 v245,v245
v_mfma_f32_16x16x32_bf16 v[36:39],v[220:223],a[12:15],v[36:39]
v_add_f32 v245,1.0,v245
v_rcp_f32 v245,v245
v_mfma_f32_16x16x32_bf16 v[44:47],v[216:219],a[64:67],v[44:47]
buffer_load_dwordx4 v233,s[80:83],s95 offen offset:2048 lds
v_mfma_f32_16x16x32_bf16 v[44:47],v[220:223],a[68:71],v[44:47]
v_mul_f32 v126,v126,v245
v_mul_f32 v246,v246,v95
v_mfma_f32_16x16x32_bf16 v[52:55],v[216:219],a[120:123],v[52:55]
v_exp_f32 v246,v246
v_mfma_f32_16x16x32_bf16 v[52:55],v[220:223],a[124:127],v[52:55]
v_mul_f32 v127,v95,v127
v_add_f32 v246,1.0,v246
v_mfma_f32_16x16x32_bf16 v[60:63],v[216:219],a[176:179],v[60:63]
buffer_load_dwordx4 v233,s[84:87],s95 offen offset:3072 lds
v_mfma_f32_16x16x32_bf16 v[60:63],v[220:223],a[180:183],v[60:63]
s_waitcnt lgkmcnt(0)
v_rcp_f32 v246,v246
v_mfma_f32_16x16x32_bf16 v[0:3],v[192:195],a[16:19],v[0:3]
s_add_u32 m0,s92,0x14000
ds_read_b64_tr_b16 v[208:209],v224 offset:1024
s_mov_b32 s95,0x280200
v_mfma_f32_16x16x32_bf16 v[0:3],v[196:199],a[20:23],v[0:3]
ds_read_b64_tr_b16 v[210:211],v224 offset:1280
v_mul_f32 v127,v127,v246
v_cvt_pk_bf16_f32 v122,v124,v125
v_mfma_f32_16x16x32_bf16 v[8:11],v[192:195],a[72:75],v[8:11]
buffer_load_dwordx4 v232,s[72:75],s95 offen lds
v_mfma_f32_16x16x32_bf16 v[8:11],v[196:199],a[76:79],v[8:11]
ds_read_b64_tr_b16 v[212:213],v224 offset:9216
ds_read_b64_tr_b16 v[214:215],v224 offset:9472
v_mfma_f32_16x16x32_bf16 v[16:19],v[192:195],a[128:131],v[16:19]
ds_read_b64_tr_b16 v[216:217],v225 offset:1024
v_cvt_pk_bf16_f32 v123,v126,v127
v_mfma_f32_16x16x32_bf16 v[16:19],v[196:199],a[132:135],v[16:19]
)"
R"(ds_read_b64_tr_b16 v[218:219],v225 offset:1280
v_bfe_u32 v231,v254,7,2
v_mfma_f32_16x16x32_bf16 v[24:27],v[192:195],a[184:187],v[24:27]
buffer_load_dwordx4 v232,s[76:79],s95 offen offset:1024 lds
v_mfma_f32_16x16x32_bf16 v[24:27],v[196:199],a[188:191],v[24:27]
ds_read_b64_tr_b16 v[220:221],v225 offset:9216
ds_read_b64_tr_b16 v[222:223],v225 offset:9472
v_mfma_f32_16x16x32_bf16 v[4:7],v[200:203],a[16:19],v[4:7]
v_mul_u32_u24 v231,896,v231
v_and_b32 v230,112,v254
v_mfma_f32_16x16x32_bf16 v[4:7],v[204:207],a[20:23],v[4:7]
v_lshl_add_u32 v231,v230,2,v231
v_and_b32 v230,496,v254
v_lshl_add_u32 v230,v230,3,s65
v_mfma_f32_16x16x32_bf16 v[12:15],v[200:203],a[72:75],v[12:15]
buffer_load_dwordx4 v233,s[80:83],s95 offen offset:2048 lds
v_mfma_f32_16x16x32_bf16 v[12:15],v[204:207],a[76:79],v[12:15]
v_min_u32 v230,458624,v230
v_add_u32 v231,s64,v231
v_mfma_f32_16x16x32_bf16 v[20:23],v[200:203],a[128:131],v[20:23]
s_and_b32 s64,%[h],2
v_mul_f32 v247,v128,v128
v_fma_f32 v247,v247,s100,v234
v_mfma_f32_16x16x32_bf16 v[20:23],v[204:207],a[132:135],v[20:23]
s_mul_i32 s64,s64,192
v_add_u32 v231,s64,v231
v_mul_f32 v247,v247,v128
v_mfma_f32_16x16x32_bf16 v[28:31],v[200:203],a[184:187],v[28:31]
buffer_load_dwordx4 v233,s[84:87],s95 offen offset:3072 lds
v_mfma_f32_16x16x32_bf16 v[28:31],v[204:207],a[188:191],v[28:31]
s_waitcnt vmcnt(16) lgkmcnt(0)
v_mul_f32 v160,v128,v160
v_mul_f32 v237,v129,v129
v_mfma_f32_16x16x32_bf16 v[32:35],v[208:211],a[16:19],v[32:35]
s_barrier
ds_read_b64_tr_b16 v[192:193],v224 offset:32768
s_add_u32 m0,s92,0x0
v_mfma_f32_16x16x32_bf16 v[32:35],v[212:215],a[20:23],v[32:35]
s_mov_b32 s95,0x300000
ds_read_b64_tr_b16 v[194:195],v224 offset:33024
v_fma_f32 v237,v237,s100,v234
v_mul_f32 v237,v237,v129
v_mfma_f32_16x16x32_bf16 v[40:43],v[208:211],a[72:75],v[40:43]
buffer_load_dwordx4 v232,s[72:75],s95 offen lds
v_mfma_f32_16x16x32_bf16 v[40:43],v[212:215],a[76:79],v[40:43]
ds_read_b64_tr_b16 v[196:197],v224 offset:40960
ds_read_b64_tr_b16 v[198:199],v224 offset:41216
v_mfma_f32_16x16x32_bf16 v[48:51],v[208:211],a[128:131],v[48:51]
ds_read_b64_tr_b16 v[200:201],v225 offset:32768
v_mul_f32 v161,v129,v161
v_mfma_f32_16x16x32_bf16 v[48:51],v[212:215],a[132:135],v[48:51]
ds_read_b64_tr_b16 v[202:203],v225 offset:33024
v_mul_f32 v238,v130,v130
v_mfma_f32_16x16x32_bf16 v[56:59],v[208:211],a[184:187],v[56:59]
buffer_load_dwordx4 v232,s[76:79],s95 offen offset:1024 lds
v_mfma_f32_16x16x32_bf16 v[56:59],v[212:215],a[188:191],v[56:59]
ds_read_b64_tr_b16 v[204:205],v225 offset:40960
ds_read_b64_tr_b16 v[206:207],v225 offset:41216
v_mfma_f32_16x16x32_bf16 v[36:39],v[216:219],a[16:19],v[36:39]
v_fma_f32 v238,v238,s100,v234
v_mul_f32 v238,v238,v130
v_mfma_f32_16x16x32_bf16 v[36:39],v[220:223],a[20:23],v[36:39]
v_mul_f32 v162,v130,v162
v_mul_f32 v239,v131,v131
v_fma_f32 v239,v239,s100,v234
v_mfma_f32_16x16x32_bf16 v[44:47],v[216:219],a[72:75],v[44:47]
buffer_load_dwordx4 v233,s[80:83],s95 offen offset:2048 lds
v_mfma_f32_16x16x32_bf16 v[44:47],v[220:223],a[76:79],v[44:47]
v_mul_f32 v239,v239,v131
v_mul_f32 v163,v131,v163
v_mfma_f32_16x16x32_bf16 v[52:55],v[216:219],a[128:131],v[52:55]
v_exp_f32 v247,v247
v_mfma_f32_16x16x32_bf16 v[52:55],v[220:223],a[132:135],v[52:55]
v_add_f32 v247,1.0,v247
v_mul_f32 v244,v136,v136
v_mfma_f32_16x16x32_bf16 v[60:63],v[216:219],a[184:187],v[60:63]
buffer_load_dwordx4 v233,s[84:87],s95 offen offset:3072 lds
v_mfma_f32_16x16x32_bf16 v[60:63],v[220:223],a[188:191],v[60:63]
s_waitcnt lgkmcnt(0)
v_rcp_f32 v247,v247
v_mfma_f32_16x16x32_bf16 v[0:3],v[192:195],a[24:27],v[0:3]
s_add_u32 m0,s92,0x4000
ds_read_b64_tr_b16 v[208:209],v224 offset:33792
s_mov_b32 s95,0x300200
v_mfma_f32_16x16x32_bf16 v[0:3],v[196:199],a[28:31],v[0:3]
ds_read_b64_tr_b16 v[210:211],v224 offset:34048
v_mul_f32 v160,v160,v247
v_fma_f32 v244,v244,s100,v234
v_mfma_f32_16x16x32_bf16 v[8:11],v[192:195],a[80:83],v[8:11]
buffer_load_dwordx4 v232,s[72:75],s95 offen lds
v_mfma_f32_16x16x32_bf16 v[8:11],v[196:199],a[84:87],v[8:11]
ds_read_b64_tr_b16 v[212:213],v224 offset:41984
ds_read_b64_tr_b16 v[214:215],v224 offset:42240
v_mfma_f32_16x16x32_bf16 v[16:19],v[192:195],a[136:139],v[16:19]
ds_read_b64_tr_b16 v[216:217],v225 offset:33792
v_mul_f32 v244,v244,v136
v_mfma_f32_16x16x32_bf16 v[16:19],v[196:199],a[140:143],v[16:19]
ds_read_b64_tr_b16 v[218:219],v225 offset:34048
v_mul_f32 v168,v136,v168
v_mfma_f32_16x16x32_bf16 v[24:27],v[192:195],a[192:195],v[24:27]
buffer_load_dwordx4 v232,s[76:79],s95 offen offset:1024 lds
v_mfma_f32_16x16x32_bf16 v[24:27],v[196:199],a[196:199],v[24:27]
ds_read_b64_tr_b16 v[220:221],v225 offset:41984
ds_read_b64_tr_b16 v[222:223],v225 offset:42240
v_mfma_f32_16x16x32_bf16 v[4:7],v[200:203],a[24:27],v[4:7]
v_mul_f32 v245,v137,v137
v_fma_f32 v245,v245,s100,v234
v_mfma_f32_16x16x32_bf16 v[4:7],v[204:207],a[28:31],v[4:7]
v_mul_f32 v245,v245,v137
v_mul_f32 v169,v137,v169
v_mul_f32 v246,v138,v138
v_mfma_f32_16x16x32_bf16 v[12:15],v[200:203],a[80:83],v[12:15]
buffer_load_dwordx4 v233,s[80:83],s95 offen offset:2048 lds
v_mfma_f32_16x16x32_bf16 v[12:15],v[204:207],a[84:87],v[12:15]
v_fma_f32 v246,v246,s100,v234
v_mul_f32 v246,v246,v138
v_mfma_f32_16x16x32_bf16 v[20:23],v[200:203],a[136:139],v[20:23]
v_mul_f32 v170,v138,v170
v_mul_f32 v247,v139,v139
v_mfma_f32_16x16x32_bf16 v[20:23],v[204:207],a[140:143],v[20:23]
v_fma_f32 v247,v247,s100,v234
v_mul_f32 v247,v247,v139
v_mfma_f32_16x16x32_bf16 v[28:31],v[200:203],a[192:195],v[28:31]
buffer_load_dwordx4 v233,s[84:87],s95 offen offset:3072 lds
v_mfma_f32_16x16x32_bf16 v[28:31],v[204:207],a[196:199],v[28:31]
s_waitcnt vmcnt(16) lgkmcnt(0)
v_mul_f32 v171,v139,v171
v_mul_f32 v241,v133,v133
v_mfma_f32_16x16x32_bf16 v[32:35],v[208:211],a[24:27],v[32:35]
s_barrier
ds_read_b64_tr_b16 v[192:193],v227 offset:32768
s_add_u32 m0,s92,0x8000
v_mfma_f32_16x16x32_bf16 v[32:35],v[212:215],a[28:31],v[32:35]
s_mov_b32 s95,0x100
ds_read_b64_tr_b16 v[194:195],v227 offset:33024
v_fma_f32 v241,v241,s100,v234
v_mul_f32 v241,v241,v133
v_mfma_f32_16x16x32_bf16 v[40:43],v[208:211],a[80:83],v[40:43]
buffer_load_dwordx4 v232,s[72:75],s95 offen lds
v_mfma_f32_16x16x32_bf16 v[40:43],v[212:215],a[84:87],v[40:43]
ds_read_b64_tr_b16 v[196:197],v227 offset:40960
ds_read_b64_tr_b16 v[198:199],v227 offset:41216
v_mfma_f32_16x16x32_bf16 v[48:51],v[208:211],a[136:139],v[48:51]
ds_read_b64_tr_b16 v[200:201],v228 offset:32768
v_mul_f32 v165,v133,v165
v_mfma_f32_16x16x32_bf16 v[48:51],v[212:215],a[140:143],v[48:51]
ds_read_b64_tr_b16 v[202:203],v228 offset:33024
v_mul_f32 v242,v134,v134
v_mfma_f32_16x16x32_bf16 v[56:59],v[208:211],a[192:195],v[56:59]
buffer_load_dwordx4 v232,s[76:79],s95 offen offset:1024 lds
v_mfma_f32_16x16x32_bf16 v[56:59],v[212:215],a[196:199],v[56:59]
ds_read_b64_tr_b16 v[204:205],v228 offset:40960
ds_read_b64_tr_b16 v[206:207],v228 offset:41216
v_mfma_f32_16x16x32_bf16 v[36:39],v[216:219],a[24:27],v[36:39]
v_exp_f32 v241,v241
v_mfma_f32_16x16x32_bf16 v[36:39],v[220:223],a[28:31],v[36:39]
v_add_f32 v241,1.0,v241
v_rcp_f32 v241,v241
v_mfma_f32_16x16x32_bf16 v[44:47],v[216:219],a[80:83],v[44:47]
buffer_load_dwordx4 v233,s[80:83],s95 offen offset:2048 lds
v_mfma_f32_16x16x32_bf16 v[44:47],v[220:223],a[84:87],v[44:47]
v_mul_f32 v165,v165,v241
v_fma_f32 v242,v242,s100,v234
v_mfma_f32_16x16x32_bf16 v[52:55],v[216:219],a[136:139],v[52:55]
v_mul_f32 v242,v242,v134
v_mul_f32 v166,v134,v166
v_mfma_f32_16x16x32_bf16 v[52:55],v[220:223],a[140:143],v[52:55]
v_exp_f32 v242,v242
v_mfma_f32_16x16x32_bf16 v[60:63],v[216:219],a[192:195],v[60:63]
buffer_load_dwordx4 v233,s[84:87],s95 offen offset:3072 lds
v_mfma_f32_16x16x32_bf16 v[60:63],v[220:223],a[196:199],v[60:63]
s_waitcnt lgkmcnt(0)
v_add_f32 v242,1.0,v242
v_mul_f32 v243,v135,v135
v_mfma_f32_16x16x32_bf16 v[0:3],v[192:195],a[32:35],v[0:3]
s_add_u32 m0,s92,0xc000
ds_read_b64_tr_b16 v[208:209],v227 offset:33792
s_mov_b32 s95,0x300
v_mfma_f32_16x16x32_bf16 v[0:3],v[196:199],a[36:39],v[0:3]
ds_read_b64_tr_b16 v[210:211],v227 offset:34048
v_rcp_f32 v242,v242
v_mfma_f32_16x16x32_bf16 v[8:11],v[192:195],a[88:91],v[8:11]
buffer_load_dwordx4 v232,s[72:75],s95 offen lds
v_mfma_f32_16x16x32_bf16 v[8:11],v[196:199],a[92:95],v[8:11]
ds_read_b64_tr_b16 v[212:213],v227 offset:41984
ds_read_b64_tr_b16 v[214:215],v227 offset:42240
v_mfma_f32_16x16x32_bf16 v[16:19],v[192:195],a[144:147],v[16:19]
ds_read_b64_tr_b16 v[216:217],v228 offset:33792
v_mul_f32 v166,v166,v242
v_mfma_f32_16x16x32_bf16 v[16:19],v[196:199],a[148:151],v[16:19]
ds_read_b64_tr_b16 v[218:219],v228 offset:34048
v_fma_f32 v243,v243,s100,v234
v_mfma_f32_16x16x32_bf16 v[24:27],v[192:195],a[200:203],v[24:27]
buffer_load_dwordx4 v232,s[76:79],s95 offen offset:1024 lds
v_mfma_f32_16x16x32_bf16 v[24:27],v[196:199],a[204:207],v[24:27]
ds_read_b64_tr_b16 v[220:221],v228 offset:41984
ds_read_b64_tr_b16 v[222:223],v228 offset:42240
v_mfma_f32_16x16x32_bf16 v[4:7],v[200:203],a[32:35],v[4:7]
v_mul_f32 v243,v243,v135
v_mul_f32 v167,v135,v167
)"
R"(v_mfma_f32_16x16x32_bf16 v[4:7],v[204:207],a[36:39],v[4:7]
v_exp_f32 v243,v243
v_mul_f32 v241,v144,v144
v_mfma_f32_16x16x32_bf16 v[12:15],v[200:203],a[88:91],v[12:15]
buffer_load_dwordx4 v233,s[80:83],s95 offen offset:2048 lds
v_mfma_f32_16x16x32_bf16 v[12:15],v[204:207],a[92:95],v[12:15]
v_add_f32 v243,1.0,v243
v_fma_f32 v241,v241,s100,v234
v_mfma_f32_16x16x32_bf16 v[20:23],v[200:203],a[144:147],v[20:23]
v_rcp_f32 v243,v243
v_mfma_f32_16x16x32_bf16 v[20:23],v[204:207],a[148:151],v[20:23]
v_mul_f32 v167,v167,v243
v_mul_f32 v241,v241,v144
v_mfma_f32_16x16x32_bf16 v[28:31],v[200:203],a[200:203],v[28:31]
buffer_load_dwordx4 v233,s[84:87],s95 offen offset:3072 lds
v_mfma_f32_16x16x32_bf16 v[28:31],v[204:207],a[204:207],v[28:31]
s_waitcnt vmcnt(16) lgkmcnt(0)
v_exp_f32 v244,v244
v_mfma_f32_16x16x32_bf16 v[32:35],v[208:211],a[32:35],v[32:35]
s_barrier
ds_read_b64_tr_b16 v[192:193],v227
s_add_u32 m0,s92,0x18000
v_mfma_f32_16x16x32_bf16 v[32:35],v[212:215],a[36:39],v[32:35]
s_mov_b32 s95,0x80100
ds_read_b64_tr_b16 v[194:195],v227 offset:256
v_add_f32 v244,1.0,v244
v_mul_f32 v176,v144,v176
v_mfma_f32_16x16x32_bf16 v[40:43],v[208:211],a[88:91],v[40:43]
buffer_load_dwordx4 v232,s[72:75],s95 offen lds
v_mfma_f32_16x16x32_bf16 v[40:43],v[212:215],a[92:95],v[40:43]
ds_read_b64_tr_b16 v[196:197],v227 offset:8192
ds_read_b64_tr_b16 v[198:199],v227 offset:8448
v_mfma_f32_16x16x32_bf16 v[48:51],v[208:211],a[144:147],v[48:51]
ds_read_b64_tr_b16 v[200:201],v228
v_mul_f32 v242,v145,v145
v_mfma_f32_16x16x32_bf16 v[48:51],v[212:215],a[148:151],v[48:51]
ds_read_b64_tr_b16 v[202:203],v228 offset:256
v_fma_f32 v242,v242,s100,v234
v_mfma_f32_16x16x32_bf16 v[56:59],v[208:211],a[200:203],v[56:59]
buffer_load_dwordx4 v232,s[76:79],s95 offen offset:1024 lds
v_mfma_f32_16x16x32_bf16 v[56:59],v[212:215],a[204:207],v[56:59]
ds_read_b64_tr_b16 v[204:205],v228 offset:8192
ds_read_b64_tr_b16 v[206:207],v228 offset:8448
v_mfma_f32_16x16x32_bf16 v[36:39],v[216:219],a[32:35],v[36:39]
v_rcp_f32 v244,v244
v_mfma_f32_16x16x32_bf16 v[36:39],v[220:223],a[36:39],v[36:39]
v_mul_f32 v168,v168,v244
v_mul_f32 v242,v242,v145
v_mul_f32 v177,v145,v177
v_mfma_f32_16x16x32_bf16 v[44:47],v[216:219],a[88:91],v[44:47]
buffer_load_dwordx4 v233,s[80:83],s95 offen offset:2048 lds
v_mfma_f32_16x16x32_bf16 v[44:47],v[220:223],a[92:95],v[44:47]
v_mul_f32 v243,v146,v146
v_fma_f32 v243,v243,s100,v234
v_mfma_f32_16x16x32_bf16 v[52:55],v[216:219],a[144:147],v[52:55]
v_mul_f32 v243,v243,v146
v_mul_f32 v178,v146,v178
v_mfma_f32_16x16x32_bf16 v[52:55],v[220:223],a[148:151],v[52:55]
v_mul_f32 v244,v147,v147
v_fma_f32 v244,v244,s100,v234
v_mfma_f32_16x16x32_bf16 v[60:63],v[216:219],a[200:203],v[60:63]
buffer_load_dwordx4 v233,s[84:87],s95 offen offset:3072 lds
v_mfma_f32_16x16x32_bf16 v[60:63],v[220:223],a[204:207],v[60:63]
s_waitcnt lgkmcnt(0)
v_mul_f32 v244,v244,v147
v_mul_f32 v179,v147,v179
v_mfma_f32_16x16x32_bf16 v[0:3],v[192:195],a[40:43],v[0:3]
s_add_u32 m0,s92,0x1c000
ds_read_b64_tr_b16 v[208:209],v227 offset:1024
s_mov_b32 s95,0x80300
v_mfma_f32_16x16x32_bf16 v[0:3],v[196:199],a[44:47],v[0:3]
ds_read_b64_tr_b16 v[210:211],v227 offset:1280
v_exp_f32 v238,v238
v_mfma_f32_16x16x32_bf16 v[8:11],v[192:195],a[96:99],v[8:11]
buffer_load_dwordx4 v232,s[72:75],s95 offen lds
v_mfma_f32_16x16x32_bf16 v[8:11],v[196:199],a[100:103],v[8:11]
ds_read_b64_tr_b16 v[212:213],v227 offset:9216
ds_read_b64_tr_b16 v[214:215],v227 offset:9472
v_mfma_f32_16x16x32_bf16 v[16:19],v[192:195],a[152:155],v[16:19]
ds_read_b64_tr_b16 v[216:217],v228 offset:1024
v_add_f32 v238,1.0,v238
v_mfma_f32_16x16x32_bf16 v[16:19],v[196:199],a[156:159],v[16:19]
ds_read_b64_tr_b16 v[218:219],v228 offset:1280
v_mul_f32 v240,v132,v132
v_mfma_f32_16x16x32_bf16 v[24:27],v[192:195],a[208:211],v[24:27]
buffer_load_dwordx4 v232,s[76:79],s95 offen offset:1024 lds
v_mfma_f32_16x16x32_bf16 v[24:27],v[196:199],a[212:215],v[24:27]
ds_read_b64_tr_b16 v[220:221],v228 offset:9216
ds_read_b64_tr_b16 v[222:223],v228 offset:9472
v_mfma_f32_16x16x32_bf16 v[4:7],v[200:203],a[40:43],v[4:7]
v_rcp_f32 v238,v238
v_mfma_f32_16x16x32_bf16 v[4:7],v[204:207],a[44:47],v[4:7]
v_mul_f32 v162,v162,v238
v_exp_f32 v239,v239
v_mfma_f32_16x16x32_bf16 v[12:15],v[200:203],a[96:99],v[12:15]
buffer_load_dwordx4 v233,s[80:83],s95 offen offset:2048 lds
v_mfma_f32_16x16x32_bf16 v[12:15],v[204:207],a[100:103],v[12:15]
v_add_f32 v239,1.0,v239
v_fma_f32 v240,v240,s100,v234
v_mfma_f32_16x16x32_bf16 v[20:23],v[200:203],a[152:155],v[20:23]
v_rcp_f32 v239,v239
v_mfma_f32_16x16x32_bf16 v[20:23],v[204:207],a[156:159],v[20:23]
v_mul_f32 v163,v163,v239
v_mul_f32 v240,v240,v132
v_mfma_f32_16x16x32_bf16 v[28:31],v[200:203],a[208:211],v[28:31]
buffer_load_dwordx4 v233,s[84:87],s95 offen offset:3072 lds
v_mfma_f32_16x16x32_bf16 v[28:31],v[204:207],a[212:215],v[28:31]
s_waitcnt vmcnt(16) lgkmcnt(0)
v_exp_f32 v240,v240
v_mfma_f32_16x16x32_bf16 v[32:35],v[208:211],a[40:43],v[32:35]
s_barrier
ds_read_b64_tr_b16 v[192:193],v224
s_add_u32 m0,s92,0x10000
v_mfma_f32_16x16x32_bf16 v[32:35],v[212:215],a[44:47],v[32:35]
s_mov_b32 s95,0x100100
ds_read_b64_tr_b16 v[194:195],v224 offset:256
v_mul_f32 v164,v132,v164
v_add_f32 v240,1.0,v240
v_mfma_f32_16x16x32_bf16 v[40:43],v[208:211],a[96:99],v[40:43]
buffer_load_dwordx4 v232,s[72:75],s95 offen lds
v_mfma_f32_16x16x32_bf16 v[40:43],v[212:215],a[100:103],v[40:43]
ds_read_b64_tr_b16 v[196:197],v224 offset:8192
ds_read_b64_tr_b16 v[198:199],v224 offset:8448
v_mfma_f32_16x16x32_bf16 v[48:51],v[208:211],a[152:155],v[48:51]
ds_read_b64_tr_b16 v[200:201],v225
v_mul_f32 v238,v141,v141
v_mfma_f32_16x16x32_bf16 v[48:51],v[212:215],a[156:159],v[48:51]
ds_read_b64_tr_b16 v[202:203],v225 offset:256
v_fma_f32 v238,v238,s100,v234
v_mfma_f32_16x16x32_bf16 v[56:59],v[208:211],a[208:211],v[56:59]
buffer_load_dwordx4 v232,s[76:79],s95 offen offset:1024 lds
v_mfma_f32_16x16x32_bf16 v[56:59],v[212:215],a[212:215],v[56:59]
ds_read_b64_tr_b16 v[204:205],v225 offset:8192
ds_read_b64_tr_b16 v[206:207],v225 offset:8448
v_mfma_f32_16x16x32_bf16 v[36:39],v[216:219],a[40:43],v[36:39]
v_rcp_f32 v240,v240
v_mfma_f32_16x16x32_bf16 v[36:39],v[220:223],a[44:47],v[36:39]
v_mul_f32 v164,v164,v240
v_mul_f32 v238,v238,v141
v_mul_f32 v173,v141,v173
v_mfma_f32_16x16x32_bf16 v[44:47],v[216:219],a[96:99],v[44:47]
buffer_load_dwordx4 v233,s[80:83],s95 offen offset:2048 lds
v_mfma_f32_16x16x32_bf16 v[44:47],v[220:223],a[100:103],v[44:47]
v_exp_f32 v238,v238
v_mfma_f32_16x16x32_bf16 v[52:55],v[216:219],a[152:155],v[52:55]
v_add_f32 v238,1.0,v238
v_mul_f32 v239,v142,v142
v_mfma_f32_16x16x32_bf16 v[52:55],v[220:223],a[156:159],v[52:55]
v_rcp_f32 v238,v238
v_mfma_f32_16x16x32_bf16 v[60:63],v[216:219],a[208:211],v[60:63]
buffer_load_dwordx4 v233,s[84:87],s95 offen offset:3072 lds
v_mfma_f32_16x16x32_bf16 v[60:63],v[220:223],a[212:215],v[60:63]
s_waitcnt lgkmcnt(0)
v_mul_f32 v173,v173,v238
v_fma_f32 v239,v239,s100,v234
v_mfma_f32_16x16x32_bf16 v[0:3],v[192:195],a[48:51],v[0:3]
s_add_u32 m0,s92,0x14000
ds_read_b64_tr_b16 v[208:209],v224 offset:1024
s_mov_b32 s95,0x100300
v_mfma_f32_16x16x32_bf16 v[0:3],v[196:199],a[52:55],v[0:3]
ds_read_b64_tr_b16 v[210:211],v224 offset:1280
v_mul_f32 v239,v239,v142
v_mul_f32 v174,v142,v174
v_mfma_f32_16x16x32_bf16 v[8:11],v[192:195],a[104:107],v[8:11]
buffer_load_dwordx4 v232,s[72:75],s95 offen lds
v_mfma_f32_16x16x32_bf16 v[8:11],v[196:199],a[108:111],v[8:11]
ds_read_b64_tr_b16 v[212:213],v224 offset:9216
ds_read_b64_tr_b16 v[214:215],v224 offset:9472
v_mfma_f32_16x16x32_bf16 v[16:19],v[192:195],a[160:163],v[16:19]
ds_read_b64_tr_b16 v[216:217],v225 offset:1024
v_cvt_pk_bf16_f32 v0,v0,v1
v_mfma_f32_16x16x32_bf16 v[16:19],v[196:199],a[164:167],v[16:19]
ds_read_b64_tr_b16 v[218:219],v225 offset:1280
v_cvt_pk_bf16_f32 v1,v2,v3
v_mfma_f32_16x16x32_bf16 v[24:27],v[192:195],a[216:219],v[24:27]
buffer_store_dwordx4 v[96:99],v254,s[88:91],s99 offen sc1
buffer_load_dwordx4 v232,s[76:79],s95 offen offset:1024 lds
v_mfma_f32_16x16x32_bf16 v[24:27],v[196:199],a[220:223],v[24:27]
s_add_u32 s99,s97,s98
buffer_store_dwordx4 v[104:107],v254,s[88:91],s99 offen sc1
v_mfma_f32_16x16x32_bf16 v[4:7],v[200:203],a[48:51],v[4:7]
s_lshr_b32 s98,%[h],2
s_mul_i32 s98,s98,28
s_add_u32 s98,s98,s101
s_and_b32 s101,%[w],2
s_add_u32 s98,s98,s101
s_and_b32 s101,%[h],2
s_mul_i32 s101,s101,6
s_add_u32 s98,s98,s101
s_add_u32 s98,s98,9
s_lshl_b32 s98,s98,10
s_add_u32 s99,s96,s98
buffer_store_dwordx4 v[112:115],v254,s[88:91],s99 offen sc1
v_mfma_f32_16x16x32_bf16 v[4:7],v[204:207],a[52:55],v[4:7]
s_add_u32 s99,s97,s98
buffer_store_dwordx4 v[120:123],v254,s[88:91],s99 offen sc1
ds_read_b64_tr_b16 v[220:221],v225 offset:9216
v_mfma_f32_16x16x32_bf16 v[12:15],v[200:203],a[104:107],v[12:15]
s_lshr_b32 s98,%[h],2
buffer_load_dwordx4 v233,s[80:83],s95 offen offset:2048 lds
)"
R"(v_mfma_f32_16x16x32_bf16 v[12:15],v[204:207],a[108:111],v[12:15]
s_mul_i32 s98,s98,28
ds_read_b64_tr_b16 v[222:223],v225 offset:9472
v_cvt_pk_bf16_f32 v2,v4,v5
v_mfma_f32_16x16x32_bf16 v[20:23],v[200:203],a[160:163],v[20:23]
s_and_b32 s101,%[w],1
v_cvt_pk_bf16_f32 v3,v6,v7
v_permlane16_swap_b32 v0,v2
v_mfma_f32_16x16x32_bf16 v[20:23],v[204:207],a[164:167],v[20:23]
s_lshl_b32 s101,s101,2
v_permlane16_swap_b32 v1,v3
v_cvt_pk_bf16_f32 v8,v8,v9
v_mfma_f32_16x16x32_bf16 v[28:31],v[200:203],a[216:219],v[28:31]
s_add_u32 s98,s98,s101
buffer_load_dwordx4 v233,s[84:87],s95 offen offset:3072 lds
v_mfma_f32_16x16x32_bf16 v[28:31],v[204:207],a[220:223],v[28:31]
s_waitcnt vmcnt(20) lgkmcnt(0)
ds_write_b128 v235,v[0:3] offset:32768
v_cvt_pk_bf16_f32 v9,v10,v11
v_mfma_f32_16x16x32_bf16 v[32:35],v[208:211],a[48:51],v[32:35]
s_barrier
ds_read_b64_tr_b16 v[192:193],v224 offset:32768
s_add_u32 m0,s92,0x0
v_mfma_f32_16x16x32_bf16 v[32:35],v[212:215],a[52:55],v[32:35]
s_mov_b32 s95,0x180100
ds_read_b64_tr_b16 v[194:195],v224 offset:33024
v_cvt_pk_bf16_f32 v10,v12,v13
v_cvt_pk_bf16_f32 v11,v14,v15
v_mfma_f32_16x16x32_bf16 v[40:43],v[208:211],a[104:107],v[40:43]
s_and_b32 s101,%[w],2
buffer_load_dwordx4 v232,s[72:75],s95 offen lds
v_mfma_f32_16x16x32_bf16 v[40:43],v[212:215],a[108:111],v[40:43]
s_add_u32 s98,s98,s101
ds_read_b64_tr_b16 v[196:197],v224 offset:40960
ds_read_b64_tr_b16 v[198:199],v224 offset:41216
v_mfma_f32_16x16x32_bf16 v[48:51],v[208:211],a[160:163],v[48:51]
s_and_b32 s101,%[h],2
ds_read_b64_tr_b16 v[200:201],v225 offset:32768
v_permlane16_swap_b32 v8,v10
v_mfma_f32_16x16x32_bf16 v[48:51],v[212:215],a[164:167],v[48:51]
s_mul_i32 s101,s101,6
ds_read_b64_tr_b16 v[202:203],v225 offset:33024
v_permlane16_swap_b32 v9,v11
v_mfma_f32_16x16x32_bf16 v[56:59],v[208:211],a[216:219],v[56:59]
s_add_u32 s98,s98,s101
buffer_load_dwordx4 v232,s[76:79],s95 offen offset:1024 lds
v_mfma_f32_16x16x32_bf16 v[56:59],v[212:215],a[220:223],v[56:59]
s_add_u32 s98,s98,0
ds_read_b64_tr_b16 v[204:205],v225 offset:40960
ds_read_b64_tr_b16 v[206:207],v225 offset:41216
v_mfma_f32_16x16x32_bf16 v[36:39],v[216:219],a[48:51],v[36:39]
s_lshl_b32 s98,s98,10
ds_write_b128 v235,v[8:11] offset:33792
v_cvt_pk_bf16_f32 v16,v16,v17
v_mfma_f32_16x16x32_bf16 v[36:39],v[220:223],a[52:55],v[36:39]
s_add_u32 s99,s96,s98
v_cvt_pk_bf16_f32 v17,v18,v19
v_cvt_pk_bf16_f32 v18,v20,v21
v_cvt_pk_bf16_f32 v19,v22,v23
v_mfma_f32_16x16x32_bf16 v[44:47],v[216:219],a[104:107],v[44:47]
s_and_b32 s101,%[w],1
buffer_load_dwordx4 v233,s[80:83],s95 offen offset:2048 lds
v_mfma_f32_16x16x32_bf16 v[44:47],v[220:223],a[108:111],v[44:47]
s_lshl_b32 s101,s101,2
v_permlane16_swap_b32 v16,v18
v_permlane16_swap_b32 v17,v19
v_mfma_f32_16x16x32_bf16 v[52:55],v[216:219],a[160:163],v[52:55]
ds_write_b128 v235,v[16:19] offset:34816
v_cvt_pk_bf16_f32 v24,v24,v25
v_mfma_f32_16x16x32_bf16 v[52:55],v[220:223],a[164:167],v[52:55]
v_cvt_pk_bf16_f32 v25,v26,v27
v_cvt_pk_bf16_f32 v26,v28,v29
v_mfma_f32_16x16x32_bf16 v[60:63],v[216:219],a[216:219],v[60:63]
buffer_load_dwordx4 v233,s[84:87],s95 offen offset:3072 lds
v_mfma_f32_16x16x32_bf16 v[60:63],v[220:223],a[220:223],v[60:63]
s_waitcnt lgkmcnt(2)
v_cvt_pk_bf16_f32 v27,v30,v31
v_permlane16_swap_b32 v24,v26
v_mfma_f32_16x16x32_bf16 v[64:67],v[192:195],a[0:3],0
s_add_u32 m0,s92,0x4000
ds_read_b64_tr_b16 v[208:209],v224 offset:33792
s_mov_b32 s95,0x180300
v_mfma_f32_16x16x32_bf16 v[64:67],v[196:199],a[4:7],v[64:67]
ds_read_b64_tr_b16 v[210:211],v224 offset:34048
v_permlane16_swap_b32 v25,v27
v_cvt_pk_bf16_f32 v32,v32,v33
v_mfma_f32_16x16x32_bf16 v[72:75],v[192:195],a[56:59],0
buffer_load_dwordx4 v232,s[72:75],s95 offen lds
v_mfma_f32_16x16x32_bf16 v[72:75],v[196:199],a[60:63],v[72:75]
ds_read_b64_tr_b16 v[212:213],v224 offset:41984
ds_read_b64_tr_b16 v[214:215],v224 offset:42240
v_mfma_f32_16x16x32_bf16 v[80:83],v[192:195],a[112:115],0
ds_read_b64_tr_b16 v[216:217],v225 offset:33792
ds_write_b128 v235,v[24:27] offset:35840
v_mfma_f32_16x16x32_bf16 v[80:83],v[196:199],a[116:119],v[80:83]
ds_read_b64_tr_b16 v[218:219],v225 offset:34048
v_cvt_pk_bf16_f32 v33,v34,v35
v_mfma_f32_16x16x32_bf16 v[88:91],v[192:195],a[168:171],0
buffer_load_dwordx4 v232,s[76:79],s95 offen offset:1024 lds
v_mfma_f32_16x16x32_bf16 v[88:91],v[196:199],a[172:175],v[88:91]
ds_read_b64_tr_b16 v[220:221],v225 offset:41984
ds_read_b64_tr_b16 v[222:223],v225 offset:42240
v_mfma_f32_16x16x32_bf16 v[68:71],v[200:203],a[0:3],0
v_cvt_pk_bf16_f32 v34,v36,v37
v_cvt_pk_bf16_f32 v35,v38,v39
v_mfma_f32_16x16x32_bf16 v[68:71],v[204:207],a[4:7],v[68:71]
v_permlane16_swap_b32 v32,v34
v_permlane16_swap_b32 v33,v35
v_cvt_pk_bf16_f32 v40,v40,v41
v_mfma_f32_16x16x32_bf16 v[76:79],v[200:203],a[56:59],0
buffer_load_dwordx4 v233,s[80:83],s95 offen offset:2048 lds
v_mfma_f32_16x16x32_bf16 v[76:79],v[204:207],a[60:63],v[76:79]
ds_write_b128 v235,v[32:35] offset:36864
v_cvt_pk_bf16_f32 v41,v42,v43
v_mfma_f32_16x16x32_bf16 v[84:87],v[200:203],a[112:115],0
v_cvt_pk_bf16_f32 v42,v44,v45
v_cvt_pk_bf16_f32 v43,v46,v47
v_mfma_f32_16x16x32_bf16 v[84:87],v[204:207],a[116:119],v[84:87]
v_permlane16_swap_b32 v40,v42
v_permlane16_swap_b32 v41,v43
v_mfma_f32_16x16x32_bf16 v[92:95],v[200:203],a[168:171],0
buffer_load_dwordx4 v233,s[84:87],s95 offen offset:3072 lds
v_mfma_f32_16x16x32_bf16 v[92:95],v[204:207],a[172:175],v[92:95]
s_waitcnt vmcnt(20) lgkmcnt(0)
ds_write_b128 v235,v[40:43] offset:37888
v_cvt_pk_bf16_f32 v48,v48,v49
v_mfma_f32_16x16x32_bf16 v[96:99],v[208:211],a[0:3],0
s_barrier
ds_read_b64_tr_b16 v[192:193],v227 offset:32768
s_add_u32 m0,s92,0x8000
v_mfma_f32_16x16x32_bf16 v[96:99],v[212:215],a[4:7],v[96:99]
s_mov_b32 s95,0x200100
ds_read_b64_tr_b16 v[194:195],v227 offset:33024
v_cvt_pk_bf16_f32 v49,v50,v51
v_cvt_pk_bf16_f32 v50,v52,v53
v_mfma_f32_16x16x32_bf16 v[104:107],v[208:211],a[56:59],0
buffer_load_dwordx4 v232,s[72:75],s95 offen lds
v_mfma_f32_16x16x32_bf16 v[104:107],v[212:215],a[60:63],v[104:107]
ds_read_b64_tr_b16 v[196:197],v227 offset:40960
ds_read_b64_tr_b16 v[198:199],v227 offset:41216
v_mfma_f32_16x16x32_bf16 v[112:115],v[208:211],a[112:115],0
ds_read_b64_tr_b16 v[200:201],v228 offset:32768
v_cvt_pk_bf16_f32 v51,v54,v55
v_mfma_f32_16x16x32_bf16 v[112:115],v[212:215],a[116:119],v[112:115]
ds_read_b64_tr_b16 v[202:203],v228 offset:33024
v_permlane16_swap_b32 v48,v50
v_mfma_f32_16x16x32_bf16 v[120:123],v[208:211],a[168:171],0
buffer_load_dwordx4 v232,s[76:79],s95 offen offset:1024 lds
v_mfma_f32_16x16x32_bf16 v[120:123],v[212:215],a[172:175],v[120:123]
ds_read_b64_tr_b16 v[204:205],v228 offset:40960
ds_read_b64_tr_b16 v[206:207],v228 offset:41216
v_mfma_f32_16x16x32_bf16 v[100:103],v[216:219],a[0:3],0
v_permlane16_swap_b32 v49,v51
v_cvt_pk_bf16_f32 v56,v56,v57
v_mfma_f32_16x16x32_bf16 v[100:103],v[220:223],a[4:7],v[100:103]
ds_write_b128 v235,v[48:51] offset:38912
v_cvt_pk_bf16_f32 v57,v58,v59
v_cvt_pk_bf16_f32 v58,v60,v61
v_mfma_f32_16x16x32_bf16 v[108:111],v[216:219],a[56:59],0
buffer_load_dwordx4 v233,s[80:83],s95 offen offset:2048 lds
v_mfma_f32_16x16x32_bf16 v[108:111],v[220:223],a[60:63],v[108:111]
v_cvt_pk_bf16_f32 v59,v62,v63
v_permlane16_swap_b32 v56,v58
v_mfma_f32_16x16x32_bf16 v[116:119],v[216:219],a[112:115],0
v_permlane16_swap_b32 v57,v59
v_mul_f32 v240,v143,v143
v_mfma_f32_16x16x32_bf16 v[116:119],v[220:223],a[116:119],v[116:119]
ds_write_b128 v235,v[56:59] offset:39936
v_fma_f32 v240,v240,s100,v234
v_mfma_f32_16x16x32_bf16 v[124:127],v[216:219],a[168:171],0
buffer_load_dwordx4 v233,s[84:87],s95 offen offset:3072 lds
v_mfma_f32_16x16x32_bf16 v[124:127],v[220:223],a[172:175],v[124:127]
s_waitcnt lgkmcnt(2)
v_exp_f32 v239,v239
v_mfma_f32_16x16x32_bf16 v[64:67],v[192:195],a[8:11],v[64:67]
s_add_u32 m0,s92,0xc000
ds_read_b64_tr_b16 v[208:209],v227 offset:33792
s_mov_b32 s95,0x200300
v_mfma_f32_16x16x32_bf16 v[64:67],v[196:199],a[12:15],v[64:67]
ds_read_b64_tr_b16 v[210:211],v227 offset:34048
v_add_f32 v239,1.0,v239
v_mul_f32 v240,v240,v143
v_mfma_f32_16x16x32_bf16 v[72:75],v[192:195],a[64:67],v[72:75]
buffer_load_dwordx4 v232,s[72:75],s95 offen lds
v_mfma_f32_16x16x32_bf16 v[72:75],v[196:199],a[68:71],v[72:75]
ds_read_b64_tr_b16 v[212:213],v227 offset:41984
ds_read_b64_tr_b16 v[214:215],v227 offset:42240
v_mfma_f32_16x16x32_bf16 v[80:83],v[192:195],a[120:123],v[80:83]
ds_read_b64_tr_b16 v[216:217],v228 offset:33792
v_mul_f32 v175,v143,v175
v_mfma_f32_16x16x32_bf16 v[80:83],v[196:199],a[124:127],v[80:83]
ds_read_b64_tr_b16 v[218:219],v228 offset:34048
v_mul_f32 v238,v152,v152
v_mfma_f32_16x16x32_bf16 v[88:91],v[192:195],a[176:179],v[88:91]
buffer_load_dwordx4 v232,s[76:79],s95 offen offset:1024 lds
v_mfma_f32_16x16x32_bf16 v[88:91],v[196:199],a[180:183],v[88:91]
ds_read_b64_tr_b16 v[220:221],v228 offset:41984
ds_read_b64_tr_b16 v[222:223],v228 offset:42240
v_mfma_f32_16x16x32_bf16 v[68:71],v[200:203],a[8:11],v[68:71]
v_rcp_f32 v239,v239
)"
R"(v_mfma_f32_16x16x32_bf16 v[68:71],v[204:207],a[12:15],v[68:71]
v_mul_f32 v174,v174,v239
v_exp_f32 v240,v240
v_mfma_f32_16x16x32_bf16 v[76:79],v[200:203],a[64:67],v[76:79]
buffer_load_dwordx4 v233,s[80:83],s95 offen offset:2048 lds
v_mfma_f32_16x16x32_bf16 v[76:79],v[204:207],a[68:71],v[76:79]
v_add_f32 v240,1.0,v240
v_fma_f32 v238,v238,s100,v234
v_mfma_f32_16x16x32_bf16 v[84:87],v[200:203],a[120:123],v[84:87]
v_rcp_f32 v240,v240
v_mfma_f32_16x16x32_bf16 v[84:87],v[204:207],a[124:127],v[84:87]
v_mul_f32 v175,v175,v240
v_mul_f32 v238,v238,v152
v_mfma_f32_16x16x32_bf16 v[92:95],v[200:203],a[176:179],v[92:95]
buffer_load_dwordx4 v233,s[84:87],s95 offen offset:3072 lds
v_mfma_f32_16x16x32_bf16 v[92:95],v[204:207],a[180:183],v[92:95]
s_waitcnt vmcnt(16) lgkmcnt(0)
v_exp_f32 v241,v241
v_mfma_f32_16x16x32_bf16 v[96:99],v[208:211],a[8:11],v[96:99]
s_barrier
ds_read_b64_tr_b16 v[192:193],v227
s_add_u32 m0,s92,0x18000
v_mfma_f32_16x16x32_bf16 v[96:99],v[212:215],a[12:15],v[96:99]
s_mov_b32 s95,0x280100
ds_read_b64_tr_b16 v[194:195],v227 offset:256
v_add_f32 v241,1.0,v241
v_mul_f32 v184,v152,v184
v_mfma_f32_16x16x32_bf16 v[104:107],v[208:211],a[64:67],v[104:107]
buffer_load_dwordx4 v232,s[72:75],s95 offen lds
v_mfma_f32_16x16x32_bf16 v[104:107],v[212:215],a[68:71],v[104:107]
ds_read_b64_tr_b16 v[196:197],v227 offset:8192
ds_read_b64_tr_b16 v[198:199],v227 offset:8448
v_mfma_f32_16x16x32_bf16 v[112:115],v[208:211],a[120:123],v[112:115]
ds_read_b64_tr_b16 v[200:201],v228
v_mul_f32 v239,v153,v153
v_mfma_f32_16x16x32_bf16 v[112:115],v[212:215],a[124:127],v[112:115]
ds_read_b64_tr_b16 v[202:203],v228 offset:256
v_fma_f32 v239,v239,s100,v234
v_mfma_f32_16x16x32_bf16 v[120:123],v[208:211],a[176:179],v[120:123]
buffer_load_dwordx4 v232,s[76:79],s95 offen offset:1024 lds
v_mfma_f32_16x16x32_bf16 v[120:123],v[212:215],a[180:183],v[120:123]
ds_read_b64_tr_b16 v[204:205],v228 offset:8192
ds_read_b64_tr_b16 v[206:207],v228 offset:8448
v_mfma_f32_16x16x32_bf16 v[100:103],v[216:219],a[8:11],v[100:103]
v_rcp_f32 v241,v241
v_mfma_f32_16x16x32_bf16 v[100:103],v[220:223],a[12:15],v[100:103]
v_mul_f32 v176,v176,v241
v_mul_f32 v239,v239,v153
v_mul_f32 v185,v153,v185
v_mfma_f32_16x16x32_bf16 v[108:111],v[216:219],a[64:67],v[108:111]
buffer_load_dwordx4 v233,s[80:83],s95 offen offset:2048 lds
v_mfma_f32_16x16x32_bf16 v[108:111],v[220:223],a[68:71],v[108:111]
v_mul_f32 v240,v154,v154
v_fma_f32 v240,v240,s100,v234
v_mfma_f32_16x16x32_bf16 v[116:119],v[216:219],a[120:123],v[116:119]
v_mul_f32 v240,v240,v154
v_mul_f32 v186,v154,v186
v_mfma_f32_16x16x32_bf16 v[116:119],v[220:223],a[124:127],v[116:119]
v_mul_f32 v241,v155,v155
v_fma_f32 v241,v241,s100,v234
v_mfma_f32_16x16x32_bf16 v[124:127],v[216:219],a[176:179],v[124:127]
buffer_load_dwordx4 v233,s[84:87],s95 offen offset:3072 lds
v_mfma_f32_16x16x32_bf16 v[124:127],v[220:223],a[180:183],v[124:127]
s_waitcnt lgkmcnt(0)
v_mul_f32 v241,v241,v155
v_mul_f32 v187,v155,v187
v_mfma_f32_16x16x32_bf16 v[64:67],v[192:195],a[16:19],v[64:67]
s_add_u32 m0,s92,0x1c000
ds_read_b64_tr_b16 v[208:209],v227 offset:1024
s_mov_b32 s95,0x280300
v_mfma_f32_16x16x32_bf16 v[64:67],v[196:199],a[20:23],v[64:67]
ds_read_b64_tr_b16 v[210:211],v227 offset:1280
v_exp_f32 v237,v237
v_mfma_f32_16x16x32_bf16 v[72:75],v[192:195],a[72:75],v[72:75]
buffer_load_dwordx4 v232,s[72:75],s95 offen lds
v_mfma_f32_16x16x32_bf16 v[72:75],v[196:199],a[76:79],v[72:75]
ds_read_b64_tr_b16 v[212:213],v227 offset:9216
ds_read_b64_tr_b16 v[214:215],v227 offset:9472
v_mfma_f32_16x16x32_bf16 v[80:83],v[192:195],a[128:131],v[80:83]
ds_read_b64_tr_b16 v[216:217],v228 offset:1024
v_add_f32 v237,1.0,v237
v_mfma_f32_16x16x32_bf16 v[80:83],v[196:199],a[132:135],v[80:83]
ds_read_b64_tr_b16 v[218:219],v228 offset:1280
v_mul_f32 v172,v140,v172
v_mfma_f32_16x16x32_bf16 v[88:91],v[192:195],a[184:187],v[88:91]
buffer_load_dwordx4 v232,s[76:79],s95 offen offset:1024 lds
v_mfma_f32_16x16x32_bf16 v[88:91],v[196:199],a[188:191],v[88:91]
ds_read_b64_tr_b16 v[220:221],v228 offset:9216
ds_read_b64_tr_b16 v[222:223],v228 offset:9472
v_mfma_f32_16x16x32_bf16 v[68:71],v[200:203],a[16:19],v[68:71]
v_rcp_f32 v237,v237
v_mfma_f32_16x16x32_bf16 v[68:71],v[204:207],a[20:23],v[68:71]
v_mul_f32 v161,v161,v237
v_mul_f32 v237,v140,v140
v_fma_f32 v237,v237,s100,v234
v_mfma_f32_16x16x32_bf16 v[76:79],v[200:203],a[72:75],v[76:79]
buffer_load_dwordx4 v233,s[80:83],s95 offen offset:2048 lds
v_mfma_f32_16x16x32_bf16 v[76:79],v[204:207],a[76:79],v[76:79]
v_mul_f32 v237,v237,v140
v_mul_f32 v180,v148,v180
v_mfma_f32_16x16x32_bf16 v[84:87],v[200:203],a[128:131],v[84:87]
v_exp_f32 v245,v245
v_mfma_f32_16x16x32_bf16 v[84:87],v[204:207],a[132:135],v[84:87]
v_add_f32 v245,1.0,v245
v_mul_f32 v181,v149,v181
v_mfma_f32_16x16x32_bf16 v[92:95],v[200:203],a[184:187],v[92:95]
buffer_load_dwordx4 v233,s[84:87],s95 offen offset:3072 lds
v_mfma_f32_16x16x32_bf16 v[92:95],v[204:207],a[188:191],v[92:95]
s_waitcnt vmcnt(16) lgkmcnt(0)
v_rcp_f32 v245,v245
v_mfma_f32_16x16x32_bf16 v[96:99],v[208:211],a[16:19],v[96:99]
s_barrier
ds_read_b64_tr_b16 v[192:193],v224
s_add_u32 m0,s92,0x10000
v_mfma_f32_16x16x32_bf16 v[96:99],v[212:215],a[20:23],v[96:99]
s_mov_b32 s95,0x300100
ds_read_b64_tr_b16 v[194:195],v224 offset:256
ds_read_b128 v[4:7],v236 offset:34816
ds_read_b128 v[12:15],v236 offset:35840
v_mfma_f32_16x16x32_bf16 v[104:107],v[208:211],a[72:75],v[104:107]
buffer_load_dwordx4 v232,s[72:75],s95 offen lds
v_mfma_f32_16x16x32_bf16 v[104:107],v[212:215],a[76:79],v[104:107]
ds_read_b64_tr_b16 v[196:197],v224 offset:8192
ds_read_b64_tr_b16 v[198:199],v224 offset:8448
v_mfma_f32_16x16x32_bf16 v[112:115],v[208:211],a[128:131],v[112:115]
ds_read_b64_tr_b16 v[200:201],v225
ds_read_b128 v[20:23],v236 offset:32768
v_mfma_f32_16x16x32_bf16 v[112:115],v[212:215],a[132:135],v[112:115]
ds_read_b64_tr_b16 v[202:203],v225 offset:256
ds_read_b128 v[28:31],v236 offset:33792
v_mfma_f32_16x16x32_bf16 v[120:123],v[208:211],a[184:187],v[120:123]
buffer_load_dwordx4 v232,s[76:79],s95 offen offset:1024 lds
v_mfma_f32_16x16x32_bf16 v[120:123],v[212:215],a[188:191],v[120:123]
ds_read_b64_tr_b16 v[204:205],v225 offset:8192
ds_read_b64_tr_b16 v[206:207],v225 offset:8448
v_mfma_f32_16x16x32_bf16 v[100:103],v[216:219],a[16:19],v[100:103]
ds_read_b128 v[36:39],v236 offset:38912
ds_read_b128 v[44:47],v236 offset:39936
v_mfma_f32_16x16x32_bf16 v[100:103],v[220:223],a[20:23],v[100:103]
ds_read_b128 v[52:55],v236 offset:36864
ds_read_b128 v[60:63],v236 offset:37888
v_mul_f32 v169,v169,v245
v_mfma_f32_16x16x32_bf16 v[108:111],v[216:219],a[72:75],v[108:111]
buffer_load_dwordx4 v233,s[80:83],s95 offen offset:2048 lds
v_mfma_f32_16x16x32_bf16 v[108:111],v[220:223],a[76:79],v[108:111]
v_exp_f32 v246,v246
v_mfma_f32_16x16x32_bf16 v[116:119],v[216:219],a[128:131],v[116:119]
v_add_f32 v246,1.0,v246
v_mul_f32 v245,v148,v148
v_mfma_f32_16x16x32_bf16 v[116:119],v[220:223],a[132:135],v[116:119]
v_rcp_f32 v246,v246
v_mfma_f32_16x16x32_bf16 v[124:127],v[216:219],a[184:187],v[124:127]
buffer_load_dwordx4 v233,s[84:87],s95 offen offset:3072 lds
v_mfma_f32_16x16x32_bf16 v[124:127],v[220:223],a[188:191],v[124:127]
s_waitcnt lgkmcnt(4)
v_mul_f32 v170,v170,v246
v_fma_f32 v245,v245,s100,v234
v_mfma_f32_16x16x32_bf16 v[64:67],v[192:195],a[24:27],v[64:67]
s_add_u32 m0,s92,0x14000
ds_read_b64_tr_b16 v[208:209],v224 offset:1024
s_mov_b32 s95,0x300300
v_mfma_f32_16x16x32_bf16 v[64:67],v[196:199],a[28:31],v[64:67]
ds_read_b64_tr_b16 v[210:211],v224 offset:1280
v_exp_f32 v247,v247
v_mfma_f32_16x16x32_bf16 v[72:75],v[192:195],a[80:83],v[72:75]
buffer_load_dwordx4 v232,s[72:75],s95 offen lds
v_mfma_f32_16x16x32_bf16 v[72:75],v[196:199],a[84:87],v[72:75]
ds_read_b64_tr_b16 v[212:213],v224 offset:9216
ds_read_b64_tr_b16 v[214:215],v224 offset:9472
v_mfma_f32_16x16x32_bf16 v[80:83],v[192:195],a[136:139],v[80:83]
ds_read_b64_tr_b16 v[216:217],v225 offset:1024
v_add_f32 v247,1.0,v247
v_mfma_f32_16x16x32_bf16 v[80:83],v[196:199],a[140:143],v[80:83]
ds_read_b64_tr_b16 v[218:219],v225 offset:1280
v_mul_f32 v245,v245,v148
v_mfma_f32_16x16x32_bf16 v[88:91],v[192:195],a[192:195],v[88:91]
buffer_load_dwordx4 v232,s[76:79],s95 offen offset:1024 lds
v_mfma_f32_16x16x32_bf16 v[88:91],v[196:199],a[196:199],v[88:91]
ds_read_b64_tr_b16 v[220:221],v225 offset:9216
ds_read_b64_tr_b16 v[222:223],v225 offset:9472
v_mfma_f32_16x16x32_bf16 v[68:71],v[200:203],a[24:27],v[68:71]
v_rcp_f32 v247,v247
v_mfma_f32_16x16x32_bf16 v[68:71],v[204:207],a[28:31],v[68:71]
v_mul_f32 v171,v171,v247
v_exp_f32 v237,v237
v_mfma_f32_16x16x32_bf16 v[76:79],v[200:203],a[80:83],v[76:79]
buffer_load_dwordx4 v233,s[80:83],s95 offen offset:2048 lds
v_mfma_f32_16x16x32_bf16 v[76:79],v[204:207],a[84:87],v[76:79]
v_add_f32 v237,1.0,v237
v_mul_f32 v246,v149,v149
v_mfma_f32_16x16x32_bf16 v[84:87],v[200:203],a[136:139],v[84:87]
v_rcp_f32 v237,v237
v_mfma_f32_16x16x32_bf16 v[84:87],v[204:207],a[140:143],v[84:87]
v_mul_f32 v172,v172,v237
v_fma_f32 v246,v246,s100,v234
v_mfma_f32_16x16x32_bf16 v[92:95],v[200:203],a[192:195],v[92:95]
buffer_load_dwordx4 v233,s[84:87],s95 offen offset:3072 lds
v_mfma_f32_16x16x32_bf16 v[92:95],v[204:207],a[196:199],v[92:95]
s_waitcnt vmcnt(16) lgkmcnt(0)
v_mul_f32 v246,v246,v149
)"
R"(v_mul_f32 v247,v150,v150
v_mfma_f32_16x16x32_bf16 v[96:99],v[208:211],a[24:27],v[96:99]
s_barrier
ds_read_b64_tr_b16 v[192:193],v224 offset:32768
s_add_u32 m0,s92,0x0
v_mfma_f32_16x16x32_bf16 v[96:99],v[212:215],a[28:31],v[96:99]
ds_read_b64_tr_b16 v[194:195],v224 offset:33024
v_fma_f32 v247,v247,s100,v234
v_mul_f32 v247,v247,v150
v_mfma_f32_16x16x32_bf16 v[104:107],v[208:211],a[80:83],v[104:107]
buffer_load_dwordx4 v252,s[24:27],s93 offen lds
v_mfma_f32_16x16x32_bf16 v[104:107],v[212:215],a[84:87],v[104:107]
ds_read_b64_tr_b16 v[196:197],v224 offset:40960
ds_read_b64_tr_b16 v[198:199],v224 offset:41216
v_mfma_f32_16x16x32_bf16 v[112:115],v[208:211],a[136:139],v[112:115]
ds_read_b64_tr_b16 v[200:201],v225 offset:32768
v_mul_f32 v182,v150,v182
v_mfma_f32_16x16x32_bf16 v[112:115],v[212:215],a[140:143],v[112:115]
ds_read_b64_tr_b16 v[202:203],v225 offset:33024
v_mul_f32 v237,v151,v151
v_mfma_f32_16x16x32_bf16 v[120:123],v[208:211],a[192:195],v[120:123]
buffer_load_dwordx4 v252,s[28:31],s93 offen offset:1024 lds
v_mfma_f32_16x16x32_bf16 v[120:123],v[212:215],a[196:199],v[120:123]
ds_read_b64_tr_b16 v[204:205],v225 offset:40960
ds_read_b64_tr_b16 v[206:207],v225 offset:41216
v_mfma_f32_16x16x32_bf16 v[100:103],v[216:219],a[24:27],v[100:103]
v_fma_f32 v237,v237,s100,v234
v_mul_f32 v237,v237,v151
v_mfma_f32_16x16x32_bf16 v[100:103],v[220:223],a[28:31],v[100:103]
v_mul_f32 v183,v151,v183
v_exp_f32 v242,v242
v_mfma_f32_16x16x32_bf16 v[108:111],v[216:219],a[80:83],v[108:111]
buffer_load_dwordx4 v252,s[32:35],s93 offen offset:2048 lds
v_mfma_f32_16x16x32_bf16 v[108:111],v[220:223],a[84:87],v[108:111]
v_add_f32 v242,1.0,v242
v_mul_f32 v188,v156,v188
v_mfma_f32_16x16x32_bf16 v[116:119],v[216:219],a[136:139],v[116:119]
v_rcp_f32 v242,v242
v_mfma_f32_16x16x32_bf16 v[116:119],v[220:223],a[140:143],v[116:119]
v_mul_f32 v177,v177,v242
v_mul_f32 v242,v156,v156
v_mfma_f32_16x16x32_bf16 v[124:127],v[216:219],a[192:195],v[124:127]
buffer_load_dwordx4 v252,s[36:39],s93 offen offset:3072 lds
v_mfma_f32_16x16x32_bf16 v[124:127],v[220:223],a[196:199],v[124:127]
s_add_u32 s93,s93,16384
s_waitcnt lgkmcnt(0)
v_fma_f32 v242,v242,s100,v234
v_mfma_f32_16x16x32_bf16 v[64:67],v[192:195],a[32:35],v[64:67]
s_add_u32 m0,s92,0x4000
ds_read_b64_tr_b16 v[208:209],v224 offset:33792
v_mul_f32 v242,v242,v156
v_mfma_f32_16x16x32_bf16 v[64:67],v[196:199],a[36:39],v[64:67]
ds_read_b64_tr_b16 v[210:211],v224 offset:34048
v_exp_f32 v243,v243
v_mfma_f32_16x16x32_bf16 v[72:75],v[192:195],a[88:91],v[72:75]
buffer_load_dwordx4 v252,s[24:27],s93 offen lds
v_mfma_f32_16x16x32_bf16 v[72:75],v[196:199],a[92:95],v[72:75]
ds_read_b64_tr_b16 v[212:213],v224 offset:41984
ds_read_b64_tr_b16 v[214:215],v224 offset:42240
v_mfma_f32_16x16x32_bf16 v[80:83],v[192:195],a[144:147],v[80:83]
ds_read_b64_tr_b16 v[216:217],v225 offset:33792
v_add_f32 v243,1.0,v243
v_mfma_f32_16x16x32_bf16 v[80:83],v[196:199],a[148:151],v[80:83]
ds_read_b64_tr_b16 v[218:219],v225 offset:34048
v_mul_f32 v189,v157,v189
v_mfma_f32_16x16x32_bf16 v[88:91],v[192:195],a[200:203],v[88:91]
buffer_load_dwordx4 v252,s[28:31],s93 offen offset:1024 lds
v_mfma_f32_16x16x32_bf16 v[88:91],v[196:199],a[204:207],v[88:91]
ds_read_b64_tr_b16 v[220:221],v225 offset:41984
ds_read_b64_tr_b16 v[222:223],v225 offset:42240
v_mfma_f32_16x16x32_bf16 v[68:71],v[200:203],a[32:35],v[68:71]
v_rcp_f32 v243,v243
v_mfma_f32_16x16x32_bf16 v[68:71],v[204:207],a[36:39],v[68:71]
v_mul_f32 v178,v178,v243
v_exp_f32 v244,v244
v_mfma_f32_16x16x32_bf16 v[76:79],v[200:203],a[88:91],v[76:79]
buffer_load_dwordx4 v252,s[32:35],s93 offen offset:2048 lds
v_mfma_f32_16x16x32_bf16 v[76:79],v[204:207],a[92:95],v[76:79]
v_add_f32 v244,1.0,v244
v_mul_f32 v243,v157,v157
v_mfma_f32_16x16x32_bf16 v[84:87],v[200:203],a[144:147],v[84:87]
v_rcp_f32 v244,v244
v_mfma_f32_16x16x32_bf16 v[84:87],v[204:207],a[148:151],v[84:87]
v_mul_f32 v179,v179,v244
v_fma_f32 v243,v243,s100,v234
v_mfma_f32_16x16x32_bf16 v[92:95],v[200:203],a[200:203],v[92:95]
buffer_load_dwordx4 v252,s[36:39],s93 offen offset:3072 lds
v_mfma_f32_16x16x32_bf16 v[92:95],v[204:207],a[204:207],v[92:95]
s_add_u32 s93,s93,16384
s_waitcnt vmcnt(16) lgkmcnt(0)
v_mul_f32 v243,v243,v157
v_mfma_f32_16x16x32_bf16 v[96:99],v[208:211],a[32:35],v[96:99]
s_barrier
ds_read_b64_tr_b16 v[192:193],v227 offset:32768
s_add_u32 m0,s92,0x8000
v_mfma_f32_16x16x32_bf16 v[96:99],v[212:215],a[36:39],v[96:99]
ds_read_b64_tr_b16 v[194:195],v227 offset:33024
v_exp_f32 v245,v245
v_mfma_f32_16x16x32_bf16 v[104:107],v[208:211],a[88:91],v[104:107]
buffer_load_dwordx4 v252,s[24:27],s93 offen lds
v_mfma_f32_16x16x32_bf16 v[104:107],v[212:215],a[92:95],v[104:107]
ds_read_b64_tr_b16 v[196:197],v227 offset:40960
ds_read_b64_tr_b16 v[198:199],v227 offset:41216
v_mfma_f32_16x16x32_bf16 v[112:115],v[208:211],a[144:147],v[112:115]
ds_read_b64_tr_b16 v[200:201],v228 offset:32768
v_add_f32 v245,1.0,v245
v_mfma_f32_16x16x32_bf16 v[112:115],v[212:215],a[148:151],v[112:115]
ds_read_b64_tr_b16 v[202:203],v228 offset:33024
v_mul_f32 v244,v158,v158
v_mfma_f32_16x16x32_bf16 v[120:123],v[208:211],a[200:203],v[120:123]
buffer_load_dwordx4 v252,s[28:31],s93 offen offset:1024 lds
v_mfma_f32_16x16x32_bf16 v[120:123],v[212:215],a[204:207],v[120:123]
ds_read_b64_tr_b16 v[204:205],v228 offset:40960
ds_read_b64_tr_b16 v[206:207],v228 offset:41216
v_mfma_f32_16x16x32_bf16 v[100:103],v[216:219],a[32:35],v[100:103]
v_rcp_f32 v245,v245
v_mfma_f32_16x16x32_bf16 v[100:103],v[220:223],a[36:39],v[100:103]
v_mul_f32 v180,v180,v245
v_fma_f32 v244,v244,s100,v234
v_mul_f32 v244,v244,v158
v_mfma_f32_16x16x32_bf16 v[108:111],v[216:219],a[88:91],v[108:111]
buffer_load_dwordx4 v252,s[32:35],s93 offen offset:2048 lds
v_mfma_f32_16x16x32_bf16 v[108:111],v[220:223],a[92:95],v[108:111]
v_mul_f32 v190,v158,v190
v_mul_f32 v245,v159,v159
v_mfma_f32_16x16x32_bf16 v[116:119],v[216:219],a[144:147],v[116:119]
v_fma_f32 v245,v245,s100,v234
v_mul_f32 v245,v245,v159
v_mfma_f32_16x16x32_bf16 v[116:119],v[220:223],a[148:151],v[116:119]
v_mul_f32 v191,v159,v191
v_cvt_pk_bf16_f32 v160,v160,v161
v_mfma_f32_16x16x32_bf16 v[124:127],v[216:219],a[200:203],v[124:127]
buffer_load_dwordx4 v252,s[36:39],s93 offen offset:3072 lds
v_mfma_f32_16x16x32_bf16 v[124:127],v[220:223],a[204:207],v[124:127]
s_add_u32 s93,s93,16384
s_waitcnt lgkmcnt(0)
v_cvt_pk_bf16_f32 v161,v162,v163
v_mfma_f32_16x16x32_bf16 v[64:67],v[192:195],a[40:43],v[64:67]
s_add_u32 m0,s92,0xc000
ds_read_b64_tr_b16 v[208:209],v227 offset:33792
v_cvt_pk_bf16_f32 v162,v164,v165
v_mfma_f32_16x16x32_bf16 v[64:67],v[196:199],a[44:47],v[64:67]
ds_read_b64_tr_b16 v[210:211],v227 offset:34048
v_cvt_pk_bf16_f32 v163,v166,v167
v_cvt_pk_bf16_f32 v168,v168,v169
v_mfma_f32_16x16x32_bf16 v[72:75],v[192:195],a[96:99],v[72:75]
buffer_load_dwordx4 v252,s[24:27],s93 offen lds
v_mfma_f32_16x16x32_bf16 v[72:75],v[196:199],a[100:103],v[72:75]
ds_read_b64_tr_b16 v[212:213],v227 offset:41984
ds_read_b64_tr_b16 v[214:215],v227 offset:42240
v_mfma_f32_16x16x32_bf16 v[80:83],v[192:195],a[152:155],v[80:83]
ds_read_b64_tr_b16 v[216:217],v228 offset:33792
v_cvt_pk_bf16_f32 v169,v170,v171
v_mfma_f32_16x16x32_bf16 v[80:83],v[196:199],a[156:159],v[80:83]
ds_read_b64_tr_b16 v[218:219],v228 offset:34048
v_cvt_pk_bf16_f32 v170,v172,v173
v_mfma_f32_16x16x32_bf16 v[88:91],v[192:195],a[208:211],v[88:91]
buffer_load_dwordx4 v252,s[28:31],s93 offen offset:1024 lds
v_mfma_f32_16x16x32_bf16 v[88:91],v[196:199],a[212:215],v[88:91]
ds_read_b64_tr_b16 v[220:221],v228 offset:41984
ds_read_b64_tr_b16 v[222:223],v228 offset:42240
v_mfma_f32_16x16x32_bf16 v[68:71],v[200:203],a[40:43],v[68:71]
buffer_store_dwordx4 v[160:163],v254,s[88:91],s99 offen sc1
v_mfma_f32_16x16x32_bf16 v[68:71],v[204:207],a[44:47],v[68:71]
s_add_u32 s99,s97,s98
v_cvt_pk_bf16_f32 v171,v174,v175
v_cvt_pk_bf16_f32 v176,v176,v177
v_cvt_pk_bf16_f32 v177,v178,v179
v_mfma_f32_16x16x32_bf16 v[76:79],v[200:203],a[96:99],v[76:79]
s_lshr_b32 s98,%[h],2
buffer_load_dwordx4 v252,s[32:35],s93 offen offset:2048 lds
v_mfma_f32_16x16x32_bf16 v[76:79],v[204:207],a[100:103],v[76:79]
s_mul_i32 s98,s98,28
buffer_store_dwordx4 v[168:171],v254,s[88:91],s99 offen sc1
v_mfma_f32_16x16x32_bf16 v[84:87],v[200:203],a[152:155],v[84:87]
s_add_u32 s98,s98,s101
v_exp_f32 v246,v246
v_mfma_f32_16x16x32_bf16 v[84:87],v[204:207],a[156:159],v[84:87]
s_and_b32 s101,%[w],2
v_exp_f32 v247,v247
v_mfma_f32_16x16x32_bf16 v[92:95],v[200:203],a[208:211],v[92:95]
s_add_u32 s98,s98,s101
buffer_load_dwordx4 v252,s[36:39],s93 offen offset:3072 lds
v_mfma_f32_16x16x32_bf16 v[92:95],v[204:207],a[212:215],v[92:95]
s_add_u32 s93,s93,16384
s_waitcnt vmcnt(18) lgkmcnt(0)
v_add_f32 v246,1.0,v246
v_mfma_f32_16x16x32_bf16 v[96:99],v[208:211],a[40:43],v[96:99]
s_barrier
ds_read_b64_tr_b16 v[192:193],v227
v_add_f32 v247,1.0,v247
v_mfma_f32_16x16x32_bf16 v[96:99],v[212:215],a[44:47],v[96:99]
s_and_b32 s101,%[h],2
ds_read_b64_tr_b16 v[194:195],v227 offset:256
v_rcp_f32 v246,v246
v_mfma_f32_16x16x32_bf16 v[104:107],v[208:211],a[96:99],v[104:107]
s_mul_i32 s101,s101,6
ds_read_b64_tr_b16 v[196:197],v227 offset:8192
v_mul_f32 v181,v181,v246
v_mfma_f32_16x16x32_bf16 v[104:107],v[212:215],a[100:103],v[104:107]
)"
R"(s_add_u32 s98,s98,s101
ds_read_b64_tr_b16 v[198:199],v227 offset:8448
v_cvt_pk_bf16_f32 v178,v180,v181
v_mfma_f32_16x16x32_bf16 v[112:115],v[208:211],a[152:155],v[112:115]
s_add_u32 s98,s98,1
ds_read_b64_tr_b16 v[200:201],v228
v_mfma_f32_16x16x32_bf16 v[112:115],v[212:215],a[156:159],v[112:115]
s_lshl_b32 s98,s98,10
ds_read_b64_tr_b16 v[202:203],v228 offset:256
v_mfma_f32_16x16x32_bf16 v[120:123],v[208:211],a[208:211],v[120:123]
s_add_u32 s99,s96,s98
ds_read_b64_tr_b16 v[204:205],v228 offset:8192
v_mfma_f32_16x16x32_bf16 v[120:123],v[212:215],a[212:215],v[120:123]
ds_read_b64_tr_b16 v[206:207],v228 offset:8448
v_mfma_f32_16x16x32_bf16 v[100:103],v[216:219],a[40:43],v[100:103]
v_rcp_f32 v247,v247
v_mfma_f32_16x16x32_bf16 v[100:103],v[220:223],a[44:47],v[100:103]
buffer_load_dword v230,v230,s[56:59],0 offen
v_mul_f32 v182,v182,v247
v_mfma_f32_16x16x32_bf16 v[108:111],v[216:219],a[96:99],v[108:111]
v_exp_f32 v237,v237
v_mfma_f32_16x16x32_bf16 v[108:111],v[220:223],a[100:103],v[108:111]
v_exp_f32 v238,v238
v_mfma_f32_16x16x32_bf16 v[116:119],v[216:219],a[152:155],v[116:119]
buffer_load_dword v231,v231,s[60:63],0 offen
v_mfma_f32_16x16x32_bf16 v[116:119],v[220:223],a[156:159],v[116:119]
v_add_f32 v237,1.0,v237
v_add_f32 v238,1.0,v238
v_mfma_f32_16x16x32_bf16 v[124:127],v[216:219],a[208:211],v[124:127]
v_rcp_f32 v237,v237
v_mfma_f32_16x16x32_bf16 v[124:127],v[220:223],a[212:215],v[124:127]
s_waitcnt lgkmcnt(0)
v_mul_f32 v183,v183,v237
v_cvt_pk_bf16_f32 v179,v182,v183
v_mfma_f32_16x16x32_bf16 v[64:67],v[192:195],a[48:51],v[64:67]
ds_read_b64_tr_b16 v[208:209],v227 offset:1024
v_mfma_f32_16x16x32_bf16 v[64:67],v[196:199],a[52:55],v[64:67]
ds_read_b64_tr_b16 v[210:211],v227 offset:1280
buffer_store_dwordx4 v[176:179],v254,s[88:91],s99 offen sc1
v_mfma_f32_16x16x32_bf16 v[72:75],v[192:195],a[104:107],v[72:75]
s_add_u32 s99,s97,s98
ds_read_b64_tr_b16 v[212:213],v227 offset:9216
v_mfma_f32_16x16x32_bf16 v[72:75],v[196:199],a[108:111],v[72:75]
ds_read_b64_tr_b16 v[214:215],v227 offset:9472
v_mfma_f32_16x16x32_bf16 v[80:83],v[192:195],a[160:163],v[80:83]
ds_read_b64_tr_b16 v[216:217],v228 offset:1024
v_cvt_pk_bf16_f32 v64,v64,v65
v_mfma_f32_16x16x32_bf16 v[80:83],v[196:199],a[164:167],v[80:83]
ds_read_b64_tr_b16 v[218:219],v228 offset:1280
v_cvt_pk_bf16_f32 v65,v66,v67
v_mfma_f32_16x16x32_bf16 v[88:91],v[192:195],a[216:219],v[88:91]
ds_read_b64_tr_b16 v[220:221],v228 offset:9216
v_cvt_pk_bf16_f32 v72,v72,v73
v_mfma_f32_16x16x32_bf16 v[88:91],v[196:199],a[220:223],v[88:91]
ds_read_b64_tr_b16 v[222:223],v228 offset:9472
v_cvt_pk_bf16_f32 v73,v74,v75
v_mfma_f32_16x16x32_bf16 v[68:71],v[200:203],a[48:51],v[68:71]
v_cvt_pk_bf16_f32 v80,v80,v81
v_cvt_pk_bf16_f32 v81,v82,v83
v_mfma_f32_16x16x32_bf16 v[68:71],v[204:207],a[52:55],v[68:71]
v_rcp_f32 v238,v238
v_mfma_f32_16x16x32_bf16 v[76:79],v[200:203],a[104:107],v[76:79]
v_cvt_pk_bf16_f32 v88,v88,v89
v_cvt_pk_bf16_f32 v89,v90,v91
v_mfma_f32_16x16x32_bf16 v[76:79],v[204:207],a[108:111],v[76:79]
v_exp_f32 v239,v239
v_mfma_f32_16x16x32_bf16 v[84:87],v[200:203],a[160:163],v[84:87]
v_mul_f32 v184,v184,v238
v_cvt_pk_bf16_f32 v66,v68,v69
v_mfma_f32_16x16x32_bf16 v[84:87],v[204:207],a[164:167],v[84:87]
v_cvt_pk_bf16_f32 v67,v70,v71
v_permlane16_swap_b32 v64,v66
v_mfma_f32_16x16x32_bf16 v[92:95],v[200:203],a[216:219],v[92:95]
v_permlane16_swap_b32 v65,v67
v_cvt_pk_bf16_f32 v74,v76,v77
v_mfma_f32_16x16x32_bf16 v[92:95],v[204:207],a[220:223],v[92:95]
s_waitcnt vmcnt(13) lgkmcnt(0)
ds_write_b128 v235,v[64:67] offset:32768
v_cvt_pk_bf16_f32 v75,v78,v79
v_mfma_f32_16x16x32_bf16 v[96:99],v[208:211],a[48:51],v[96:99]
s_barrier
ds_read_b128 v[192:195],v248
v_permlane16_swap_b32 v72,v74
v_mfma_f32_16x16x32_bf16 v[96:99],v[212:215],a[52:55],v[96:99]
v_permlane16_swap_b32 v73,v75
v_cvt_pk_bf16_f32 v82,v84,v85
v_cvt_pk_bf16_f32 v83,v86,v87
v_mfma_f32_16x16x32_bf16 v[104:107],v[208:211],a[104:107],v[104:107]
ds_read_b128 v[196:199],v249
ds_write_b128 v235,v[72:75] offset:33792
v_mfma_f32_16x16x32_bf16 v[104:107],v[212:215],a[108:111],v[104:107]
v_permlane16_swap_b32 v80,v82
v_permlane16_swap_b32 v81,v83
v_mfma_f32_16x16x32_bf16 v[112:115],v[208:211],a[160:163],v[112:115]
ds_read_b128 v[200:203],v248 offset:1024
ds_write_b128 v235,v[80:83] offset:34816
v_mfma_f32_16x16x32_bf16 v[112:115],v[212:215],a[164:167],v[112:115]
s_add_u32 m0,s92,0x10000
v_cvt_pk_bf16_f32 v90,v92,v93
v_cvt_pk_bf16_f32 v91,v94,v95
v_mfma_f32_16x16x32_bf16 v[120:123],v[208:211],a[216:219],v[120:123]
ds_read_b128 v[204:207],v249 offset:1024
v_permlane16_swap_b32 v88,v90
v_mfma_f32_16x16x32_bf16 v[120:123],v[212:215],a[220:223],v[120:123]
v_permlane16_swap_b32 v89,v91
v_cvt_pk_bf16_f32 v96,v96,v97
v_mfma_f32_16x16x32_bf16 v[100:103],v[216:219],a[48:51],v[100:103]
ds_write_b128 v235,v[88:91] offset:35840
v_cvt_pk_bf16_f32 v97,v98,v99
v_mfma_f32_16x16x32_bf16 v[100:103],v[220:223],a[52:55],v[100:103]
buffer_load_dwordx4 v253,s[40:43],s94 offen lds
v_cvt_pk_bf16_f32 v104,v104,v105
v_mfma_f32_16x16x32_bf16 v[108:111],v[216:219],a[104:107],v[108:111]
v_cvt_pk_bf16_f32 v105,v106,v107
v_cvt_pk_bf16_f32 v112,v112,v113
v_mfma_f32_16x16x32_bf16 v[108:111],v[220:223],a[108:111],v[108:111]
v_cvt_pk_bf16_f32 v113,v114,v115
v_cvt_pk_bf16_f32 v120,v120,v121
v_mfma_f32_16x16x32_bf16 v[116:119],v[216:219],a[160:163],v[116:119]
buffer_load_dwordx4 v253,s[44:47],s94 offen offset:1024 lds
v_mfma_f32_16x16x32_bf16 v[116:119],v[220:223],a[164:167],v[116:119]
v_cvt_pk_bf16_f32 v98,v100,v101
v_cvt_pk_bf16_f32 v99,v102,v103
v_mfma_f32_16x16x32_bf16 v[124:127],v[216:219],a[216:219],v[124:127]
v_permlane16_swap_b32 v96,v98
v_permlane16_swap_b32 v97,v99
v_mfma_f32_16x16x32_bf16 v[124:127],v[220:223],a[220:223],v[124:127]
s_waitcnt lgkmcnt(1)
ds_write_b128 v235,v[96:99] offset:36864
v_cvt_pk_bf16_f32 v106,v108,v109
v_mfma_f32_16x16x32_bf16 v[128:131],v[192:195],v[0:3],0
ds_read_b128 v[208:211],v248 offset:8192
v_cvt_pk_bf16_f32 v107,v110,v111
v_mfma_f32_16x16x32_bf16 v[128:131],v[196:199],v[4:7],v[128:131]
v_permlane16_swap_b32 v104,v106
v_permlane16_swap_b32 v105,v107
v_cvt_pk_bf16_f32 v114,v116,v117
v_mfma_f32_16x16x32_bf16 v[128:131],v[200:203],v[32:35],v[128:131]
ds_read_b128 v[212:215],v249 offset:8192
ds_write_b128 v235,v[104:107] offset:37888
v_mfma_f32_16x16x32_bf16 v[128:131],v[204:207],v[36:39],v[128:131]
v_cvt_pk_bf16_f32 v115,v118,v119
v_permlane16_swap_b32 v112,v114
v_mfma_f32_16x16x32_bf16 v[136:139],v[192:195],v[8:11],0
ds_read_b128 v[216:219],v248 offset:9216
v_permlane16_swap_b32 v113,v115
v_mfma_f32_16x16x32_bf16 v[136:139],v[196:199],v[12:15],v[136:139]
ds_write_b128 v235,v[112:115] offset:38912
v_cvt_pk_bf16_f32 v121,v122,v123
v_mfma_f32_16x16x32_bf16 v[136:139],v[200:203],v[40:43],v[136:139]
ds_read_b128 v[220:223],v249 offset:9216
v_cvt_pk_bf16_f32 v122,v124,v125
v_mfma_f32_16x16x32_bf16 v[136:139],v[204:207],v[44:47],v[136:139]
v_cvt_pk_bf16_f32 v123,v126,v127
v_permlane16_swap_b32 v120,v122
v_mfma_f32_16x16x32_bf16 v[144:147],v[192:195],v[16:19],0
v_permlane16_swap_b32 v121,v123
v_add_f32 v239,1.0,v239
v_mfma_f32_16x16x32_bf16 v[144:147],v[196:199],v[20:23],v[144:147]
buffer_load_dwordx4 v253,s[48:51],s94 offen offset:2048 lds
ds_write_b128 v235,v[120:123] offset:39936
v_mfma_f32_16x16x32_bf16 v[144:147],v[200:203],v[48:51],v[144:147]
v_rcp_f32 v239,v239
v_mfma_f32_16x16x32_bf16 v[144:147],v[204:207],v[52:55],v[144:147]
v_mul_f32 v185,v185,v239
v_cvt_pk_bf16_f32 v184,v184,v185
v_mfma_f32_16x16x32_bf16 v[152:155],v[192:195],v[24:27],0
buffer_load_dwordx4 v253,s[52:55],s94 offen offset:3072 lds
v_mfma_f32_16x16x32_bf16 v[152:155],v[196:199],v[28:31],v[152:155]
s_add_u32 s94,s94,16384
v_exp_f32 v240,v240
v_mfma_f32_16x16x32_bf16 v[152:155],v[200:203],v[56:59],v[152:155]
v_exp_f32 v241,v241
v_mfma_f32_16x16x32_bf16 v[152:155],v[204:207],v[60:63],v[152:155]
s_waitcnt vmcnt(7) lgkmcnt(0)
v_add_f32 v240,1.0,v240
v_add_f32 v241,1.0,v241
v_mfma_f32_16x16x32_bf16 v[132:135],v[208:211],v[0:3],0
s_barrier
ds_read_b128 v[192:195],v248 offset:32768
v_mfma_f32_16x16x32_bf16 v[132:135],v[212:215],v[4:7],v[132:135]
v_rcp_f32 v240,v240
v_mfma_f32_16x16x32_bf16 v[132:135],v[216:219],v[32:35],v[132:135]
ds_read_b128 v[196:199],v249 offset:32768
v_mul_f32 v186,v186,v240
v_mfma_f32_16x16x32_bf16 v[132:135],v[220:223],v[36:39],v[132:135]
v_rcp_f32 v241,v241
v_mfma_f32_16x16x32_bf16 v[140:143],v[208:211],v[8:11],0
ds_read_b128 v[200:203],v248 offset:33792
v_mul_f32 v187,v187,v241
v_mfma_f32_16x16x32_bf16 v[140:143],v[212:215],v[12:15],v[140:143]
s_add_u32 m0,s92,0x14000
v_cvt_pk_bf16_f32 v185,v186,v187
v_mov_b32 v240,0x3f803f80
v_mfma_f32_16x16x32_bf16 v[140:143],v[216:219],v[40:43],v[140:143]
ds_read_b128 v[204:207],v249 offset:33792
v_mov_b32 v241,0x3f803f80
v_mfma_f32_16x16x32_bf16 v[140:143],v[220:223],v[44:47],v[140:143]
v_exp_f32 v242,v242
v_mfma_f32_16x16x32_bf16 v[148:151],v[208:211],v[16:19],0
)"
R"(v_exp_f32 v243,v243
v_mfma_f32_16x16x32_bf16 v[148:151],v[212:215],v[20:23],v[148:151]
buffer_load_dwordx4 v253,s[40:43],s94 offen lds
v_add_f32 v242,1.0,v242
v_mfma_f32_16x16x32_bf16 v[148:151],v[216:219],v[48:51],v[148:151]
v_rcp_f32 v242,v242
v_mfma_f32_16x16x32_bf16 v[148:151],v[220:223],v[52:55],v[148:151]
v_mul_f32 v188,v188,v242
v_add_f32 v243,1.0,v243
v_mfma_f32_16x16x32_bf16 v[156:159],v[208:211],v[24:27],0
buffer_load_dwordx4 v253,s[44:47],s94 offen offset:1024 lds
v_mfma_f32_16x16x32_bf16 v[156:159],v[212:215],v[28:31],v[156:159]
v_rcp_f32 v243,v243
v_mfma_f32_16x16x32_bf16 v[156:159],v[216:219],v[56:59],v[156:159]
v_mul_f32 v189,v189,v243
v_cvt_pk_bf16_f32 v186,v188,v189
v_mfma_f32_16x16x32_bf16 v[156:159],v[220:223],v[60:63],v[156:159]
s_waitcnt lgkmcnt(0)
v_exp_f32 v244,v244
v_mfma_f32_16x16x32_bf16 v[160:163],v[192:195],v[0:3],0
ds_read_b128 v[208:211],v248 offset:40960
v_add_f32 v244,1.0,v244
v_mfma_f32_16x16x32_bf16 v[160:163],v[196:199],v[4:7],v[160:163]
v_rcp_f32 v244,v244
v_mov_b32 v242,0x3f803f80
v_mfma_f32_16x16x32_bf16 v[160:163],v[200:203],v[32:35],v[160:163]
ds_read_b128 v[212:215],v249 offset:40960
v_mul_f32 v190,v190,v244
v_mfma_f32_16x16x32_bf16 v[160:163],v[204:207],v[36:39],v[160:163]
v_exp_f32 v245,v245
v_mfma_f32_16x16x32_bf16 v[168:171],v[192:195],v[8:11],0
ds_read_b128 v[216:219],v248 offset:41984
v_add_f32 v245,1.0,v245
v_mfma_f32_16x16x32_bf16 v[168:171],v[196:199],v[12:15],v[168:171]
v_rcp_f32 v245,v245
v_mfma_f32_16x16x32_bf16 v[168:171],v[200:203],v[40:43],v[168:171]
ds_read_b128 v[220:223],v249 offset:41984
v_mul_f32 v191,v191,v245
v_mfma_f32_16x16x32_bf16 v[168:171],v[204:207],v[44:47],v[168:171]
v_cvt_pk_bf16_f32 v187,v190,v191
v_mov_b32 v243,0x3f803f80
v_mfma_f32_16x16x32_bf16 v[176:179],v[192:195],v[16:19],0
buffer_store_dwordx4 v[184:187],v254,s[88:91],s99 offen sc1
v_mfma_f32_16x16x32_bf16 v[176:179],v[196:199],v[20:23],v[176:179]
s_waitcnt lgkmcnt(0)
buffer_load_dwordx4 v253,s[48:51],s94 offen offset:2048 lds
v_mfma_f32_16x16x32_bf16 v[176:179],v[200:203],v[48:51],v[176:179]
s_barrier
ds_read_b128 v[68:71],v236 offset:34816
ds_read_b128 v[76:79],v236 offset:35840
v_mfma_f32_16x16x32_bf16 v[176:179],v[204:207],v[52:55],v[176:179]
ds_read_b128 v[84:87],v236 offset:32768
ds_read_b128 v[92:95],v236 offset:33792
v_mfma_f32_16x16x32_bf16 v[184:187],v[192:195],v[24:27],0
buffer_load_dwordx4 v253,s[52:55],s94 offen offset:3072 lds
v_mfma_f32_16x16x32_bf16 v[184:187],v[196:199],v[28:31],v[184:187]
s_add_u32 s94,s94,16384
ds_read_b128 v[100:103],v236 offset:38912
ds_read_b128 v[108:111],v236 offset:39936
v_mfma_f32_16x16x32_bf16 v[184:187],v[200:203],v[56:59],v[184:187]
ds_read_b128 v[116:119],v236 offset:36864
ds_read_b128 v[124:127],v236 offset:37888
v_mfma_f32_16x16x32_bf16 v[184:187],v[204:207],v[60:63],v[184:187]
s_waitcnt lgkmcnt(8)
v_mfma_f32_16x16x32_bf16 v[164:167],v[208:211],v[0:3],0
ds_read_b128 v[192:195],v248 offset:2048
v_mfma_f32_16x16x32_bf16 v[164:167],v[212:215],v[4:7],v[164:167]
v_mfma_f32_16x16x32_bf16 v[164:167],v[216:219],v[32:35],v[164:167]
ds_read_b128 v[196:199],v249 offset:2048
v_mfma_f32_16x16x32_bf16 v[164:167],v[220:223],v[36:39],v[164:167]
v_mfma_f32_16x16x32_bf16 v[172:175],v[208:211],v[8:11],0
ds_read_b128 v[200:203],v248 offset:3072
v_mfma_f32_16x16x32_bf16 v[172:175],v[212:215],v[12:15],v[172:175]
s_add_u32 m0,s92,0x18000
v_mfma_f32_16x16x32_bf16 v[172:175],v[216:219],v[40:43],v[172:175]
ds_read_b128 v[204:207],v249 offset:3072
v_mfma_f32_16x16x32_bf16 v[172:175],v[220:223],v[44:47],v[172:175]
v_mfma_f32_16x16x32_bf16 v[180:183],v[208:211],v[16:19],0
v_mfma_f32_16x16x32_bf16 v[180:183],v[212:215],v[20:23],v[180:183]
buffer_load_dwordx4 v253,s[40:43],s94 offen lds
v_mfma_f32_16x16x32_bf16 v[180:183],v[216:219],v[48:51],v[180:183]
v_mfma_f32_16x16x32_bf16 v[180:183],v[220:223],v[52:55],v[180:183]
v_mfma_f32_16x16x32_bf16 v[188:191],v[208:211],v[24:27],0
buffer_load_dwordx4 v253,s[44:47],s94 offen offset:1024 lds
v_mfma_f32_16x16x32_bf16 v[188:191],v[212:215],v[28:31],v[188:191]
v_mfma_f32_16x16x32_bf16 v[188:191],v[216:219],v[56:59],v[188:191]
v_mfma_f32_16x16x32_bf16 v[188:191],v[220:223],v[60:63],v[188:191]
s_waitcnt lgkmcnt(0)
v_mfma_f32_16x16x32_bf16 v[128:131],v[192:195],v[64:67],v[128:131]
s_waitcnt lgkmcnt(4)
ds_read_b128 v[208:211],v248 offset:10240
v_accvgpr_read_b32 v224,a255
v_mfma_f32_16x16x32_bf16 v[128:131],v[196:199],v[68:71],v[128:131]
s_waitcnt vmcnt(63)
v_add_u32 v255,v255,v224
v_mfma_f32_16x16x32_bf16 v[128:131],v[200:203],v[96:99],v[128:131]
s_mov_b32 s72,0x3db8aa3b
ds_read_b128 v[212:215],v249 offset:10240
v_mfma_f32_16x16x32_bf16 v[128:131],v[204:207],v[100:103],v[128:131]
s_mov_b32 s73,0xc2000000
v_mfma_f32_16x16x32_bf16 v[136:139],v[192:195],v[72:75],v[136:139]
s_mov_b32 s74,14
ds_read_b128 v[216:219],v248 offset:11264
v_mfma_f32_16x16x32_bf16 v[136:139],v[196:199],v[76:79],v[136:139]
s_lshr_b32 s78,%[h],1
v_readlane_b32 s75,v255,s78
v_mfma_f32_16x16x32_bf16 v[136:139],v[200:203],v[104:107],v[136:139]
s_lshr_b32 s79,%[tb],2
ds_read_b128 v[220:223],v249 offset:11264
v_max3_f32 v244,v128,v129,v130
v_mfma_f32_16x16x32_bf16 v[136:139],v[204:207],v[108:111],v[136:139]
s_lshl_b32 s79,s79,3
v_cvt_pk_bf16_f32 v128,0,v128
v_cvt_pk_bf16_f32 v129,0,v129
v_mfma_f32_16x16x32_bf16 v[144:147],v[192:195],v[80:83],v[144:147]
s_lshr_b32 s78,%[h],1
v_cvt_pk_bf16_f32 v130,0,v130
v_mfma_f32_16x16x32_bf16 v[144:147],v[196:199],v[84:87],v[144:147]
s_add_u32 s79,s79,s78
buffer_load_dwordx4 v253,s[48:51],s94 offen offset:2048 lds
v_max3_f32 v245,v136,v137,v138
v_mfma_f32_16x16x32_bf16 v[144:147],v[200:203],v[112:115],v[144:147]
s_lshl_b32 s79,s79,7
v_cvt_pk_bf16_f32 v136,0,v136
v_cvt_pk_bf16_f32 v137,0,v137
v_mfma_f32_16x16x32_bf16 v[144:147],v[204:207],v[116:119],v[144:147]
s_lshl_b32 s78,%[w],15
v_cvt_pk_bf16_f32 v138,0,v138
v_mfma_f32_16x16x32_bf16 v[152:155],v[192:195],v[88:91],v[152:155]
s_add_u32 s79,s79,s78
buffer_load_dwordx4 v253,s[52:55],s94 offen offset:3072 lds
v_mfma_f32_16x16x32_bf16 v[152:155],v[196:199],v[92:95],v[152:155]
s_add_u32 s94,s94,16384
v_mfma_f32_16x16x32_bf16 v[152:155],v[200:203],v[120:123],v[152:155]
s_add_u32 s79,s79,0x2b20000
v_max3_f32 v246,v144,v145,v146
v_cvt_pk_bf16_f32 v144,0,v144
v_mfma_f32_16x16x32_bf16 v[152:155],v[204:207],v[124:127],v[152:155]
s_waitcnt lgkmcnt(0)
v_cvt_pk_bf16_f32 v145,0,v145
v_cvt_pk_bf16_f32 v146,0,v146
v_mfma_f32_16x16x32_bf16 v[132:135],v[208:211],v[64:67],v[132:135]
s_sub_u32 s78,s79,131072
ds_read_b128 v[192:195],v248 offset:34816
v_mfma_f32_16x16x32_bf16 v[132:135],v[212:215],v[68:71],v[132:135]
s_mov_b64 s[56:57],%[wd]
v_max3_f32 v247,v152,v153,v154
v_cvt_pk_bf16_f32 v152,0,v152
v_cvt_pk_bf16_f32 v153,0,v153
v_mfma_f32_16x16x32_bf16 v[132:135],v[216:219],v[96:99],v[132:135]
s_mov_b32 s58,0x62000
ds_read_b128 v[196:199],v249 offset:34816
v_cvt_pk_bf16_f32 v154,0,v154
v_mfma_f32_16x16x32_bf16 v[132:135],v[220:223],v[100:103],v[132:135]
s_mov_b32 s59,0x20000
v_mfma_f32_16x16x32_bf16 v[140:143],v[208:211],v[72:75],v[140:143]
s_mov_b64 s[60:61],%[wd]
ds_read_b128 v[200:203],v248 offset:35840
v_mfma_f32_16x16x32_bf16 v[140:143],v[212:215],v[76:79],v[140:143]
s_add_u32 m0,s92,0x1c000
v_mfma_f32_16x16x32_bf16 v[140:143],v[216:219],v[104:107],v[140:143]
s_add_u32 s60,s60,0x1800
s_addc_u32 s61,s61,0
ds_read_b128 v[204:207],v249 offset:35840
v_max3_f32 v244,v131,v132,v244
v_mfma_f32_16x16x32_bf16 v[140:143],v[220:223],v[108:111],v[140:143]
s_mov_b32 s62,0x60800
v_max3_f32 v244,v133,v134,v244
v_max_f32 v244,v135,v244
v_mfma_f32_16x16x32_bf16 v[148:151],v[208:211],v[80:83],v[148:151]
s_mov_b32 s63,0x20000
v_cvt_pk_bf16_f32 v131,0,v131
v_cvt_pk_bf16_f32 v132,0,v132
v_mfma_f32_16x16x32_bf16 v[148:151],v[212:215],v[84:87],v[148:151]
s_mov_b64 s[64:65],%[wd]
buffer_load_dwordx4 v253,s[40:43],s94 offen lds
v_max3_f32 v245,v139,v140,v245
v_mfma_f32_16x16x32_bf16 v[148:151],v[216:219],v[112:115],v[148:151]
s_add_u32 s64,s64,0x3000
s_addc_u32 s65,s65,0
v_max3_f32 v245,v141,v142,v245
v_max_f32 v245,v143,v245
v_mfma_f32_16x16x32_bf16 v[148:151],v[220:223],v[116:119],v[148:151]
s_mov_b32 s66,0x5f000
v_cvt_pk_bf16_f32 v139,0,v139
v_cvt_pk_bf16_f32 v133,0,v133
v_mfma_f32_16x16x32_bf16 v[156:159],v[208:211],v[88:91],v[156:159]
s_mov_b32 s67,0x20000
buffer_load_dwordx4 v253,s[44:47],s94 offen offset:1024 lds
v_mfma_f32_16x16x32_bf16 v[156:159],v[212:215],v[92:95],v[156:159]
s_mov_b64 s[68:69],%[wd]
v_max3_f32 v246,v147,v148,v246
v_max3_f32 v246,v149,v150,v246
v_mfma_f32_16x16x32_bf16 v[156:159],v[216:219],v[120:123],v[156:159]
s_add_u32 s68,s68,0x4800
s_addc_u32 s69,s69,0
v_max_f32 v246,v151,v246
v_cvt_pk_bf16_f32 v147,0,v147
v_mfma_f32_16x16x32_bf16 v[156:159],v[220:223],v[124:127],v[156:159]
s_waitcnt lgkmcnt(0)
)"
R"(v_cvt_pk_bf16_f32 v134,0,v134
v_cvt_pk_bf16_f32 v135,0,v135
v_mfma_f32_16x16x32_bf16 v[160:163],v[192:195],v[64:67],v[160:163]
s_mov_b32 s70,0x5d800
ds_read_b128 v[208:211],v248 offset:43008
v_cvt_pk_bf16_f32 v140,0,v140
v_mfma_f32_16x16x32_bf16 v[160:163],v[196:199],v[68:71],v[160:163]
s_mov_b32 s71,0x20000
v_max3_f32 v247,v155,v156,v247
v_max3_f32 v247,v157,v158,v247
v_max_f32 v247,v159,v247
v_mfma_f32_16x16x32_bf16 v[160:163],v[200:203],v[96:99],v[160:163]
ds_read_b128 v[212:215],v249 offset:43008
v_cvt_pk_bf16_f32 v155,0,v155
v_mfma_f32_16x16x32_bf16 v[160:163],v[204:207],v[100:103],v[160:163]
v_cvt_pk_bf16_f32 v141,0,v141
v_cvt_pk_bf16_f32 v142,0,v142
v_mfma_f32_16x16x32_bf16 v[168:171],v[192:195],v[72:75],v[168:171]
ds_read_b128 v[216:219],v248 offset:44032
v_cvt_pk_bf16_f32 v143,0,v143
v_mfma_f32_16x16x32_bf16 v[168:171],v[196:199],v[76:79],v[168:171]
v_cvt_pk_bf16_f32 v148,0,v148
v_cvt_pk_bf16_f32 v149,0,v149
v_mfma_f32_16x16x32_bf16 v[168:171],v[200:203],v[104:107],v[168:171]
ds_read_b128 v[220:223],v249 offset:44032
v_cvt_pk_bf16_f32 v150,0,v150
v_mfma_f32_16x16x32_bf16 v[168:171],v[204:207],v[108:111],v[168:171]
v_cvt_pk_bf16_f32 v151,0,v151
v_cvt_pk_bf16_f32 v156,0,v156
v_mfma_f32_16x16x32_bf16 v[176:179],v[192:195],v[80:83],v[176:179]
v_cvt_pk_bf16_f32 v157,0,v157
v_cvt_pk_bf16_f32 v158,0,v158
v_mfma_f32_16x16x32_bf16 v[176:179],v[196:199],v[84:87],v[176:179]
buffer_load_dwordx4 v253,s[48:51],s94 offen offset:2048 lds
v_cvt_pk_bf16_f32 v159,0,v159
v_mfma_f32_16x16x32_bf16 v[176:179],v[200:203],v[112:115],v[176:179]
v_max3_f32 v244,v160,v161,v244
v_max3_f32 v244,v162,v163,v244
v_mfma_f32_16x16x32_bf16 v[176:179],v[204:207],v[116:119],v[176:179]
v_max3_f32 v245,v168,v169,v245
v_max3_f32 v245,v170,v171,v245
v_mfma_f32_16x16x32_bf16 v[184:187],v[192:195],v[88:91],v[184:187]
buffer_load_dwordx4 v253,s[52:55],s94 offen offset:3072 lds
v_mfma_f32_16x16x32_bf16 v[184:187],v[196:199],v[92:95],v[184:187]
s_add_u32 s94,s94,16384
v_mfma_f32_16x16x32_bf16 v[184:187],v[200:203],v[120:123],v[184:187]
v_mfma_f32_16x16x32_bf16 v[184:187],v[204:207],v[124:127],v[184:187]
s_waitcnt vmcnt(8) lgkmcnt(0)
v_max3_f32 v246,v176,v177,v246
v_max3_f32 v246,v178,v179,v246
v_mfma_f32_16x16x32_bf16 v[164:167],v[208:211],v[64:67],v[164:167]
s_barrier
ds_read_b64_tr_b16 v[192:193],v250
v_mfma_f32_16x16x32_bf16 v[164:167],v[212:215],v[68:71],v[164:167]
ds_read_b64_tr_b16 v[194:195],v250 offset:8192
v_max3_f32 v247,v184,v185,v247
v_max3_f32 v247,v186,v187,v247
v_mfma_f32_16x16x32_bf16 v[164:167],v[216:219],v[96:99],v[164:167]
ds_read_b64_tr_b16 v[196:197],v251
v_mfma_f32_16x16x32_bf16 v[164:167],v[220:223],v[100:103],v[164:167]
ds_read_b64_tr_b16 v[198:199],v251 offset:8192
v_mfma_f32_16x16x32_bf16 v[172:175],v[208:211],v[72:75],v[172:175]
ds_read_b64_tr_b16 v[200:201],v250 offset:512
v_mfma_f32_16x16x32_bf16 v[172:175],v[212:215],v[76:79],v[172:175]
s_add_u32 m0,s92,0x0
ds_read_b64_tr_b16 v[202:203],v250 offset:8704
v_mfma_f32_16x16x32_bf16 v[172:175],v[216:219],v[104:107],v[172:175]
ds_read_b64_tr_b16 v[204:205],v251 offset:512
v_max3_f32 v244,v164,v165,v244
v_mfma_f32_16x16x32_bf16 v[172:175],v[220:223],v[108:111],v[172:175]
ds_read_b64_tr_b16 v[206:207],v251 offset:8704
v_max3_f32 v244,v166,v167,v244
v_mfma_f32_16x16x32_bf16 v[180:183],v[208:211],v[80:83],v[180:183]
v_mfma_f32_16x16x32_bf16 v[180:183],v[212:215],v[84:87],v[180:183]
buffer_load_dwordx4 v252,s[24:27],s93 offen lds
v_mfma_f32_16x16x32_bf16 v[180:183],v[216:219],v[112:115],v[180:183]
v_mfma_f32_16x16x32_bf16 v[180:183],v[220:223],v[116:119],v[180:183]
v_mfma_f32_16x16x32_bf16 v[188:191],v[208:211],v[88:91],v[188:191]
buffer_load_dwordx4 v252,s[28:31],s93 offen offset:1024 lds
v_mfma_f32_16x16x32_bf16 v[188:191],v[212:215],v[92:95],v[188:191]
v_max3_f32 v245,v172,v173,v245
v_max3_f32 v245,v174,v175,v245
v_mfma_f32_16x16x32_bf16 v[188:191],v[216:219],v[120:123],v[188:191]
v_mfma_f32_16x16x32_bf16 v[188:191],v[220:223],v[124:127],v[188:191]
s_waitcnt lgkmcnt(0)
v_max3_f32 v246,v180,v181,v246
v_max3_f32 v246,v182,v183,v246
s_nop 4
v_max3_f32 v247,v188,v189,v247
v_max3_f32 v247,v190,v191,v247
v_mov_b32 v224,v244
v_mov_b32 v225,v245
v_mov_b32 v226,v246
v_mov_b32 v227,v247
v_permlane16_swap_b32 v244,v224
v_permlane16_swap_b32 v245,v225
v_permlane16_swap_b32 v246,v226
v_permlane16_swap_b32 v247,v227
v_max_f32 v244,v244,v224
v_max_f32 v245,v245,v225
v_max_f32 v246,v246,v226
v_max_f32 v247,v247,v227
v_mov_b32 v224,v244
v_mov_b32 v225,v245
v_mov_b32 v226,v246
v_mov_b32 v227,v247
v_permlane32_swap_b32 v244,v224
v_permlane32_swap_b32 v245,v225
v_permlane32_swap_b32 v246,v226
v_permlane32_swap_b32 v247,v227
v_max_f32 v244,v244,v224
v_max_f32 v245,v245,v225
v_max_f32 v246,v246,v226
v_max_f32 v247,v247,v227
ds_write_b128 v235,v[244:247] offset:32768
s_waitcnt lgkmcnt(0)
s_barrier
ds_read_b128 v[224:227],v236 offset:32768
buffer_load_dwordx4 v252,s[32:35],s93 offen offset:2048 lds
buffer_load_dwordx4 v252,s[36:39],s93 offen offset:3072 lds
s_add_u32 s93,s93,16384
s_waitcnt lgkmcnt(0)
v_max_f32 v244,v244,v226
v_max_f32 v245,v245,v227
v_max_f32 v246,v246,v224
v_max_f32 v247,v247,v225
v_cvt_pk_bf16_f32 v244,0,v244
v_cvt_pk_bf16_f32 v245,0,v245
v_cvt_pk_bf16_f32 v246,0,v246
v_cvt_pk_bf16_f32 v247,0,v247
v_mul_f32 v244,s72,v244
v_mul_f32 v245,s72,v245
v_mul_f32 v246,s72,v246
v_mul_f32 v247,s72,v247
v_sub_f32 v244,0xc2c00000,v244
v_sub_f32 v245,0xc2c00000,v245
v_sub_f32 v246,0xc2c00000,v246
v_sub_f32 v247,0xc2c00000,v247
s_add_u32 m0,s92,0x4000
s_nop 0
buffer_load_dwordx4 v252,s[24:27],s93 offen lds
v_fma_f32 v128,v128,s72,v244
v_fma_f32 v129,v129,s72,v244
v_fma_f32 v130,v130,s72,v244
v_fma_f32 v131,v131,s72,v244
v_fma_f32 v136,v136,s72,v245
v_fma_f32 v137,v137,s72,v245
v_fma_f32 v138,v138,s72,v245
v_fma_f32 v139,v139,s72,v245
v_fma_f32 v144,v144,s72,v246
v_fma_f32 v145,v145,s72,v246
v_fma_f32 v146,v146,s72,v246
v_fma_f32 v147,v147,s72,v246
v_fma_f32 v152,v152,s72,v247
buffer_load_dwordx4 v252,s[28:31],s93 offen offset:1024 lds
v_fma_f32 v153,v153,s72,v247
v_fma_f32 v154,v154,s72,v247
v_fma_f32 v155,v155,s72,v247
v_fma_f32 v132,v132,s72,v244
v_fma_f32 v133,v133,s72,v244
v_fma_f32 v134,v134,s72,v244
v_fma_f32 v135,v135,s72,v244
v_fma_f32 v140,v140,s72,v245
v_fma_f32 v141,v141,s72,v245
v_fma_f32 v142,v142,s72,v245
v_fma_f32 v143,v143,s72,v245
v_fma_f32 v148,v148,s72,v246
v_fma_f32 v149,v149,s72,v246
buffer_load_dwordx4 v252,s[32:35],s93 offen offset:2048 lds
v_fma_f32 v150,v150,s72,v246
v_fma_f32 v151,v151,s72,v246
v_fma_f32 v156,v156,s72,v247
v_fma_f32 v157,v157,s72,v247
v_fma_f32 v158,v158,s72,v247
v_fma_f32 v159,v159,s72,v247
v_exp_f32 v128,v128
v_exp_f32 v129,v129
v_exp_f32 v130,v130
v_exp_f32 v131,v131
v_exp_f32 v132,v132
v_exp_f32 v133,v133
v_exp_f32 v134,v134
v_exp_f32 v135,v135
buffer_load_dwordx4 v252,s[36:39],s93 offen offset:3072 lds
s_add_u32 s93,s93,16384
v_exp_f32 v136,v136
v_exp_f32 v137,v137
v_exp_f32 v138,v138
v_exp_f32 v139,v139
v_exp_f32 v140,v140
v_exp_f32 v141,v141
v_exp_f32 v142,v142
v_exp_f32 v143,v143
v_exp_f32 v144,v144
v_exp_f32 v145,v145
v_exp_f32 v146,v146
v_exp_f32 v147,v147
v_exp_f32 v148,v148
s_add_u32 m0,s92,0x8000
s_nop 0
)"
R"(buffer_load_dwordx4 v252,s[24:27],s93 offen lds
v_exp_f32 v149,v149
v_exp_f32 v150,v150
v_exp_f32 v151,v151
v_exp_f32 v152,v152
v_exp_f32 v153,v153
v_exp_f32 v154,v154
v_exp_f32 v155,v155
v_exp_f32 v156,v156
v_exp_f32 v157,v157
v_exp_f32 v158,v158
v_exp_f32 v159,v159
v_cvt_pk_bf16_f32 v128,v128,v129
v_cvt_pk_bf16_f32 v129,v130,v131
buffer_load_dwordx4 v252,s[28:31],s93 offen offset:1024 lds
v_cvt_pk_bf16_f32 v130,v132,v133
v_cvt_pk_bf16_f32 v131,v134,v135
v_cvt_pk_bf16_f32 v136,v136,v137
v_cvt_pk_bf16_f32 v137,v138,v139
v_cvt_pk_bf16_f32 v138,v140,v141
v_cvt_pk_bf16_f32 v139,v142,v143
v_cvt_pk_bf16_f32 v144,v144,v145
v_cvt_pk_bf16_f32 v145,v146,v147
v_cvt_pk_bf16_f32 v146,v148,v149
v_cvt_pk_bf16_f32 v147,v150,v151
v_cvt_pk_bf16_f32 v152,v152,v153
v_cvt_pk_bf16_f32 v153,v154,v155
v_cvt_pk_bf16_f32 v154,v156,v157
v_cvt_pk_bf16_f32 v155,v158,v159
v_mfma_f32_16x16x32_bf16 a[0:3],v[192:195],v[128:131],0
ds_read_b64_tr_b16 v[208:209],v250 offset:1024
v_cvt_pk_bf16_f32 v160,0,v160
v_mfma_f32_16x16x32_bf16 a[4:7],v[196:199],v[128:131],0
ds_read_b64_tr_b16 v[210:211],v250 offset:9216
v_fma_f32 v160,v160,s72,v244
v_mfma_f32_16x16x32_bf16 a[8:11],v[200:203],v[128:131],0
ds_read_b64_tr_b16 v[212:213],v251 offset:1024
v_cvt_pk_bf16_f32 v161,0,v161
v_mfma_f32_16x16x32_bf16 a[12:15],v[204:207],v[128:131],0
ds_read_b64_tr_b16 v[214:215],v251 offset:9216
v_fma_f32 v161,v161,s72,v244
v_mfma_f32_16x16x32_bf16 a[64:67],v[192:195],v[136:139],0
ds_read_b64_tr_b16 v[216:217],v250 offset:1536
v_cvt_pk_bf16_f32 v162,0,v162
v_mfma_f32_16x16x32_bf16 a[68:71],v[196:199],v[136:139],0
ds_read_b64_tr_b16 v[218:219],v250 offset:9728
v_fma_f32 v162,v162,s72,v244
v_mfma_f32_16x16x32_bf16 a[72:75],v[200:203],v[136:139],0
ds_read_b64_tr_b16 v[220:221],v251 offset:1536
v_cvt_pk_bf16_f32 v163,0,v163
v_mfma_f32_16x16x32_bf16 a[76:79],v[204:207],v[136:139],0
ds_read_b64_tr_b16 v[222:223],v251 offset:9728
v_fma_f32 v163,v163,s72,v244
v_mfma_f32_16x16x32_bf16 a[128:131],v[192:195],v[144:147],0
v_cvt_pk_bf16_f32 v168,0,v168
v_fma_f32 v168,v168,s72,v245
v_mfma_f32_16x16x32_bf16 a[132:135],v[196:199],v[144:147],0
buffer_load_dwordx4 v252,s[32:35],s93 offen offset:2048 lds
v_mfma_f32_16x16x32_bf16 a[136:139],v[200:203],v[144:147],0
v_cvt_pk_bf16_f32 v169,0,v169
v_fma_f32 v169,v169,s72,v245
v_mfma_f32_16x16x32_bf16 a[140:143],v[204:207],v[144:147],0
v_cvt_pk_bf16_f32 v170,0,v170
v_fma_f32 v170,v170,s72,v245
v_mfma_f32_16x16x32_bf16 a[192:195],v[192:195],v[152:155],0
v_cvt_pk_bf16_f32 v171,0,v171
v_fma_f32 v171,v171,s72,v245
v_mfma_f32_16x16x32_bf16 a[196:199],v[196:199],v[152:155],0
buffer_load_dwordx4 v252,s[36:39],s93 offen offset:3072 lds
v_mfma_f32_16x16x32_bf16 a[200:203],v[200:203],v[152:155],0
s_add_u32 s93,s93,16384
v_cvt_pk_bf16_f32 v176,0,v176
v_fma_f32 v176,v176,s72,v246
v_mfma_f32_16x16x32_bf16 a[204:207],v[204:207],v[152:155],0
v_cvt_pk_bf16_f32 v177,0,v177
v_fma_f32 v177,v177,s72,v246
v_mfma_f32_16x16x32_bf16 v[224:227],v[240:243],v[128:131],0
s_waitcnt lgkmcnt(0)
v_cvt_pk_bf16_f32 v178,0,v178
v_fma_f32 v178,v178,s72,v246
v_mfma_f32_16x16x32_bf16 a[16:19],v[208:211],v[128:131],0
ds_read_b64_tr_b16 v[192:193],v250 offset:2048
v_cvt_pk_bf16_f32 v179,0,v179
v_mfma_f32_16x16x32_bf16 a[80:83],v[208:211],v[136:139],0
ds_read_b64_tr_b16 v[194:195],v250 offset:10240
v_fma_f32 v179,v179,s72,v246
v_mfma_f32_16x16x32_bf16 a[144:147],v[208:211],v[144:147],0
ds_read_b64_tr_b16 v[196:197],v251 offset:2048
v_cvt_pk_bf16_f32 v184,0,v184
v_mfma_f32_16x16x32_bf16 a[208:211],v[208:211],v[152:155],0
ds_read_b64_tr_b16 v[198:199],v251 offset:10240
v_fma_f32 v184,v184,s72,v247
v_mfma_f32_16x16x32_bf16 a[20:23],v[212:215],v[128:131],0
ds_read_b64_tr_b16 v[200:201],v250 offset:2560
v_cvt_pk_bf16_f32 v185,0,v185
v_mfma_f32_16x16x32_bf16 a[84:87],v[212:215],v[136:139],0
s_add_u32 m0,s92,0xc000
ds_read_b64_tr_b16 v[202:203],v250 offset:10752
v_fma_f32 v185,v185,s72,v247
v_mfma_f32_16x16x32_bf16 a[148:151],v[212:215],v[144:147],0
ds_read_b64_tr_b16 v[204:205],v251 offset:2560
v_cvt_pk_bf16_f32 v186,0,v186
v_mfma_f32_16x16x32_bf16 a[212:215],v[212:215],v[152:155],0
ds_read_b64_tr_b16 v[206:207],v251 offset:10752
v_fma_f32 v186,v186,s72,v247
v_mfma_f32_16x16x32_bf16 a[24:27],v[216:219],v[128:131],0
v_cvt_pk_bf16_f32 v187,0,v187
v_fma_f32 v187,v187,s72,v247
v_mfma_f32_16x16x32_bf16 a[88:91],v[216:219],v[136:139],0
buffer_load_dwordx4 v252,s[24:27],s93 offen lds
v_mfma_f32_16x16x32_bf16 a[152:155],v[216:219],v[144:147],0
v_cvt_pk_bf16_f32 v164,0,v164
v_fma_f32 v164,v164,s72,v244
v_mfma_f32_16x16x32_bf16 a[216:219],v[216:219],v[152:155],0
v_cvt_pk_bf16_f32 v165,0,v165
v_fma_f32 v165,v165,s72,v244
v_mfma_f32_16x16x32_bf16 a[28:31],v[220:223],v[128:131],0
v_cvt_pk_bf16_f32 v166,0,v166
v_fma_f32 v166,v166,s72,v244
v_mfma_f32_16x16x32_bf16 a[92:95],v[220:223],v[136:139],0
buffer_load_dwordx4 v252,s[28:31],s93 offen offset:1024 lds
v_mfma_f32_16x16x32_bf16 a[156:159],v[220:223],v[144:147],0
v_cvt_pk_bf16_f32 v167,0,v167
v_fma_f32 v167,v167,s72,v244
v_mfma_f32_16x16x32_bf16 a[220:223],v[220:223],v[152:155],0
v_cvt_pk_bf16_f32 v172,0,v172
v_fma_f32 v172,v172,s72,v245
v_mfma_f32_16x16x32_bf16 v[228:231],v[240:243],v[136:139],0
s_waitcnt lgkmcnt(0)
v_cvt_pk_bf16_f32 v173,0,v173
v_fma_f32 v173,v173,s72,v245
v_mfma_f32_16x16x32_bf16 a[32:35],v[192:195],v[128:131],0
ds_read_b64_tr_b16 v[208:209],v250 offset:3072
v_cvt_pk_bf16_f32 v174,0,v174
v_mfma_f32_16x16x32_bf16 a[96:99],v[192:195],v[136:139],0
ds_read_b64_tr_b16 v[210:211],v250 offset:11264
v_fma_f32 v174,v174,s72,v245
v_mfma_f32_16x16x32_bf16 a[160:163],v[192:195],v[144:147],0
ds_read_b64_tr_b16 v[212:213],v251 offset:3072
v_cvt_pk_bf16_f32 v175,0,v175
v_mfma_f32_16x16x32_bf16 a[224:227],v[192:195],v[152:155],0
ds_read_b64_tr_b16 v[214:215],v251 offset:11264
v_fma_f32 v175,v175,s72,v245
v_mfma_f32_16x16x32_bf16 a[36:39],v[196:199],v[128:131],0
ds_read_b64_tr_b16 v[216:217],v250 offset:3584
v_cvt_pk_bf16_f32 v180,0,v180
v_mfma_f32_16x16x32_bf16 a[100:103],v[196:199],v[136:139],0
ds_read_b64_tr_b16 v[218:219],v250 offset:11776
v_fma_f32 v180,v180,s72,v246
v_mfma_f32_16x16x32_bf16 a[164:167],v[196:199],v[144:147],0
ds_read_b64_tr_b16 v[220:221],v251 offset:3584
v_cvt_pk_bf16_f32 v181,0,v181
v_mfma_f32_16x16x32_bf16 a[228:231],v[196:199],v[152:155],0
ds_read_b64_tr_b16 v[222:223],v251 offset:11776
v_fma_f32 v181,v181,s72,v246
v_mfma_f32_16x16x32_bf16 a[40:43],v[200:203],v[128:131],0
v_cvt_pk_bf16_f32 v182,0,v182
v_fma_f32 v182,v182,s72,v246
v_mfma_f32_16x16x32_bf16 a[104:107],v[200:203],v[136:139],0
buffer_load_dwordx4 v252,s[32:35],s93 offen offset:2048 lds
v_mfma_f32_16x16x32_bf16 a[168:171],v[200:203],v[144:147],0
v_cvt_pk_bf16_f32 v183,0,v183
v_fma_f32 v183,v183,s72,v246
v_mfma_f32_16x16x32_bf16 a[232:235],v[200:203],v[152:155],0
v_cvt_pk_bf16_f32 v188,0,v188
v_fma_f32 v188,v188,s72,v247
v_mfma_f32_16x16x32_bf16 a[44:47],v[204:207],v[128:131],0
v_cvt_pk_bf16_f32 v189,0,v189
v_fma_f32 v189,v189,s72,v247
v_mfma_f32_16x16x32_bf16 a[108:111],v[204:207],v[136:139],0
buffer_load_dwordx4 v252,s[36:39],s93 offen offset:3072 lds
v_mfma_f32_16x16x32_bf16 a[172:175],v[204:207],v[144:147],0
s_add_u32 s93,s93,16384
v_cvt_pk_bf16_f32 v190,0,v190
v_fma_f32 v190,v190,s72,v247
v_mfma_f32_16x16x32_bf16 a[236:239],v[204:207],v[152:155],0
v_cvt_pk_bf16_f32 v191,0,v191
v_fma_f32 v191,v191,s72,v247
v_mfma_f32_16x16x32_bf16 v[232:235],v[240:243],v[144:147],0
s_waitcnt vmcnt(8) lgkmcnt(0)
v_mfma_f32_16x16x32_bf16 a[48:51],v[208:211],v[128:131],0
s_barrier
ds_read_b128 v[192:195],v248
v_mfma_f32_16x16x32_bf16 a[112:115],v[208:211],v[136:139],0
s_add_u32 s75,s75,1
v_mov_b32 v254,s75
v_mfma_f32_16x16x32_bf16 a[176:179],v[208:211],v[144:147],0
ds_read_b128 v[196:199],v249
v_mfma_f32_16x16x32_bf16 a[240:243],v[208:211],v[152:155],0
buffer_store_dword v254,off,s[88:91],s78 sc1
v_mfma_f32_16x16x32_bf16 a[52:55],v[212:215],v[128:131],0
ds_read_b128 v[200:203],v248 offset:1024
v_mfma_f32_16x16x32_bf16 a[116:119],v[212:215],v[136:139],0
s_add_u32 m0,s92,0x10000
buffer_store_dword v240,off,s[88:91],s78 offset:4 sc1
v_mfma_f32_16x16x32_bf16 a[180:183],v[212:215],v[144:147],0
ds_read_b128 v[204:207],v249 offset:1024
v_mfma_f32_16x16x32_bf16 a[244:247],v[212:215],v[152:155],0
v_mfma_f32_16x16x32_bf16 a[56:59],v[216:219],v[128:131],0
v_mfma_f32_16x16x32_bf16 a[120:123],v[216:219],v[136:139],0
)"
R"(buffer_load_dwordx4 v253,s[40:43],s94 offen lds
v_mfma_f32_16x16x32_bf16 a[184:187],v[216:219],v[144:147],0
v_mfma_f32_16x16x32_bf16 a[248:251],v[216:219],v[152:155],0
v_mfma_f32_16x16x32_bf16 a[60:63],v[220:223],v[128:131],0
v_mfma_f32_16x16x32_bf16 a[124:127],v[220:223],v[136:139],0
buffer_load_dwordx4 v253,s[44:47],s94 offen offset:1024 lds
v_mfma_f32_16x16x32_bf16 a[188:191],v[220:223],v[144:147],0
v_mfma_f32_16x16x32_bf16 a[252:255],v[220:223],v[152:155],0
v_mfma_f32_16x16x32_bf16 v[236:239],v[240:243],v[152:155],0
s_waitcnt lgkmcnt(0)
.p2align 5
s_nop 0
s_nop 0
Lloopb_%=:
v_mfma_f32_16x16x32_bf16 v[128:131],v[192:195],v[0:3],0
ds_read_b128 v[208:211],v248 offset:2048
v_mfma_f32_16x16x32_bf16 v[128:131],v[196:199],v[4:7],v[128:131]
v_exp_f32 v160,v160
v_mfma_f32_16x16x32_bf16 v[128:131],v[200:203],v[32:35],v[128:131]
ds_read_b128 v[212:215],v249 offset:2048
v_mfma_f32_16x16x32_bf16 v[128:131],v[204:207],v[36:39],v[128:131]
v_exp_f32 v161,v161
v_mfma_f32_16x16x32_bf16 v[136:139],v[192:195],v[8:11],0
ds_read_b128 v[216:219],v248 offset:3072
v_cvt_pk_bf16_f32 v160,v160,v161
v_mfma_f32_16x16x32_bf16 v[136:139],v[196:199],v[12:15],v[136:139]
v_exp_f32 v162,v162
v_mfma_f32_16x16x32_bf16 v[136:139],v[200:203],v[40:43],v[136:139]
ds_read_b128 v[220:223],v249 offset:3072
v_mfma_f32_16x16x32_bf16 v[136:139],v[204:207],v[44:47],v[136:139]
v_exp_f32 v163,v163
v_mfma_f32_16x16x32_bf16 v[144:147],v[192:195],v[16:19],0
v_exp_f32 v164,v164
v_mfma_f32_16x16x32_bf16 v[144:147],v[196:199],v[20:23],v[144:147]
buffer_load_dwordx4 v253,s[48:51],s94 offen offset:2048 lds
v_mfma_f32_16x16x32_bf16 v[144:147],v[200:203],v[48:51],v[144:147]
v_exp_f32 v165,v165
v_mfma_f32_16x16x32_bf16 v[144:147],v[204:207],v[52:55],v[144:147]
v_exp_f32 v166,v166
v_mfma_f32_16x16x32_bf16 v[152:155],v[192:195],v[24:27],0
buffer_load_dwordx4 v253,s[52:55],s94 offen offset:3072 lds
v_mfma_f32_16x16x32_bf16 v[152:155],v[196:199],v[28:31],v[152:155]
s_add_u32 s94,s94,16384
v_exp_f32 v167,v167
v_mfma_f32_16x16x32_bf16 v[152:155],v[200:203],v[56:59],v[152:155]
v_exp_f32 v168,v168
v_mfma_f32_16x16x32_bf16 v[152:155],v[204:207],v[60:63],v[152:155]
s_waitcnt lgkmcnt(0)
v_exp_f32 v169,v169
v_mfma_f32_16x16x32_bf16 v[128:131],v[208:211],v[64:67],v[128:131]
ds_read_b128 v[192:195],v248 offset:8192
v_cvt_pk_bf16_f32 v161,v162,v163
v_mfma_f32_16x16x32_bf16 v[128:131],v[212:215],v[68:71],v[128:131]
v_exp_f32 v170,v170
v_mfma_f32_16x16x32_bf16 v[128:131],v[216:219],v[96:99],v[128:131]
ds_read_b128 v[196:199],v249 offset:8192
v_cvt_pk_bf16_f32 v162,v164,v165
v_mfma_f32_16x16x32_bf16 v[128:131],v[220:223],v[100:103],v[128:131]
v_exp_f32 v171,v171
v_mfma_f32_16x16x32_bf16 v[136:139],v[208:211],v[72:75],v[136:139]
ds_read_b128 v[200:203],v248 offset:9216
v_cvt_pk_bf16_f32 v163,v166,v167
v_mfma_f32_16x16x32_bf16 v[136:139],v[212:215],v[76:79],v[136:139]
s_add_u32 m0,s92,0x14000
v_exp_f32 v172,v172
v_mfma_f32_16x16x32_bf16 v[136:139],v[216:219],v[104:107],v[136:139]
ds_read_b128 v[204:207],v249 offset:9216
v_cvt_pk_bf16_f32 v168,v168,v169
v_mfma_f32_16x16x32_bf16 v[136:139],v[220:223],v[108:111],v[136:139]
v_exp_f32 v173,v173
v_mfma_f32_16x16x32_bf16 v[144:147],v[208:211],v[80:83],v[144:147]
v_exp_f32 v174,v174
v_mfma_f32_16x16x32_bf16 v[144:147],v[212:215],v[84:87],v[144:147]
buffer_load_dwordx4 v253,s[40:43],s94 offen lds
v_mfma_f32_16x16x32_bf16 v[144:147],v[216:219],v[112:115],v[144:147]
v_exp_f32 v175,v175
v_mfma_f32_16x16x32_bf16 v[144:147],v[220:223],v[116:119],v[144:147]
v_exp_f32 v176,v176
v_mfma_f32_16x16x32_bf16 v[152:155],v[208:211],v[88:91],v[152:155]
buffer_load_dwordx4 v253,s[44:47],s94 offen offset:1024 lds
v_mfma_f32_16x16x32_bf16 v[152:155],v[212:215],v[92:95],v[152:155]
v_exp_f32 v177,v177
v_mfma_f32_16x16x32_bf16 v[152:155],v[216:219],v[120:123],v[152:155]
v_exp_f32 v178,v178
v_mfma_f32_16x16x32_bf16 v[152:155],v[220:223],v[124:127],v[152:155]
s_waitcnt lgkmcnt(0)
v_exp_f32 v179,v179
v_mfma_f32_16x16x32_bf16 v[132:135],v[192:195],v[0:3],0
ds_read_b128 v[208:211],v248 offset:10240
v_cvt_pk_bf16_f32 v169,v170,v171
v_mfma_f32_16x16x32_bf16 v[132:135],v[196:199],v[4:7],v[132:135]
v_exp_f32 v180,v180
v_mfma_f32_16x16x32_bf16 v[132:135],v[200:203],v[32:35],v[132:135]
ds_read_b128 v[212:215],v249 offset:10240
v_cvt_pk_bf16_f32 v170,v172,v173
v_mfma_f32_16x16x32_bf16 v[132:135],v[204:207],v[36:39],v[132:135]
v_exp_f32 v181,v181
v_mfma_f32_16x16x32_bf16 v[140:143],v[192:195],v[8:11],0
ds_read_b128 v[216:219],v248 offset:11264
v_cvt_pk_bf16_f32 v171,v174,v175
v_mfma_f32_16x16x32_bf16 v[140:143],v[196:199],v[12:15],v[140:143]
v_exp_f32 v182,v182
v_mfma_f32_16x16x32_bf16 v[140:143],v[200:203],v[40:43],v[140:143]
ds_read_b128 v[220:223],v249 offset:11264
v_cvt_pk_bf16_f32 v176,v176,v177
v_mfma_f32_16x16x32_bf16 v[140:143],v[204:207],v[44:47],v[140:143]
v_exp_f32 v183,v183
v_mfma_f32_16x16x32_bf16 v[148:151],v[192:195],v[16:19],0
v_exp_f32 v184,v184
v_mfma_f32_16x16x32_bf16 v[148:151],v[196:199],v[20:23],v[148:151]
buffer_load_dwordx4 v253,s[48:51],s94 offen offset:2048 lds
v_mfma_f32_16x16x32_bf16 v[148:151],v[200:203],v[48:51],v[148:151]
v_exp_f32 v185,v185
v_mfma_f32_16x16x32_bf16 v[148:151],v[204:207],v[52:55],v[148:151]
v_exp_f32 v186,v186
v_mfma_f32_16x16x32_bf16 v[156:159],v[192:195],v[24:27],0
buffer_load_dwordx4 v253,s[52:55],s94 offen offset:3072 lds
v_mfma_f32_16x16x32_bf16 v[156:159],v[196:199],v[28:31],v[156:159]
s_add_u32 s94,s94,16384
v_exp_f32 v187,v187
v_mfma_f32_16x16x32_bf16 v[156:159],v[200:203],v[56:59],v[156:159]
v_exp_f32 v188,v188
v_mfma_f32_16x16x32_bf16 v[156:159],v[204:207],v[60:63],v[156:159]
s_waitcnt vmcnt(16) lgkmcnt(0)
v_exp_f32 v189,v189
v_mfma_f32_16x16x32_bf16 v[132:135],v[208:211],v[64:67],v[132:135]
s_barrier
ds_read_b64_tr_b16 v[192:193],v250 offset:32768
v_cvt_pk_bf16_f32 v177,v178,v179
v_mfma_f32_16x16x32_bf16 v[132:135],v[212:215],v[68:71],v[132:135]
ds_read_b64_tr_b16 v[194:195],v250 offset:40960
v_cvt_pk_bf16_f32 v178,v180,v181
v_mfma_f32_16x16x32_bf16 v[132:135],v[216:219],v[96:99],v[132:135]
ds_read_b64_tr_b16 v[196:197],v251 offset:32768
v_cvt_pk_bf16_f32 v179,v182,v183
v_mfma_f32_16x16x32_bf16 v[132:135],v[220:223],v[100:103],v[132:135]
ds_read_b64_tr_b16 v[198:199],v251 offset:40960
v_cvt_pk_bf16_f32 v184,v184,v185
v_mfma_f32_16x16x32_bf16 v[140:143],v[208:211],v[72:75],v[140:143]
ds_read_b64_tr_b16 v[200:201],v250 offset:33280
v_cvt_pk_bf16_f32 v185,v186,v187
v_mfma_f32_16x16x32_bf16 v[140:143],v[212:215],v[76:79],v[140:143]
s_add_u32 m0,s92,0x0
ds_read_b64_tr_b16 v[202:203],v250 offset:41472
v_cvt_pk_bf16_f32 v186,v188,v189
v_mfma_f32_16x16x32_bf16 v[140:143],v[216:219],v[104:107],v[140:143]
ds_read_b64_tr_b16 v[204:205],v251 offset:33280
v_cvt_pk_bf16_f32 v128,0,v128
v_mfma_f32_16x16x32_bf16 v[140:143],v[220:223],v[108:111],v[140:143]
ds_read_b64_tr_b16 v[206:207],v251 offset:41472
v_fma_f32 v128,v128,s72,v244
v_mfma_f32_16x16x32_bf16 v[148:151],v[208:211],v[80:83],v[148:151]
v_exp_f32 v190,v190
v_mfma_f32_16x16x32_bf16 v[148:151],v[212:215],v[84:87],v[148:151]
buffer_load_dwordx4 v252,s[24:27],s93 offen lds
v_mfma_f32_16x16x32_bf16 v[148:151],v[216:219],v[112:115],v[148:151]
v_exp_f32 v191,v191
v_mfma_f32_16x16x32_bf16 v[148:151],v[220:223],v[116:119],v[148:151]
v_cvt_pk_bf16_f32 v187,v190,v191
v_cvt_pk_bf16_f32 v129,0,v129
v_mfma_f32_16x16x32_bf16 v[156:159],v[208:211],v[88:91],v[156:159]
buffer_load_dwordx4 v252,s[28:31],s93 offen offset:1024 lds
v_mfma_f32_16x16x32_bf16 v[156:159],v[212:215],v[92:95],v[156:159]
v_fma_f32 v129,v129,s72,v244
v_cvt_pk_bf16_f32 v130,0,v130
v_mfma_f32_16x16x32_bf16 v[156:159],v[216:219],v[120:123],v[156:159]
v_fma_f32 v130,v130,s72,v244
v_cvt_pk_bf16_f32 v131,0,v131
v_mfma_f32_16x16x32_bf16 v[156:159],v[220:223],v[124:127],v[156:159]
s_waitcnt lgkmcnt(0)
v_fma_f32 v131,v131,s72,v244
v_max3_f32 v164,v128,v129,v129
v_mfma_f32_16x16x32_bf16 a[0:3],v[192:195],v[160:163],a[0:3]
ds_read_b64_tr_b16 v[208:209],v250 offset:33792
v_max3_f32 v164,v130,v131,v164
v_mfma_f32_16x16x32_bf16 a[4:7],v[196:199],v[160:163],a[4:7]
ds_read_b64_tr_b16 v[210:211],v250 offset:41984
v_cvt_pk_bf16_f32 v136,0,v136
v_mfma_f32_16x16x32_bf16 a[8:11],v[200:203],v[160:163],a[8:11]
ds_read_b64_tr_b16 v[212:213],v251 offset:33792
v_fma_f32 v136,v136,s72,v245
v_mfma_f32_16x16x32_bf16 a[12:15],v[204:207],v[160:163],a[12:15]
ds_read_b64_tr_b16 v[214:215],v251 offset:41984
v_cvt_pk_bf16_f32 v137,0,v137
v_mfma_f32_16x16x32_bf16 a[64:67],v[192:195],v[168:171],a[64:67]
ds_read_b64_tr_b16 v[216:217],v250 offset:34304
v_fma_f32 v137,v137,s72,v245
v_mfma_f32_16x16x32_bf16 a[68:71],v[196:199],v[168:171],a[68:71]
ds_read_b64_tr_b16 v[218:219],v250 offset:42496
v_cvt_pk_bf16_f32 v138,0,v138
v_mfma_f32_16x16x32_bf16 a[72:75],v[200:203],v[168:171],a[72:75]
ds_read_b64_tr_b16 v[220:221],v251 offset:34304
v_fma_f32 v138,v138,s72,v245
v_mfma_f32_16x16x32_bf16 a[76:79],v[204:207],v[168:171],a[76:79]
ds_read_b64_tr_b16 v[222:223],v251 offset:42496
v_cvt_pk_bf16_f32 v139,0,v139
v_mfma_f32_16x16x32_bf16 a[128:131],v[192:195],v[176:179],a[128:131]
v_fma_f32 v139,v139,s72,v245
v_max3_f32 v164,v136,v137,v164
v_mfma_f32_16x16x32_bf16 a[132:135],v[196:199],v[176:179],a[132:135]
)"
R"(buffer_load_dwordx4 v252,s[32:35],s93 offen offset:2048 lds
v_mfma_f32_16x16x32_bf16 a[136:139],v[200:203],v[176:179],a[136:139]
v_max3_f32 v164,v138,v139,v164
v_cvt_pk_bf16_f32 v144,0,v144
v_mfma_f32_16x16x32_bf16 a[140:143],v[204:207],v[176:179],a[140:143]
v_fma_f32 v144,v144,s72,v246
v_cvt_pk_bf16_f32 v145,0,v145
v_mfma_f32_16x16x32_bf16 a[192:195],v[192:195],v[184:187],a[192:195]
v_fma_f32 v145,v145,s72,v246
v_cvt_pk_bf16_f32 v146,0,v146
v_mfma_f32_16x16x32_bf16 a[196:199],v[196:199],v[184:187],a[196:199]
buffer_load_dwordx4 v252,s[36:39],s93 offen offset:3072 lds
v_mfma_f32_16x16x32_bf16 a[200:203],v[200:203],v[184:187],a[200:203]
s_add_u32 s93,s93,16384
v_fma_f32 v146,v146,s72,v246
v_cvt_pk_bf16_f32 v147,0,v147
v_mfma_f32_16x16x32_bf16 a[204:207],v[204:207],v[184:187],a[204:207]
v_fma_f32 v147,v147,s72,v246
v_max3_f32 v164,v144,v145,v164
v_mfma_f32_16x16x32_bf16 v[224:227],v[240:243],v[160:163],v[224:227]
s_waitcnt lgkmcnt(0)
v_max3_f32 v164,v146,v147,v164
v_cvt_pk_bf16_f32 v152,0,v152
v_mfma_f32_16x16x32_bf16 a[16:19],v[208:211],v[160:163],a[16:19]
ds_read_b64_tr_b16 v[192:193],v250 offset:34816
v_fma_f32 v152,v152,s72,v247
v_mfma_f32_16x16x32_bf16 a[80:83],v[208:211],v[168:171],a[80:83]
ds_read_b64_tr_b16 v[194:195],v250 offset:43008
v_cvt_pk_bf16_f32 v153,0,v153
v_mfma_f32_16x16x32_bf16 a[144:147],v[208:211],v[176:179],a[144:147]
ds_read_b64_tr_b16 v[196:197],v251 offset:34816
v_fma_f32 v153,v153,s72,v247
v_mfma_f32_16x16x32_bf16 a[208:211],v[208:211],v[184:187],a[208:211]
ds_read_b64_tr_b16 v[198:199],v251 offset:43008
v_cvt_pk_bf16_f32 v154,0,v154
v_mfma_f32_16x16x32_bf16 a[20:23],v[212:215],v[160:163],a[20:23]
ds_read_b64_tr_b16 v[200:201],v250 offset:35328
v_fma_f32 v154,v154,s72,v247
v_mfma_f32_16x16x32_bf16 a[84:87],v[212:215],v[168:171],a[84:87]
s_add_u32 m0,s92,0x4000
ds_read_b64_tr_b16 v[202:203],v250 offset:43520
v_cvt_pk_bf16_f32 v155,0,v155
v_mfma_f32_16x16x32_bf16 a[148:151],v[212:215],v[176:179],a[148:151]
ds_read_b64_tr_b16 v[204:205],v251 offset:35328
v_fma_f32 v155,v155,s72,v247
v_mfma_f32_16x16x32_bf16 a[212:215],v[212:215],v[184:187],a[212:215]
ds_read_b64_tr_b16 v[206:207],v251 offset:43520
v_max3_f32 v164,v152,v153,v164
v_mfma_f32_16x16x32_bf16 a[24:27],v[216:219],v[160:163],a[24:27]
v_max3_f32 v164,v154,v155,v164
v_cvt_pk_bf16_f32 v132,0,v132
v_mfma_f32_16x16x32_bf16 a[88:91],v[216:219],v[168:171],a[88:91]
buffer_load_dwordx4 v252,s[24:27],s93 offen lds
v_mfma_f32_16x16x32_bf16 a[152:155],v[216:219],v[176:179],a[152:155]
v_fma_f32 v132,v132,s72,v244
v_cvt_pk_bf16_f32 v133,0,v133
v_mfma_f32_16x16x32_bf16 a[216:219],v[216:219],v[184:187],a[216:219]
v_fma_f32 v133,v133,s72,v244
v_cvt_pk_bf16_f32 v134,0,v134
v_mfma_f32_16x16x32_bf16 a[28:31],v[220:223],v[160:163],a[28:31]
v_fma_f32 v134,v134,s72,v244
v_cvt_pk_bf16_f32 v135,0,v135
v_mfma_f32_16x16x32_bf16 a[92:95],v[220:223],v[168:171],a[92:95]
buffer_load_dwordx4 v252,s[28:31],s93 offen offset:1024 lds
v_mfma_f32_16x16x32_bf16 a[156:159],v[220:223],v[176:179],a[156:159]
v_fma_f32 v135,v135,s72,v244
v_max3_f32 v164,v132,v133,v164
v_mfma_f32_16x16x32_bf16 a[220:223],v[220:223],v[184:187],a[220:223]
v_max3_f32 v164,v134,v135,v164
v_cvt_pk_bf16_f32 v140,0,v140
v_mfma_f32_16x16x32_bf16 v[228:231],v[240:243],v[168:171],v[228:231]
s_waitcnt lgkmcnt(0)
v_fma_f32 v140,v140,s72,v245
v_cvt_pk_bf16_f32 v141,0,v141
v_mfma_f32_16x16x32_bf16 a[32:35],v[192:195],v[160:163],a[32:35]
ds_read_b64_tr_b16 v[208:209],v250 offset:35840
v_fma_f32 v141,v141,s72,v245
v_mfma_f32_16x16x32_bf16 a[96:99],v[192:195],v[168:171],a[96:99]
ds_read_b64_tr_b16 v[210:211],v250 offset:44032
v_cvt_pk_bf16_f32 v142,0,v142
v_mfma_f32_16x16x32_bf16 a[160:163],v[192:195],v[176:179],a[160:163]
ds_read_b64_tr_b16 v[212:213],v251 offset:35840
v_fma_f32 v142,v142,s72,v245
v_mfma_f32_16x16x32_bf16 a[224:227],v[192:195],v[184:187],a[224:227]
ds_read_b64_tr_b16 v[214:215],v251 offset:44032
v_cvt_pk_bf16_f32 v143,0,v143
v_mfma_f32_16x16x32_bf16 a[36:39],v[196:199],v[160:163],a[36:39]
ds_read_b64_tr_b16 v[216:217],v250 offset:36352
v_fma_f32 v143,v143,s72,v245
v_mfma_f32_16x16x32_bf16 a[100:103],v[196:199],v[168:171],a[100:103]
ds_read_b64_tr_b16 v[218:219],v250 offset:44544
v_max3_f32 v164,v140,v141,v164
v_mfma_f32_16x16x32_bf16 a[164:167],v[196:199],v[176:179],a[164:167]
ds_read_b64_tr_b16 v[220:221],v251 offset:36352
v_max3_f32 v164,v142,v143,v164
v_mfma_f32_16x16x32_bf16 a[228:231],v[196:199],v[184:187],a[228:231]
ds_read_b64_tr_b16 v[222:223],v251 offset:44544
v_cvt_pk_bf16_f32 v148,0,v148
v_mfma_f32_16x16x32_bf16 a[40:43],v[200:203],v[160:163],a[40:43]
v_fma_f32 v148,v148,s72,v246
v_cvt_pk_bf16_f32 v149,0,v149
v_mfma_f32_16x16x32_bf16 a[104:107],v[200:203],v[168:171],a[104:107]
buffer_load_dwordx4 v252,s[32:35],s93 offen offset:2048 lds
v_mfma_f32_16x16x32_bf16 a[168:171],v[200:203],v[176:179],a[168:171]
v_fma_f32 v149,v149,s72,v246
v_cvt_pk_bf16_f32 v150,0,v150
v_mfma_f32_16x16x32_bf16 a[232:235],v[200:203],v[184:187],a[232:235]
v_fma_f32 v150,v150,s72,v246
v_cvt_pk_bf16_f32 v151,0,v151
v_mfma_f32_16x16x32_bf16 a[44:47],v[204:207],v[160:163],a[44:47]
v_fma_f32 v151,v151,s72,v246
v_max3_f32 v164,v148,v149,v164
v_mfma_f32_16x16x32_bf16 a[108:111],v[204:207],v[168:171],a[108:111]
buffer_load_dwordx4 v252,s[36:39],s93 offen offset:3072 lds
v_mfma_f32_16x16x32_bf16 a[172:175],v[204:207],v[176:179],a[172:175]
s_add_u32 s93,s93,16384
v_max3_f32 v164,v150,v151,v164
v_cvt_pk_bf16_f32 v156,0,v156
v_mfma_f32_16x16x32_bf16 a[236:239],v[204:207],v[184:187],a[236:239]
v_fma_f32 v156,v156,s72,v247
v_cvt_pk_bf16_f32 v157,0,v157
v_mfma_f32_16x16x32_bf16 v[232:235],v[240:243],v[176:179],v[232:235]
s_waitcnt vmcnt(16) lgkmcnt(0)
v_fma_f32 v157,v157,s72,v247
v_cvt_pk_bf16_f32 v158,0,v158
v_mfma_f32_16x16x32_bf16 a[48:51],v[208:211],v[160:163],a[48:51]
s_barrier
ds_read_b128 v[192:195],v248 offset:32768
v_fma_f32 v158,v158,s72,v247
v_mfma_f32_16x16x32_bf16 a[112:115],v[208:211],v[168:171],a[112:115]
v_cvt_pk_bf16_f32 v159,0,v159
v_fma_f32 v159,v159,s72,v247
v_mfma_f32_16x16x32_bf16 a[176:179],v[208:211],v[176:179],a[176:179]
ds_read_b128 v[196:199],v249 offset:32768
v_max3_f32 v164,v156,v157,v164
v_mfma_f32_16x16x32_bf16 a[240:243],v[208:211],v[184:187],a[240:243]
v_max3_f32 v164,v158,v159,v164
v_cmp_lt_f32 vcc,s73,v164
v_mfma_f32_16x16x32_bf16 a[52:55],v[212:215],v[160:163],a[52:55]
ds_read_b128 v[200:203],v248 offset:33792
v_mfma_f32_16x16x32_bf16 a[116:119],v[212:215],v[168:171],a[116:119]
s_add_u32 m0,s92,0x18000
v_mfma_f32_16x16x32_bf16 a[180:183],v[212:215],v[176:179],a[180:183]
ds_read_b128 v[204:207],v249 offset:33792
v_mfma_f32_16x16x32_bf16 a[244:247],v[212:215],v[184:187],a[244:247]
v_mfma_f32_16x16x32_bf16 a[56:59],v[216:219],v[160:163],a[56:59]
v_mfma_f32_16x16x32_bf16 a[120:123],v[216:219],v[168:171],a[120:123]
buffer_load_dwordx4 v253,s[40:43],s94 offen lds
v_mfma_f32_16x16x32_bf16 a[184:187],v[216:219],v[176:179],a[184:187]
v_mfma_f32_16x16x32_bf16 a[248:251],v[216:219],v[184:187],a[248:251]
v_mfma_f32_16x16x32_bf16 a[60:63],v[220:223],v[160:163],a[60:63]
v_mfma_f32_16x16x32_bf16 a[124:127],v[220:223],v[168:171],a[124:127]
buffer_load_dwordx4 v253,s[44:47],s94 offen offset:1024 lds
v_mfma_f32_16x16x32_bf16 a[188:191],v[220:223],v[176:179],a[188:191]
v_mfma_f32_16x16x32_bf16 a[252:255],v[220:223],v[184:187],a[252:255]
v_mfma_f32_16x16x32_bf16 v[236:239],v[240:243],v[184:187],v[236:239]
s_waitcnt lgkmcnt(0)
s_cbranch_vccnz Lstub0b_%=
Lback0b_%=:
v_mfma_f32_16x16x32_bf16 v[160:163],v[192:195],v[0:3],0
s_sub_u32 s74,s74,1
ds_read_b128 v[208:211],v248 offset:34816
v_mfma_f32_16x16x32_bf16 v[160:163],v[196:199],v[4:7],v[160:163]
v_exp_f32 v128,v128
v_mfma_f32_16x16x32_bf16 v[160:163],v[200:203],v[32:35],v[160:163]
ds_read_b128 v[212:215],v249 offset:34816
v_mfma_f32_16x16x32_bf16 v[160:163],v[204:207],v[36:39],v[160:163]
v_exp_f32 v129,v129
v_mfma_f32_16x16x32_bf16 v[168:171],v[192:195],v[8:11],0
ds_read_b128 v[216:219],v248 offset:35840
v_cvt_pk_bf16_f32 v128,v128,v129
v_mfma_f32_16x16x32_bf16 v[168:171],v[196:199],v[12:15],v[168:171]
v_exp_f32 v130,v130
v_mfma_f32_16x16x32_bf16 v[168:171],v[200:203],v[40:43],v[168:171]
ds_read_b128 v[220:223],v249 offset:35840
v_mfma_f32_16x16x32_bf16 v[168:171],v[204:207],v[44:47],v[168:171]
v_exp_f32 v131,v131
v_mfma_f32_16x16x32_bf16 v[176:179],v[192:195],v[16:19],0
v_exp_f32 v132,v132
v_mfma_f32_16x16x32_bf16 v[176:179],v[196:199],v[20:23],v[176:179]
buffer_load_dwordx4 v253,s[48:51],s94 offen offset:2048 lds
v_mfma_f32_16x16x32_bf16 v[176:179],v[200:203],v[48:51],v[176:179]
v_exp_f32 v133,v133
v_mfma_f32_16x16x32_bf16 v[176:179],v[204:207],v[52:55],v[176:179]
v_exp_f32 v134,v134
v_mfma_f32_16x16x32_bf16 v[184:187],v[192:195],v[24:27],0
buffer_load_dwordx4 v253,s[52:55],s94 offen offset:3072 lds
v_mfma_f32_16x16x32_bf16 v[184:187],v[196:199],v[28:31],v[184:187]
s_add_u32 s94,s94,16384
v_exp_f32 v135,v135
v_mfma_f32_16x16x32_bf16 v[184:187],v[200:203],v[56:59],v[184:187]
v_exp_f32 v136,v136
v_mfma_f32_16x16x32_bf16 v[184:187],v[204:207],v[60:63],v[184:187]
s_waitcnt lgkmcnt(0)
v_exp_f32 v137,v137
v_mfma_f32_16x16x32_bf16 v[160:163],v[208:211],v[64:67],v[160:163]
ds_read_b128 v[192:195],v248 offset:40960
v_cvt_pk_bf16_f32 v129,v130,v131
v_mfma_f32_16x16x32_bf16 v[160:163],v[212:215],v[68:71],v[160:163]
v_exp_f32 v138,v138
)"
R"(v_mfma_f32_16x16x32_bf16 v[160:163],v[216:219],v[96:99],v[160:163]
ds_read_b128 v[196:199],v249 offset:40960
v_cvt_pk_bf16_f32 v130,v132,v133
v_mfma_f32_16x16x32_bf16 v[160:163],v[220:223],v[100:103],v[160:163]
v_exp_f32 v139,v139
v_mfma_f32_16x16x32_bf16 v[168:171],v[208:211],v[72:75],v[168:171]
ds_read_b128 v[200:203],v248 offset:41984
v_cvt_pk_bf16_f32 v131,v134,v135
v_mfma_f32_16x16x32_bf16 v[168:171],v[212:215],v[76:79],v[168:171]
s_add_u32 m0,s92,0x1c000
v_exp_f32 v140,v140
v_mfma_f32_16x16x32_bf16 v[168:171],v[216:219],v[104:107],v[168:171]
ds_read_b128 v[204:207],v249 offset:41984
v_cvt_pk_bf16_f32 v136,v136,v137
v_mfma_f32_16x16x32_bf16 v[168:171],v[220:223],v[108:111],v[168:171]
v_exp_f32 v141,v141
v_mfma_f32_16x16x32_bf16 v[176:179],v[208:211],v[80:83],v[176:179]
v_exp_f32 v142,v142
v_mfma_f32_16x16x32_bf16 v[176:179],v[212:215],v[84:87],v[176:179]
buffer_load_dwordx4 v253,s[40:43],s94 offen lds
v_mfma_f32_16x16x32_bf16 v[176:179],v[216:219],v[112:115],v[176:179]
v_exp_f32 v143,v143
v_mfma_f32_16x16x32_bf16 v[176:179],v[220:223],v[116:119],v[176:179]
v_exp_f32 v144,v144
v_mfma_f32_16x16x32_bf16 v[184:187],v[208:211],v[88:91],v[184:187]
buffer_load_dwordx4 v253,s[44:47],s94 offen offset:1024 lds
v_mfma_f32_16x16x32_bf16 v[184:187],v[212:215],v[92:95],v[184:187]
v_exp_f32 v145,v145
v_mfma_f32_16x16x32_bf16 v[184:187],v[216:219],v[120:123],v[184:187]
v_exp_f32 v146,v146
v_mfma_f32_16x16x32_bf16 v[184:187],v[220:223],v[124:127],v[184:187]
s_waitcnt lgkmcnt(0)
v_exp_f32 v147,v147
v_mfma_f32_16x16x32_bf16 v[164:167],v[192:195],v[0:3],0
ds_read_b128 v[208:211],v248 offset:43008
v_cvt_pk_bf16_f32 v137,v138,v139
v_mfma_f32_16x16x32_bf16 v[164:167],v[196:199],v[4:7],v[164:167]
v_exp_f32 v148,v148
v_mfma_f32_16x16x32_bf16 v[164:167],v[200:203],v[32:35],v[164:167]
ds_read_b128 v[212:215],v249 offset:43008
v_cvt_pk_bf16_f32 v138,v140,v141
v_mfma_f32_16x16x32_bf16 v[164:167],v[204:207],v[36:39],v[164:167]
v_exp_f32 v149,v149
v_mfma_f32_16x16x32_bf16 v[172:175],v[192:195],v[8:11],0
ds_read_b128 v[216:219],v248 offset:44032
v_cvt_pk_bf16_f32 v139,v142,v143
v_mfma_f32_16x16x32_bf16 v[172:175],v[196:199],v[12:15],v[172:175]
v_exp_f32 v150,v150
v_mfma_f32_16x16x32_bf16 v[172:175],v[200:203],v[40:43],v[172:175]
ds_read_b128 v[220:223],v249 offset:44032
v_cvt_pk_bf16_f32 v144,v144,v145
v_mfma_f32_16x16x32_bf16 v[172:175],v[204:207],v[44:47],v[172:175]
v_exp_f32 v151,v151
v_mfma_f32_16x16x32_bf16 v[180:183],v[192:195],v[16:19],0
v_exp_f32 v152,v152
v_mfma_f32_16x16x32_bf16 v[180:183],v[196:199],v[20:23],v[180:183]
buffer_load_dwordx4 v253,s[48:51],s94 offen offset:2048 lds
v_mfma_f32_16x16x32_bf16 v[180:183],v[200:203],v[48:51],v[180:183]
v_exp_f32 v153,v153
v_mfma_f32_16x16x32_bf16 v[180:183],v[204:207],v[52:55],v[180:183]
v_exp_f32 v154,v154
v_mfma_f32_16x16x32_bf16 v[188:191],v[192:195],v[24:27],0
buffer_load_dwordx4 v253,s[52:55],s94 offen offset:3072 lds
v_mfma_f32_16x16x32_bf16 v[188:191],v[196:199],v[28:31],v[188:191]
s_add_u32 s94,s94,16384
v_exp_f32 v155,v155
v_mfma_f32_16x16x32_bf16 v[188:191],v[200:203],v[56:59],v[188:191]
v_exp_f32 v156,v156
v_mfma_f32_16x16x32_bf16 v[188:191],v[204:207],v[60:63],v[188:191]
s_waitcnt vmcnt(16) lgkmcnt(0)
v_exp_f32 v157,v157
v_mfma_f32_16x16x32_bf16 v[164:167],v[208:211],v[64:67],v[164:167]
s_barrier
ds_read_b64_tr_b16 v[192:193],v250
v_cvt_pk_bf16_f32 v145,v146,v147
v_mfma_f32_16x16x32_bf16 v[164:167],v[212:215],v[68:71],v[164:167]
ds_read_b64_tr_b16 v[194:195],v250 offset:8192
v_cvt_pk_bf16_f32 v146,v148,v149
v_mfma_f32_16x16x32_bf16 v[164:167],v[216:219],v[96:99],v[164:167]
ds_read_b64_tr_b16 v[196:197],v251
v_cvt_pk_bf16_f32 v147,v150,v151
v_mfma_f32_16x16x32_bf16 v[164:167],v[220:223],v[100:103],v[164:167]
ds_read_b64_tr_b16 v[198:199],v251 offset:8192
v_cvt_pk_bf16_f32 v152,v152,v153
v_mfma_f32_16x16x32_bf16 v[172:175],v[208:211],v[72:75],v[172:175]
ds_read_b64_tr_b16 v[200:201],v250 offset:512
v_cvt_pk_bf16_f32 v153,v154,v155
v_mfma_f32_16x16x32_bf16 v[172:175],v[212:215],v[76:79],v[172:175]
s_add_u32 m0,s92,0x8000
ds_read_b64_tr_b16 v[202:203],v250 offset:8704
v_cvt_pk_bf16_f32 v154,v156,v157
v_mfma_f32_16x16x32_bf16 v[172:175],v[216:219],v[104:107],v[172:175]
ds_read_b64_tr_b16 v[204:205],v251 offset:512
v_cvt_pk_bf16_f32 v160,0,v160
v_mfma_f32_16x16x32_bf16 v[172:175],v[220:223],v[108:111],v[172:175]
ds_read_b64_tr_b16 v[206:207],v251 offset:8704
v_fma_f32 v160,v160,s72,v244
v_mfma_f32_16x16x32_bf16 v[180:183],v[208:211],v[80:83],v[180:183]
v_exp_f32 v158,v158
v_mfma_f32_16x16x32_bf16 v[180:183],v[212:215],v[84:87],v[180:183]
buffer_load_dwordx4 v252,s[24:27],s93 offen lds
v_mfma_f32_16x16x32_bf16 v[180:183],v[216:219],v[112:115],v[180:183]
v_exp_f32 v159,v159
v_mfma_f32_16x16x32_bf16 v[180:183],v[220:223],v[116:119],v[180:183]
v_cvt_pk_bf16_f32 v155,v158,v159
v_cvt_pk_bf16_f32 v161,0,v161
v_mfma_f32_16x16x32_bf16 v[188:191],v[208:211],v[88:91],v[188:191]
buffer_load_dwordx4 v252,s[28:31],s93 offen offset:1024 lds
v_mfma_f32_16x16x32_bf16 v[188:191],v[212:215],v[92:95],v[188:191]
v_fma_f32 v161,v161,s72,v244
v_cvt_pk_bf16_f32 v162,0,v162
v_mfma_f32_16x16x32_bf16 v[188:191],v[216:219],v[120:123],v[188:191]
v_fma_f32 v162,v162,s72,v244
v_cvt_pk_bf16_f32 v163,0,v163
v_mfma_f32_16x16x32_bf16 v[188:191],v[220:223],v[124:127],v[188:191]
s_waitcnt lgkmcnt(0)
v_fma_f32 v163,v163,s72,v244
v_max3_f32 v132,v160,v161,v161
v_mfma_f32_16x16x32_bf16 a[0:3],v[192:195],v[128:131],a[0:3]
ds_read_b64_tr_b16 v[208:209],v250 offset:1024
v_max3_f32 v132,v162,v163,v132
v_mfma_f32_16x16x32_bf16 a[4:7],v[196:199],v[128:131],a[4:7]
ds_read_b64_tr_b16 v[210:211],v250 offset:9216
v_cvt_pk_bf16_f32 v168,0,v168
v_mfma_f32_16x16x32_bf16 a[8:11],v[200:203],v[128:131],a[8:11]
ds_read_b64_tr_b16 v[212:213],v251 offset:1024
v_fma_f32 v168,v168,s72,v245
v_mfma_f32_16x16x32_bf16 a[12:15],v[204:207],v[128:131],a[12:15]
ds_read_b64_tr_b16 v[214:215],v251 offset:9216
v_cvt_pk_bf16_f32 v169,0,v169
v_mfma_f32_16x16x32_bf16 a[64:67],v[192:195],v[136:139],a[64:67]
ds_read_b64_tr_b16 v[216:217],v250 offset:1536
v_fma_f32 v169,v169,s72,v245
v_mfma_f32_16x16x32_bf16 a[68:71],v[196:199],v[136:139],a[68:71]
ds_read_b64_tr_b16 v[218:219],v250 offset:9728
v_cvt_pk_bf16_f32 v170,0,v170
v_mfma_f32_16x16x32_bf16 a[72:75],v[200:203],v[136:139],a[72:75]
ds_read_b64_tr_b16 v[220:221],v251 offset:1536
v_fma_f32 v170,v170,s72,v245
v_mfma_f32_16x16x32_bf16 a[76:79],v[204:207],v[136:139],a[76:79]
ds_read_b64_tr_b16 v[222:223],v251 offset:9728
v_cvt_pk_bf16_f32 v171,0,v171
v_mfma_f32_16x16x32_bf16 a[128:131],v[192:195],v[144:147],a[128:131]
v_fma_f32 v171,v171,s72,v245
v_max3_f32 v132,v168,v169,v132
v_mfma_f32_16x16x32_bf16 a[132:135],v[196:199],v[144:147],a[132:135]
buffer_load_dwordx4 v252,s[32:35],s93 offen offset:2048 lds
v_mfma_f32_16x16x32_bf16 a[136:139],v[200:203],v[144:147],a[136:139]
v_max3_f32 v132,v170,v171,v132
v_cvt_pk_bf16_f32 v176,0,v176
v_mfma_f32_16x16x32_bf16 a[140:143],v[204:207],v[144:147],a[140:143]
v_fma_f32 v176,v176,s72,v246
v_cvt_pk_bf16_f32 v177,0,v177
v_mfma_f32_16x16x32_bf16 a[192:195],v[192:195],v[152:155],a[192:195]
v_fma_f32 v177,v177,s72,v246
v_cvt_pk_bf16_f32 v178,0,v178
v_mfma_f32_16x16x32_bf16 a[196:199],v[196:199],v[152:155],a[196:199]
buffer_load_dwordx4 v252,s[36:39],s93 offen offset:3072 lds
v_mfma_f32_16x16x32_bf16 a[200:203],v[200:203],v[152:155],a[200:203]
s_add_u32 s93,s93,16384
v_fma_f32 v178,v178,s72,v246
v_cvt_pk_bf16_f32 v179,0,v179
v_mfma_f32_16x16x32_bf16 a[204:207],v[204:207],v[152:155],a[204:207]
v_fma_f32 v179,v179,s72,v246
v_max3_f32 v132,v176,v177,v132
v_mfma_f32_16x16x32_bf16 v[224:227],v[240:243],v[128:131],v[224:227]
s_waitcnt lgkmcnt(0)
v_max3_f32 v132,v178,v179,v132
v_cvt_pk_bf16_f32 v184,0,v184
v_mfma_f32_16x16x32_bf16 a[16:19],v[208:211],v[128:131],a[16:19]
ds_read_b64_tr_b16 v[192:193],v250 offset:2048
v_fma_f32 v184,v184,s72,v247
v_mfma_f32_16x16x32_bf16 a[80:83],v[208:211],v[136:139],a[80:83]
ds_read_b64_tr_b16 v[194:195],v250 offset:10240
v_cvt_pk_bf16_f32 v185,0,v185
v_mfma_f32_16x16x32_bf16 a[144:147],v[208:211],v[144:147],a[144:147]
ds_read_b64_tr_b16 v[196:197],v251 offset:2048
v_fma_f32 v185,v185,s72,v247
v_mfma_f32_16x16x32_bf16 a[208:211],v[208:211],v[152:155],a[208:211]
ds_read_b64_tr_b16 v[198:199],v251 offset:10240
v_cvt_pk_bf16_f32 v186,0,v186
v_mfma_f32_16x16x32_bf16 a[20:23],v[212:215],v[128:131],a[20:23]
ds_read_b64_tr_b16 v[200:201],v250 offset:2560
v_fma_f32 v186,v186,s72,v247
v_mfma_f32_16x16x32_bf16 a[84:87],v[212:215],v[136:139],a[84:87]
s_add_u32 m0,s92,0xc000
ds_read_b64_tr_b16 v[202:203],v250 offset:10752
v_cvt_pk_bf16_f32 v187,0,v187
v_mfma_f32_16x16x32_bf16 a[148:151],v[212:215],v[144:147],a[148:151]
ds_read_b64_tr_b16 v[204:205],v251 offset:2560
v_fma_f32 v187,v187,s72,v247
v_mfma_f32_16x16x32_bf16 a[212:215],v[212:215],v[152:155],a[212:215]
ds_read_b64_tr_b16 v[206:207],v251 offset:10752
v_max3_f32 v132,v184,v185,v132
v_mfma_f32_16x16x32_bf16 a[24:27],v[216:219],v[128:131],a[24:27]
v_max3_f32 v132,v186,v187,v132
v_cvt_pk_bf16_f32 v164,0,v164
v_mfma_f32_16x16x32_bf16 a[88:91],v[216:219],v[136:139],a[88:91]
buffer_load_dwordx4 v252,s[24:27],s93 offen lds
v_mfma_f32_16x16x32_bf16 a[152:155],v[216:219],v[144:147],a[152:155]
)"
R"(v_fma_f32 v164,v164,s72,v244
v_cvt_pk_bf16_f32 v165,0,v165
v_mfma_f32_16x16x32_bf16 a[216:219],v[216:219],v[152:155],a[216:219]
v_fma_f32 v165,v165,s72,v244
v_cvt_pk_bf16_f32 v166,0,v166
v_mfma_f32_16x16x32_bf16 a[28:31],v[220:223],v[128:131],a[28:31]
v_fma_f32 v166,v166,s72,v244
v_cvt_pk_bf16_f32 v167,0,v167
v_mfma_f32_16x16x32_bf16 a[92:95],v[220:223],v[136:139],a[92:95]
buffer_load_dwordx4 v252,s[28:31],s93 offen offset:1024 lds
v_mfma_f32_16x16x32_bf16 a[156:159],v[220:223],v[144:147],a[156:159]
v_fma_f32 v167,v167,s72,v244
v_max3_f32 v132,v164,v165,v132
v_mfma_f32_16x16x32_bf16 a[220:223],v[220:223],v[152:155],a[220:223]
v_max3_f32 v132,v166,v167,v132
v_cvt_pk_bf16_f32 v172,0,v172
v_mfma_f32_16x16x32_bf16 v[228:231],v[240:243],v[136:139],v[228:231]
s_waitcnt lgkmcnt(0)
v_fma_f32 v172,v172,s72,v245
v_cvt_pk_bf16_f32 v173,0,v173
v_mfma_f32_16x16x32_bf16 a[32:35],v[192:195],v[128:131],a[32:35]
ds_read_b64_tr_b16 v[208:209],v250 offset:3072
v_fma_f32 v173,v173,s72,v245
v_mfma_f32_16x16x32_bf16 a[96:99],v[192:195],v[136:139],a[96:99]
ds_read_b64_tr_b16 v[210:211],v250 offset:11264
v_cvt_pk_bf16_f32 v174,0,v174
v_mfma_f32_16x16x32_bf16 a[160:163],v[192:195],v[144:147],a[160:163]
ds_read_b64_tr_b16 v[212:213],v251 offset:3072
v_fma_f32 v174,v174,s72,v245
v_mfma_f32_16x16x32_bf16 a[224:227],v[192:195],v[152:155],a[224:227]
ds_read_b64_tr_b16 v[214:215],v251 offset:11264
v_cvt_pk_bf16_f32 v175,0,v175
v_mfma_f32_16x16x32_bf16 a[36:39],v[196:199],v[128:131],a[36:39]
ds_read_b64_tr_b16 v[216:217],v250 offset:3584
v_fma_f32 v175,v175,s72,v245
v_mfma_f32_16x16x32_bf16 a[100:103],v[196:199],v[136:139],a[100:103]
ds_read_b64_tr_b16 v[218:219],v250 offset:11776
v_max3_f32 v132,v172,v173,v132
v_mfma_f32_16x16x32_bf16 a[164:167],v[196:199],v[144:147],a[164:167]
ds_read_b64_tr_b16 v[220:221],v251 offset:3584
v_max3_f32 v132,v174,v175,v132
v_mfma_f32_16x16x32_bf16 a[228:231],v[196:199],v[152:155],a[228:231]
ds_read_b64_tr_b16 v[222:223],v251 offset:11776
v_cvt_pk_bf16_f32 v180,0,v180
v_mfma_f32_16x16x32_bf16 a[40:43],v[200:203],v[128:131],a[40:43]
v_fma_f32 v180,v180,s72,v246
v_cvt_pk_bf16_f32 v181,0,v181
v_mfma_f32_16x16x32_bf16 a[104:107],v[200:203],v[136:139],a[104:107]
buffer_load_dwordx4 v252,s[32:35],s93 offen offset:2048 lds
v_mfma_f32_16x16x32_bf16 a[168:171],v[200:203],v[144:147],a[168:171]
v_fma_f32 v181,v181,s72,v246
v_cvt_pk_bf16_f32 v182,0,v182
v_mfma_f32_16x16x32_bf16 a[232:235],v[200:203],v[152:155],a[232:235]
v_fma_f32 v182,v182,s72,v246
v_cvt_pk_bf16_f32 v183,0,v183
v_mfma_f32_16x16x32_bf16 a[44:47],v[204:207],v[128:131],a[44:47]
v_fma_f32 v183,v183,s72,v246
v_max3_f32 v132,v180,v181,v132
v_mfma_f32_16x16x32_bf16 a[108:111],v[204:207],v[136:139],a[108:111]
buffer_load_dwordx4 v252,s[36:39],s93 offen offset:3072 lds
v_mfma_f32_16x16x32_bf16 a[172:175],v[204:207],v[144:147],a[172:175]
s_add_u32 s93,s93,16384
v_max3_f32 v132,v182,v183,v132
v_cvt_pk_bf16_f32 v188,0,v188
v_mfma_f32_16x16x32_bf16 a[236:239],v[204:207],v[152:155],a[236:239]
v_fma_f32 v188,v188,s72,v247
v_cvt_pk_bf16_f32 v189,0,v189
v_mfma_f32_16x16x32_bf16 v[232:235],v[240:243],v[144:147],v[232:235]
s_waitcnt vmcnt(16) lgkmcnt(0)
v_fma_f32 v189,v189,s72,v247
v_cvt_pk_bf16_f32 v190,0,v190
v_mfma_f32_16x16x32_bf16 a[48:51],v[208:211],v[128:131],a[48:51]
s_barrier
ds_read_b128 v[192:195],v248
v_fma_f32 v190,v190,s72,v247
v_mfma_f32_16x16x32_bf16 a[112:115],v[208:211],v[136:139],a[112:115]
v_cvt_pk_bf16_f32 v191,0,v191
v_fma_f32 v191,v191,s72,v247
v_mfma_f32_16x16x32_bf16 a[176:179],v[208:211],v[144:147],a[176:179]
ds_read_b128 v[196:199],v249
v_max3_f32 v132,v188,v189,v132
v_mfma_f32_16x16x32_bf16 a[240:243],v[208:211],v[152:155],a[240:243]
v_max3_f32 v132,v190,v191,v132
v_cmp_lt_f32 vcc,s73,v132
v_mfma_f32_16x16x32_bf16 a[52:55],v[212:215],v[128:131],a[52:55]
ds_read_b128 v[200:203],v248 offset:1024
v_mfma_f32_16x16x32_bf16 a[116:119],v[212:215],v[136:139],a[116:119]
s_add_u32 m0,s92,0x10000
v_mfma_f32_16x16x32_bf16 a[180:183],v[212:215],v[144:147],a[180:183]
ds_read_b128 v[204:207],v249 offset:1024
v_mfma_f32_16x16x32_bf16 a[244:247],v[212:215],v[152:155],a[244:247]
v_mfma_f32_16x16x32_bf16 a[56:59],v[216:219],v[128:131],a[56:59]
v_mfma_f32_16x16x32_bf16 a[120:123],v[216:219],v[136:139],a[120:123]
buffer_load_dwordx4 v253,s[40:43],s94 offen lds
v_mfma_f32_16x16x32_bf16 a[184:187],v[216:219],v[144:147],a[184:187]
v_mfma_f32_16x16x32_bf16 a[248:251],v[216:219],v[152:155],a[248:251]
v_mfma_f32_16x16x32_bf16 a[60:63],v[220:223],v[128:131],a[60:63]
v_mfma_f32_16x16x32_bf16 a[124:127],v[220:223],v[136:139],a[124:127]
buffer_load_dwordx4 v253,s[44:47],s94 offen offset:1024 lds
v_mfma_f32_16x16x32_bf16 a[188:191],v[220:223],v[144:147],a[188:191]
v_mfma_f32_16x16x32_bf16 a[252:255],v[220:223],v[152:155],a[252:255]
v_mfma_f32_16x16x32_bf16 v[236:239],v[240:243],v[152:155],v[236:239]
s_waitcnt lgkmcnt(0)
s_cbranch_vccnz Lstub1b_%=
Lback1b_%=:
s_cmp_gt_u32 s74,1
s_cbranch_scc1 Lloopb_%=
s_cmp_eq_u32 s74,1
s_cbranch_scc1 Lf1lb_%=
s_waitcnt vmcnt(16)
v_cmp_eq_u32 vcc,v254,v255
s_nop 1
s_cbranch_vccnz Lspin1b_%=
Lok1b_%=:
v_mfma_f32_16x16x32_bf16 v[128:131],v[192:195],v[0:3],0
s_and_b32 s99,%[w],1
ds_read_b128 v[208:211],v248 offset:2048
v_mbcnt_lo_u32_b32 v254,-1,0
v_mfma_f32_16x16x32_bf16 v[128:131],v[196:199],v[4:7],v[128:131]
s_mul_i32 s99,s99,28672
v_mbcnt_hi_u32_b32 v254,-1,v254
v_bfe_u32 v252,v254,2,3
v_mfma_f32_16x16x32_bf16 v[128:131],v[200:203],v[32:35],v[128:131]
s_lshr_b32 s101,%[w],1
ds_read_b128 v[212:215],v249 offset:2048
v_mul_u32_u24 v252,896,v252
v_mfma_f32_16x16x32_bf16 v[128:131],v[204:207],v[36:39],v[128:131]
s_lshl_b32 s101,s101,7
v_and_b32 v254,48,v254
v_lshl_add_u32 v252,v254,1,v252
v_mfma_f32_16x16x32_bf16 v[136:139],v[192:195],v[8:11],0
s_add_u32 s99,s99,s101
ds_read_b128 v[216:219],v248 offset:3072
v_mbcnt_lo_u32_b32 v254,-1,0
v_mfma_f32_16x16x32_bf16 v[136:139],v[196:199],v[12:15],v[136:139]
s_and_b32 s101,%[h],2
v_mbcnt_hi_u32_b32 v254,-1,v254
v_and_b32 v254,3,v254
v_mfma_f32_16x16x32_bf16 v[136:139],v[200:203],v[40:43],v[136:139]
s_mul_i32 s101,s101,192
ds_read_b128 v[220:223],v249 offset:3072
v_lshlrev_b32 v254,4,v254
v_mfma_f32_16x16x32_bf16 v[136:139],v[204:207],v[44:47],v[136:139]
s_add_u32 s99,s99,s101
v_xor_b32 v252,v252,v254
v_add_u32 v252,s99,v252
v_mfma_f32_16x16x32_bf16 v[144:147],v[192:195],v[16:19],0
s_add_u32 s75,s75,1
v_exp_f32 v160,v160
v_mfma_f32_16x16x32_bf16 v[144:147],v[196:199],v[20:23],v[144:147]
s_mul_i32 s86,%[tb],57344
buffer_load_dwordx4 v253,s[48:51],s94 offen offset:2048 lds
v_mfma_f32_16x16x32_bf16 v[144:147],v[200:203],v[48:51],v[144:147]
s_lshr_b32 s101,%[h],2
v_exp_f32 v161,v161
v_mfma_f32_16x16x32_bf16 v[144:147],v[204:207],v[52:55],v[144:147]
s_mul_i32 s101,s101,28672
v_exp_f32 v162,v162
v_mfma_f32_16x16x32_bf16 v[152:155],v[192:195],v[24:27],0
s_add_u32 s86,s86,s101
buffer_load_dwordx4 v253,s[52:55],s94 offen offset:3072 lds
v_mfma_f32_16x16x32_bf16 v[152:155],v[196:199],v[28:31],v[152:155]
s_add_u32 s94,s94,16384
v_exp_f32 v163,v163
v_mfma_f32_16x16x32_bf16 v[152:155],v[200:203],v[56:59],v[152:155]
s_add_u32 s86,s86,0x700000
v_exp_f32 v164,v164
v_mfma_f32_16x16x32_bf16 v[152:155],v[204:207],v[60:63],v[152:155]
s_waitcnt lgkmcnt(0)
v_exp_f32 v165,v165
v_mfma_f32_16x16x32_bf16 v[128:131],v[208:211],v[64:67],v[128:131]
ds_read_b128 v[192:195],v248 offset:8192
v_cvt_pk_bf16_f32 v160,v160,v161
v_mfma_f32_16x16x32_bf16 v[128:131],v[212:215],v[68:71],v[128:131]
v_exp_f32 v166,v166
v_mfma_f32_16x16x32_bf16 v[128:131],v[216:219],v[96:99],v[128:131]
ds_read_b128 v[196:199],v249 offset:8192
v_cvt_pk_bf16_f32 v161,v162,v163
v_mfma_f32_16x16x32_bf16 v[128:131],v[220:223],v[100:103],v[128:131]
v_exp_f32 v167,v167
v_mfma_f32_16x16x32_bf16 v[136:139],v[208:211],v[72:75],v[136:139]
ds_read_b128 v[200:203],v248 offset:9216
v_cvt_pk_bf16_f32 v162,v164,v165
v_mfma_f32_16x16x32_bf16 v[136:139],v[212:215],v[76:79],v[136:139]
s_add_u32 m0,s92,0x14000
v_exp_f32 v168,v168
v_mfma_f32_16x16x32_bf16 v[136:139],v[216:219],v[104:107],v[136:139]
ds_read_b128 v[204:207],v249 offset:9216
v_cvt_pk_bf16_f32 v163,v166,v167
v_mfma_f32_16x16x32_bf16 v[136:139],v[220:223],v[108:111],v[136:139]
v_exp_f32 v169,v169
v_mfma_f32_16x16x32_bf16 v[144:147],v[208:211],v[80:83],v[144:147]
v_exp_f32 v170,v170
v_mfma_f32_16x16x32_bf16 v[144:147],v[212:215],v[84:87],v[144:147]
buffer_load_dwordx4 v253,s[40:43],s94 offen lds
v_mfma_f32_16x16x32_bf16 v[144:147],v[216:219],v[112:115],v[144:147]
v_exp_f32 v171,v171
v_mfma_f32_16x16x32_bf16 v[144:147],v[220:223],v[116:119],v[144:147]
v_exp_f32 v172,v172
v_mfma_f32_16x16x32_bf16 v[152:155],v[208:211],v[88:91],v[152:155]
)"
R"(buffer_load_dwordx4 v253,s[44:47],s94 offen offset:1024 lds
v_mfma_f32_16x16x32_bf16 v[152:155],v[212:215],v[92:95],v[152:155]
v_exp_f32 v173,v173
v_mfma_f32_16x16x32_bf16 v[152:155],v[216:219],v[120:123],v[152:155]
v_exp_f32 v174,v174
v_mfma_f32_16x16x32_bf16 v[152:155],v[220:223],v[124:127],v[152:155]
s_waitcnt lgkmcnt(0)
v_exp_f32 v175,v175
v_mfma_f32_16x16x32_bf16 v[132:135],v[192:195],v[0:3],0
ds_read_b128 v[208:211],v248 offset:10240
v_cvt_pk_bf16_f32 v168,v168,v169
v_mfma_f32_16x16x32_bf16 v[132:135],v[196:199],v[4:7],v[132:135]
v_exp_f32 v176,v176
v_mfma_f32_16x16x32_bf16 v[132:135],v[200:203],v[32:35],v[132:135]
ds_read_b128 v[212:215],v249 offset:10240
v_cvt_pk_bf16_f32 v169,v170,v171
v_mfma_f32_16x16x32_bf16 v[132:135],v[204:207],v[36:39],v[132:135]
v_exp_f32 v177,v177
v_mfma_f32_16x16x32_bf16 v[140:143],v[192:195],v[8:11],0
ds_read_b128 v[216:219],v248 offset:11264
v_cvt_pk_bf16_f32 v170,v172,v173
v_mfma_f32_16x16x32_bf16 v[140:143],v[196:199],v[12:15],v[140:143]
v_exp_f32 v178,v178
v_mfma_f32_16x16x32_bf16 v[140:143],v[200:203],v[40:43],v[140:143]
ds_read_b128 v[220:223],v249 offset:11264
v_cvt_pk_bf16_f32 v171,v174,v175
v_mfma_f32_16x16x32_bf16 v[140:143],v[204:207],v[44:47],v[140:143]
v_exp_f32 v179,v179
v_mfma_f32_16x16x32_bf16 v[148:151],v[192:195],v[16:19],0
v_exp_f32 v180,v180
v_mfma_f32_16x16x32_bf16 v[148:151],v[196:199],v[20:23],v[148:151]
buffer_load_dwordx4 v253,s[48:51],s94 offen offset:2048 lds
v_mfma_f32_16x16x32_bf16 v[148:151],v[200:203],v[48:51],v[148:151]
v_exp_f32 v181,v181
v_mfma_f32_16x16x32_bf16 v[148:151],v[204:207],v[52:55],v[148:151]
v_exp_f32 v182,v182
v_mfma_f32_16x16x32_bf16 v[156:159],v[192:195],v[24:27],0
buffer_load_dwordx4 v253,s[52:55],s94 offen offset:3072 lds
v_mfma_f32_16x16x32_bf16 v[156:159],v[196:199],v[28:31],v[156:159]
s_add_u32 s94,s94,16384
v_exp_f32 v183,v183
v_mfma_f32_16x16x32_bf16 v[156:159],v[200:203],v[56:59],v[156:159]
v_exp_f32 v184,v184
v_mfma_f32_16x16x32_bf16 v[156:159],v[204:207],v[60:63],v[156:159]
s_waitcnt vmcnt(16) lgkmcnt(0)
v_exp_f32 v185,v185
v_mfma_f32_16x16x32_bf16 v[132:135],v[208:211],v[64:67],v[132:135]
s_barrier
ds_read_b64_tr_b16 v[192:193],v250 offset:32768
v_cvt_pk_bf16_f32 v176,v176,v177
v_mfma_f32_16x16x32_bf16 v[132:135],v[212:215],v[68:71],v[132:135]
ds_read_b64_tr_b16 v[194:195],v250 offset:40960
v_cvt_pk_bf16_f32 v177,v178,v179
v_mfma_f32_16x16x32_bf16 v[132:135],v[216:219],v[96:99],v[132:135]
ds_read_b64_tr_b16 v[196:197],v251 offset:32768
v_cvt_pk_bf16_f32 v178,v180,v181
v_mfma_f32_16x16x32_bf16 v[132:135],v[220:223],v[100:103],v[132:135]
ds_read_b64_tr_b16 v[198:199],v251 offset:40960
v_cvt_pk_bf16_f32 v179,v182,v183
v_mfma_f32_16x16x32_bf16 v[140:143],v[208:211],v[72:75],v[140:143]
ds_read_b64_tr_b16 v[200:201],v250 offset:33280
v_cvt_pk_bf16_f32 v184,v184,v185
v_mfma_f32_16x16x32_bf16 v[140:143],v[212:215],v[76:79],v[140:143]
s_add_u32 m0,s92,0x0
ds_read_b64_tr_b16 v[202:203],v250 offset:41472
v_mbcnt_lo_u32_b32 v255,-1,0
v_mfma_f32_16x16x32_bf16 v[140:143],v[216:219],v[104:107],v[140:143]
s_mov_b32 s95,0x0
ds_read_b64_tr_b16 v[204:205],v251 offset:33280
v_mbcnt_hi_u32_b32 v255,-1,v255
v_mfma_f32_16x16x32_bf16 v[140:143],v[220:223],v[108:111],v[140:143]
ds_read_b64_tr_b16 v[206:207],v251 offset:41472
v_and_b32 v255,7,v255
v_mfma_f32_16x16x32_bf16 v[148:151],v[208:211],v[80:83],v[148:151]
v_exp_f32 v186,v186
v_mfma_f32_16x16x32_bf16 v[148:151],v[212:215],v[84:87],v[148:151]
buffer_load_dwordx4 v252,s[56:59],s95 offen lds
v_mfma_f32_16x16x32_bf16 v[148:151],v[216:219],v[112:115],v[148:151]
v_exp_f32 v187,v187
v_mfma_f32_16x16x32_bf16 v[148:151],v[220:223],v[116:119],v[148:151]
v_exp_f32 v188,v188
v_mfma_f32_16x16x32_bf16 v[156:159],v[208:211],v[88:91],v[156:159]
buffer_load_dwordx4 v252,s[60:63],s95 offen offset:1024 lds
v_mfma_f32_16x16x32_bf16 v[156:159],v[212:215],v[92:95],v[156:159]
v_exp_f32 v189,v189
v_mfma_f32_16x16x32_bf16 v[156:159],v[216:219],v[120:123],v[156:159]
v_exp_f32 v190,v190
v_mfma_f32_16x16x32_bf16 v[156:159],v[220:223],v[124:127],v[156:159]
s_waitcnt lgkmcnt(0)
v_exp_f32 v191,v191
v_mfma_f32_16x16x32_bf16 a[0:3],v[192:195],v[160:163],a[0:3]
ds_read_b64_tr_b16 v[208:209],v250 offset:33792
v_cvt_pk_bf16_f32 v185,v186,v187
v_mfma_f32_16x16x32_bf16 a[4:7],v[196:199],v[160:163],a[4:7]
ds_read_b64_tr_b16 v[210:211],v250 offset:41984
v_cvt_pk_bf16_f32 v186,v188,v189
v_mfma_f32_16x16x32_bf16 a[8:11],v[200:203],v[160:163],a[8:11]
ds_read_b64_tr_b16 v[212:213],v251 offset:33792
v_cvt_pk_bf16_f32 v187,v190,v191
v_mfma_f32_16x16x32_bf16 a[12:15],v[204:207],v[160:163],a[12:15]
ds_read_b64_tr_b16 v[214:215],v251 offset:41984
v_lshlrev_b32 v255,7,v255
v_mfma_f32_16x16x32_bf16 a[64:67],v[192:195],v[168:171],a[64:67]
ds_read_b64_tr_b16 v[216:217],v250 offset:34304
v_mov_b32 v254,s75
v_mfma_f32_16x16x32_bf16 a[68:71],v[196:199],v[168:171],a[68:71]
ds_read_b64_tr_b16 v[218:219],v250 offset:42496
v_cvt_pk_bf16_f32 v128,0,v128
v_mfma_f32_16x16x32_bf16 a[72:75],v[200:203],v[168:171],a[72:75]
ds_read_b64_tr_b16 v[220:221],v251 offset:34304
v_fma_f32 v128,v128,s72,v244
v_mfma_f32_16x16x32_bf16 a[76:79],v[204:207],v[168:171],a[76:79]
ds_read_b64_tr_b16 v[222:223],v251 offset:42496
v_cvt_pk_bf16_f32 v129,0,v129
v_mfma_f32_16x16x32_bf16 a[128:131],v[192:195],v[176:179],a[128:131]
buffer_load_dword v255,v255,s[88:91],s97 offen offset:4 sc1
v_mfma_f32_16x16x32_bf16 a[132:135],v[196:199],v[176:179],a[132:135]
buffer_load_dwordx4 v252,s[64:67],s95 offen offset:2048 lds
v_mfma_f32_16x16x32_bf16 a[136:139],v[200:203],v[176:179],a[136:139]
buffer_store_dword v254,off,s[88:91],s78 sc1
v_mfma_f32_16x16x32_bf16 a[140:143],v[204:207],v[176:179],a[140:143]
v_fma_f32 v129,v129,s72,v244
v_cvt_pk_bf16_f32 v130,0,v130
v_mfma_f32_16x16x32_bf16 a[192:195],v[192:195],v[184:187],a[192:195]
v_fma_f32 v130,v130,s72,v244
v_cvt_pk_bf16_f32 v131,0,v131
v_mfma_f32_16x16x32_bf16 a[196:199],v[196:199],v[184:187],a[196:199]
buffer_load_dwordx4 v252,s[68:71],s95 offen offset:3072 lds
v_mfma_f32_16x16x32_bf16 a[200:203],v[200:203],v[184:187],a[200:203]
v_fma_f32 v131,v131,s72,v244
v_max3_f32 v164,v128,v129,v129
v_mfma_f32_16x16x32_bf16 a[204:207],v[204:207],v[184:187],a[204:207]
v_max3_f32 v164,v130,v131,v164
v_cvt_pk_bf16_f32 v136,0,v136
v_mfma_f32_16x16x32_bf16 v[224:227],v[240:243],v[160:163],v[224:227]
s_waitcnt lgkmcnt(0)
v_fma_f32 v136,v136,s72,v245
v_cvt_pk_bf16_f32 v137,0,v137
v_mfma_f32_16x16x32_bf16 a[16:19],v[208:211],v[160:163],a[16:19]
ds_read_b64_tr_b16 v[192:193],v250 offset:34816
v_fma_f32 v137,v137,s72,v245
v_mfma_f32_16x16x32_bf16 a[80:83],v[208:211],v[168:171],a[80:83]
ds_read_b64_tr_b16 v[194:195],v250 offset:43008
v_cvt_pk_bf16_f32 v138,0,v138
v_mfma_f32_16x16x32_bf16 a[144:147],v[208:211],v[176:179],a[144:147]
ds_read_b64_tr_b16 v[196:197],v251 offset:34816
v_fma_f32 v138,v138,s72,v245
v_mfma_f32_16x16x32_bf16 a[208:211],v[208:211],v[184:187],a[208:211]
ds_read_b64_tr_b16 v[198:199],v251 offset:43008
v_cvt_pk_bf16_f32 v139,0,v139
v_mfma_f32_16x16x32_bf16 a[20:23],v[212:215],v[160:163],a[20:23]
ds_read_b64_tr_b16 v[200:201],v250 offset:35328
v_fma_f32 v139,v139,s72,v245
v_mfma_f32_16x16x32_bf16 a[84:87],v[212:215],v[168:171],a[84:87]
s_add_u32 m0,s92,0x4000
ds_read_b64_tr_b16 v[202:203],v250 offset:43520
v_max3_f32 v164,v136,v137,v164
v_mfma_f32_16x16x32_bf16 a[148:151],v[212:215],v[176:179],a[148:151]
s_mov_b32 s95,0x100
ds_read_b64_tr_b16 v[204:205],v251 offset:35328
v_max3_f32 v164,v138,v139,v164
v_mfma_f32_16x16x32_bf16 a[212:215],v[212:215],v[184:187],a[212:215]
ds_read_b64_tr_b16 v[206:207],v251 offset:43520
v_cvt_pk_bf16_f32 v144,0,v144
v_mfma_f32_16x16x32_bf16 a[24:27],v[216:219],v[160:163],a[24:27]
v_fma_f32 v144,v144,s72,v246
v_cvt_pk_bf16_f32 v145,0,v145
v_mfma_f32_16x16x32_bf16 a[88:91],v[216:219],v[168:171],a[88:91]
buffer_load_dwordx4 v252,s[56:59],s95 offen lds
v_mfma_f32_16x16x32_bf16 a[152:155],v[216:219],v[176:179],a[152:155]
v_fma_f32 v145,v145,s72,v246
v_cvt_pk_bf16_f32 v146,0,v146
v_mfma_f32_16x16x32_bf16 a[216:219],v[216:219],v[184:187],a[216:219]
v_fma_f32 v146,v146,s72,v246
v_cvt_pk_bf16_f32 v147,0,v147
v_mfma_f32_16x16x32_bf16 a[28:31],v[220:223],v[160:163],a[28:31]
v_fma_f32 v147,v147,s72,v246
v_max3_f32 v164,v144,v145,v164
v_mfma_f32_16x16x32_bf16 a[92:95],v[220:223],v[168:171],a[92:95]
buffer_load_dwordx4 v252,s[60:63],s95 offen offset:1024 lds
v_mfma_f32_16x16x32_bf16 a[156:159],v[220:223],v[176:179],a[156:159]
v_max3_f32 v164,v146,v147,v164
v_cvt_pk_bf16_f32 v152,0,v152
v_mfma_f32_16x16x32_bf16 a[220:223],v[220:223],v[184:187],a[220:223]
v_fma_f32 v152,v152,s72,v247
v_cvt_pk_bf16_f32 v153,0,v153
v_mfma_f32_16x16x32_bf16 v[228:231],v[240:243],v[168:171],v[228:231]
s_waitcnt lgkmcnt(0)
v_fma_f32 v153,v153,s72,v247
v_cvt_pk_bf16_f32 v154,0,v154
v_mfma_f32_16x16x32_bf16 a[32:35],v[192:195],v[160:163],a[32:35]
ds_read_b64_tr_b16 v[208:209],v250 offset:35840
v_fma_f32 v154,v154,s72,v247
v_mfma_f32_16x16x32_bf16 a[96:99],v[192:195],v[168:171],a[96:99]
ds_read_b64_tr_b16 v[210:211],v250 offset:44032
v_cvt_pk_bf16_f32 v155,0,v155
v_mfma_f32_16x16x32_bf16 a[160:163],v[192:195],v[176:179],a[160:163]
ds_read_b64_tr_b16 v[212:213],v251 offset:35840
v_fma_f32 v155,v155,s72,v247
v_mfma_f32_16x16x32_bf16 a[224:227],v[192:195],v[184:187],a[224:227]
)"
R"(ds_read_b64_tr_b16 v[214:215],v251 offset:44032
v_max3_f32 v164,v152,v153,v164
v_mfma_f32_16x16x32_bf16 a[36:39],v[196:199],v[160:163],a[36:39]
ds_read_b64_tr_b16 v[216:217],v250 offset:36352
v_max3_f32 v164,v154,v155,v164
v_mfma_f32_16x16x32_bf16 a[100:103],v[196:199],v[168:171],a[100:103]
ds_read_b64_tr_b16 v[218:219],v250 offset:44544
v_cvt_pk_bf16_f32 v132,0,v132
v_mfma_f32_16x16x32_bf16 a[164:167],v[196:199],v[176:179],a[164:167]
ds_read_b64_tr_b16 v[220:221],v251 offset:36352
v_fma_f32 v132,v132,s72,v244
v_mfma_f32_16x16x32_bf16 a[228:231],v[196:199],v[184:187],a[228:231]
ds_read_b64_tr_b16 v[222:223],v251 offset:44544
v_cvt_pk_bf16_f32 v133,0,v133
v_mfma_f32_16x16x32_bf16 a[40:43],v[200:203],v[160:163],a[40:43]
v_fma_f32 v133,v133,s72,v244
v_cvt_pk_bf16_f32 v134,0,v134
v_mfma_f32_16x16x32_bf16 a[104:107],v[200:203],v[168:171],a[104:107]
buffer_load_dwordx4 v252,s[64:67],s95 offen offset:2048 lds
v_mfma_f32_16x16x32_bf16 a[168:171],v[200:203],v[176:179],a[168:171]
v_fma_f32 v134,v134,s72,v244
v_cvt_pk_bf16_f32 v135,0,v135
v_mfma_f32_16x16x32_bf16 a[232:235],v[200:203],v[184:187],a[232:235]
v_fma_f32 v135,v135,s72,v244
v_max3_f32 v164,v132,v133,v164
v_mfma_f32_16x16x32_bf16 a[44:47],v[204:207],v[160:163],a[44:47]
v_max3_f32 v164,v134,v135,v164
v_cvt_pk_bf16_f32 v140,0,v140
v_mfma_f32_16x16x32_bf16 a[108:111],v[204:207],v[168:171],a[108:111]
buffer_load_dwordx4 v252,s[68:71],s95 offen offset:3072 lds
v_mfma_f32_16x16x32_bf16 a[172:175],v[204:207],v[176:179],a[172:175]
v_fma_f32 v140,v140,s72,v245
v_cvt_pk_bf16_f32 v141,0,v141
v_mfma_f32_16x16x32_bf16 a[236:239],v[204:207],v[184:187],a[236:239]
v_fma_f32 v141,v141,s72,v245
v_cvt_pk_bf16_f32 v142,0,v142
v_mfma_f32_16x16x32_bf16 v[232:235],v[240:243],v[176:179],v[232:235]
s_waitcnt vmcnt(18) lgkmcnt(0)
v_fma_f32 v142,v142,s72,v245
v_cvt_pk_bf16_f32 v143,0,v143
v_mfma_f32_16x16x32_bf16 a[48:51],v[208:211],v[160:163],a[48:51]
s_barrier
ds_read_b128 v[192:195],v248 offset:32768
v_fma_f32 v143,v143,s72,v245
v_mfma_f32_16x16x32_bf16 a[112:115],v[208:211],v[168:171],a[112:115]
s_waitcnt vmcnt(7)
v_max3_f32 v164,v140,v141,v164
v_max3_f32 v164,v142,v143,v164
v_mfma_f32_16x16x32_bf16 a[176:179],v[208:211],v[176:179],a[176:179]
ds_read_b128 v[196:199],v249 offset:32768
v_cvt_pk_bf16_f32 v148,0,v148
v_mfma_f32_16x16x32_bf16 a[240:243],v[208:211],v[184:187],a[240:243]
v_fma_f32 v148,v148,s72,v246
v_cvt_pk_bf16_f32 v149,0,v149
v_mfma_f32_16x16x32_bf16 a[52:55],v[212:215],v[160:163],a[52:55]
ds_read_b128 v[200:203],v248 offset:33792
v_fma_f32 v149,v149,s72,v246
v_mfma_f32_16x16x32_bf16 a[116:119],v[212:215],v[168:171],a[116:119]
s_add_u32 m0,s92,0x18000
v_cvt_pk_bf16_f32 v150,0,v150
v_fma_f32 v150,v150,s72,v246
v_mfma_f32_16x16x32_bf16 a[180:183],v[212:215],v[176:179],a[180:183]
ds_read_b128 v[204:207],v249 offset:33792
v_cvt_pk_bf16_f32 v151,0,v151
v_mfma_f32_16x16x32_bf16 a[244:247],v[212:215],v[184:187],a[244:247]
v_fma_f32 v151,v151,s72,v246
v_max3_f32 v164,v148,v149,v164
v_mfma_f32_16x16x32_bf16 a[56:59],v[216:219],v[160:163],a[56:59]
v_max3_f32 v164,v150,v151,v164
v_cvt_pk_bf16_f32 v156,0,v156
v_mfma_f32_16x16x32_bf16 a[120:123],v[216:219],v[168:171],a[120:123]
buffer_load_dwordx4 v253,s[40:43],s94 offen lds
v_mfma_f32_16x16x32_bf16 a[184:187],v[216:219],v[176:179],a[184:187]
v_fma_f32 v156,v156,s72,v247
v_cvt_pk_bf16_f32 v157,0,v157
v_mfma_f32_16x16x32_bf16 a[248:251],v[216:219],v[184:187],a[248:251]
v_fma_f32 v157,v157,s72,v247
v_cvt_pk_bf16_f32 v158,0,v158
v_mfma_f32_16x16x32_bf16 a[60:63],v[220:223],v[160:163],a[60:63]
v_fma_f32 v158,v158,s72,v247
v_cvt_pk_bf16_f32 v159,0,v159
v_mfma_f32_16x16x32_bf16 a[124:127],v[220:223],v[168:171],a[124:127]
buffer_load_dwordx4 v253,s[44:47],s94 offen offset:1024 lds
v_mfma_f32_16x16x32_bf16 a[188:191],v[220:223],v[176:179],a[188:191]
v_fma_f32 v159,v159,s72,v247
v_max3_f32 v164,v156,v157,v164
v_mfma_f32_16x16x32_bf16 a[252:255],v[220:223],v[184:187],a[252:255]
v_max3_f32 v164,v158,v159,v164
v_cmp_lt_f32 vcc,s73,v164
v_mfma_f32_16x16x32_bf16 v[236:239],v[240:243],v[184:187],v[236:239]
s_waitcnt lgkmcnt(0)
v_cmp_ne_u32 s[84:85],0,v255
s_nop 0
s_lshr_b32 s85,s84,1
s_or_b32 s84,s84,s85
s_and_b32 s84,s84,85
s_cbranch_vccnz Lstub2b_%=
Lback2b_%=:
s_cmp_lg_u32 s84,85
s_cbranch_scc1 Lroleb_%=
Lok3b_%=:
s_getpc_b64 s[80:81]
Lpc1b_%=:
s_mov_b32 s82,Lendb_%=-Lpc1b_%=
s_mov_b32 s83,0x20000
v_mfma_f32_16x16x32_bf16 v[160:163],v[192:195],v[0:3],0
s_lshr_b32 s98,%[tb],2
ds_read_b128 v[208:211],v248 offset:34816
v_mbcnt_lo_u32_b32 v255,-1,0
v_mfma_f32_16x16x32_bf16 v[160:163],v[196:199],v[4:7],v[160:163]
s_lshl_b32 s98,s98,10
v_exp_f32 v128,v128
v_mfma_f32_16x16x32_bf16 v[160:163],v[200:203],v[32:35],v[160:163]
s_add_u32 s98,s98,0x2b20000
ds_read_b128 v[212:215],v249 offset:34816
v_mbcnt_hi_u32_b32 v255,-1,v255
v_mfma_f32_16x16x32_bf16 v[160:163],v[204:207],v[36:39],v[160:163]
s_lshl_b32 s100,%[w],15
v_exp_f32 v129,v129
v_mfma_f32_16x16x32_bf16 v[168:171],v[192:195],v[8:11],0
s_add_u32 s98,s98,s100
ds_read_b128 v[216:219],v248 offset:35840
v_cvt_pk_bf16_f32 v128,v128,v129
v_mfma_f32_16x16x32_bf16 v[168:171],v[196:199],v[12:15],v[168:171]
s_and_b32 s96,%[tb],124
v_exp_f32 v130,v130
v_mfma_f32_16x16x32_bf16 v[168:171],v[200:203],v[40:43],v[168:171]
s_add_u32 s96,s96,%[w]
ds_read_b128 v[220:223],v249 offset:35840
v_lshlrev_b32 v255,4,v255
v_mfma_f32_16x16x32_bf16 v[168:171],v[204:207],v[44:47],v[168:171]
s_lshl_b32 s96,s96,10
v_exp_f32 v131,v131
v_mfma_f32_16x16x32_bf16 v[176:179],v[192:195],v[16:19],0
s_lshr_b32 s96,s96,1
v_exp_f32 v132,v132
v_mfma_f32_16x16x32_bf16 v[176:179],v[196:199],v[20:23],v[176:179]
buffer_load_dwordx4 v253,s[48:51],s94 offen offset:2048 lds
v_mfma_f32_16x16x32_bf16 v[176:179],v[200:203],v[48:51],v[176:179]
v_exp_f32 v133,v133
v_mfma_f32_16x16x32_bf16 v[176:179],v[204:207],v[52:55],v[176:179]
v_exp_f32 v134,v134
v_mfma_f32_16x16x32_bf16 v[184:187],v[192:195],v[24:27],0
buffer_load_dwordx4 v253,s[52:55],s94 offen offset:3072 lds
v_mfma_f32_16x16x32_bf16 v[184:187],v[196:199],v[28:31],v[184:187]
s_add_u32 s94,s94,16384
v_exp_f32 v135,v135
v_mfma_f32_16x16x32_bf16 v[184:187],v[200:203],v[56:59],v[184:187]
v_exp_f32 v136,v136
v_mfma_f32_16x16x32_bf16 v[184:187],v[204:207],v[60:63],v[184:187]
s_waitcnt lgkmcnt(0)
v_exp_f32 v137,v137
v_mfma_f32_16x16x32_bf16 v[160:163],v[208:211],v[64:67],v[160:163]
ds_read_b128 v[192:195],v248 offset:40960
v_cvt_pk_bf16_f32 v129,v130,v131
v_mfma_f32_16x16x32_bf16 v[160:163],v[212:215],v[68:71],v[160:163]
v_exp_f32 v138,v138
v_mfma_f32_16x16x32_bf16 v[160:163],v[216:219],v[96:99],v[160:163]
ds_read_b128 v[196:199],v249 offset:40960
v_cvt_pk_bf16_f32 v130,v132,v133
v_mfma_f32_16x16x32_bf16 v[160:163],v[220:223],v[100:103],v[160:163]
v_exp_f32 v139,v139
v_mfma_f32_16x16x32_bf16 v[168:171],v[208:211],v[72:75],v[168:171]
ds_read_b128 v[200:203],v248 offset:41984
v_cvt_pk_bf16_f32 v131,v134,v135
v_mfma_f32_16x16x32_bf16 v[168:171],v[212:215],v[76:79],v[168:171]
s_add_u32 m0,s92,0x1c000
v_exp_f32 v140,v140
v_mfma_f32_16x16x32_bf16 v[168:171],v[216:219],v[104:107],v[168:171]
ds_read_b128 v[204:207],v249 offset:41984
v_cvt_pk_bf16_f32 v136,v136,v137
v_mfma_f32_16x16x32_bf16 v[168:171],v[220:223],v[108:111],v[168:171]
v_exp_f32 v141,v141
v_mfma_f32_16x16x32_bf16 v[176:179],v[208:211],v[80:83],v[176:179]
v_exp_f32 v142,v142
v_mfma_f32_16x16x32_bf16 v[176:179],v[212:215],v[84:87],v[176:179]
buffer_load_dwordx4 v253,s[40:43],s94 offen lds
v_mfma_f32_16x16x32_bf16 v[176:179],v[216:219],v[112:115],v[176:179]
v_exp_f32 v143,v143
v_mfma_f32_16x16x32_bf16 v[176:179],v[220:223],v[116:119],v[176:179]
v_exp_f32 v144,v144
v_mfma_f32_16x16x32_bf16 v[184:187],v[208:211],v[88:91],v[184:187]
buffer_load_dwordx4 v253,s[44:47],s94 offen offset:1024 lds
v_mfma_f32_16x16x32_bf16 v[184:187],v[212:215],v[92:95],v[184:187]
v_exp_f32 v145,v145
v_mfma_f32_16x16x32_bf16 v[184:187],v[216:219],v[120:123],v[184:187]
v_exp_f32 v146,v146
v_mfma_f32_16x16x32_bf16 v[184:187],v[220:223],v[124:127],v[184:187]
s_waitcnt lgkmcnt(0)
v_exp_f32 v147,v147
v_mfma_f32_16x16x32_bf16 v[164:167],v[192:195],v[0:3],0
ds_read_b128 v[208:211],v248 offset:43008
v_cvt_pk_bf16_f32 v137,v138,v139
v_mfma_f32_16x16x32_bf16 v[164:167],v[196:199],v[4:7],v[164:167]
v_exp_f32 v148,v148
v_mfma_f32_16x16x32_bf16 v[164:167],v[200:203],v[32:35],v[164:167]
ds_read_b128 v[212:215],v249 offset:43008
v_cvt_pk_bf16_f32 v138,v140,v141
v_mfma_f32_16x16x32_bf16 v[164:167],v[204:207],v[36:39],v[164:167]
v_exp_f32 v149,v149
)"
R"(v_mfma_f32_16x16x32_bf16 v[172:175],v[192:195],v[8:11],0
ds_read_b128 v[216:219],v248 offset:44032
v_cvt_pk_bf16_f32 v139,v142,v143
v_mfma_f32_16x16x32_bf16 v[172:175],v[196:199],v[12:15],v[172:175]
v_exp_f32 v150,v150
v_mfma_f32_16x16x32_bf16 v[172:175],v[200:203],v[40:43],v[172:175]
ds_read_b128 v[220:223],v249 offset:44032
v_cvt_pk_bf16_f32 v144,v144,v145
v_mfma_f32_16x16x32_bf16 v[172:175],v[204:207],v[44:47],v[172:175]
v_exp_f32 v151,v151
v_mfma_f32_16x16x32_bf16 v[180:183],v[192:195],v[16:19],0
v_exp_f32 v152,v152
v_mfma_f32_16x16x32_bf16 v[180:183],v[196:199],v[20:23],v[180:183]
buffer_load_dwordx4 v253,s[48:51],s94 offen offset:2048 lds
v_mfma_f32_16x16x32_bf16 v[180:183],v[200:203],v[48:51],v[180:183]
v_exp_f32 v153,v153
v_mfma_f32_16x16x32_bf16 v[180:183],v[204:207],v[52:55],v[180:183]
v_exp_f32 v154,v154
v_mfma_f32_16x16x32_bf16 v[188:191],v[192:195],v[24:27],0
buffer_load_dwordx4 v253,s[52:55],s94 offen offset:3072 lds
v_mfma_f32_16x16x32_bf16 v[188:191],v[196:199],v[28:31],v[188:191]
s_add_u32 s94,s94,16384
v_exp_f32 v155,v155
v_mfma_f32_16x16x32_bf16 v[188:191],v[200:203],v[56:59],v[188:191]
v_exp_f32 v156,v156
v_mfma_f32_16x16x32_bf16 v[188:191],v[204:207],v[60:63],v[188:191]
s_waitcnt vmcnt(18) lgkmcnt(0)
v_exp_f32 v157,v157
v_mfma_f32_16x16x32_bf16 v[164:167],v[208:211],v[64:67],v[164:167]
s_barrier
ds_read_b64_tr_b16 v[192:193],v250
v_cvt_pk_bf16_f32 v145,v146,v147
v_mfma_f32_16x16x32_bf16 v[164:167],v[212:215],v[68:71],v[164:167]
ds_read_b64_tr_b16 v[194:195],v250 offset:8192
v_cvt_pk_bf16_f32 v146,v148,v149
v_mfma_f32_16x16x32_bf16 v[164:167],v[216:219],v[96:99],v[164:167]
ds_read_b64_tr_b16 v[196:197],v251
v_cvt_pk_bf16_f32 v147,v150,v151
v_mfma_f32_16x16x32_bf16 v[164:167],v[220:223],v[100:103],v[164:167]
ds_read_b64_tr_b16 v[198:199],v251 offset:8192
v_cvt_pk_bf16_f32 v152,v152,v153
v_mfma_f32_16x16x32_bf16 v[172:175],v[208:211],v[72:75],v[172:175]
ds_read_b64_tr_b16 v[200:201],v250 offset:512
v_cvt_pk_bf16_f32 v153,v154,v155
v_mfma_f32_16x16x32_bf16 v[172:175],v[212:215],v[76:79],v[172:175]
s_add_u32 m0,s92,0x20000
ds_read_b64_tr_b16 v[202:203],v250 offset:8704
v_cvt_pk_bf16_f32 v154,v156,v157
v_mfma_f32_16x16x32_bf16 v[172:175],v[216:219],v[104:107],v[172:175]
s_mov_b32 s95,0xe000
ds_read_b64_tr_b16 v[204:205],v251 offset:512
v_cvt_pk_bf16_f32 v160,0,v160
v_mfma_f32_16x16x32_bf16 v[172:175],v[220:223],v[108:111],v[172:175]
ds_read_b64_tr_b16 v[206:207],v251 offset:8704
v_fma_f32 v160,v160,s72,v244
v_mfma_f32_16x16x32_bf16 v[180:183],v[208:211],v[80:83],v[180:183]
v_exp_f32 v158,v158
v_mfma_f32_16x16x32_bf16 v[180:183],v[212:215],v[84:87],v[180:183]
buffer_load_dwordx4 v252,s[56:59],s95 offen lds
v_mfma_f32_16x16x32_bf16 v[180:183],v[216:219],v[112:115],v[180:183]
v_exp_f32 v159,v159
v_mfma_f32_16x16x32_bf16 v[180:183],v[220:223],v[116:119],v[180:183]
buffer_load_dwordx4 v252,s[60:63],s95 offen offset:1024 lds
v_mfma_f32_16x16x32_bf16 v[188:191],v[208:211],v[88:91],v[188:191]
v_cvt_pk_bf16_f32 v155,v158,v159
v_cvt_pk_bf16_f32 v161,0,v161
v_mfma_f32_16x16x32_bf16 v[188:191],v[212:215],v[92:95],v[188:191]
buffer_load_dwordx4 v252,s[64:67],s95 offen offset:2048 lds
v_mfma_f32_16x16x32_bf16 v[188:191],v[216:219],v[120:123],v[188:191]
v_fma_f32 v161,v161,s72,v244
v_cvt_pk_bf16_f32 v162,0,v162
v_mfma_f32_16x16x32_bf16 v[188:191],v[220:223],v[124:127],v[188:191]
s_waitcnt lgkmcnt(0)
v_fma_f32 v162,v162,s72,v244
v_cvt_pk_bf16_f32 v163,0,v163
v_mfma_f32_16x16x32_bf16 a[0:3],v[192:195],v[128:131],a[0:3]
s_and_b32 s100,%[w],1
ds_read_b64_tr_b16 v[208:209],v250 offset:1024
v_mov_b32 v127,1
v_mfma_f32_16x16x32_bf16 a[4:7],v[196:199],v[128:131],a[4:7]
s_lshl_b32 s100,s100,5
s_mov_b64 exec,1
buffer_atomic_add v127,off,s[88:91],s98 offset:64 sc0
s_mov_b64 exec,-1
v_mfma_f32_16x16x32_bf16 a[8:11],v[200:203],v[128:131],a[8:11]
buffer_load_dwordx4 v252,s[68:71],s95 offen offset:3072 lds
v_mfma_f32_16x16x32_bf16 a[12:15],v[204:207],v[128:131],a[12:15]
ds_read_b64_tr_b16 v[210:211],v250 offset:9216
ds_read_b64_tr_b16 v[212:213],v251 offset:1024
v_mfma_f32_16x16x32_bf16 a[64:67],v[192:195],v[136:139],a[64:67]
buffer_load_dwordx4 v[64:67],v255,s[88:91],s86 offen sc1
v_mfma_f32_16x16x32_bf16 a[68:71],v[196:199],v[136:139],a[68:71]
ds_read_b64_tr_b16 v[214:215],v251 offset:9216
ds_read_b64_tr_b16 v[216:217],v250 offset:1536
v_mfma_f32_16x16x32_bf16 a[72:75],v[200:203],v[136:139],a[72:75]
ds_read_b64_tr_b16 v[218:219],v250 offset:9728
ds_read_b64_tr_b16 v[220:221],v251 offset:1536
v_mfma_f32_16x16x32_bf16 a[76:79],v[204:207],v[136:139],a[76:79]
buffer_load_dwordx4 v[68:71],v255,s[88:91],s86 offen offset:1024 sc1
v_mfma_f32_16x16x32_bf16 a[128:131],v[192:195],v[144:147],a[128:131]
ds_read_b64_tr_b16 v[222:223],v251 offset:9728
v_fma_f32 v163,v163,s72,v244
v_mfma_f32_16x16x32_bf16 a[132:135],v[196:199],v[144:147],a[132:135]
v_max3_f32 v132,v160,v161,v161
v_max3_f32 v132,v162,v163,v132
v_mfma_f32_16x16x32_bf16 a[136:139],v[200:203],v[144:147],a[136:139]
buffer_load_dwordx4 v[72:75],v255,s[88:91],s86 offen offset:2048 sc1
v_mfma_f32_16x16x32_bf16 a[140:143],v[204:207],v[144:147],a[140:143]
v_cvt_pk_bf16_f32 v168,0,v168
v_fma_f32 v168,v168,s72,v245
v_mfma_f32_16x16x32_bf16 a[192:195],v[192:195],v[152:155],a[192:195]
v_cvt_pk_bf16_f32 v169,0,v169
v_fma_f32 v169,v169,s72,v245
v_mfma_f32_16x16x32_bf16 a[196:199],v[196:199],v[152:155],a[196:199]
buffer_load_dwordx4 v[76:79],v255,s[88:91],s86 offen offset:3072 sc1
v_mfma_f32_16x16x32_bf16 a[200:203],v[200:203],v[152:155],a[200:203]
s_add_u32 s86,s86,4096
v_cvt_pk_bf16_f32 v170,0,v170
v_fma_f32 v170,v170,s72,v245
v_mfma_f32_16x16x32_bf16 a[204:207],v[204:207],v[152:155],a[204:207]
buffer_load_dwordx4 v[80:83],v255,s[88:91],s86 offen sc1
v_mfma_f32_16x16x32_bf16 v[224:227],v[240:243],v[128:131],v[224:227]
s_waitcnt lgkmcnt(0)
v_cvt_pk_bf16_f32 v171,0,v171
v_fma_f32 v171,v171,s72,v245
v_mfma_f32_16x16x32_bf16 a[16:19],v[208:211],v[128:131],a[16:19]
ds_read_b64_tr_b16 v[192:193],v250 offset:2048
v_max3_f32 v132,v168,v169,v132
v_mfma_f32_16x16x32_bf16 a[80:83],v[208:211],v[136:139],a[80:83]
ds_read_b64_tr_b16 v[194:195],v250 offset:10240
v_max3_f32 v132,v170,v171,v132
v_mfma_f32_16x16x32_bf16 a[144:147],v[208:211],v[144:147],a[144:147]
buffer_load_dwordx4 v[84:87],v255,s[88:91],s86 offen offset:1024 sc1
v_mfma_f32_16x16x32_bf16 a[208:211],v[208:211],v[152:155],a[208:211]
ds_read_b64_tr_b16 v[196:197],v251 offset:2048
ds_read_b64_tr_b16 v[198:199],v251 offset:10240
v_mfma_f32_16x16x32_bf16 a[20:23],v[212:215],v[128:131],a[20:23]
ds_read_b64_tr_b16 v[200:201],v250 offset:2560
v_cvt_pk_bf16_f32 v176,0,v176
v_mfma_f32_16x16x32_bf16 a[84:87],v[212:215],v[136:139],a[84:87]
buffer_load_dwordx4 v[88:91],v255,s[88:91],s86 offen offset:2048 sc1
v_mfma_f32_16x16x32_bf16 a[148:151],v[212:215],v[144:147],a[148:151]
ds_read_b64_tr_b16 v[202:203],v250 offset:10752
ds_read_b64_tr_b16 v[204:205],v251 offset:2560
v_mfma_f32_16x16x32_bf16 a[212:215],v[212:215],v[152:155],a[212:215]
ds_read_b64_tr_b16 v[206:207],v251 offset:10752
v_fma_f32 v176,v176,s72,v246
v_mfma_f32_16x16x32_bf16 a[24:27],v[216:219],v[128:131],a[24:27]
s_add_u32 m0,s92,0x24000
buffer_load_dwordx4 v[92:95],v255,s[88:91],s86 offen offset:3072 sc1
v_mfma_f32_16x16x32_bf16 a[88:91],v[216:219],v[136:139],a[88:91]
s_mov_b32 s95,0xe100
v_cvt_pk_bf16_f32 v177,0,v177
v_fma_f32 v177,v177,s72,v246
v_mfma_f32_16x16x32_bf16 a[152:155],v[216:219],v[144:147],a[152:155]
v_cvt_pk_bf16_f32 v178,0,v178
v_fma_f32 v178,v178,s72,v246
v_mfma_f32_16x16x32_bf16 a[216:219],v[216:219],v[152:155],a[216:219]
v_cvt_pk_bf16_f32 v179,0,v179
v_fma_f32 v179,v179,s72,v246
v_mfma_f32_16x16x32_bf16 a[28:31],v[220:223],v[128:131],a[28:31]
buffer_load_dwordx4 v252,s[56:59],s95 offen lds
v_mfma_f32_16x16x32_bf16 a[92:95],v[220:223],v[136:139],a[92:95]
v_max3_f32 v132,v176,v177,v132
v_max3_f32 v132,v178,v179,v132
v_mfma_f32_16x16x32_bf16 a[156:159],v[220:223],v[144:147],a[156:159]
v_cvt_pk_bf16_f32 v184,0,v184
v_fma_f32 v184,v184,s72,v247
v_mfma_f32_16x16x32_bf16 a[220:223],v[220:223],v[152:155],a[220:223]
buffer_load_dwordx4 v252,s[60:63],s95 offen offset:1024 lds
v_mfma_f32_16x16x32_bf16 v[228:231],v[240:243],v[136:139],v[228:231]
s_waitcnt lgkmcnt(0)
v_cvt_pk_bf16_f32 v185,0,v185
v_fma_f32 v185,v185,s72,v247
v_mfma_f32_16x16x32_bf16 a[32:35],v[192:195],v[128:131],a[32:35]
ds_read_b64_tr_b16 v[208:209],v250 offset:3072
v_cvt_pk_bf16_f32 v186,0,v186
v_mfma_f32_16x16x32_bf16 a[96:99],v[192:195],v[136:139],a[96:99]
ds_read_b64_tr_b16 v[210:211],v250 offset:11264
v_fma_f32 v186,v186,s72,v247
v_mfma_f32_16x16x32_bf16 a[160:163],v[192:195],v[144:147],a[160:163]
buffer_load_dwordx4 v252,s[64:67],s95 offen offset:2048 lds
v_mfma_f32_16x16x32_bf16 a[224:227],v[192:195],v[152:155],a[224:227]
ds_read_b64_tr_b16 v[212:213],v251 offset:3072
ds_read_b64_tr_b16 v[214:215],v251 offset:11264
v_mfma_f32_16x16x32_bf16 a[36:39],v[196:199],v[128:131],a[36:39]
ds_read_b64_tr_b16 v[216:217],v250 offset:3584
v_cvt_pk_bf16_f32 v187,0,v187
v_mfma_f32_16x16x32_bf16 a[100:103],v[196:199],v[136:139],a[100:103]
buffer_load_dwordx4 v252,s[68:71],s95 offen offset:3072 lds
v_mfma_f32_16x16x32_bf16 a[164:167],v[196:199],v[144:147],a[164:167]
s_add_u32 m0,s92,0x8000
ds_read_b64_tr_b16 v[218:219],v250 offset:11776
ds_read_b64_tr_b16 v[220:221],v251 offset:3584
v_mfma_f32_16x16x32_bf16 a[228:231],v[196:199],v[152:155],a[228:231]
s_mov_b32 s95,0x1c000
ds_read_b64_tr_b16 v[222:223],v251 offset:11776
v_fma_f32 v187,v187,s72,v247
v_mfma_f32_16x16x32_bf16 a[40:43],v[200:203],v[128:131],a[40:43]
buffer_load_dwordx4 v252,s[56:59],s95 offen lds
)"
R"(v_mfma_f32_16x16x32_bf16 a[104:107],v[200:203],v[136:139],a[104:107]
v_max3_f32 v132,v184,v185,v132
v_max3_f32 v132,v186,v187,v132
v_mfma_f32_16x16x32_bf16 a[168:171],v[200:203],v[144:147],a[168:171]
v_cvt_pk_bf16_f32 v164,0,v164
v_fma_f32 v164,v164,s72,v244
v_mfma_f32_16x16x32_bf16 a[232:235],v[200:203],v[152:155],a[232:235]
v_cvt_pk_bf16_f32 v165,0,v165
v_fma_f32 v165,v165,s72,v244
v_mfma_f32_16x16x32_bf16 a[44:47],v[204:207],v[128:131],a[44:47]
buffer_load_dwordx4 v252,s[60:63],s95 offen offset:1024 lds
v_mfma_f32_16x16x32_bf16 a[108:111],v[204:207],v[136:139],a[108:111]
v_cvt_pk_bf16_f32 v166,0,v166
v_fma_f32 v166,v166,s72,v244
v_mfma_f32_16x16x32_bf16 a[172:175],v[204:207],v[144:147],a[172:175]
v_cvt_pk_bf16_f32 v167,0,v167
v_fma_f32 v167,v167,s72,v244
v_mfma_f32_16x16x32_bf16 a[236:239],v[204:207],v[152:155],a[236:239]
buffer_load_dwordx4 v252,s[64:67],s95 offen offset:2048 lds
v_mfma_f32_16x16x32_bf16 v[232:235],v[240:243],v[144:147],v[232:235]
s_waitcnt vmcnt(28) lgkmcnt(0)
v_max3_f32 v132,v164,v165,v132
v_max3_f32 v132,v166,v167,v132
v_mfma_f32_16x16x32_bf16 a[48:51],v[208:211],v[128:131],a[48:51]
s_barrier
v_cvt_pk_bf16_f32 v172,0,v172
v_fma_f32 v172,v172,s72,v245
v_mfma_f32_16x16x32_bf16 a[112:115],v[208:211],v[136:139],a[112:115]
v_cvt_pk_bf16_f32 v173,0,v173
v_fma_f32 v173,v173,s72,v245
v_mfma_f32_16x16x32_bf16 a[176:179],v[208:211],v[144:147],a[176:179]
buffer_load_dwordx4 v252,s[68:71],s95 offen offset:3072 lds
v_mfma_f32_16x16x32_bf16 a[240:243],v[208:211],v[152:155],a[240:243]
s_add_u32 m0,s92,0xc000
s_mov_b32 s95,0x1c100
v_cvt_pk_bf16_f32 v174,0,v174
v_mfma_f32_16x16x32_bf16 a[52:55],v[212:215],v[128:131],a[52:55]
v_fma_f32 v174,v174,s72,v245
v_cvt_pk_bf16_f32 v175,0,v175
v_mfma_f32_16x16x32_bf16 a[116:119],v[212:215],v[136:139],a[116:119]
buffer_load_dwordx4 v252,s[56:59],s95 offen lds
v_mfma_f32_16x16x32_bf16 a[180:183],v[212:215],v[144:147],a[180:183]
v_fma_f32 v175,v175,s72,v245
v_max3_f32 v132,v172,v173,v132
v_mfma_f32_16x16x32_bf16 a[244:247],v[212:215],v[152:155],a[244:247]
v_max3_f32 v132,v174,v175,v132
v_cvt_pk_bf16_f32 v180,0,v180
v_mfma_f32_16x16x32_bf16 a[56:59],v[216:219],v[128:131],a[56:59]
buffer_load_dwordx4 v252,s[60:63],s95 offen offset:1024 lds
v_mfma_f32_16x16x32_bf16 a[120:123],v[216:219],v[136:139],a[120:123]
v_fma_f32 v180,v180,s72,v246
v_cvt_pk_bf16_f32 v181,0,v181
v_mfma_f32_16x16x32_bf16 a[184:187],v[216:219],v[144:147],a[184:187]
v_fma_f32 v181,v181,s72,v246
v_cvt_pk_bf16_f32 v182,0,v182
v_mfma_f32_16x16x32_bf16 a[248:251],v[216:219],v[152:155],a[248:251]
v_fma_f32 v182,v182,s72,v246
v_cvt_pk_bf16_f32 v183,0,v183
v_mfma_f32_16x16x32_bf16 a[60:63],v[220:223],v[128:131],a[60:63]
buffer_load_dwordx4 v252,s[64:67],s95 offen offset:2048 lds
v_mfma_f32_16x16x32_bf16 a[124:127],v[220:223],v[136:139],a[124:127]
v_fma_f32 v183,v183,s72,v246
v_max3_f32 v132,v180,v181,v132
v_mfma_f32_16x16x32_bf16 a[188:191],v[220:223],v[144:147],a[188:191]
v_max3_f32 v132,v182,v183,v132
v_cvt_pk_bf16_f32 v188,0,v188
v_mfma_f32_16x16x32_bf16 a[252:255],v[220:223],v[152:155],a[252:255]
buffer_load_dwordx4 v252,s[68:71],s95 offen offset:3072 lds
v_mfma_f32_16x16x32_bf16 v[236:239],v[240:243],v[152:155],v[236:239]
v_fma_f32 v188,v188,s72,v247
v_cvt_pk_bf16_f32 v189,0,v189
v_fma_f32 v189,v189,s72,v247
v_cvt_pk_bf16_f32 v190,0,v190
v_fma_f32 v190,v190,s72,v247
v_cvt_pk_bf16_f32 v191,0,v191
v_fma_f32 v191,v191,s72,v247
v_max3_f32 v132,v188,v189,v132
v_max3_f32 v132,v190,v191,v132
v_cmp_lt_f32 vcc,s73,v132
v_mbcnt_lo_u32_b32 v248,-1,0
v_mbcnt_hi_u32_b32 v248,-1,v248
v_bfe_u32 v249,v248,2,3
v_lshlrev_b32 v120,6,v249
v_and_b32 v249,3,v248
v_lshl_add_u32 v120,v249,3,v120
v_bfe_u32 v249,v248,4,1
v_lshl_add_u32 v120,v249,5,v120
v_lshrrev_b32 v249,5,v248
v_lshl_add_u32 v120,v249,10,v120
v_xor_b32 v120,s100,v120
v_add_u32 v122,65536,v120
v_add_u32 v124,131072,v120
v_and_b32 v126,48,v255
v_lshl_add_u32 v126,v126,3,s96
buffer_load_dword v126,v126,s[80:83],0 offen
s_cbranch_vccnz Lstub3b_%=
Lback3b_%=:
ds_read_b64_tr_b16 v[192:193],v120
ds_read_b64_tr_b16 v[194:195],v120 offset:2048
ds_read_b64_tr_b16 v[196:197],v120 offset:512
ds_read_b64_tr_b16 v[198:199],v120 offset:2560
ds_read_b64_tr_b16 v[200:201],v120 offset:8192
ds_read_b64_tr_b16 v[202:203],v120 offset:10240
ds_read_b64_tr_b16 v[204:205],v120 offset:8704
ds_read_b64_tr_b16 v[206:207],v120 offset:10752
ds_read_b64_tr_b16 v[208:209],v120 offset:16384
ds_read_b64_tr_b16 v[210:211],v120 offset:18432
ds_read_b64_tr_b16 v[212:213],v120 offset:16896
ds_read_b64_tr_b16 v[214:215],v120 offset:18944
ds_read_b64_tr_b16 v[216:217],v120 offset:24576
ds_read_b64_tr_b16 v[218:219],v120 offset:26624
ds_read_b64_tr_b16 v[220:221],v120 offset:25088
ds_read_b64_tr_b16 v[222:223],v120 offset:27136
s_add_u32 s86,s86,4096
buffer_load_dwordx4 v[96:99],v255,s[88:91],s86 offen sc1
v_exp_f32 v160,v160
v_exp_f32 v161,v161
s_mov_b64 s[28:29],%[wo]
s_add_u32 s28,s28,0x1800
s_addc_u32 s29,s29,0
buffer_load_dwordx4 v[100:103],v255,s[88:91],s86 offen offset:1024 sc1
v_exp_f32 v162,v162
v_exp_f32 v163,v163
s_mov_b32 s30,0x6e800
s_mov_b32 s31,0x20000
s_mov_b64 s[32:33],%[wo]
buffer_load_dwordx4 v[104:107],v255,s[88:91],s86 offen offset:2048 sc1
v_exp_f32 v164,v164
v_exp_f32 v165,v165
s_add_u32 s32,s32,0x3000
s_addc_u32 s33,s33,0
s_mov_b32 s34,0x6d000
buffer_load_dwordx4 v[108:111],v255,s[88:91],s86 offen offset:3072 sc1
v_exp_f32 v166,v166
v_exp_f32 v167,v167
v_exp_f32 v168,v168
s_mov_b32 s35,0x20000
s_mov_b64 s[36:37],%[wo]
s_add_u32 s36,s36,0x4800
s_addc_u32 s37,s37,0
s_add_u32 s86,s86,4096
buffer_load_dwordx4 v[112:115],v255,s[88:91],s86 offen sc1
v_exp_f32 v169,v169
v_exp_f32 v170,v170
s_mov_b32 s38,0x6b800
s_mov_b32 s39,0x20000
s_mov_b64 s[24:25],%[wo]
buffer_load_dwordx4 v[116:119],v255,s[88:91],s86 offen offset:1024 sc1
v_exp_f32 v171,v171
v_exp_f32 v172,v172
s_mov_b32 s26,0x70000
s_mov_b32 s27,0x20000
s_mov_b64 s[40:41],s[88:89]
s_add_u32 m0,s92,0x10000
s_mov_b32 s95,0x2a000
buffer_load_dwordx4 v252,s[56:59],s95 offen lds
v_exp_f32 v173,v173
v_exp_f32 v174,v174
v_exp_f32 v175,v175
s_mov_b32 s43,0x20000
s_mov_b64 s[44:45],%[x]
s_mov_b32 s47,0x20000
buffer_load_dwordx4 v252,s[60:63],s95 offen offset:1024 lds
v_exp_f32 v176,v176
v_exp_f32 v177,v177
s_mov_b64 s[48:49],%[out]
s_mov_b32 s51,0x20000
s_waitcnt vmcnt(30)
buffer_load_dwordx4 v252,s[64:67],s95 offen offset:2048 lds
v_exp_f32 v178,v178
v_exp_f32 v179,v179
v_readfirstlane_b32 s53,v127
s_and_b32 s85,%[w],1
s_sub_u32 s53,6,s53
buffer_load_dwordx4 v252,s[68:71],s95 offen offset:3072 lds
v_exp_f32 v180,v180
v_exp_f32 v181,v181
s_and_b32 s53,s53,7
s_lshl_b32 s53,s53,1
s_add_u32 s53,s53,s85
s_add_u32 m0,s92,0x14000
s_mov_b32 s95,0x2a100
buffer_load_dwordx4 v252,s[56:59],s95 offen lds
v_exp_f32 v182,v182
v_exp_f32 v183,v183
v_exp_f32 v184,v184
s_cmp_lt_u32 s53,14
s_cselect_b32 s42,45350912,0
s_cselect_b32 s46,1835008,0
s_cselect_b32 s50,1835008,0
s_cselect_b32 s84,0,255
s_cselect_b32 s52,1,0
buffer_load_dwordx4 v252,s[60:63],s95 offen offset:1024 lds
v_exp_f32 v185,v185
v_exp_f32 v186,v186
s_lshr_b32 s81,s53,1
s_cmp_ge_u32 s81,0
s_addc_u32 s81,s81,1
buffer_load_dwordx4 v252,s[64:67],s95 offen offset:2048 lds
v_exp_f32 v187,v187
)"
R"(v_exp_f32 v188,v188
buffer_load_dwordx4 v252,s[68:71],s95 offen offset:3072 lds
v_exp_f32 v189,v189
v_exp_f32 v190,v190
v_exp_f32 v191,v191
s_waitcnt vmcnt(33)
v_swap_b32 v66,v68
v_swap_b32 v67,v69
s_waitcnt lgkmcnt(8)
s_nop 0
v_mfma_f32_16x16x32_bf16 v[0:3],v[192:195],v[64:67],0
s_lshr_b32 s83,%[tb],3
ds_read_b64_tr_b16 v[128:129],v120 offset:4096
ds_read_b64_tr_b16 v[130:131],v120 offset:6144
v_mfma_f32_16x16x32_bf16 v[4:7],v[192:195],v[68:71],0
s_mul_i32 s83,s83,458752
ds_read_b64_tr_b16 v[132:133],v120 offset:4608
v_cvt_pk_bf16_f32 v160,v160,v161
v_mfma_f32_16x16x32_bf16 v[8:11],v[196:199],v[64:67],0
s_and_b32 s98,%[tb],6
ds_read_b64_tr_b16 v[134:135],v120 offset:6656
v_cvt_pk_bf16_f32 v161,v162,v163
v_mfma_f32_16x16x32_bf16 v[12:15],v[196:199],v[68:71],0
s_waitcnt lgkmcnt(4)
ds_read_b64_tr_b16 v[136:137],v120 offset:12288
v_cvt_pk_bf16_f32 v162,v164,v165
v_mfma_f32_16x16x32_bf16 v[16:19],v[200:203],v[64:67],0
s_lshl_b32 s98,s98,9
ds_read_b64_tr_b16 v[138:139],v120 offset:14336
ds_read_b64_tr_b16 v[140:141],v120 offset:12800
v_mfma_f32_16x16x32_bf16 v[20:23],v[200:203],v[68:71],0
s_add_u32 s83,s83,s98
ds_read_b64_tr_b16 v[142:143],v120 offset:14848
v_cvt_pk_bf16_f32 v163,v166,v167
v_mfma_f32_16x16x32_bf16 v[24:27],v[204:207],v[64:67],0
s_waitcnt vmcnt(31)
ds_read_b64_tr_b16 v[144:145],v120 offset:20480
v_swap_b32 v74,v76
v_mfma_f32_16x16x32_bf16 v[28:31],v[204:207],v[68:71],0
s_lshr_b32 s54,%[h],2
ds_read_b64_tr_b16 v[146:147],v120 offset:22528
v_swap_b32 v75,v77
v_mfma_f32_16x16x32_bf16 v[32:35],v[208:211],v[64:67],0
s_mul_i32 s54,s54,114688
ds_read_b64_tr_b16 v[148:149],v120 offset:20992
ds_read_b64_tr_b16 v[150:151],v120 offset:23040
v_mfma_f32_16x16x32_bf16 v[36:39],v[208:211],v[68:71],0
s_add_u32 s54,s54,s83
ds_read_b64_tr_b16 v[152:153],v120 offset:28672
v_cvt_pk_bf16_f32 v168,v168,v169
v_mfma_f32_16x16x32_bf16 v[40:43],v[212:215],v[64:67],0
s_and_b32 s98,%[h],1
ds_read_b64_tr_b16 v[154:155],v120 offset:30720
v_cvt_pk_bf16_f32 v169,v170,v171
v_mfma_f32_16x16x32_bf16 v[44:47],v[212:215],v[68:71],0
s_lshl_b32 s98,s98,12
ds_read_b64_tr_b16 v[156:157],v120 offset:29184
v_cvt_pk_bf16_f32 v170,v172,v173
v_mfma_f32_16x16x32_bf16 v[48:51],v[216:219],v[64:67],0
s_add_u32 s54,s54,s98
ds_read_b64_tr_b16 v[158:159],v120 offset:31232
v_cvt_pk_bf16_f32 v171,v174,v175
v_mfma_f32_16x16x32_bf16 v[52:55],v[216:219],v[68:71],0
s_and_b32 s98,%[h],2
v_cvt_pk_bf16_f32 v176,v176,v177
v_cvt_pk_bf16_f32 v177,v178,v179
v_mfma_f32_16x16x32_bf16 v[56:59],v[220:223],v[64:67],0
s_mul_i32 s98,s98,24576
buffer_load_dwordx4 v[64:67],v255,s[88:91],s86 offen offset:2048 sc1
v_mfma_f32_16x16x32_bf16 v[60:63],v[220:223],v[68:71],0
s_waitcnt lgkmcnt(8)
buffer_load_dwordx4 v[68:71],v255,s[88:91],s86 offen offset:3072 sc1
v_mfma_f32_16x16x32_bf16 v[0:3],v[128:131],v[72:75],v[0:3]
s_add_u32 s86,s86,4096
v_cvt_pk_bf16_f32 v178,v180,v181
v_cvt_pk_bf16_f32 v179,v182,v183
v_mfma_f32_16x16x32_bf16 v[4:7],v[128:131],v[76:79],v[4:7]
s_add_u32 s54,s54,s98
v_cvt_pk_bf16_f32 v184,v184,v185
v_cvt_pk_bf16_f32 v185,v186,v187
v_mfma_f32_16x16x32_bf16 v[8:11],v[132:135],v[72:75],v[8:11]
s_add_u32 m0,s92,0x0
v_cvt_pk_bf16_f32 v186,v188,v189
v_cvt_pk_bf16_f32 v187,v190,v191
v_mfma_f32_16x16x32_bf16 v[12:15],v[132:135],v[76:79],v[12:15]
s_waitcnt vmcnt(25) lgkmcnt(0)
v_mfma_f32_16x16x32_bf16 v[16:19],v[136:139],v[72:75],v[16:19]
s_barrier
ds_read_b64_tr_b16 v[192:193],v124
ds_read_b64_tr_b16 v[194:195],v124 offset:2048
v_mfma_f32_16x16x32_bf16 v[20:23],v[136:139],v[76:79],v[20:23]
s_mov_b32 s95,0x38000
ds_read_b64_tr_b16 v[196:197],v124 offset:512
ds_read_b64_tr_b16 v[198:199],v124 offset:2560
v_mfma_f32_16x16x32_bf16 v[24:27],v[140:143],v[72:75],v[24:27]
s_waitcnt vmcnt(31)
buffer_load_dwordx4 v252,s[56:59],s95 offen lds
v_mfma_f32_16x16x32_bf16 v[28:31],v[140:143],v[76:79],v[28:31]
s_lshl_b32 s98,s53,1
ds_read_b64_tr_b16 v[200:201],v124 offset:8192
ds_read_b64_tr_b16 v[202:203],v124 offset:10240
v_mfma_f32_16x16x32_bf16 v[32:35],v[144:147],v[72:75],v[32:35]
s_lshl_b32 s82,s98,5
ds_read_b64_tr_b16 v[204:205],v124 offset:8704
ds_read_b64_tr_b16 v[206:207],v124 offset:10752
v_mfma_f32_16x16x32_bf16 v[36:39],v[144:147],v[76:79],v[36:39]
s_lshl_b32 s98,s98,12
buffer_load_dwordx4 v252,s[60:63],s95 offen offset:1024 lds
v_mfma_f32_16x16x32_bf16 v[40:43],v[148:151],v[72:75],v[40:43]
s_add_u32 s83,s83,s98
ds_read_b64_tr_b16 v[208:209],v124 offset:16384
ds_read_b64_tr_b16 v[210:211],v124 offset:18432
v_mfma_f32_16x16x32_bf16 v[44:47],v[148:151],v[76:79],v[44:47]
s_mul_i32 s98,%[tb],14336
ds_read_b64_tr_b16 v[212:213],v124 offset:16896
ds_read_b64_tr_b16 v[214:215],v124 offset:18944
v_mfma_f32_16x16x32_bf16 v[48:51],v[152:155],v[72:75],v[48:51]
s_add_u32 s82,s82,s98
buffer_load_dwordx4 v252,s[64:67],s95 offen offset:2048 lds
v_mfma_f32_16x16x32_bf16 v[52:55],v[152:155],v[76:79],v[52:55]
s_mov_b32 s55,%[tb]
ds_read_b64_tr_b16 v[216:217],v124 offset:24576
ds_read_b64_tr_b16 v[218:219],v124 offset:26624
v_mfma_f32_16x16x32_bf16 v[56:59],v[156:159],v[72:75],v[56:59]
s_mul_i32 s55,s55,14336
buffer_load_dwordx4 v252,s[68:71],s95 offen offset:3072 lds
v_swap_b32 v82,v84
v_swap_b32 v83,v85
v_mfma_f32_16x16x32_bf16 v[60:63],v[156:159],v[76:79],v[60:63]
s_waitcnt lgkmcnt(6)
ds_read_b64_tr_b16 v[220:221],v124 offset:25088
ds_read_b64_tr_b16 v[222:223],v124 offset:27136
v_mfma_f32_16x16x32_bf16 v[0:3],v[192:195],v[80:83],v[0:3]
s_add_u32 m0,s92,0x4000
ds_read_b64_tr_b16 v[128:129],v124 offset:4096
ds_read_b64_tr_b16 v[130:131],v124 offset:6144
v_mfma_f32_16x16x32_bf16 v[4:7],v[192:195],v[84:87],v[4:7]
s_mov_b32 s95,0x38100
ds_read_b64_tr_b16 v[132:133],v124 offset:4608
v_mfma_f32_16x16x32_bf16 v[8:11],v[196:199],v[80:83],v[8:11]
s_lshl_b32 s98,s53,11
buffer_load_dwordx4 v252,s[56:59],s95 offen lds
v_mfma_f32_16x16x32_bf16 v[12:15],v[196:199],v[84:87],v[12:15]
s_waitcnt lgkmcnt(3)
ds_read_b64_tr_b16 v[134:135],v124 offset:6656
ds_read_b64_tr_b16 v[136:137],v124 offset:12288
v_mfma_f32_16x16x32_bf16 v[16:19],v[200:203],v[80:83],v[16:19]
s_add_u32 s55,s55,s98
ds_read_b64_tr_b16 v[138:139],v124 offset:14336
ds_read_b64_tr_b16 v[140:141],v124 offset:12800
v_mfma_f32_16x16x32_bf16 v[20:23],v[200:203],v[84:87],v[20:23]
s_add_u32 s55,s55,0xe00000
buffer_load_dwordx4 v[72:75],v255,s[88:91],s86 offen sc1
v_mfma_f32_16x16x32_bf16 v[24:27],v[204:207],v[80:83],v[24:27]
s_waitcnt vmcnt(35)
buffer_load_dwordx4 v252,s[60:63],s95 offen offset:1024 lds
v_mfma_f32_16x16x32_bf16 v[28:31],v[204:207],v[84:87],v[28:31]
s_lshr_b32 s80,%[tb],2
buffer_load_dwordx4 v[76:79],v255,s[88:91],s86 offen offset:1024 sc1
v_mfma_f32_16x16x32_bf16 v[32:35],v[208:211],v[80:83],v[32:35]
s_lshl_b32 s80,s80,10
ds_read_b64_tr_b16 v[142:143],v124 offset:14848
ds_read_b64_tr_b16 v[144:145],v124 offset:20480
v_mfma_f32_16x16x32_bf16 v[36:39],v[208:211],v[84:87],v[36:39]
s_add_u32 s80,s80,0x2b20000
ds_read_b64_tr_b16 v[146:147],v124 offset:22528
ds_read_b64_tr_b16 v[148:149],v124 offset:20992
v_mfma_f32_16x16x32_bf16 v[40:43],v[212:215],v[80:83],v[40:43]
s_add_u32 s101,s54,0
buffer_load_dwordx4 v252,s[64:67],s95 offen offset:2048 lds
v_mfma_f32_16x16x32_bf16 v[44:47],v[212:215],v[84:87],v[44:47]
s_and_b32 s100,%[w],1
ds_read_b64_tr_b16 v[150:151],v124 offset:23040
ds_read_b64_tr_b16 v[152:153],v124 offset:28672
v_mfma_f32_16x16x32_bf16 v[48:51],v[216:219],v[80:83],v[48:51]
s_lshl_b32 s100,s100,14
ds_read_b64_tr_b16 v[154:155],v124 offset:30720
ds_read_b64_tr_b16 v[156:157],v124 offset:29184
v_mfma_f32_16x16x32_bf16 v[52:55],v[216:219],v[84:87],v[52:55]
s_lshr_b32 s96,%[h],1
ds_read_b64_tr_b16 v[158:159],v124 offset:31232
v_swap_b32 v90,v92
v_mfma_f32_16x16x32_bf16 v[56:59],v[220:223],v[80:83],v[56:59]
s_mul_i32 s96,s96,1835008
buffer_load_dwordx4 v252,s[68:71],s95 offen offset:3072 lds
v_swap_b32 v91,v93
v_mfma_f32_16x16x32_bf16 v[60:63],v[220:223],v[84:87],v[60:63]
s_waitcnt lgkmcnt(8)
buffer_load_dwordx4 v[80:83],v255,s[88:91],s86 offen offset:2048 sc1
v_mfma_f32_16x16x32_bf16 v[0:3],v[128:131],v[88:91],v[0:3]
buffer_load_dwordx4 v[84:87],v255,s[88:91],s86 offen offset:3072 sc1
v_mfma_f32_16x16x32_bf16 v[4:7],v[128:131],v[92:95],v[4:7]
s_add_u32 s86,s86,4096
v_mfma_f32_16x16x32_bf16 v[8:11],v[132:135],v[88:91],v[8:11]
s_add_u32 m0,s92,0x20000
v_mfma_f32_16x16x32_bf16 v[12:15],v[132:135],v[92:95],v[12:15]
s_waitcnt vmcnt(29) lgkmcnt(0)
v_mfma_f32_16x16x32_bf16 v[16:19],v[136:139],v[88:91],v[16:19]
s_barrier
ds_read_b64_tr_b16 v[192:193],v120 offset:32768
)"
R"(ds_read_b64_tr_b16 v[194:195],v120 offset:34816
v_mfma_f32_16x16x32_bf16 v[20:23],v[136:139],v[92:95],v[20:23]
s_mov_b32 s95,0x46000
ds_read_b64_tr_b16 v[196:197],v120 offset:33280
ds_read_b64_tr_b16 v[198:199],v120 offset:35328
v_mfma_f32_16x16x32_bf16 v[24:27],v[140:143],v[88:91],v[24:27]
s_waitcnt vmcnt(26)
buffer_load_dwordx4 v252,s[56:59],s95 offen lds
v_mfma_f32_16x16x32_bf16 v[28:31],v[140:143],v[92:95],v[28:31]
ds_read_b64_tr_b16 v[200:201],v120 offset:40960
ds_read_b64_tr_b16 v[202:203],v120 offset:43008
v_mfma_f32_16x16x32_bf16 v[32:35],v[144:147],v[88:91],v[32:35]
ds_read_b64_tr_b16 v[204:205],v120 offset:41472
ds_read_b64_tr_b16 v[206:207],v120 offset:43520
v_mfma_f32_16x16x32_bf16 v[36:39],v[144:147],v[92:95],v[36:39]
buffer_load_dwordx4 v252,s[60:63],s95 offen offset:1024 lds
v_mfma_f32_16x16x32_bf16 v[40:43],v[148:151],v[88:91],v[40:43]
ds_read_b64_tr_b16 v[208:209],v120 offset:49152
ds_read_b64_tr_b16 v[210:211],v120 offset:51200
v_mfma_f32_16x16x32_bf16 v[44:47],v[148:151],v[92:95],v[44:47]
ds_read_b64_tr_b16 v[212:213],v120 offset:49664
ds_read_b64_tr_b16 v[214:215],v120 offset:51712
v_mfma_f32_16x16x32_bf16 v[48:51],v[152:155],v[88:91],v[48:51]
buffer_load_dwordx4 v252,s[64:67],s95 offen offset:2048 lds
v_mfma_f32_16x16x32_bf16 v[52:55],v[152:155],v[92:95],v[52:55]
ds_read_b64_tr_b16 v[216:217],v120 offset:57344
ds_read_b64_tr_b16 v[218:219],v120 offset:59392
v_mfma_f32_16x16x32_bf16 v[56:59],v[156:159],v[88:91],v[56:59]
buffer_load_dwordx4 v252,s[68:71],s95 offen offset:3072 lds
v_swap_b32 v98,v100
v_swap_b32 v99,v101
v_mfma_f32_16x16x32_bf16 v[60:63],v[156:159],v[92:95],v[60:63]
s_waitcnt lgkmcnt(6)
ds_read_b64_tr_b16 v[220:221],v120 offset:57856
ds_read_b64_tr_b16 v[222:223],v120 offset:59904
v_mfma_f32_16x16x32_bf16 v[0:3],v[192:195],v[96:99],v[0:3]
s_add_u32 m0,s92,0x24000
ds_read_b64_tr_b16 v[128:129],v120 offset:36864
ds_read_b64_tr_b16 v[130:131],v120 offset:38912
v_mfma_f32_16x16x32_bf16 v[4:7],v[192:195],v[100:103],v[4:7]
s_mov_b32 s95,0x46100
ds_read_b64_tr_b16 v[132:133],v120 offset:37376
v_mfma_f32_16x16x32_bf16 v[8:11],v[196:199],v[96:99],v[8:11]
buffer_load_dwordx4 v252,s[56:59],s95 offen lds
v_mfma_f32_16x16x32_bf16 v[12:15],v[196:199],v[100:103],v[12:15]
s_waitcnt lgkmcnt(3)
ds_read_b64_tr_b16 v[134:135],v120 offset:39424
ds_read_b64_tr_b16 v[136:137],v120 offset:45056
v_mfma_f32_16x16x32_bf16 v[16:19],v[200:203],v[96:99],v[16:19]
ds_read_b64_tr_b16 v[138:139],v120 offset:47104
ds_read_b64_tr_b16 v[140:141],v120 offset:45568
v_mfma_f32_16x16x32_bf16 v[20:23],v[200:203],v[100:103],v[20:23]
buffer_load_dwordx4 v[88:91],v255,s[88:91],s86 offen sc1
v_mfma_f32_16x16x32_bf16 v[24:27],v[204:207],v[96:99],v[24:27]
s_waitcnt vmcnt(30)
buffer_load_dwordx4 v252,s[60:63],s95 offen offset:1024 lds
v_mfma_f32_16x16x32_bf16 v[28:31],v[204:207],v[100:103],v[28:31]
buffer_load_dwordx4 v[92:95],v255,s[88:91],s86 offen offset:1024 sc1
v_mfma_f32_16x16x32_bf16 v[32:35],v[208:211],v[96:99],v[32:35]
ds_read_b64_tr_b16 v[142:143],v120 offset:47616
ds_read_b64_tr_b16 v[144:145],v120 offset:53248
v_mfma_f32_16x16x32_bf16 v[36:39],v[208:211],v[100:103],v[36:39]
ds_read_b64_tr_b16 v[146:147],v120 offset:55296
ds_read_b64_tr_b16 v[148:149],v120 offset:53760
v_mfma_f32_16x16x32_bf16 v[40:43],v[212:215],v[96:99],v[40:43]
buffer_load_dwordx4 v252,s[64:67],s95 offen offset:2048 lds
v_mfma_f32_16x16x32_bf16 v[44:47],v[212:215],v[100:103],v[44:47]
ds_read_b64_tr_b16 v[150:151],v120 offset:55808
ds_read_b64_tr_b16 v[152:153],v120 offset:61440
v_mfma_f32_16x16x32_bf16 v[48:51],v[216:219],v[96:99],v[48:51]
ds_read_b64_tr_b16 v[154:155],v120 offset:63488
ds_read_b64_tr_b16 v[156:157],v120 offset:61952
v_mfma_f32_16x16x32_bf16 v[52:55],v[216:219],v[100:103],v[52:55]
ds_read_b64_tr_b16 v[158:159],v120 offset:64000
v_swap_b32 v106,v108
v_mfma_f32_16x16x32_bf16 v[56:59],v[220:223],v[96:99],v[56:59]
buffer_load_dwordx4 v252,s[68:71],s95 offen offset:3072 lds
v_swap_b32 v107,v109
v_mfma_f32_16x16x32_bf16 v[60:63],v[220:223],v[100:103],v[60:63]
s_waitcnt lgkmcnt(8)
buffer_load_dwordx4 v[96:99],v255,s[88:91],s86 offen offset:2048 sc1
v_mfma_f32_16x16x32_bf16 v[0:3],v[128:131],v[104:107],v[0:3]
buffer_load_dwordx4 v[100:103],v255,s[88:91],s86 offen offset:3072 sc1
v_mfma_f32_16x16x32_bf16 v[4:7],v[128:131],v[108:111],v[4:7]
s_add_u32 s86,s86,4096
v_mfma_f32_16x16x32_bf16 v[8:11],v[132:135],v[104:107],v[8:11]
s_add_u32 m0,s92,0x8000
v_mfma_f32_16x16x32_bf16 v[12:15],v[132:135],v[108:111],v[12:15]
s_waitcnt vmcnt(26) lgkmcnt(0)
v_mfma_f32_16x16x32_bf16 v[16:19],v[136:139],v[104:107],v[16:19]
s_barrier
ds_read_b64_tr_b16 v[192:193],v122
ds_read_b64_tr_b16 v[194:195],v122 offset:2048
v_mfma_f32_16x16x32_bf16 v[20:23],v[136:139],v[108:111],v[20:23]
s_mov_b32 s95,0x54000
ds_read_b64_tr_b16 v[196:197],v122 offset:512
ds_read_b64_tr_b16 v[198:199],v122 offset:2560
v_mfma_f32_16x16x32_bf16 v[24:27],v[140:143],v[104:107],v[24:27]
s_waitcnt vmcnt(34)
buffer_load_dwordx4 v252,s[56:59],s95 offen lds
v_mfma_f32_16x16x32_bf16 v[28:31],v[140:143],v[108:111],v[28:31]
ds_read_b64_tr_b16 v[200:201],v122 offset:8192
ds_read_b64_tr_b16 v[202:203],v122 offset:10240
v_mfma_f32_16x16x32_bf16 v[32:35],v[144:147],v[104:107],v[32:35]
ds_read_b64_tr_b16 v[204:205],v122 offset:8704
ds_read_b64_tr_b16 v[206:207],v122 offset:10752
v_mfma_f32_16x16x32_bf16 v[36:39],v[144:147],v[108:111],v[36:39]
buffer_load_dwordx4 v252,s[60:63],s95 offen offset:1024 lds
v_mfma_f32_16x16x32_bf16 v[40:43],v[148:151],v[104:107],v[40:43]
ds_read_b64_tr_b16 v[208:209],v122 offset:16384
ds_read_b64_tr_b16 v[210:211],v122 offset:18432
v_mfma_f32_16x16x32_bf16 v[44:47],v[148:151],v[108:111],v[44:47]
ds_read_b64_tr_b16 v[212:213],v122 offset:16896
ds_read_b64_tr_b16 v[214:215],v122 offset:18944
v_mfma_f32_16x16x32_bf16 v[48:51],v[152:155],v[104:107],v[48:51]
buffer_load_dwordx4 v252,s[64:67],s95 offen offset:2048 lds
v_mfma_f32_16x16x32_bf16 v[52:55],v[152:155],v[108:111],v[52:55]
ds_read_b64_tr_b16 v[216:217],v122 offset:24576
ds_read_b64_tr_b16 v[218:219],v122 offset:26624
v_mfma_f32_16x16x32_bf16 v[56:59],v[156:159],v[104:107],v[56:59]
buffer_load_dwordx4 v252,s[68:71],s95 offen offset:3072 lds
v_swap_b32 v114,v116
v_swap_b32 v115,v117
v_mfma_f32_16x16x32_bf16 v[60:63],v[156:159],v[108:111],v[60:63]
s_waitcnt lgkmcnt(6)
ds_read_b64_tr_b16 v[220:221],v122 offset:25088
ds_read_b64_tr_b16 v[222:223],v122 offset:27136
v_mfma_f32_16x16x32_bf16 v[0:3],v[192:195],v[112:115],v[0:3]
s_add_u32 m0,s92,0xc000
ds_read_b64_tr_b16 v[128:129],v122 offset:4096
ds_read_b64_tr_b16 v[130:131],v122 offset:6144
v_mfma_f32_16x16x32_bf16 v[4:7],v[192:195],v[116:119],v[4:7]
s_mov_b32 s95,0x54100
ds_read_b64_tr_b16 v[132:133],v122 offset:4608
v_mfma_f32_16x16x32_bf16 v[8:11],v[196:199],v[112:115],v[8:11]
buffer_load_dwordx4 v252,s[56:59],s95 offen lds
v_mfma_f32_16x16x32_bf16 v[12:15],v[196:199],v[116:119],v[12:15]
s_waitcnt lgkmcnt(3)
ds_read_b64_tr_b16 v[134:135],v122 offset:6656
ds_read_b64_tr_b16 v[136:137],v122 offset:12288
v_mfma_f32_16x16x32_bf16 v[16:19],v[200:203],v[112:115],v[16:19]
ds_read_b64_tr_b16 v[138:139],v122 offset:14336
ds_read_b64_tr_b16 v[140:141],v122 offset:12800
v_mfma_f32_16x16x32_bf16 v[20:23],v[200:203],v[116:119],v[20:23]
buffer_load_dwordx4 v[104:107],v255,s[88:91],s86 offen sc1
v_mfma_f32_16x16x32_bf16 v[24:27],v[204:207],v[112:115],v[24:27]
s_waitcnt vmcnt(30)
buffer_load_dwordx4 v252,s[60:63],s95 offen offset:1024 lds
v_mfma_f32_16x16x32_bf16 v[28:31],v[204:207],v[116:119],v[28:31]
buffer_load_dwordx4 v[108:111],v255,s[88:91],s86 offen offset:1024 sc1
v_mfma_f32_16x16x32_bf16 v[32:35],v[208:211],v[112:115],v[32:35]
ds_read_b64_tr_b16 v[142:143],v122 offset:14848
ds_read_b64_tr_b16 v[144:145],v122 offset:20480
v_mfma_f32_16x16x32_bf16 v[36:39],v[208:211],v[116:119],v[36:39]
ds_read_b64_tr_b16 v[146:147],v122 offset:22528
ds_read_b64_tr_b16 v[148:149],v122 offset:20992
v_mfma_f32_16x16x32_bf16 v[40:43],v[212:215],v[112:115],v[40:43]
buffer_load_dwordx4 v252,s[64:67],s95 offen offset:2048 lds
v_mfma_f32_16x16x32_bf16 v[44:47],v[212:215],v[116:119],v[44:47]
ds_read_b64_tr_b16 v[150:151],v122 offset:23040
ds_read_b64_tr_b16 v[152:153],v122 offset:28672
v_mfma_f32_16x16x32_bf16 v[48:51],v[216:219],v[112:115],v[48:51]
ds_read_b64_tr_b16 v[154:155],v122 offset:30720
ds_read_b64_tr_b16 v[156:157],v122 offset:29184
v_mfma_f32_16x16x32_bf16 v[52:55],v[216:219],v[116:119],v[52:55]
ds_read_b64_tr_b16 v[158:159],v122 offset:31232
v_swap_b32 v66,v68
v_mfma_f32_16x16x32_bf16 v[56:59],v[220:223],v[112:115],v[56:59]
buffer_load_dwordx4 v252,s[68:71],s95 offen offset:3072 lds
v_swap_b32 v67,v69
v_mfma_f32_16x16x32_bf16 v[60:63],v[220:223],v[116:119],v[60:63]
s_waitcnt lgkmcnt(8)
buffer_load_dwordx4 v[112:115],v255,s[88:91],s86 offen offset:2048 sc1
v_mfma_f32_16x16x32_bf16 v[0:3],v[128:131],v[64:67],v[0:3]
s_lshr_b32 s98,%[w],1
buffer_load_dwordx4 v[116:119],v255,s[88:91],s86 offen offset:3072 sc1
v_mfma_f32_16x16x32_bf16 v[4:7],v[128:131],v[68:71],v[4:7]
s_mul_i32 s98,s98,57216
v_add_u32 v252,s98,v252
v_mfma_f32_16x16x32_bf16 v[8:11],v[132:135],v[64:67],v[8:11]
s_and_b32 s98,%[h],2
v_mfma_f32_16x16x32_bf16 v[12:15],v[132:135],v[68:71],v[12:15]
s_waitcnt vmcnt(26) lgkmcnt(0)
v_mfma_f32_16x16x32_bf16 v[16:19],v[136:139],v[64:67],v[16:19]
s_barrier
ds_read_b64_tr_b16 v[192:193],v120
ds_read_b64_tr_b16 v[194:195],v120 offset:2048
v_mfma_f32_16x16x32_bf16 v[20:23],v[136:139],v[68:71],v[20:23]
s_mul_i32 s98,s98,192
ds_read_b64_tr_b16 v[196:197],v120 offset:512
ds_read_b64_tr_b16 v[198:199],v120 offset:2560
v_mfma_f32_16x16x32_bf16 v[24:27],v[140:143],v[64:67],v[24:27]
s_waitcnt vmcnt(28)
ds_read_b64_tr_b16 v[200:201],v120 offset:8192
ds_read_b64_tr_b16 v[202:203],v120 offset:10240
v_mfma_f32_16x16x32_bf16 v[28:31],v[140:143],v[68:71],v[28:31]
ds_read_b64_tr_b16 v[204:205],v120 offset:8704
ds_read_b64_tr_b16 v[206:207],v120 offset:10752
v_mfma_f32_16x16x32_bf16 v[32:35],v[144:147],v[64:67],v[32:35]
ds_read_b64_tr_b16 v[208:209],v120 offset:16384
)"
R"(v_swap_b32 v74,v76
v_mfma_f32_16x16x32_bf16 v[36:39],v[144:147],v[68:71],v[36:39]
ds_read_b64_tr_b16 v[210:211],v120 offset:18432
ds_read_b64_tr_b16 v[212:213],v120 offset:16896
v_mfma_f32_16x16x32_bf16 v[40:43],v[148:151],v[64:67],v[40:43]
ds_read_b64_tr_b16 v[214:215],v120 offset:18944
ds_read_b64_tr_b16 v[216:217],v120 offset:24576
v_mfma_f32_16x16x32_bf16 v[44:47],v[148:151],v[68:71],v[44:47]
ds_read_b64_tr_b16 v[218:219],v120 offset:26624
ds_read_b64_tr_b16 v[220:221],v120 offset:25088
v_mfma_f32_16x16x32_bf16 v[48:51],v[152:155],v[64:67],v[48:51]
ds_read_b64_tr_b16 v[222:223],v120 offset:27136
v_swap_b32 v75,v77
v_mfma_f32_16x16x32_bf16 v[52:55],v[152:155],v[68:71],v[52:55]
v_subrev_u32 v252,s98,v252
v_mfma_f32_16x16x32_bf16 v[56:59],v[156:159],v[64:67],v[56:59]
s_and_b32 s98,%[w],2
v_mfma_f32_16x16x32_bf16 v[60:63],v[156:159],v[68:71],v[60:63]
s_waitcnt lgkmcnt(8)
v_mfma_f32_16x16x32_bf16 v[0:3],v[192:195],v[72:75],v[0:3]
s_lshl_b32 s98,s98,8
ds_read_b64_tr_b16 v[128:129],v120 offset:4096
ds_read_b64_tr_b16 v[130:131],v120 offset:6144
v_mfma_f32_16x16x32_bf16 v[4:7],v[192:195],v[76:79],v[4:7]
s_add_u32 s98,s98,s100
ds_read_b64_tr_b16 v[132:133],v120 offset:4608
v_mfma_f32_16x16x32_bf16 v[8:11],v[196:199],v[72:75],v[8:11]
s_xor_b32 s100,s98,16384
ds_read_b64_tr_b16 v[134:135],v120 offset:6656
v_mfma_f32_16x16x32_bf16 v[12:15],v[196:199],v[76:79],v[12:15]
s_waitcnt lgkmcnt(4)
ds_read_b64_tr_b16 v[136:137],v120 offset:12288
v_mfma_f32_16x16x32_bf16 v[16:19],v[200:203],v[72:75],v[16:19]
ds_read_b64_tr_b16 v[138:139],v120 offset:14336
ds_read_b64_tr_b16 v[140:141],v120 offset:12800
v_mfma_f32_16x16x32_bf16 v[20:23],v[200:203],v[76:79],v[20:23]
ds_read_b64_tr_b16 v[142:143],v120 offset:14848
v_mfma_f32_16x16x32_bf16 v[24:27],v[204:207],v[72:75],v[24:27]
s_waitcnt vmcnt(24)
ds_read_b64_tr_b16 v[144:145],v120 offset:20480
v_swap_b32 v82,v84
v_mfma_f32_16x16x32_bf16 v[28:31],v[204:207],v[76:79],v[28:31]
ds_read_b64_tr_b16 v[146:147],v120 offset:22528
v_swap_b32 v83,v85
v_mfma_f32_16x16x32_bf16 v[32:35],v[208:211],v[72:75],v[32:35]
ds_read_b64_tr_b16 v[148:149],v120 offset:20992
ds_read_b64_tr_b16 v[150:151],v120 offset:23040
v_mfma_f32_16x16x32_bf16 v[36:39],v[208:211],v[76:79],v[36:39]
ds_read_b64_tr_b16 v[152:153],v120 offset:28672
v_mfma_f32_16x16x32_bf16 v[40:43],v[212:215],v[72:75],v[40:43]
ds_read_b64_tr_b16 v[154:155],v120 offset:30720
v_mfma_f32_16x16x32_bf16 v[44:47],v[212:215],v[76:79],v[44:47]
ds_read_b64_tr_b16 v[156:157],v120 offset:29184
v_mfma_f32_16x16x32_bf16 v[48:51],v[216:219],v[72:75],v[48:51]
ds_read_b64_tr_b16 v[158:159],v120 offset:31232
v_mfma_f32_16x16x32_bf16 v[52:55],v[216:219],v[76:79],v[52:55]
v_mfma_f32_16x16x32_bf16 v[56:59],v[220:223],v[72:75],v[56:59]
v_mfma_f32_16x16x32_bf16 v[60:63],v[220:223],v[76:79],v[60:63]
s_waitcnt lgkmcnt(8)
v_mfma_f32_16x16x32_bf16 v[0:3],v[128:131],v[80:83],v[0:3]
v_mfma_f32_16x16x32_bf16 v[4:7],v[128:131],v[84:87],v[4:7]
v_mfma_f32_16x16x32_bf16 v[8:11],v[132:135],v[80:83],v[8:11]
v_mfma_f32_16x16x32_bf16 v[12:15],v[132:135],v[84:87],v[12:15]
s_waitcnt vmcnt(14) lgkmcnt(0)
v_mfma_f32_16x16x32_bf16 v[16:19],v[136:139],v[80:83],v[16:19]
s_barrier
ds_read_b64_tr_b16 v[192:193],v124
ds_read_b64_tr_b16 v[194:195],v124 offset:2048
v_mfma_f32_16x16x32_bf16 v[20:23],v[136:139],v[84:87],v[20:23]
s_add_u32 m0,s92,0x0
ds_read_b64_tr_b16 v[196:197],v124 offset:512
ds_read_b64_tr_b16 v[198:199],v124 offset:2560
v_mfma_f32_16x16x32_bf16 v[24:27],v[140:143],v[80:83],v[24:27]
s_mov_b32 s95,0x0
ds_read_b64_tr_b16 v[200:201],v124 offset:8192
ds_read_b64_tr_b16 v[202:203],v124 offset:10240
v_mfma_f32_16x16x32_bf16 v[28:31],v[140:143],v[84:87],v[28:31]
s_waitcnt vmcnt(16)
ds_read_b64_tr_b16 v[204:205],v124 offset:8704
ds_read_b64_tr_b16 v[206:207],v124 offset:10752
v_mfma_f32_16x16x32_bf16 v[32:35],v[144:147],v[80:83],v[32:35]
ds_read_b64_tr_b16 v[208:209],v124 offset:16384
v_swap_b32 v90,v92
v_mfma_f32_16x16x32_bf16 v[36:39],v[144:147],v[84:87],v[36:39]
buffer_load_dwordx4 v252,s[24:27],s95 offen lds
v_mfma_f32_16x16x32_bf16 v[40:43],v[148:151],v[80:83],v[40:43]
ds_read_b64_tr_b16 v[210:211],v124 offset:18432
ds_read_b64_tr_b16 v[212:213],v124 offset:16896
v_mfma_f32_16x16x32_bf16 v[44:47],v[148:151],v[84:87],v[44:47]
ds_read_b64_tr_b16 v[214:215],v124 offset:18944
ds_read_b64_tr_b16 v[216:217],v124 offset:24576
v_mfma_f32_16x16x32_bf16 v[48:51],v[152:155],v[80:83],v[48:51]
buffer_load_dwordx4 v252,s[28:31],s95 offen offset:1024 lds
v_mfma_f32_16x16x32_bf16 v[52:55],v[152:155],v[84:87],v[52:55]
ds_read_b64_tr_b16 v[218:219],v124 offset:26624
ds_read_b64_tr_b16 v[220:221],v124 offset:25088
v_mfma_f32_16x16x32_bf16 v[56:59],v[156:159],v[80:83],v[56:59]
v_swap_b32 v91,v93
v_mfma_f32_16x16x32_bf16 v[60:63],v[156:159],v[84:87],v[60:63]
s_waitcnt lgkmcnt(7)
ds_read_b64_tr_b16 v[222:223],v124 offset:27136
v_mfma_f32_16x16x32_bf16 v[0:3],v[192:195],v[88:91],v[0:3]
ds_read_b64_tr_b16 v[128:129],v124 offset:4096
ds_read_b64_tr_b16 v[130:131],v124 offset:6144
v_mfma_f32_16x16x32_bf16 v[4:7],v[192:195],v[92:95],v[4:7]
ds_read_b64_tr_b16 v[132:133],v124 offset:4608
v_mfma_f32_16x16x32_bf16 v[8:11],v[196:199],v[88:91],v[8:11]
ds_read_b64_tr_b16 v[134:135],v124 offset:6656
v_mfma_f32_16x16x32_bf16 v[12:15],v[196:199],v[92:95],v[12:15]
s_waitcnt lgkmcnt(4)
ds_read_b64_tr_b16 v[136:137],v124 offset:12288
v_mfma_f32_16x16x32_bf16 v[16:19],v[200:203],v[88:91],v[16:19]
ds_read_b64_tr_b16 v[138:139],v124 offset:14336
ds_read_b64_tr_b16 v[140:141],v124 offset:12800
v_mfma_f32_16x16x32_bf16 v[20:23],v[200:203],v[92:95],v[20:23]
ds_read_b64_tr_b16 v[142:143],v124 offset:14848
v_mfma_f32_16x16x32_bf16 v[24:27],v[204:207],v[88:91],v[24:27]
s_waitcnt vmcnt(14)
ds_read_b64_tr_b16 v[144:145],v124 offset:20480
v_swap_b32 v98,v100
v_mfma_f32_16x16x32_bf16 v[28:31],v[204:207],v[92:95],v[28:31]
ds_read_b64_tr_b16 v[146:147],v124 offset:22528
v_swap_b32 v99,v101
v_mfma_f32_16x16x32_bf16 v[32:35],v[208:211],v[88:91],v[32:35]
ds_read_b64_tr_b16 v[148:149],v124 offset:20992
ds_read_b64_tr_b16 v[150:151],v124 offset:23040
v_mfma_f32_16x16x32_bf16 v[36:39],v[208:211],v[92:95],v[36:39]
buffer_load_dwordx4 v252,s[32:35],s95 offen offset:2048 lds
v_mfma_f32_16x16x32_bf16 v[40:43],v[212:215],v[88:91],v[40:43]
ds_read_b64_tr_b16 v[152:153],v124 offset:28672
ds_read_b64_tr_b16 v[154:155],v124 offset:30720
v_mfma_f32_16x16x32_bf16 v[44:47],v[212:215],v[92:95],v[44:47]
ds_read_b64_tr_b16 v[156:157],v124 offset:29184
v_mfma_f32_16x16x32_bf16 v[48:51],v[216:219],v[88:91],v[48:51]
buffer_load_dwordx4 v252,s[36:39],s95 offen offset:3072 lds
v_mfma_f32_16x16x32_bf16 v[52:55],v[216:219],v[92:95],v[52:55]
ds_read_b64_tr_b16 v[158:159],v124 offset:31232
v_mfma_f32_16x16x32_bf16 v[56:59],v[220:223],v[88:91],v[56:59]
v_mfma_f32_16x16x32_bf16 v[60:63],v[220:223],v[92:95],v[60:63]
s_waitcnt lgkmcnt(8)
v_mfma_f32_16x16x32_bf16 v[0:3],v[128:131],v[96:99],v[0:3]
v_mfma_f32_16x16x32_bf16 v[4:7],v[128:131],v[100:103],v[4:7]
v_mfma_f32_16x16x32_bf16 v[8:11],v[132:135],v[96:99],v[8:11]
v_mfma_f32_16x16x32_bf16 v[12:15],v[132:135],v[100:103],v[12:15]
s_waitcnt vmcnt(6) lgkmcnt(0)
v_mfma_f32_16x16x32_bf16 v[16:19],v[136:139],v[96:99],v[16:19]
s_barrier
ds_read_b64_tr_b16 v[192:193],v120 offset:32768
ds_read_b64_tr_b16 v[194:195],v120 offset:34816
v_mfma_f32_16x16x32_bf16 v[20:23],v[136:139],v[100:103],v[20:23]
s_add_u32 m0,s92,0x4000
ds_read_b64_tr_b16 v[196:197],v120 offset:33280
ds_read_b64_tr_b16 v[198:199],v120 offset:35328
v_mfma_f32_16x16x32_bf16 v[24:27],v[140:143],v[96:99],v[24:27]
s_mov_b32 s95,0x38000
ds_read_b64_tr_b16 v[200:201],v120 offset:40960
ds_read_b64_tr_b16 v[202:203],v120 offset:43008
v_mfma_f32_16x16x32_bf16 v[28:31],v[140:143],v[100:103],v[28:31]
s_waitcnt vmcnt(8)
ds_read_b64_tr_b16 v[204:205],v120 offset:41472
ds_read_b64_tr_b16 v[206:207],v120 offset:43520
v_mfma_f32_16x16x32_bf16 v[32:35],v[144:147],v[96:99],v[32:35]
ds_read_b64_tr_b16 v[208:209],v120 offset:49152
v_swap_b32 v106,v108
v_mfma_f32_16x16x32_bf16 v[36:39],v[144:147],v[100:103],v[36:39]
buffer_load_dwordx4 v252,s[24:27],s95 offen lds
v_mfma_f32_16x16x32_bf16 v[40:43],v[148:151],v[96:99],v[40:43]
ds_read_b64_tr_b16 v[210:211],v120 offset:51200
ds_read_b64_tr_b16 v[212:213],v120 offset:49664
v_mfma_f32_16x16x32_bf16 v[44:47],v[148:151],v[100:103],v[44:47]
ds_read_b64_tr_b16 v[214:215],v120 offset:51712
ds_read_b64_tr_b16 v[216:217],v120 offset:57344
v_mfma_f32_16x16x32_bf16 v[48:51],v[152:155],v[96:99],v[48:51]
buffer_load_dwordx4 v252,s[28:31],s95 offen offset:1024 lds
v_mfma_f32_16x16x32_bf16 v[52:55],v[152:155],v[100:103],v[52:55]
ds_read_b64_tr_b16 v[218:219],v120 offset:59392
ds_read_b64_tr_b16 v[220:221],v120 offset:57856
v_mfma_f32_16x16x32_bf16 v[56:59],v[156:159],v[96:99],v[56:59]
v_swap_b32 v107,v109
v_mfma_f32_16x16x32_bf16 v[60:63],v[156:159],v[100:103],v[60:63]
s_waitcnt lgkmcnt(7)
ds_read_b64_tr_b16 v[222:223],v120 offset:59904
v_mfma_f32_16x16x32_bf16 v[0:3],v[192:195],v[104:107],v[0:3]
ds_read_b64_tr_b16 v[128:129],v120 offset:36864
ds_read_b64_tr_b16 v[130:131],v120 offset:38912
v_mfma_f32_16x16x32_bf16 v[4:7],v[192:195],v[108:111],v[4:7]
ds_read_b64_tr_b16 v[132:133],v120 offset:37376
v_mfma_f32_16x16x32_bf16 v[8:11],v[196:199],v[104:107],v[8:11]
ds_read_b64_tr_b16 v[134:135],v120 offset:39424
v_mfma_f32_16x16x32_bf16 v[12:15],v[196:199],v[108:111],v[12:15]
s_waitcnt lgkmcnt(4)
ds_read_b64_tr_b16 v[136:137],v120 offset:45056
v_mfma_f32_16x16x32_bf16 v[16:19],v[200:203],v[104:107],v[16:19]
ds_read_b64_tr_b16 v[138:139],v120 offset:47104
ds_read_b64_tr_b16 v[140:141],v120 offset:45568
v_mfma_f32_16x16x32_bf16 v[20:23],v[200:203],v[108:111],v[20:23]
ds_read_b64_tr_b16 v[142:143],v120 offset:47616
v_mfma_f32_16x16x32_bf16 v[24:27],v[204:207],v[104:107],v[24:27]
s_waitcnt vmcnt(6)
ds_read_b64_tr_b16 v[144:145],v120 offset:53248
)"
R"(v_swap_b32 v114,v116
v_mfma_f32_16x16x32_bf16 v[28:31],v[204:207],v[108:111],v[28:31]
ds_read_b64_tr_b16 v[146:147],v120 offset:55296
v_swap_b32 v115,v117
v_mfma_f32_16x16x32_bf16 v[32:35],v[208:211],v[104:107],v[32:35]
ds_read_b64_tr_b16 v[148:149],v120 offset:53760
ds_read_b64_tr_b16 v[150:151],v120 offset:55808
v_mfma_f32_16x16x32_bf16 v[36:39],v[208:211],v[108:111],v[36:39]
buffer_load_dwordx4 v252,s[32:35],s95 offen offset:2048 lds
v_mfma_f32_16x16x32_bf16 v[40:43],v[212:215],v[104:107],v[40:43]
ds_read_b64_tr_b16 v[152:153],v120 offset:61440
ds_read_b64_tr_b16 v[154:155],v120 offset:63488
v_mfma_f32_16x16x32_bf16 v[44:47],v[212:215],v[108:111],v[44:47]
ds_read_b64_tr_b16 v[156:157],v120 offset:61952
v_mfma_f32_16x16x32_bf16 v[48:51],v[216:219],v[104:107],v[48:51]
buffer_load_dwordx4 v252,s[36:39],s95 offen offset:3072 lds
v_mfma_f32_16x16x32_bf16 v[52:55],v[216:219],v[108:111],v[52:55]
ds_read_b64_tr_b16 v[158:159],v120 offset:64000
v_mfma_f32_16x16x32_bf16 v[56:59],v[220:223],v[104:107],v[56:59]
v_mfma_f32_16x16x32_bf16 v[60:63],v[220:223],v[108:111],v[60:63]
s_waitcnt lgkmcnt(8)
v_mfma_f32_16x16x32_bf16 v[0:3],v[128:131],v[112:115],v[0:3]
v_mfma_f32_16x16x32_bf16 v[4:7],v[128:131],v[116:119],v[4:7]
v_mov_b32 v128,v246
v_mov_b32 v130,v247
v_mfma_f32_16x16x32_bf16 v[8:11],v[132:135],v[112:115],v[8:11]
v_add_u32 v246,s98,v124
v_add_u32 v247,s100,v124
v_mfma_f32_16x16x32_bf16 v[12:15],v[132:135],v[116:119],v[12:15]
s_waitcnt vmcnt(63) lgkmcnt(0)
v_cvt_pk_bf16_f32 v0,v0,v1
v_cvt_pk_bf16_f32 v1,v2,v3
v_mfma_f32_16x16x32_bf16 v[16:19],v[136:139],v[112:115],v[16:19]
s_barrier
ds_read_b64_tr_b16 v[192:193],v250 offset:32768
ds_read_b64_tr_b16 v[194:195],v250 offset:40960
v_mfma_f32_16x16x32_bf16 v[20:23],v[136:139],v[116:119],v[20:23]
ds_read_b64_tr_b16 v[196:197],v251 offset:32768
v_cvt_pk_bf16_f32 v2,v4,v5
v_mfma_f32_16x16x32_bf16 v[24:27],v[140:143],v[112:115],v[24:27]
ds_read_b64_tr_b16 v[198:199],v251 offset:40960
v_cvt_pk_bf16_f32 v3,v6,v7
v_mfma_f32_16x16x32_bf16 v[28:31],v[140:143],v[116:119],v[28:31]
ds_read_b64_tr_b16 v[200:201],v250 offset:33280
ds_read_b64_tr_b16 v[202:203],v250 offset:41472
v_mfma_f32_16x16x32_bf16 v[32:35],v[144:147],v[112:115],v[32:35]
ds_read_b64_tr_b16 v[204:205],v251 offset:33280
v_cvt_pk_bf16_f32 v8,v8,v9
v_mfma_f32_16x16x32_bf16 v[36:39],v[144:147],v[116:119],v[36:39]
ds_read_b64_tr_b16 v[206:207],v251 offset:41472
v_cvt_pk_bf16_f32 v9,v10,v11
v_mfma_f32_16x16x32_bf16 v[40:43],v[148:151],v[112:115],v[40:43]
buffer_store_dwordx4 v[0:3],v255,s[88:91],s101 offen sc1
v_mfma_f32_16x16x32_bf16 v[44:47],v[148:151],v[116:119],v[44:47]
s_add_u32 s101,s54,8192
v_cvt_pk_bf16_f32 v10,v12,v13
v_cvt_pk_bf16_f32 v11,v14,v15
v_mfma_f32_16x16x32_bf16 v[48:51],v[152:155],v[112:115],v[48:51]
buffer_store_dwordx4 v[8:11],v255,s[88:91],s101 offen sc1
v_mfma_f32_16x16x32_bf16 v[52:55],v[152:155],v[116:119],v[52:55]
s_add_u32 s101,s54,16384
v_cvt_pk_bf16_f32 v16,v16,v17
v_cvt_pk_bf16_f32 v17,v18,v19
v_mfma_f32_16x16x32_bf16 v[56:59],v[156:159],v[112:115],v[56:59]
v_cvt_pk_bf16_f32 v18,v20,v21
v_cvt_pk_bf16_f32 v19,v22,v23
v_mfma_f32_16x16x32_bf16 v[60:63],v[156:159],v[116:119],v[60:63]
s_waitcnt lgkmcnt(0)
buffer_store_dwordx4 v[16:19],v255,s[88:91],s101 offen sc1
v_mfma_f32_16x16x32_bf16 v[224:227],v[240:243],v[160:163],v[224:227]
s_lshl_b32 s99,%[w],13
ds_read_b64_tr_b16 v[208:209],v250 offset:33792
v_cvt_pk_bf16_f32 v24,v24,v25
v_mfma_f32_16x16x32_bf16 v[228:231],v[240:243],v[168:171],v[228:231]
s_add_u32 s99,s99,32768
ds_read_b64_tr_b16 v[210:211],v250 offset:41984
v_add_u32 v248,s99,v255
v_mfma_f32_16x16x32_bf16 v[232:235],v[240:243],v[176:179],v[232:235]
s_add_u32 s101,s54,24576
ds_read_b64_tr_b16 v[212:213],v251 offset:33792
v_add_u32 v249,65536,v248
v_mfma_f32_16x16x32_bf16 v[236:239],v[240:243],v[184:187],v[236:239]
ds_read_b64_tr_b16 v[214:215],v251 offset:41984
v_xor_b32 v254,16384,v249
v_mfma_f32_16x16x32_bf16 a[0:3],v[192:195],v[160:163],a[0:3]
v_cvt_pk_bf16_f32 v25,v26,v27
v_mov_b32 v129,v232
v_mfma_f32_16x16x32_bf16 a[4:7],v[196:199],v[160:163],a[4:7]
ds_read_b64_tr_b16 v[216:217],v250 offset:34304
v_cvt_pk_bf16_f32 v26,v28,v29
v_mfma_f32_16x16x32_bf16 a[8:11],v[200:203],v[160:163],a[8:11]
ds_read_b64_tr_b16 v[218:219],v250 offset:42496
v_mov_b32 v131,v236
v_mfma_f32_16x16x32_bf16 a[12:15],v[204:207],v[160:163],a[12:15]
ds_read_b64_tr_b16 v[220:221],v251 offset:34304
ds_write_b128 v254,v[128:131] offset:32768
v_mfma_f32_16x16x32_bf16 a[64:67],v[192:195],v[168:171],a[64:67]
ds_read_b64_tr_b16 v[222:223],v251 offset:42496
v_cvt_pk_bf16_f32 v27,v30,v31
v_mfma_f32_16x16x32_bf16 a[68:71],v[196:199],v[168:171],a[68:71]
buffer_store_dwordx4 v[24:27],v255,s[88:91],s101 offen sc1
v_mfma_f32_16x16x32_bf16 a[72:75],v[200:203],v[168:171],a[72:75]
s_add_u32 s101,s54,32768
v_cvt_pk_bf16_f32 v32,v32,v33
v_cvt_pk_bf16_f32 v33,v34,v35
v_mfma_f32_16x16x32_bf16 a[76:79],v[204:207],v[168:171],a[76:79]
v_cvt_pk_bf16_f32 v34,v36,v37
v_cvt_pk_bf16_f32 v35,v38,v39
v_mfma_f32_16x16x32_bf16 a[128:131],v[192:195],v[176:179],a[128:131]
buffer_store_dwordx4 v[32:35],v255,s[88:91],s101 offen sc1
v_mfma_f32_16x16x32_bf16 a[132:135],v[196:199],v[176:179],a[132:135]
s_add_u32 s101,s54,40960
v_cvt_pk_bf16_f32 v40,v40,v41
v_cvt_pk_bf16_f32 v41,v42,v43
v_mfma_f32_16x16x32_bf16 a[136:139],v[200:203],v[176:179],a[136:139]
v_cvt_pk_bf16_f32 v42,v44,v45
v_cvt_pk_bf16_f32 v43,v46,v47
v_mfma_f32_16x16x32_bf16 a[140:143],v[204:207],v[176:179],a[140:143]
buffer_store_dwordx4 v[40:43],v255,s[88:91],s101 offen sc1
v_mfma_f32_16x16x32_bf16 a[192:195],v[192:195],v[184:187],a[192:195]
s_add_u32 s101,s54,49152
ds_write_b128 v248,a[128:131]
v_cvt_pk_bf16_f32 v48,v48,v49
v_mfma_f32_16x16x32_bf16 a[196:199],v[196:199],v[184:187],a[196:199]
ds_write_b128 v248,a[132:135] offset:1024
v_cvt_pk_bf16_f32 v49,v50,v51
v_mfma_f32_16x16x32_bf16 a[200:203],v[200:203],v[184:187],a[200:203]
ds_write_b128 v248,a[136:139] offset:2048
v_cvt_pk_bf16_f32 v50,v52,v53
v_mfma_f32_16x16x32_bf16 a[204:207],v[204:207],v[184:187],a[204:207]
s_waitcnt lgkmcnt(3)
ds_write_b128 v248,a[140:143] offset:3072
v_cvt_pk_bf16_f32 v51,v54,v55
v_mfma_f32_16x16x32_bf16 a[16:19],v[208:211],v[160:163],a[16:19]
ds_read_b64_tr_b16 v[192:193],v250 offset:34816
v_cvt_pk_bf16_f32 v56,v56,v57
v_mfma_f32_16x16x32_bf16 a[80:83],v[208:211],v[168:171],a[80:83]
ds_read_b64_tr_b16 v[194:195],v250 offset:43008
v_cvt_pk_bf16_f32 v57,v58,v59
v_mfma_f32_16x16x32_bf16 a[144:147],v[208:211],v[176:179],a[144:147]
ds_read_b64_tr_b16 v[196:197],v251 offset:34816
v_cvt_pk_bf16_f32 v58,v60,v61
v_mfma_f32_16x16x32_bf16 a[208:211],v[208:211],v[184:187],a[208:211]
ds_read_b64_tr_b16 v[198:199],v251 offset:43008
v_cvt_pk_bf16_f32 v59,v62,v63
v_mfma_f32_16x16x32_bf16 a[20:23],v[212:215],v[160:163],a[20:23]
ds_read_b64_tr_b16 v[200:201],v250 offset:35328
v_xor_b32 v253,16384,v248
v_mfma_f32_16x16x32_bf16 a[84:87],v[212:215],v[168:171],a[84:87]
ds_read_b64_tr_b16 v[202:203],v250 offset:43520
ds_write_b128 v248,a[192:195] offset:4096
v_mfma_f32_16x16x32_bf16 a[148:151],v[212:215],v[176:179],a[148:151]
ds_read_b64_tr_b16 v[204:205],v251 offset:35328
ds_write_b128 v248,a[196:199] offset:5120
v_mfma_f32_16x16x32_bf16 a[212:215],v[212:215],v[184:187],a[212:215]
ds_read_b64_tr_b16 v[206:207],v251 offset:43520
ds_write_b128 v248,a[200:203] offset:6144
v_mfma_f32_16x16x32_bf16 a[24:27],v[216:219],v[160:163],a[24:27]
buffer_store_dwordx4 v[48:51],v255,s[88:91],s101 offen sc1
v_mfma_f32_16x16x32_bf16 a[88:91],v[216:219],v[168:171],a[88:91]
s_add_u32 s101,s54,57344
buffer_store_dwordx4 v[56:59],v255,s[88:91],s101 offen sc1
v_mfma_f32_16x16x32_bf16 a[152:155],v[216:219],v[176:179],a[152:155]
ds_write_b128 v248,a[204:207] offset:7168
ds_write_b128 v248,a[144:147] offset:32768
v_mfma_f32_16x16x32_bf16 a[216:219],v[216:219],v[184:187],a[216:219]
ds_write_b128 v248,a[148:151] offset:33792
ds_write_b128 v248,a[208:211] offset:36864
v_mfma_f32_16x16x32_bf16 a[28:31],v[220:223],v[160:163],a[28:31]
ds_write_b128 v248,a[212:215] offset:37888
v_add_u32 v240,s98,v120
v_mfma_f32_16x16x32_bf16 a[92:95],v[220:223],v[168:171],a[92:95]
ds_write_b128 v248,a[152:155] offset:34816
v_add_u32 v241,s100,v120
v_mfma_f32_16x16x32_bf16 a[156:159],v[220:223],v[176:179],a[156:159]
ds_write_b128 v248,a[216:219] offset:38912
v_add_u32 v242,s98,v122
v_mfma_f32_16x16x32_bf16 a[220:223],v[220:223],v[184:187],a[220:223]
s_waitcnt lgkmcnt(8)
v_add_u32 v243,s100,v122
v_lshlrev_b32 v226,3,v255
v_mfma_f32_16x16x32_bf16 a[32:35],v[192:195],v[160:163],a[32:35]
s_lshl_b32 s100,%[w],10
ds_read_b64_tr_b16 v[208:209],v250 offset:35840
ds_write_b128 v248,a[156:159] offset:35840
v_mfma_f32_16x16x32_bf16 a[96:99],v[192:195],v[168:171],a[96:99]
s_add_u32 s96,s96,s100
ds_read_b64_tr_b16 v[210:211],v250 offset:44032
ds_write_b128 v248,a[220:223] offset:39936
v_mfma_f32_16x16x32_bf16 a[160:163],v[192:195],v[176:179],a[160:163]
s_add_u32 s96,s96,0xe00000
ds_read_b64_tr_b16 v[212:213],v251 offset:35840
v_and_b32 v226,896,v226
v_mfma_f32_16x16x32_bf16 a[224:227],v[192:195],v[184:187],a[224:227]
s_xor_b32 s100,%[tb],2
ds_read_b64_tr_b16 v[214:215],v251 offset:44032
v_mov_b32 v231,v254
v_mfma_f32_16x16x32_bf16 a[36:39],v[196:199],v[160:163],a[36:39]
s_mul_i32 s100,s100,14336
ds_read_b64_tr_b16 v[216:217],v250 offset:36352
)"
R"(v_xor_b32 v225,8192,v254
v_mfma_f32_16x16x32_bf16 a[100:103],v[196:199],v[168:171],a[100:103]
s_add_u32 s93,s96,s100
ds_read_b64_tr_b16 v[218:219],v250 offset:44544
v_mfma_f32_16x16x32_bf16 a[164:167],v[196:199],v[176:179],a[164:167]
s_mul_i32 s100,%[tb],14336
ds_read_b64_tr_b16 v[220:221],v251 offset:36352
v_mfma_f32_16x16x32_bf16 a[228:231],v[196:199],v[184:187],a[228:231]
s_add_u32 s96,s96,s100
ds_read_b64_tr_b16 v[222:223],v251 offset:44544
v_mfma_f32_16x16x32_bf16 a[40:43],v[200:203],v[160:163],a[40:43]
v_mfma_f32_16x16x32_bf16 a[104:107],v[200:203],v[168:171],a[104:107]
v_mfma_f32_16x16x32_bf16 a[168:171],v[200:203],v[176:179],a[168:171]
v_mfma_f32_16x16x32_bf16 a[232:235],v[200:203],v[184:187],a[232:235]
v_mfma_f32_16x16x32_bf16 a[44:47],v[204:207],v[160:163],a[44:47]
v_mfma_f32_16x16x32_bf16 a[108:111],v[204:207],v[168:171],a[108:111]
v_mfma_f32_16x16x32_bf16 a[172:175],v[204:207],v[176:179],a[172:175]
v_mfma_f32_16x16x32_bf16 a[236:239],v[204:207],v[184:187],a[236:239]
s_waitcnt lgkmcnt(0)
v_mfma_f32_16x16x32_bf16 a[48:51],v[208:211],v[160:163],a[48:51]
s_barrier
ds_read_b128 v[132:135],v249 offset:32768
ds_write_b128 v249,a[160:163] offset:32768
v_mfma_f32_16x16x32_bf16 a[112:115],v[208:211],v[168:171],a[112:115]
ds_write_b128 v249,a[164:167] offset:33792
ds_write_b128 v249,a[168:171] offset:34816
v_mfma_f32_16x16x32_bf16 a[176:179],v[208:211],v[176:179],a[176:179]
ds_write_b128 v249,a[172:175] offset:35840
ds_write_b128 v249,a[224:227] offset:36864
v_mfma_f32_16x16x32_bf16 a[240:243],v[208:211],v[184:187],a[240:243]
ds_write_b128 v249,a[228:231] offset:37888
ds_write_b128 v249,a[232:235] offset:38912
v_mfma_f32_16x16x32_bf16 a[52:55],v[212:215],v[160:163],a[52:55]
ds_write_b128 v249,a[236:239] offset:39936
v_mfma_f32_16x16x32_bf16 a[116:119],v[212:215],v[168:171],a[116:119]
ds_write_b128 v249,a[176:179]
v_mfma_f32_16x16x32_bf16 a[180:183],v[212:215],v[176:179],a[180:183]
v_mfma_f32_16x16x32_bf16 a[244:247],v[212:215],v[184:187],a[244:247]
ds_write_b128 v249,a[240:243] offset:4096
v_mfma_f32_16x16x32_bf16 a[56:59],v[216:219],v[160:163],a[56:59]
s_waitcnt lgkmcnt(10)
v_min_f32 v136,v244,v132
v_min_f32 v137,v245,v134
v_mfma_f32_16x16x32_bf16 a[120:123],v[216:219],v[168:171],a[120:123]
v_sub_f32 v232,v136,v244
v_sub_f32 v234,v137,v245
v_mfma_f32_16x16x32_bf16 a[184:187],v[216:219],v[176:179],a[184:187]
ds_write_b128 v249,a[180:183] offset:1024
v_sub_f32 v236,v136,v132
v_mfma_f32_16x16x32_bf16 a[248:251],v[216:219],v[184:187],a[248:251]
ds_write_b128 v249,a[244:247] offset:5120
v_sub_f32 v238,v137,v134
v_mfma_f32_16x16x32_bf16 a[60:63],v[220:223],v[160:163],a[60:63]
v_exp_f32 v232,v232
v_mfma_f32_16x16x32_bf16 a[124:127],v[220:223],v[168:171],a[124:127]
v_exp_f32 v234,v234
v_mfma_f32_16x16x32_bf16 a[188:191],v[220:223],v[176:179],a[188:191]
v_exp_f32 v236,v236
v_mfma_f32_16x16x32_bf16 a[252:255],v[220:223],v[184:187],a[252:255]
v_exp_f32 v238,v238
v_mul_f32 v136,v224,v232
v_mul_f32 v137,v228,v234
v_fma_f32 v136,v133,v236,v136
v_fma_f32 v137,v135,v238,v137
v_rcp_f32 v136,v136
v_rcp_f32 v137,v137
v_mul_f32 v232,v232,v136
v_mul_f32 v234,v234,v137
v_mul_f32 v236,v236,v136
v_mul_f32 v238,v238,v137
ds_write_b128 v249,a[184:187] offset:2048
ds_write_b128 v249,a[188:191] offset:3072
ds_write_b128 v249,a[248:251] offset:6144
ds_write_b128 v249,a[252:255] offset:7168
v_lshrrev_b32 v228,6,v255
v_mul_u32_u24 v227,896,v228
v_lshlrev_b32 v228,3,v228
v_xor_b32 v228,v228,v255
v_and_b32 v228,48,v228
v_add_u32 v227,v227,v228
v_xor_b32 v228,16384,v254
v_xor_b32 v224,16384,v225
s_waitcnt vmcnt(8) lgkmcnt(0)
s_barrier
ds_read_b128 v[0:3],v253
ds_read_b128 v[4:7],v253 offset:1024
ds_read_b128 v[8:11],v253 offset:2048
ds_read_b128 v[12:15],v253 offset:3072
ds_read_b128 v[16:19],v253 offset:32768
ds_read_b128 v[20:23],v253 offset:33792
ds_read_b128 v[24:27],v253 offset:34816
ds_read_b128 v[28:31],v253 offset:35840
ds_read_b128 v[64:67],v253 offset:4096
ds_read_b128 v[68:71],v253 offset:5120
ds_read_b128 v[72:75],v253 offset:6144
ds_read_b128 v[76:79],v253 offset:7168
ds_read_b128 v[80:83],v253 offset:36864
ds_read_b128 v[84:87],v253 offset:37888
ds_read_b128 v[88:91],v253 offset:38912
ds_read_b128 v[92:95],v253 offset:39936
ds_read_b128 v[32:35],v231 offset:32768
ds_read_b128 v[36:39],v231 offset:33792
ds_read_b128 v[40:43],v231 offset:34816
ds_read_b128 v[44:47],v231 offset:35840
ds_read_b128 v[48:51],v231
ds_read_b128 v[52:55],v231 offset:1024
ds_read_b128 v[56:59],v231 offset:2048
ds_read_b128 v[60:63],v231 offset:3072
ds_read_b128 v[96:99],v231 offset:36864
ds_read_b128 v[100:103],v231 offset:37888
ds_read_b128 v[104:107],v231 offset:38912
ds_read_b128 v[108:111],v231 offset:39936
ds_read_b128 v[112:115],v231 offset:4096
ds_read_b128 v[116:119],v231 offset:5120
ds_read_b128 v[120:123],v231 offset:6144
ds_read_b128 v[124:127],v231 offset:7168
s_waitcnt lgkmcnt(15)
v_accvgpr_read_b32 v248,a0
v_accvgpr_read_b32 v249,a1
v_accvgpr_read_b32 v250,a2
v_accvgpr_read_b32 v251,a3
v_pk_mul_f32 v[0:1],v[0:1],v[236:237] op_sel_hi:[1,0]
v_pk_mul_f32 v[2:3],v[2:3],v[236:237] op_sel_hi:[1,0]
v_pk_fma_f32 v[0:1],v[248:249],v[232:233],v[0:1] op_sel_hi:[1,0,1]
v_pk_fma_f32 v[2:3],v[250:251],v[232:233],v[2:3] op_sel_hi:[1,0,1]
v_accvgpr_read_b32 v248,a4
v_accvgpr_read_b32 v249,a5
v_accvgpr_read_b32 v250,a6
v_accvgpr_read_b32 v251,a7
v_pk_mul_f32 v[4:5],v[4:5],v[236:237] op_sel_hi:[1,0]
v_pk_mul_f32 v[6:7],v[6:7],v[236:237] op_sel_hi:[1,0]
v_pk_fma_f32 v[4:5],v[248:249],v[232:233],v[4:5] op_sel_hi:[1,0,1]
v_pk_fma_f32 v[6:7],v[250:251],v[232:233],v[6:7] op_sel_hi:[1,0,1]
v_cvt_pk_bf16_f32 v128,v0,v1
v_cvt_pk_bf16_f32 v129,v2,v3
v_cvt_pk_bf16_f32 v130,v4,v5
v_cvt_pk_bf16_f32 v131,v6,v7
v_accvgpr_read_b32 v248,a8
v_accvgpr_read_b32 v249,a9
v_accvgpr_read_b32 v250,a10
v_accvgpr_read_b32 v251,a11
v_pk_mul_f32 v[8:9],v[8:9],v[236:237] op_sel_hi:[1,0]
v_pk_mul_f32 v[10:11],v[10:11],v[236:237] op_sel_hi:[1,0]
v_pk_fma_f32 v[8:9],v[248:249],v[232:233],v[8:9] op_sel_hi:[1,0,1]
v_pk_fma_f32 v[10:11],v[250:251],v[232:233],v[10:11] op_sel_hi:[1,0,1]
v_accvgpr_read_b32 v248,a12
v_accvgpr_read_b32 v249,a13
v_accvgpr_read_b32 v250,a14
v_accvgpr_read_b32 v251,a15
v_pk_mul_f32 v[12:13],v[12:13],v[236:237] op_sel_hi:[1,0]
v_pk_mul_f32 v[14:15],v[14:15],v[236:237] op_sel_hi:[1,0]
v_pk_fma_f32 v[12:13],v[248:249],v[232:233],v[12:13] op_sel_hi:[1,0,1]
v_pk_fma_f32 v[14:15],v[250:251],v[232:233],v[14:15] op_sel_hi:[1,0,1]
v_cvt_pk_bf16_f32 v132,v8,v9
v_cvt_pk_bf16_f32 v133,v10,v11
v_cvt_pk_bf16_f32 v134,v12,v13
v_cvt_pk_bf16_f32 v135,v14,v15
s_waitcnt lgkmcnt(0)
s_barrier
s_add_u32 m0,s92,0x8000
s_mov_b32 s95,0x80
buffer_load_dwordx4 v252,s[24:27],s95 offen lds
v_accvgpr_read_b32 v248,a16
v_accvgpr_read_b32 v249,a17
v_accvgpr_read_b32 v250,a18
v_accvgpr_read_b32 v251,a19
v_pk_mul_f32 v[16:17],v[16:17],v[236:237] op_sel_hi:[1,0]
v_pk_mul_f32 v[18:19],v[18:19],v[236:237] op_sel_hi:[1,0]
v_pk_fma_f32 v[16:17],v[248:249],v[232:233],v[16:17] op_sel_hi:[1,0,1]
v_pk_fma_f32 v[18:19],v[250:251],v[232:233],v[18:19] op_sel_hi:[1,0,1]
v_accvgpr_read_b32 v248,a20
v_accvgpr_read_b32 v249,a21
v_accvgpr_read_b32 v250,a22
v_accvgpr_read_b32 v251,a23
v_pk_mul_f32 v[20:21],v[20:21],v[236:237] op_sel_hi:[1,0]
v_pk_mul_f32 v[22:23],v[22:23],v[236:237] op_sel_hi:[1,0]
v_pk_fma_f32 v[20:21],v[248:249],v[232:233],v[20:21] op_sel_hi:[1,0,1]
v_pk_fma_f32 v[22:23],v[250:251],v[232:233],v[22:23] op_sel_hi:[1,0,1]
v_cvt_pk_bf16_f32 v136,v16,v17
v_cvt_pk_bf16_f32 v137,v18,v19
v_cvt_pk_bf16_f32 v138,v20,v21
v_cvt_pk_bf16_f32 v139,v22,v23
ds_write_b128 v231,v[136:139] offset:2048
ds_write_b128 v231,v[128:131]
buffer_load_dwordx4 v252,s[28:31],s95 offen offset:1024 lds
v_accvgpr_read_b32 v248,a24
v_accvgpr_read_b32 v249,a25
v_accvgpr_read_b32 v250,a26
v_accvgpr_read_b32 v251,a27
v_pk_mul_f32 v[24:25],v[24:25],v[236:237] op_sel_hi:[1,0]
v_pk_mul_f32 v[26:27],v[26:27],v[236:237] op_sel_hi:[1,0]
v_pk_fma_f32 v[24:25],v[248:249],v[232:233],v[24:25] op_sel_hi:[1,0,1]
v_pk_fma_f32 v[26:27],v[250:251],v[232:233],v[26:27] op_sel_hi:[1,0,1]
v_accvgpr_read_b32 v248,a28
v_accvgpr_read_b32 v249,a29
v_accvgpr_read_b32 v250,a30
v_accvgpr_read_b32 v251,a31
v_pk_mul_f32 v[28:29],v[28:29],v[236:237] op_sel_hi:[1,0]
v_pk_mul_f32 v[30:31],v[30:31],v[236:237] op_sel_hi:[1,0]
v_pk_fma_f32 v[28:29],v[248:249],v[232:233],v[28:29] op_sel_hi:[1,0,1]
)"
R"(v_pk_fma_f32 v[30:31],v[250:251],v[232:233],v[30:31] op_sel_hi:[1,0,1]
v_cvt_pk_bf16_f32 v140,v24,v25
v_cvt_pk_bf16_f32 v141,v26,v27
v_cvt_pk_bf16_f32 v142,v28,v29
v_cvt_pk_bf16_f32 v143,v30,v31
ds_write_b128 v231,v[140:143] offset:3072
ds_write_b128 v231,v[132:135] offset:1024
buffer_load_dwordx4 v252,s[32:35],s95 offen offset:2048 lds
v_accvgpr_read_b32 v248,a64
v_accvgpr_read_b32 v249,a65
v_accvgpr_read_b32 v250,a66
v_accvgpr_read_b32 v251,a67
v_pk_mul_f32 v[64:65],v[64:65],v[238:239] op_sel_hi:[1,0]
v_pk_mul_f32 v[66:67],v[66:67],v[238:239] op_sel_hi:[1,0]
v_pk_fma_f32 v[64:65],v[248:249],v[234:235],v[64:65] op_sel_hi:[1,0,1]
v_pk_fma_f32 v[66:67],v[250:251],v[234:235],v[66:67] op_sel_hi:[1,0,1]
v_accvgpr_read_b32 v248,a68
v_accvgpr_read_b32 v249,a69
v_accvgpr_read_b32 v250,a70
v_accvgpr_read_b32 v251,a71
v_pk_mul_f32 v[68:69],v[68:69],v[238:239] op_sel_hi:[1,0]
v_pk_mul_f32 v[70:71],v[70:71],v[238:239] op_sel_hi:[1,0]
v_pk_fma_f32 v[68:69],v[248:249],v[234:235],v[68:69] op_sel_hi:[1,0,1]
v_pk_fma_f32 v[70:71],v[250:251],v[234:235],v[70:71] op_sel_hi:[1,0,1]
v_cvt_pk_bf16_f32 v160,v64,v65
v_cvt_pk_bf16_f32 v161,v66,v67
v_cvt_pk_bf16_f32 v162,v68,v69
v_cvt_pk_bf16_f32 v163,v70,v71
ds_write_b128 v231,v[160:163] offset:4096
buffer_load_dwordx4 v252,s[36:39],s95 offen offset:3072 lds
v_accvgpr_read_b32 v248,a72
v_accvgpr_read_b32 v249,a73
v_accvgpr_read_b32 v250,a74
v_accvgpr_read_b32 v251,a75
v_pk_mul_f32 v[72:73],v[72:73],v[238:239] op_sel_hi:[1,0]
v_pk_mul_f32 v[74:75],v[74:75],v[238:239] op_sel_hi:[1,0]
v_pk_fma_f32 v[72:73],v[248:249],v[234:235],v[72:73] op_sel_hi:[1,0,1]
v_pk_fma_f32 v[74:75],v[250:251],v[234:235],v[74:75] op_sel_hi:[1,0,1]
v_accvgpr_read_b32 v248,a76
v_accvgpr_read_b32 v249,a77
v_accvgpr_read_b32 v250,a78
v_accvgpr_read_b32 v251,a79
v_pk_mul_f32 v[76:77],v[76:77],v[238:239] op_sel_hi:[1,0]
v_pk_mul_f32 v[78:79],v[78:79],v[238:239] op_sel_hi:[1,0]
v_pk_fma_f32 v[76:77],v[248:249],v[234:235],v[76:77] op_sel_hi:[1,0,1]
v_pk_fma_f32 v[78:79],v[250:251],v[234:235],v[78:79] op_sel_hi:[1,0,1]
v_cvt_pk_bf16_f32 v164,v72,v73
v_cvt_pk_bf16_f32 v165,v74,v75
v_cvt_pk_bf16_f32 v166,v76,v77
v_cvt_pk_bf16_f32 v167,v78,v79
ds_write_b128 v231,v[164:167] offset:5120
s_add_u32 m0,s92,0xc000
s_mov_b32 s95,0x38080
buffer_load_dwordx4 v252,s[24:27],s95 offen lds
v_accvgpr_read_b32 v248,a80
v_accvgpr_read_b32 v249,a81
v_accvgpr_read_b32 v250,a82
v_accvgpr_read_b32 v251,a83
v_pk_mul_f32 v[80:81],v[80:81],v[238:239] op_sel_hi:[1,0]
v_pk_mul_f32 v[82:83],v[82:83],v[238:239] op_sel_hi:[1,0]
v_pk_fma_f32 v[80:81],v[248:249],v[234:235],v[80:81] op_sel_hi:[1,0,1]
v_pk_fma_f32 v[82:83],v[250:251],v[234:235],v[82:83] op_sel_hi:[1,0,1]
v_accvgpr_read_b32 v248,a84
v_accvgpr_read_b32 v249,a85
v_accvgpr_read_b32 v250,a86
v_accvgpr_read_b32 v251,a87
v_pk_mul_f32 v[84:85],v[84:85],v[238:239] op_sel_hi:[1,0]
v_pk_mul_f32 v[86:87],v[86:87],v[238:239] op_sel_hi:[1,0]
v_pk_fma_f32 v[84:85],v[248:249],v[234:235],v[84:85] op_sel_hi:[1,0,1]
v_pk_fma_f32 v[86:87],v[250:251],v[234:235],v[86:87] op_sel_hi:[1,0,1]
v_cvt_pk_bf16_f32 v168,v80,v81
v_cvt_pk_bf16_f32 v169,v82,v83
v_cvt_pk_bf16_f32 v170,v84,v85
v_cvt_pk_bf16_f32 v171,v86,v87
ds_write_b128 v231,v[168:171] offset:6144
buffer_load_dwordx4 v252,s[28:31],s95 offen offset:1024 lds
v_accvgpr_read_b32 v248,a88
v_accvgpr_read_b32 v249,a89
v_accvgpr_read_b32 v250,a90
v_accvgpr_read_b32 v251,a91
v_pk_mul_f32 v[88:89],v[88:89],v[238:239] op_sel_hi:[1,0]
v_pk_mul_f32 v[90:91],v[90:91],v[238:239] op_sel_hi:[1,0]
v_pk_fma_f32 v[88:89],v[248:249],v[234:235],v[88:89] op_sel_hi:[1,0,1]
v_pk_fma_f32 v[90:91],v[250:251],v[234:235],v[90:91] op_sel_hi:[1,0,1]
v_accvgpr_read_b32 v248,a92
v_accvgpr_read_b32 v249,a93
v_accvgpr_read_b32 v250,a94
v_accvgpr_read_b32 v251,a95
v_pk_mul_f32 v[92:93],v[92:93],v[238:239] op_sel_hi:[1,0]
v_pk_mul_f32 v[94:95],v[94:95],v[238:239] op_sel_hi:[1,0]
v_pk_fma_f32 v[92:93],v[248:249],v[234:235],v[92:93] op_sel_hi:[1,0,1]
v_pk_fma_f32 v[94:95],v[250:251],v[234:235],v[94:95] op_sel_hi:[1,0,1]
v_cvt_pk_bf16_f32 v172,v88,v89
v_cvt_pk_bf16_f32 v173,v90,v91
v_cvt_pk_bf16_f32 v174,v92,v93
v_cvt_pk_bf16_f32 v175,v94,v95
ds_write_b128 v231,v[172:175] offset:7168
s_waitcnt vmcnt(6) lgkmcnt(0)
s_barrier
v_mov_b32 v229,1
buffer_store_dword v229,off,s[88:91],s79 sc1
ds_read_b128 a[128:131],v228
ds_read_b128 a[132:135],v228 offset:1024
ds_read_b128 a[136:139],v228 offset:2048
ds_read_b128 a[140:143],v228 offset:3072
ds_read_b128 a[160:163],v228 offset:4096
ds_read_b128 a[164:167],v228 offset:5120
ds_read_b128 a[168:171],v228 offset:6144
ds_read_b128 a[172:175],v228 offset:7168
ds_read_b128 a[192:195],v225
ds_read_b128 a[196:199],v225 offset:1024
ds_read_b128 a[200:203],v225 offset:2048
ds_read_b128 a[204:207],v225 offset:3072
ds_read_b128 a[224:227],v225 offset:4096
ds_read_b128 a[228:231],v225 offset:5120
ds_read_b128 a[232:235],v225 offset:6144
ds_read_b128 a[236:239],v225 offset:7168
ds_read_b128 a[0:3],v224
ds_read_b128 a[4:7],v224 offset:1024
ds_read_b128 a[8:11],v224 offset:2048
ds_read_b128 a[12:15],v224 offset:3072
ds_read_b128 a[64:67],v224 offset:4096
ds_read_b128 a[68:71],v224 offset:5120
ds_read_b128 a[72:75],v224 offset:6144
ds_read_b128 a[76:79],v224 offset:7168
ds_read_b64_tr_b16 v[192:193],v240
ds_read_b64_tr_b16 v[194:195],v240 offset:2048
ds_read_b64_tr_b16 v[196:197],v240 offset:4096
ds_read_b64_tr_b16 v[198:199],v240 offset:6144
ds_read_b64_tr_b16 v[200:201],v240 offset:8192
ds_read_b64_tr_b16 v[202:203],v240 offset:10240
ds_read_b64_tr_b16 v[204:205],v240 offset:12288
ds_read_b64_tr_b16 v[206:207],v240 offset:14336
buffer_load_dwordx4 v252,s[32:35],s95 offen offset:2048 lds
v_accvgpr_read_b32 v248,a32
v_accvgpr_read_b32 v249,a33
v_accvgpr_read_b32 v250,a34
v_accvgpr_read_b32 v251,a35
v_pk_mul_f32 v[32:33],v[32:33],v[236:237] op_sel_hi:[1,0]
v_pk_mul_f32 v[34:35],v[34:35],v[236:237] op_sel_hi:[1,0]
v_pk_fma_f32 v[32:33],v[248:249],v[232:233],v[32:33] op_sel_hi:[1,0,1]
v_pk_fma_f32 v[34:35],v[250:251],v[232:233],v[34:35] op_sel_hi:[1,0,1]
v_accvgpr_read_b32 v248,a36
v_accvgpr_read_b32 v249,a37
buffer_load_dwordx4 v252,s[36:39],s95 offen offset:3072 lds
v_accvgpr_read_b32 v250,a38
v_accvgpr_read_b32 v251,a39
v_pk_mul_f32 v[36:37],v[36:37],v[236:237] op_sel_hi:[1,0]
v_pk_mul_f32 v[38:39],v[38:39],v[236:237] op_sel_hi:[1,0]
v_pk_fma_f32 v[36:37],v[248:249],v[232:233],v[36:37] op_sel_hi:[1,0,1]
v_pk_fma_f32 v[38:39],v[250:251],v[232:233],v[38:39] op_sel_hi:[1,0,1]
v_cvt_pk_bf16_f32 v144,v32,v33
v_cvt_pk_bf16_f32 v145,v34,v35
v_cvt_pk_bf16_f32 v146,v36,v37
v_cvt_pk_bf16_f32 v147,v38,v39
ds_write_b128 v231,v[144:147] offset:32768
s_add_u32 m0,s92,0x10000
s_mov_b32 s95,0x1c000
buffer_load_dwordx4 v252,s[24:27],s95 offen lds
v_accvgpr_read_b32 v248,a40
v_accvgpr_read_b32 v249,a41
v_accvgpr_read_b32 v250,a42
v_accvgpr_read_b32 v251,a43
v_pk_mul_f32 v[40:41],v[40:41],v[236:237] op_sel_hi:[1,0]
v_pk_mul_f32 v[42:43],v[42:43],v[236:237] op_sel_hi:[1,0]
v_pk_fma_f32 v[40:41],v[248:249],v[232:233],v[40:41] op_sel_hi:[1,0,1]
v_pk_fma_f32 v[42:43],v[250:251],v[232:233],v[42:43] op_sel_hi:[1,0,1]
v_accvgpr_read_b32 v248,a44
v_accvgpr_read_b32 v249,a45
buffer_load_dwordx4 v252,s[28:31],s95 offen offset:1024 lds
v_accvgpr_read_b32 v250,a46
v_accvgpr_read_b32 v251,a47
v_pk_mul_f32 v[44:45],v[44:45],v[236:237] op_sel_hi:[1,0]
v_pk_mul_f32 v[46:47],v[46:47],v[236:237] op_sel_hi:[1,0]
v_pk_fma_f32 v[44:45],v[248:249],v[232:233],v[44:45] op_sel_hi:[1,0,1]
v_pk_fma_f32 v[46:47],v[250:251],v[232:233],v[46:47] op_sel_hi:[1,0,1]
v_cvt_pk_bf16_f32 v148,v40,v41
v_cvt_pk_bf16_f32 v149,v42,v43
v_cvt_pk_bf16_f32 v150,v44,v45
v_cvt_pk_bf16_f32 v151,v46,v47
ds_write_b128 v231,v[148:151] offset:33792
buffer_load_dwordx4 v252,s[32:35],s95 offen offset:2048 lds
v_accvgpr_read_b32 v248,a48
v_accvgpr_read_b32 v249,a49
v_accvgpr_read_b32 v250,a50
v_accvgpr_read_b32 v251,a51
v_pk_mul_f32 v[48:49],v[48:49],v[236:237] op_sel_hi:[1,0]
v_pk_mul_f32 v[50:51],v[50:51],v[236:237] op_sel_hi:[1,0]
v_pk_fma_f32 v[48:49],v[248:249],v[232:233],v[48:49] op_sel_hi:[1,0,1]
v_pk_fma_f32 v[50:51],v[250:251],v[232:233],v[50:51] op_sel_hi:[1,0,1]
v_accvgpr_read_b32 v248,a52
v_accvgpr_read_b32 v249,a53
v_accvgpr_read_b32 v250,a54
v_accvgpr_read_b32 v251,a55
v_pk_mul_f32 v[52:53],v[52:53],v[236:237] op_sel_hi:[1,0]
v_pk_mul_f32 v[54:55],v[54:55],v[236:237] op_sel_hi:[1,0]
v_pk_fma_f32 v[52:53],v[248:249],v[232:233],v[52:53] op_sel_hi:[1,0,1]
v_pk_fma_f32 v[54:55],v[250:251],v[232:233],v[54:55] op_sel_hi:[1,0,1]
v_cvt_pk_bf16_f32 v152,v48,v49
v_cvt_pk_bf16_f32 v153,v50,v51
)"
R"(v_cvt_pk_bf16_f32 v154,v52,v53
v_cvt_pk_bf16_f32 v155,v54,v55
ds_write_b128 v231,v[152:155] offset:34816
buffer_load_dwordx4 v252,s[36:39],s95 offen offset:3072 lds
v_accvgpr_read_b32 v248,a56
v_accvgpr_read_b32 v249,a57
v_accvgpr_read_b32 v250,a58
v_accvgpr_read_b32 v251,a59
v_pk_mul_f32 v[56:57],v[56:57],v[236:237] op_sel_hi:[1,0]
v_pk_mul_f32 v[58:59],v[58:59],v[236:237] op_sel_hi:[1,0]
v_pk_fma_f32 v[56:57],v[248:249],v[232:233],v[56:57] op_sel_hi:[1,0,1]
v_pk_fma_f32 v[58:59],v[250:251],v[232:233],v[58:59] op_sel_hi:[1,0,1]
v_accvgpr_read_b32 v248,a60
v_accvgpr_read_b32 v249,a61
v_accvgpr_read_b32 v250,a62
v_accvgpr_read_b32 v251,a63
v_pk_mul_f32 v[60:61],v[60:61],v[236:237] op_sel_hi:[1,0]
v_pk_mul_f32 v[62:63],v[62:63],v[236:237] op_sel_hi:[1,0]
v_pk_fma_f32 v[60:61],v[248:249],v[232:233],v[60:61] op_sel_hi:[1,0,1]
v_pk_fma_f32 v[62:63],v[250:251],v[232:233],v[62:63] op_sel_hi:[1,0,1]
v_cvt_pk_bf16_f32 v156,v56,v57
v_cvt_pk_bf16_f32 v157,v58,v59
v_cvt_pk_bf16_f32 v158,v60,v61
v_cvt_pk_bf16_f32 v159,v62,v63
ds_write_b128 v231,v[156:159] offset:35840
s_add_u32 m0,s92,0x14000
s_mov_b32 s95,0x54000
buffer_load_dwordx4 v252,s[24:27],s95 offen lds
v_accvgpr_read_b32 v248,a96
v_accvgpr_read_b32 v249,a97
v_accvgpr_read_b32 v250,a98
v_accvgpr_read_b32 v251,a99
v_pk_mul_f32 v[96:97],v[96:97],v[238:239] op_sel_hi:[1,0]
v_pk_mul_f32 v[98:99],v[98:99],v[238:239] op_sel_hi:[1,0]
v_pk_fma_f32 v[96:97],v[248:249],v[234:235],v[96:97] op_sel_hi:[1,0,1]
v_pk_fma_f32 v[98:99],v[250:251],v[234:235],v[98:99] op_sel_hi:[1,0,1]
v_accvgpr_read_b32 v248,a100
v_accvgpr_read_b32 v249,a101
v_accvgpr_read_b32 v250,a102
v_accvgpr_read_b32 v251,a103
v_pk_mul_f32 v[100:101],v[100:101],v[238:239] op_sel_hi:[1,0]
v_pk_mul_f32 v[102:103],v[102:103],v[238:239] op_sel_hi:[1,0]
v_pk_fma_f32 v[100:101],v[248:249],v[234:235],v[100:101] op_sel_hi:[1,0,1]
v_pk_fma_f32 v[102:103],v[250:251],v[234:235],v[102:103] op_sel_hi:[1,0,1]
v_cvt_pk_bf16_f32 v176,v96,v97
v_cvt_pk_bf16_f32 v177,v98,v99
v_cvt_pk_bf16_f32 v178,v100,v101
v_cvt_pk_bf16_f32 v179,v102,v103
ds_write_b128 v231,v[176:179] offset:36864
buffer_load_dwordx4 v252,s[28:31],s95 offen offset:1024 lds
v_accvgpr_read_b32 v248,a104
v_accvgpr_read_b32 v249,a105
v_accvgpr_read_b32 v250,a106
v_accvgpr_read_b32 v251,a107
v_pk_mul_f32 v[104:105],v[104:105],v[238:239] op_sel_hi:[1,0]
v_pk_mul_f32 v[106:107],v[106:107],v[238:239] op_sel_hi:[1,0]
v_pk_fma_f32 v[104:105],v[248:249],v[234:235],v[104:105] op_sel_hi:[1,0,1]
v_pk_fma_f32 v[106:107],v[250:251],v[234:235],v[106:107] op_sel_hi:[1,0,1]
v_accvgpr_read_b32 v248,a108
v_accvgpr_read_b32 v249,a109
v_accvgpr_read_b32 v250,a110
v_accvgpr_read_b32 v251,a111
v_pk_mul_f32 v[108:109],v[108:109],v[238:239] op_sel_hi:[1,0]
v_pk_mul_f32 v[110:111],v[110:111],v[238:239] op_sel_hi:[1,0]
v_pk_fma_f32 v[108:109],v[248:249],v[234:235],v[108:109] op_sel_hi:[1,0,1]
v_pk_fma_f32 v[110:111],v[250:251],v[234:235],v[110:111] op_sel_hi:[1,0,1]
v_cvt_pk_bf16_f32 v180,v104,v105
v_cvt_pk_bf16_f32 v181,v106,v107
v_cvt_pk_bf16_f32 v182,v108,v109
v_cvt_pk_bf16_f32 v183,v110,v111
ds_write_b128 v231,v[180:183] offset:37888
buffer_load_dwordx4 v252,s[32:35],s95 offen offset:2048 lds
v_accvgpr_read_b32 v248,a112
v_accvgpr_read_b32 v249,a113
v_accvgpr_read_b32 v250,a114
v_accvgpr_read_b32 v251,a115
v_pk_mul_f32 v[112:113],v[112:113],v[238:239] op_sel_hi:[1,0]
v_pk_mul_f32 v[114:115],v[114:115],v[238:239] op_sel_hi:[1,0]
v_pk_fma_f32 v[112:113],v[248:249],v[234:235],v[112:113] op_sel_hi:[1,0,1]
v_pk_fma_f32 v[114:115],v[250:251],v[234:235],v[114:115] op_sel_hi:[1,0,1]
v_accvgpr_read_b32 v248,a116
v_accvgpr_read_b32 v249,a117
v_accvgpr_read_b32 v250,a118
v_accvgpr_read_b32 v251,a119
v_pk_mul_f32 v[116:117],v[116:117],v[238:239] op_sel_hi:[1,0]
v_pk_mul_f32 v[118:119],v[118:119],v[238:239] op_sel_hi:[1,0]
v_pk_fma_f32 v[116:117],v[248:249],v[234:235],v[116:117] op_sel_hi:[1,0,1]
v_pk_fma_f32 v[118:119],v[250:251],v[234:235],v[118:119] op_sel_hi:[1,0,1]
v_cvt_pk_bf16_f32 v184,v112,v113
v_cvt_pk_bf16_f32 v185,v114,v115
v_cvt_pk_bf16_f32 v186,v116,v117
v_cvt_pk_bf16_f32 v187,v118,v119
ds_write_b128 v231,v[184:187] offset:38912
buffer_load_dwordx4 v252,s[36:39],s95 offen offset:3072 lds
v_accvgpr_read_b32 v248,a120
v_accvgpr_read_b32 v249,a121
v_accvgpr_read_b32 v250,a122
v_accvgpr_read_b32 v251,a123
v_pk_mul_f32 v[120:121],v[120:121],v[238:239] op_sel_hi:[1,0]
v_pk_mul_f32 v[122:123],v[122:123],v[238:239] op_sel_hi:[1,0]
v_pk_fma_f32 v[120:121],v[248:249],v[234:235],v[120:121] op_sel_hi:[1,0,1]
v_pk_fma_f32 v[122:123],v[250:251],v[234:235],v[122:123] op_sel_hi:[1,0,1]
v_accvgpr_read_b32 v248,a124
v_accvgpr_read_b32 v249,a125
v_accvgpr_read_b32 v250,a126
v_accvgpr_read_b32 v251,a127
v_pk_mul_f32 v[124:125],v[124:125],v[238:239] op_sel_hi:[1,0]
v_pk_mul_f32 v[126:127],v[126:127],v[238:239] op_sel_hi:[1,0]
v_pk_fma_f32 v[124:125],v[248:249],v[234:235],v[124:125] op_sel_hi:[1,0,1]
v_pk_fma_f32 v[126:127],v[250:251],v[234:235],v[126:127] op_sel_hi:[1,0,1]
v_cvt_pk_bf16_f32 v188,v120,v121
v_cvt_pk_bf16_f32 v189,v122,v123
v_cvt_pk_bf16_f32 v190,v124,v125
v_cvt_pk_bf16_f32 v191,v126,v127
ds_write_b128 v231,v[188:191] offset:39936
s_waitcnt lgkmcnt(8)
v_mfma_f32_16x16x32_bf16 v[0:3],v[192:195],v[128:131],0
s_add_u32 s99,s96,0x0
ds_read_b64_tr_b16 v[208:209],v241
v_mfma_f32_16x16x32_bf16 v[0:3],v[196:199],v[132:135],v[0:3]
s_mov_b64 s[56:57],s[88:89]
ds_read_b64_tr_b16 v[210:211],v241 offset:2048
v_mfma_f32_16x16x32_bf16 v[0:3],v[200:203],v[136:139],v[0:3]
s_add_u32 s98,s82,14336
ds_read_b64_tr_b16 v[212:213],v241 offset:4096
v_mfma_f32_16x16x32_bf16 v[0:3],v[204:207],v[140:143],v[0:3]
ds_read_b64_tr_b16 v[214:215],v241 offset:6144
v_mfma_f32_16x16x32_bf16 v[4:7],v[192:195],v[160:163],0
ds_read_b64_tr_b16 v[216:217],v241 offset:8192
v_mfma_f32_16x16x32_bf16 v[4:7],v[196:199],v[164:167],v[4:7]
ds_read_b64_tr_b16 v[218:219],v241 offset:10240
v_mfma_f32_16x16x32_bf16 v[4:7],v[200:203],v[168:171],v[4:7]
ds_read_b64_tr_b16 v[220:221],v241 offset:12288
v_mfma_f32_16x16x32_bf16 v[4:7],v[204:207],v[172:175],v[4:7]
ds_read_b64_tr_b16 v[222:223],v241 offset:14336
v_mfma_f32_16x16x32_bf16 v[8:11],v[192:195],a[128:131],0
buffer_load_dwordx4 v[232:235],v227,s[44:47],s82 offen
v_mfma_f32_16x16x32_bf16 v[8:11],v[196:199],a[132:135],v[8:11]
buffer_load_dwordx4 v[236:239],v227,s[44:47],s98 offen
v_mfma_f32_16x16x32_bf16 v[8:11],v[200:203],a[136:139],v[8:11]
v_mfma_f32_16x16x32_bf16 v[8:11],v[204:207],a[140:143],v[8:11]
v_mfma_f32_16x16x32_bf16 v[12:15],v[192:195],a[160:163],0
v_mfma_f32_16x16x32_bf16 v[12:15],v[196:199],a[164:167],v[12:15]
v_mfma_f32_16x16x32_bf16 v[12:15],v[200:203],a[168:171],v[12:15]
v_mfma_f32_16x16x32_bf16 v[12:15],v[204:207],a[172:175],v[12:15]
s_waitcnt vmcnt(10) lgkmcnt(0)
v_mfma_f32_16x16x32_bf16 v[0:3],v[208:211],a[192:195],v[0:3]
s_barrier
ds_read_b64_tr_b16 v[192:193],v240 offset:32768
s_add_u32 m0,s92,0x18000
v_mfma_f32_16x16x32_bf16 v[0:3],v[212:215],a[196:199],v[0:3]
s_mov_b32 s95,0x1c080
ds_read_b64_tr_b16 v[194:195],v240 offset:34816
ds_read_b128 a[144:147],v228 offset:32768
v_mfma_f32_16x16x32_bf16 v[0:3],v[216:219],a[200:203],v[0:3]
buffer_load_dwordx4 v252,s[24:27],s95 offen lds
v_mfma_f32_16x16x32_bf16 v[0:3],v[220:223],a[204:207],v[0:3]
ds_read_b64_tr_b16 v[196:197],v240 offset:36864
ds_read_b64_tr_b16 v[198:199],v240 offset:38912
v_mfma_f32_16x16x32_bf16 v[4:7],v[208:211],a[224:227],v[4:7]
ds_read_b64_tr_b16 v[200:201],v240 offset:40960
ds_read_b128 a[148:151],v228 offset:33792
v_mfma_f32_16x16x32_bf16 v[4:7],v[212:215],a[228:231],v[4:7]
ds_read_b64_tr_b16 v[202:203],v240 offset:43008
ds_read_b128 a[152:155],v228 offset:34816
v_mfma_f32_16x16x32_bf16 v[4:7],v[216:219],a[232:235],v[4:7]
buffer_load_dwordx4 v252,s[28:31],s95 offen offset:1024 lds
v_mfma_f32_16x16x32_bf16 v[4:7],v[220:223],a[236:239],v[4:7]
ds_read_b64_tr_b16 v[204:205],v240 offset:45056
ds_read_b64_tr_b16 v[206:207],v240 offset:47104
v_mfma_f32_16x16x32_bf16 v[8:11],v[208:211],a[0:3],v[8:11]
ds_read_b128 a[156:159],v228 offset:35840
ds_read_b128 a[176:179],v228 offset:36864
v_mfma_f32_16x16x32_bf16 v[8:11],v[212:215],a[4:7],v[8:11]
ds_read_b128 a[180:183],v228 offset:37888
ds_read_b128 a[184:187],v228 offset:38912
v_mfma_f32_16x16x32_bf16 v[8:11],v[216:219],a[8:11],v[8:11]
buffer_load_dwordx4 v252,s[32:35],s95 offen offset:2048 lds
v_mfma_f32_16x16x32_bf16 v[8:11],v[220:223],a[12:15],v[8:11]
ds_read_b128 a[188:191],v228 offset:39936
ds_read_b128 a[208:211],v225 offset:32768
v_mfma_f32_16x16x32_bf16 v[12:15],v[208:211],a[64:67],v[12:15]
ds_read_b128 a[212:215],v225 offset:33792
ds_read_b128 a[216:219],v225 offset:34816
v_mfma_f32_16x16x32_bf16 v[12:15],v[212:215],a[68:71],v[12:15]
ds_read_b128 a[220:223],v225 offset:35840
ds_read_b128 a[240:243],v225 offset:36864
v_mfma_f32_16x16x32_bf16 v[12:15],v[216:219],a[72:75],v[12:15]
buffer_load_dwordx4 v252,s[36:39],s95 offen offset:3072 lds
v_mfma_f32_16x16x32_bf16 v[12:15],v[220:223],a[76:79],v[12:15]
s_waitcnt lgkmcnt(10)
ds_read_b128 a[244:247],v225 offset:37888
ds_read_b128 a[248:251],v225 offset:38912
v_mfma_f32_16x16x32_bf16 v[16:19],v[192:195],v[128:131],0
s_add_u32 m0,s92,0x1c000
ds_read_b64_tr_b16 v[208:209],v241 offset:32768
s_mov_b32 s95,0x54080
v_mfma_f32_16x16x32_bf16 v[16:19],v[196:199],v[132:135],v[16:19]
ds_read_b64_tr_b16 v[210:211],v241 offset:34816
ds_read_b128 a[252:255],v225 offset:39936
)"
R"(v_mfma_f32_16x16x32_bf16 v[16:19],v[200:203],v[136:139],v[16:19]
buffer_load_dwordx4 v252,s[24:27],s95 offen lds
v_mfma_f32_16x16x32_bf16 v[16:19],v[204:207],v[140:143],v[16:19]
ds_read_b64_tr_b16 v[212:213],v241 offset:36864
ds_read_b64_tr_b16 v[214:215],v241 offset:38912
v_mfma_f32_16x16x32_bf16 v[20:23],v[192:195],v[160:163],0
ds_read_b64_tr_b16 v[216:217],v241 offset:40960
ds_read_b128 a[16:19],v224 offset:32768
v_mfma_f32_16x16x32_bf16 v[20:23],v[196:199],v[164:167],v[20:23]
ds_read_b64_tr_b16 v[218:219],v241 offset:43008
ds_read_b128 a[20:23],v224 offset:33792
v_mfma_f32_16x16x32_bf16 v[20:23],v[200:203],v[168:171],v[20:23]
buffer_load_dwordx4 v252,s[28:31],s95 offen offset:1024 lds
v_mfma_f32_16x16x32_bf16 v[20:23],v[204:207],v[172:175],v[20:23]
ds_read_b64_tr_b16 v[220:221],v241 offset:45056
ds_read_b64_tr_b16 v[222:223],v241 offset:47104
v_mfma_f32_16x16x32_bf16 v[24:27],v[192:195],a[128:131],0
ds_read_b128 a[24:27],v224 offset:34816
ds_read_b128 a[28:31],v224 offset:35840
v_mfma_f32_16x16x32_bf16 v[24:27],v[196:199],a[132:135],v[24:27]
ds_read_b128 a[80:83],v224 offset:36864
ds_read_b128 a[84:87],v224 offset:37888
v_mfma_f32_16x16x32_bf16 v[24:27],v[200:203],a[136:139],v[24:27]
buffer_load_dwordx4 v252,s[32:35],s95 offen offset:2048 lds
v_mfma_f32_16x16x32_bf16 v[24:27],v[204:207],a[140:143],v[24:27]
ds_read_b128 a[88:91],v224 offset:38912
ds_read_b128 a[92:95],v224 offset:39936
v_mfma_f32_16x16x32_bf16 v[28:31],v[192:195],a[160:163],0
v_mfma_f32_16x16x32_bf16 v[28:31],v[196:199],a[164:167],v[28:31]
v_mfma_f32_16x16x32_bf16 v[28:31],v[200:203],a[168:171],v[28:31]
buffer_load_dwordx4 v252,s[36:39],s95 offen offset:3072 lds
v_mfma_f32_16x16x32_bf16 v[28:31],v[204:207],a[172:175],v[28:31]
s_waitcnt vmcnt(10) lgkmcnt(0)
v_mfma_f32_16x16x32_bf16 v[16:19],v[208:211],a[192:195],v[16:19]
s_barrier
ds_read_b64_tr_b16 v[192:193],v242
s_add_u32 m0,s92,0x0
v_mfma_f32_16x16x32_bf16 v[16:19],v[212:215],a[196:199],v[16:19]
s_mov_b32 s95,0x100
ds_read_b64_tr_b16 v[194:195],v242 offset:2048
v_mfma_f32_16x16x32_bf16 v[16:19],v[216:219],a[200:203],v[16:19]
buffer_load_dwordx4 v252,s[24:27],s95 offen lds
v_mfma_f32_16x16x32_bf16 v[16:19],v[220:223],a[204:207],v[16:19]
ds_read_b64_tr_b16 v[196:197],v242 offset:4096
ds_read_b64_tr_b16 v[198:199],v242 offset:6144
v_mfma_f32_16x16x32_bf16 v[20:23],v[208:211],a[224:227],v[20:23]
ds_read_b64_tr_b16 v[200:201],v242 offset:8192
v_mfma_f32_16x16x32_bf16 v[20:23],v[212:215],a[228:231],v[20:23]
ds_read_b64_tr_b16 v[202:203],v242 offset:10240
v_mfma_f32_16x16x32_bf16 v[20:23],v[216:219],a[232:235],v[20:23]
buffer_load_dwordx4 v252,s[28:31],s95 offen offset:1024 lds
v_mfma_f32_16x16x32_bf16 v[20:23],v[220:223],a[236:239],v[20:23]
ds_read_b64_tr_b16 v[204:205],v242 offset:12288
ds_read_b64_tr_b16 v[206:207],v242 offset:14336
v_mfma_f32_16x16x32_bf16 v[24:27],v[208:211],a[0:3],v[24:27]
v_mfma_f32_16x16x32_bf16 v[24:27],v[212:215],a[4:7],v[24:27]
v_mfma_f32_16x16x32_bf16 v[24:27],v[216:219],a[8:11],v[24:27]
buffer_load_dwordx4 v252,s[32:35],s95 offen offset:2048 lds
v_mfma_f32_16x16x32_bf16 v[24:27],v[220:223],a[12:15],v[24:27]
v_mfma_f32_16x16x32_bf16 v[28:31],v[208:211],a[64:67],v[28:31]
v_mfma_f32_16x16x32_bf16 v[28:31],v[212:215],a[68:71],v[28:31]
v_mfma_f32_16x16x32_bf16 v[28:31],v[216:219],a[72:75],v[28:31]
buffer_load_dwordx4 v252,s[36:39],s95 offen offset:3072 lds
v_mfma_f32_16x16x32_bf16 v[28:31],v[220:223],a[76:79],v[28:31]
s_waitcnt lgkmcnt(0)
v_mfma_f32_16x16x32_bf16 v[0:3],v[192:195],v[144:147],v[0:3]
s_add_u32 m0,s92,0x4000
ds_read_b64_tr_b16 v[208:209],v243
s_mov_b32 s95,0x38100
v_mfma_f32_16x16x32_bf16 v[0:3],v[196:199],v[148:151],v[0:3]
ds_read_b64_tr_b16 v[210:211],v243 offset:2048
v_mfma_f32_16x16x32_bf16 v[0:3],v[200:203],v[152:155],v[0:3]
buffer_load_dwordx4 v252,s[24:27],s95 offen lds
v_mfma_f32_16x16x32_bf16 v[0:3],v[204:207],v[156:159],v[0:3]
ds_read_b64_tr_b16 v[212:213],v243 offset:4096
ds_read_b64_tr_b16 v[214:215],v243 offset:6144
v_mfma_f32_16x16x32_bf16 v[4:7],v[192:195],v[176:179],v[4:7]
ds_read_b64_tr_b16 v[216:217],v243 offset:8192
v_mfma_f32_16x16x32_bf16 v[4:7],v[196:199],v[180:183],v[4:7]
ds_read_b64_tr_b16 v[218:219],v243 offset:10240
v_mfma_f32_16x16x32_bf16 v[4:7],v[200:203],v[184:187],v[4:7]
buffer_load_dwordx4 v252,s[28:31],s95 offen offset:1024 lds
v_mfma_f32_16x16x32_bf16 v[4:7],v[204:207],v[188:191],v[4:7]
ds_read_b64_tr_b16 v[220:221],v243 offset:12288
ds_read_b64_tr_b16 v[222:223],v243 offset:14336
v_mfma_f32_16x16x32_bf16 v[8:11],v[192:195],a[144:147],v[8:11]
v_mfma_f32_16x16x32_bf16 v[8:11],v[196:199],a[148:151],v[8:11]
v_mfma_f32_16x16x32_bf16 v[8:11],v[200:203],a[152:155],v[8:11]
buffer_load_dwordx4 v252,s[32:35],s95 offen offset:2048 lds
v_mfma_f32_16x16x32_bf16 v[8:11],v[204:207],a[156:159],v[8:11]
v_mfma_f32_16x16x32_bf16 v[12:15],v[192:195],a[176:179],v[12:15]
v_mfma_f32_16x16x32_bf16 v[12:15],v[196:199],a[180:183],v[12:15]
v_mfma_f32_16x16x32_bf16 v[12:15],v[200:203],a[184:187],v[12:15]
buffer_load_dwordx4 v252,s[36:39],s95 offen offset:3072 lds
v_mfma_f32_16x16x32_bf16 v[12:15],v[204:207],a[188:191],v[12:15]
s_waitcnt vmcnt(8) lgkmcnt(0)
v_mfma_f32_16x16x32_bf16 v[0:3],v[208:211],a[208:211],v[0:3]
s_barrier
ds_read_b64_tr_b16 v[192:193],v242 offset:32768
s_add_u32 m0,s92,0x8000
v_mfma_f32_16x16x32_bf16 v[0:3],v[212:215],a[212:215],v[0:3]
s_mov_b32 s95,0x1c100
ds_read_b64_tr_b16 v[194:195],v242 offset:34816
v_mfma_f32_16x16x32_bf16 v[0:3],v[216:219],a[216:219],v[0:3]
buffer_load_dwordx4 v252,s[24:27],s95 offen lds
v_mfma_f32_16x16x32_bf16 v[0:3],v[220:223],a[220:223],v[0:3]
ds_read_b64_tr_b16 v[196:197],v242 offset:36864
ds_read_b64_tr_b16 v[198:199],v242 offset:38912
v_mfma_f32_16x16x32_bf16 v[4:7],v[208:211],a[240:243],v[4:7]
ds_read_b64_tr_b16 v[200:201],v242 offset:40960
v_mfma_f32_16x16x32_bf16 v[4:7],v[212:215],a[244:247],v[4:7]
ds_read_b64_tr_b16 v[202:203],v242 offset:43008
v_mfma_f32_16x16x32_bf16 v[4:7],v[216:219],a[248:251],v[4:7]
buffer_load_dwordx4 v252,s[28:31],s95 offen offset:1024 lds
v_mfma_f32_16x16x32_bf16 v[4:7],v[220:223],a[252:255],v[4:7]
ds_read_b64_tr_b16 v[204:205],v242 offset:45056
ds_read_b64_tr_b16 v[206:207],v242 offset:47104
v_mfma_f32_16x16x32_bf16 v[8:11],v[208:211],a[16:19],v[8:11]
v_cvt_pk_bf16_f32 v32,v0,v1
v_cvt_pk_bf16_f32 v33,v2,v3
v_mfma_f32_16x16x32_bf16 v[8:11],v[212:215],a[20:23],v[8:11]
v_mfma_f32_16x16x32_bf16 v[8:11],v[216:219],a[24:27],v[8:11]
buffer_load_dwordx4 v252,s[32:35],s95 offen offset:2048 lds
v_mfma_f32_16x16x32_bf16 v[8:11],v[220:223],a[28:31],v[8:11]
v_cvt_pk_bf16_f32 v34,v4,v5
v_cvt_pk_bf16_f32 v35,v6,v7
v_mfma_f32_16x16x32_bf16 v[12:15],v[208:211],a[80:83],v[12:15]
buffer_store_dwordx4 v[32:35],v255,s[88:91],s99 offen sc1
v_mfma_f32_16x16x32_bf16 v[12:15],v[212:215],a[84:87],v[12:15]
s_add_u32 s99,s93,0x0
v_mfma_f32_16x16x32_bf16 v[12:15],v[216:219],a[88:91],v[12:15]
buffer_load_dwordx4 v252,s[36:39],s95 offen offset:3072 lds
v_mfma_f32_16x16x32_bf16 v[12:15],v[220:223],a[92:95],v[12:15]
s_waitcnt lgkmcnt(0)
v_cvt_pk_bf16_f32 v36,v8,v9
v_cvt_pk_bf16_f32 v37,v10,v11
v_mfma_f32_16x16x32_bf16 v[16:19],v[192:195],v[144:147],v[16:19]
s_add_u32 m0,s92,0xc000
ds_read_b64_tr_b16 v[208:209],v243 offset:32768
s_mov_b32 s95,0x54100
v_mfma_f32_16x16x32_bf16 v[16:19],v[196:199],v[148:151],v[16:19]
ds_read_b64_tr_b16 v[210:211],v243 offset:34816
v_cvt_pk_bf16_f32 v38,v12,v13
v_mfma_f32_16x16x32_bf16 v[16:19],v[200:203],v[152:155],v[16:19]
buffer_load_dwordx4 v252,s[24:27],s95 offen lds
v_mfma_f32_16x16x32_bf16 v[16:19],v[204:207],v[156:159],v[16:19]
ds_read_b64_tr_b16 v[212:213],v243 offset:36864
ds_read_b64_tr_b16 v[214:215],v243 offset:38912
v_mfma_f32_16x16x32_bf16 v[20:23],v[192:195],v[176:179],v[20:23]
ds_read_b64_tr_b16 v[216:217],v243 offset:40960
v_cvt_pk_bf16_f32 v39,v14,v15
v_mfma_f32_16x16x32_bf16 v[20:23],v[196:199],v[180:183],v[20:23]
ds_read_b64_tr_b16 v[218:219],v243 offset:43008
v_mfma_f32_16x16x32_bf16 v[20:23],v[200:203],v[184:187],v[20:23]
buffer_load_dwordx4 v252,s[28:31],s95 offen offset:1024 lds
v_mfma_f32_16x16x32_bf16 v[20:23],v[204:207],v[188:191],v[20:23]
ds_read_b64_tr_b16 v[220:221],v243 offset:45056
ds_read_b64_tr_b16 v[222:223],v243 offset:47104
v_mfma_f32_16x16x32_bf16 v[24:27],v[192:195],a[144:147],v[24:27]
buffer_store_dwordx4 v[36:39],v255,s[88:91],s99 offen sc1
v_mfma_f32_16x16x32_bf16 v[24:27],v[196:199],a[148:151],v[24:27]
s_add_u32 s99,s96,0x1000
v_mfma_f32_16x16x32_bf16 v[24:27],v[200:203],a[152:155],v[24:27]
buffer_load_dwordx4 v252,s[32:35],s95 offen offset:2048 lds
v_mfma_f32_16x16x32_bf16 v[24:27],v[204:207],a[156:159],v[24:27]
v_mfma_f32_16x16x32_bf16 v[28:31],v[192:195],a[176:179],v[28:31]
v_mfma_f32_16x16x32_bf16 v[28:31],v[196:199],a[180:183],v[28:31]
v_mfma_f32_16x16x32_bf16 v[28:31],v[200:203],a[184:187],v[28:31]
buffer_load_dwordx4 v252,s[36:39],s95 offen offset:3072 lds
v_mfma_f32_16x16x32_bf16 v[28:31],v[204:207],a[188:191],v[28:31]
s_waitcnt vmcnt(10) lgkmcnt(0)
v_mfma_f32_16x16x32_bf16 v[16:19],v[208:211],a[208:211],v[16:19]
s_barrier
ds_read_b64_tr_b16 v[192:193],v240
s_add_u32 m0,s92,0x20000
v_mfma_f32_16x16x32_bf16 v[16:19],v[212:215],a[212:215],v[16:19]
s_mov_b32 s95,0x180
ds_read_b64_tr_b16 v[194:195],v240 offset:2048
v_mfma_f32_16x16x32_bf16 v[16:19],v[216:219],a[216:219],v[16:19]
buffer_load_dwordx4 v252,s[24:27],s95 offen lds
v_mfma_f32_16x16x32_bf16 v[16:19],v[220:223],a[220:223],v[16:19]
ds_read_b64_tr_b16 v[196:197],v240 offset:4096
ds_read_b64_tr_b16 v[198:199],v240 offset:6144
v_mfma_f32_16x16x32_bf16 v[20:23],v[208:211],a[240:243],v[20:23]
ds_read_b64_tr_b16 v[200:201],v240 offset:8192
v_mfma_f32_16x16x32_bf16 v[20:23],v[212:215],a[244:247],v[20:23]
ds_read_b64_tr_b16 v[202:203],v240 offset:10240
v_mfma_f32_16x16x32_bf16 v[20:23],v[216:219],a[248:251],v[20:23]
buffer_load_dwordx4 v252,s[28:31],s95 offen offset:1024 lds
v_mfma_f32_16x16x32_bf16 v[20:23],v[220:223],a[252:255],v[20:23]
ds_read_b64_tr_b16 v[204:205],v240 offset:12288
ds_read_b64_tr_b16 v[206:207],v240 offset:14336
v_mfma_f32_16x16x32_bf16 v[24:27],v[208:211],a[16:19],v[24:27]
v_cvt_pk_bf16_f32 v40,v16,v17
v_cvt_pk_bf16_f32 v41,v18,v19
v_mfma_f32_16x16x32_bf16 v[24:27],v[212:215],a[20:23],v[24:27]
v_mfma_f32_16x16x32_bf16 v[24:27],v[216:219],a[24:27],v[24:27]
buffer_load_dwordx4 v252,s[32:35],s95 offen offset:2048 lds
v_mfma_f32_16x16x32_bf16 v[24:27],v[220:223],a[28:31],v[24:27]
v_cvt_pk_bf16_f32 v42,v20,v21
)"
R"(v_cvt_pk_bf16_f32 v43,v22,v23
v_mfma_f32_16x16x32_bf16 v[28:31],v[208:211],a[80:83],v[28:31]
buffer_store_dwordx4 v[40:43],v255,s[88:91],s99 offen sc1
v_mfma_f32_16x16x32_bf16 v[28:31],v[212:215],a[84:87],v[28:31]
s_add_u32 s99,s93,0x1000
v_mfma_f32_16x16x32_bf16 v[28:31],v[216:219],a[88:91],v[28:31]
buffer_load_dwordx4 v252,s[36:39],s95 offen offset:3072 lds
v_mfma_f32_16x16x32_bf16 v[28:31],v[220:223],a[92:95],v[28:31]
s_waitcnt lgkmcnt(0)
v_cvt_pk_bf16_f32 v44,v24,v25
v_cvt_pk_bf16_f32 v45,v26,v27
v_mfma_f32_16x16x32_bf16 v[0:3],v[192:195],v[128:131],0
s_add_u32 m0,s92,0x24000
ds_read_b64_tr_b16 v[208:209],v241
s_mov_b32 s95,0x38180
v_mfma_f32_16x16x32_bf16 v[0:3],v[196:199],v[132:135],v[0:3]
ds_read_b64_tr_b16 v[210:211],v241 offset:2048
v_cvt_pk_bf16_f32 v46,v28,v29
v_mfma_f32_16x16x32_bf16 v[0:3],v[200:203],v[136:139],v[0:3]
buffer_load_dwordx4 v252,s[24:27],s95 offen lds
v_mfma_f32_16x16x32_bf16 v[0:3],v[204:207],v[140:143],v[0:3]
ds_read_b64_tr_b16 v[212:213],v241 offset:4096
ds_read_b64_tr_b16 v[214:215],v241 offset:6144
v_mfma_f32_16x16x32_bf16 v[4:7],v[192:195],v[160:163],0
ds_read_b64_tr_b16 v[216:217],v241 offset:8192
v_cvt_pk_bf16_f32 v47,v30,v31
v_mfma_f32_16x16x32_bf16 v[4:7],v[196:199],v[164:167],v[4:7]
ds_read_b64_tr_b16 v[218:219],v241 offset:10240
v_mfma_f32_16x16x32_bf16 v[4:7],v[200:203],v[168:171],v[4:7]
buffer_load_dwordx4 v252,s[28:31],s95 offen offset:1024 lds
v_mfma_f32_16x16x32_bf16 v[4:7],v[204:207],v[172:175],v[4:7]
ds_read_b64_tr_b16 v[220:221],v241 offset:12288
ds_read_b64_tr_b16 v[222:223],v241 offset:14336
v_mfma_f32_16x16x32_bf16 v[8:11],v[192:195],a[128:131],0
buffer_store_dwordx4 v[44:47],v255,s[88:91],s99 offen sc1
v_mfma_f32_16x16x32_bf16 v[8:11],v[196:199],a[132:135],v[8:11]
s_add_u32 s99,s96,0x2000
v_mfma_f32_16x16x32_bf16 v[8:11],v[200:203],a[136:139],v[8:11]
buffer_load_dwordx4 v252,s[32:35],s95 offen offset:2048 lds
v_mfma_f32_16x16x32_bf16 v[8:11],v[204:207],a[140:143],v[8:11]
v_mfma_f32_16x16x32_bf16 v[12:15],v[192:195],a[160:163],0
v_mfma_f32_16x16x32_bf16 v[12:15],v[196:199],a[164:167],v[12:15]
v_mfma_f32_16x16x32_bf16 v[12:15],v[200:203],a[168:171],v[12:15]
buffer_load_dwordx4 v252,s[36:39],s95 offen offset:3072 lds
v_mfma_f32_16x16x32_bf16 v[12:15],v[204:207],a[172:175],v[12:15]
s_waitcnt vmcnt(10) lgkmcnt(0)
v_mfma_f32_16x16x32_bf16 v[0:3],v[208:211],a[192:195],v[0:3]
s_barrier
ds_read_b64_tr_b16 v[192:193],v240 offset:32768
s_add_u32 m0,s92,0x10000
v_mfma_f32_16x16x32_bf16 v[0:3],v[212:215],a[196:199],v[0:3]
s_mov_b32 s95,0x1c180
ds_read_b64_tr_b16 v[194:195],v240 offset:34816
v_mfma_f32_16x16x32_bf16 v[0:3],v[216:219],a[200:203],v[0:3]
buffer_load_dwordx4 v252,s[24:27],s95 offen lds
v_mfma_f32_16x16x32_bf16 v[0:3],v[220:223],a[204:207],v[0:3]
ds_read_b64_tr_b16 v[196:197],v240 offset:36864
ds_read_b64_tr_b16 v[198:199],v240 offset:38912
v_mfma_f32_16x16x32_bf16 v[4:7],v[208:211],a[224:227],v[4:7]
ds_read_b64_tr_b16 v[200:201],v240 offset:40960
v_mfma_f32_16x16x32_bf16 v[4:7],v[212:215],a[228:231],v[4:7]
ds_read_b64_tr_b16 v[202:203],v240 offset:43008
v_mfma_f32_16x16x32_bf16 v[4:7],v[216:219],a[232:235],v[4:7]
buffer_load_dwordx4 v252,s[28:31],s95 offen offset:1024 lds
v_mfma_f32_16x16x32_bf16 v[4:7],v[220:223],a[236:239],v[4:7]
ds_read_b64_tr_b16 v[204:205],v240 offset:45056
ds_read_b64_tr_b16 v[206:207],v240 offset:47104
v_mfma_f32_16x16x32_bf16 v[8:11],v[208:211],a[0:3],v[8:11]
v_mfma_f32_16x16x32_bf16 v[8:11],v[212:215],a[4:7],v[8:11]
v_mfma_f32_16x16x32_bf16 v[8:11],v[216:219],a[8:11],v[8:11]
buffer_load_dwordx4 v252,s[32:35],s95 offen offset:2048 lds
v_mfma_f32_16x16x32_bf16 v[8:11],v[220:223],a[12:15],v[8:11]
v_mfma_f32_16x16x32_bf16 v[12:15],v[208:211],a[64:67],v[12:15]
v_mfma_f32_16x16x32_bf16 v[12:15],v[212:215],a[68:71],v[12:15]
v_mfma_f32_16x16x32_bf16 v[12:15],v[216:219],a[72:75],v[12:15]
buffer_load_dwordx4 v252,s[36:39],s95 offen offset:3072 lds
v_mfma_f32_16x16x32_bf16 v[12:15],v[220:223],a[76:79],v[12:15]
s_waitcnt lgkmcnt(0)
v_mfma_f32_16x16x32_bf16 v[0:3],v[192:195],v[144:147],v[0:3]
s_add_u32 m0,s92,0x14000
ds_read_b64_tr_b16 v[208:209],v241 offset:32768
s_mov_b32 s95,0x54180
v_mfma_f32_16x16x32_bf16 v[0:3],v[196:199],v[148:151],v[0:3]
ds_read_b64_tr_b16 v[210:211],v241 offset:34816
v_mfma_f32_16x16x32_bf16 v[0:3],v[200:203],v[152:155],v[0:3]
buffer_load_dwordx4 v252,s[24:27],s95 offen lds
v_mfma_f32_16x16x32_bf16 v[0:3],v[204:207],v[156:159],v[0:3]
ds_read_b64_tr_b16 v[212:213],v241 offset:36864
ds_read_b64_tr_b16 v[214:215],v241 offset:38912
v_mfma_f32_16x16x32_bf16 v[4:7],v[192:195],v[176:179],v[4:7]
ds_read_b64_tr_b16 v[216:217],v241 offset:40960
v_mfma_f32_16x16x32_bf16 v[4:7],v[196:199],v[180:183],v[4:7]
ds_read_b64_tr_b16 v[218:219],v241 offset:43008
v_mfma_f32_16x16x32_bf16 v[4:7],v[200:203],v[184:187],v[4:7]
buffer_load_dwordx4 v252,s[28:31],s95 offen offset:1024 lds
v_mfma_f32_16x16x32_bf16 v[4:7],v[204:207],v[188:191],v[4:7]
ds_read_b64_tr_b16 v[220:221],v241 offset:45056
ds_read_b64_tr_b16 v[222:223],v241 offset:47104
v_mfma_f32_16x16x32_bf16 v[8:11],v[192:195],a[144:147],v[8:11]
v_mfma_f32_16x16x32_bf16 v[8:11],v[196:199],a[148:151],v[8:11]
v_mfma_f32_16x16x32_bf16 v[8:11],v[200:203],a[152:155],v[8:11]
buffer_load_dwordx4 v252,s[32:35],s95 offen offset:2048 lds
v_mfma_f32_16x16x32_bf16 v[8:11],v[204:207],a[156:159],v[8:11]
v_mfma_f32_16x16x32_bf16 v[12:15],v[192:195],a[176:179],v[12:15]
v_mfma_f32_16x16x32_bf16 v[12:15],v[196:199],a[180:183],v[12:15]
v_mfma_f32_16x16x32_bf16 v[12:15],v[200:203],a[184:187],v[12:15]
buffer_load_dwordx4 v252,s[36:39],s95 offen offset:3072 lds
v_mfma_f32_16x16x32_bf16 v[12:15],v[204:207],a[188:191],v[12:15]
s_waitcnt vmcnt(8) lgkmcnt(0)
v_mfma_f32_16x16x32_bf16 v[0:3],v[208:211],a[208:211],v[0:3]
s_barrier
ds_read_b64_tr_b16 v[192:193],v246
s_add_u32 m0,s92,0x18000
v_mfma_f32_16x16x32_bf16 v[0:3],v[212:215],a[212:215],v[0:3]
s_mov_b32 s95,0x200
ds_read_b64_tr_b16 v[194:195],v246 offset:2048
v_mov_b32 v229,2
v_mfma_f32_16x16x32_bf16 v[0:3],v[216:219],a[216:219],v[0:3]
buffer_load_dwordx4 v252,s[24:27],s95 offen lds
v_mfma_f32_16x16x32_bf16 v[0:3],v[220:223],a[220:223],v[0:3]
ds_read_b64_tr_b16 v[196:197],v246 offset:4096
ds_read_b64_tr_b16 v[198:199],v246 offset:6144
v_mfma_f32_16x16x32_bf16 v[4:7],v[208:211],a[240:243],v[4:7]
ds_read_b64_tr_b16 v[200:201],v246 offset:8192
v_mfma_f32_16x16x32_bf16 v[4:7],v[212:215],a[244:247],v[4:7]
ds_read_b64_tr_b16 v[202:203],v246 offset:10240
v_mfma_f32_16x16x32_bf16 v[4:7],v[216:219],a[248:251],v[4:7]
buffer_load_dwordx4 v252,s[28:31],s95 offen offset:1024 lds
v_mfma_f32_16x16x32_bf16 v[4:7],v[220:223],a[252:255],v[4:7]
ds_read_b64_tr_b16 v[204:205],v246 offset:12288
ds_read_b64_tr_b16 v[206:207],v246 offset:14336
v_mfma_f32_16x16x32_bf16 v[8:11],v[208:211],a[16:19],v[8:11]
buffer_store_dword v229,off,s[88:91],s79 sc1
v_mfma_f32_16x16x32_bf16 v[8:11],v[212:215],a[20:23],v[8:11]
v_cvt_pk_bf16_f32 v48,v0,v1
v_cvt_pk_bf16_f32 v49,v2,v3
v_mfma_f32_16x16x32_bf16 v[8:11],v[216:219],a[24:27],v[8:11]
buffer_load_dwordx4 v252,s[32:35],s95 offen offset:2048 lds
v_mfma_f32_16x16x32_bf16 v[8:11],v[220:223],a[28:31],v[8:11]
v_cvt_pk_bf16_f32 v50,v4,v5
v_cvt_pk_bf16_f32 v51,v6,v7
v_mfma_f32_16x16x32_bf16 v[12:15],v[208:211],a[80:83],v[12:15]
buffer_store_dwordx4 v[48:51],v255,s[88:91],s99 offen sc1
v_mfma_f32_16x16x32_bf16 v[12:15],v[212:215],a[84:87],v[12:15]
s_add_u32 s99,s93,0x2000
v_mfma_f32_16x16x32_bf16 v[12:15],v[216:219],a[88:91],v[12:15]
buffer_load_dwordx4 v252,s[36:39],s95 offen offset:3072 lds
v_mfma_f32_16x16x32_bf16 v[12:15],v[220:223],a[92:95],v[12:15]
s_waitcnt lgkmcnt(0)
v_cvt_pk_bf16_f32 v52,v8,v9
v_cvt_pk_bf16_f32 v53,v10,v11
v_mfma_f32_16x16x32_bf16 v[16:19],v[192:195],v[128:131],0
s_add_u32 m0,s92,0x1c000
ds_read_b64_tr_b16 v[208:209],v247
s_mov_b32 s95,0x38200
v_mfma_f32_16x16x32_bf16 v[16:19],v[196:199],v[132:135],v[16:19]
ds_read_b64_tr_b16 v[210:211],v247 offset:2048
v_cvt_pk_bf16_f32 v54,v12,v13
v_mfma_f32_16x16x32_bf16 v[16:19],v[200:203],v[136:139],v[16:19]
buffer_load_dwordx4 v252,s[24:27],s95 offen lds
v_mfma_f32_16x16x32_bf16 v[16:19],v[204:207],v[140:143],v[16:19]
ds_read_b64_tr_b16 v[212:213],v247 offset:4096
ds_read_b64_tr_b16 v[214:215],v247 offset:6144
v_mfma_f32_16x16x32_bf16 v[20:23],v[192:195],v[160:163],0
ds_read_b64_tr_b16 v[216:217],v247 offset:8192
v_cvt_pk_bf16_f32 v55,v14,v15
v_mfma_f32_16x16x32_bf16 v[20:23],v[196:199],v[164:167],v[20:23]
ds_read_b64_tr_b16 v[218:219],v247 offset:10240
v_mfma_f32_16x16x32_bf16 v[20:23],v[200:203],v[168:171],v[20:23]
buffer_load_dwordx4 v252,s[28:31],s95 offen offset:1024 lds
v_mfma_f32_16x16x32_bf16 v[20:23],v[204:207],v[172:175],v[20:23]
ds_read_b64_tr_b16 v[220:221],v247 offset:12288
ds_read_b64_tr_b16 v[222:223],v247 offset:14336
v_mfma_f32_16x16x32_bf16 v[24:27],v[192:195],a[128:131],0
buffer_store_dwordx4 v[52:55],v255,s[88:91],s99 offen sc1
v_mfma_f32_16x16x32_bf16 v[24:27],v[196:199],a[132:135],v[24:27]
s_add_u32 s99,s96,0x3000
v_mfma_f32_16x16x32_bf16 v[24:27],v[200:203],a[136:139],v[24:27]
buffer_load_dwordx4 v252,s[32:35],s95 offen offset:2048 lds
v_mfma_f32_16x16x32_bf16 v[24:27],v[204:207],a[140:143],v[24:27]
v_mfma_f32_16x16x32_bf16 v[28:31],v[192:195],a[160:163],0
v_mfma_f32_16x16x32_bf16 v[28:31],v[196:199],a[164:167],v[28:31]
v_mfma_f32_16x16x32_bf16 v[28:31],v[200:203],a[168:171],v[28:31]
buffer_load_dwordx4 v252,s[36:39],s95 offen offset:3072 lds
v_mfma_f32_16x16x32_bf16 v[28:31],v[204:207],a[172:175],v[28:31]
s_waitcnt vmcnt(11) lgkmcnt(0)
v_mfma_f32_16x16x32_bf16 v[16:19],v[208:211],a[192:195],v[16:19]
s_barrier
ds_read_b64_tr_b16 v[192:193],v242
s_add_u32 m0,s92,0x0
v_mfma_f32_16x16x32_bf16 v[16:19],v[212:215],a[196:199],v[16:19]
s_mov_b32 s95,0x1c200
ds_read_b64_tr_b16 v[194:195],v242 offset:2048
v_mov_b32 v229,3
v_mfma_f32_16x16x32_bf16 v[16:19],v[216:219],a[200:203],v[16:19]
buffer_load_dwordx4 v252,s[24:27],s95 offen lds
v_mfma_f32_16x16x32_bf16 v[16:19],v[220:223],a[204:207],v[16:19]
ds_read_b64_tr_b16 v[196:197],v242 offset:4096
ds_read_b64_tr_b16 v[198:199],v242 offset:6144
v_mfma_f32_16x16x32_bf16 v[20:23],v[208:211],a[224:227],v[20:23]
)"
R"(ds_read_b64_tr_b16 v[200:201],v242 offset:8192
v_mfma_f32_16x16x32_bf16 v[20:23],v[212:215],a[228:231],v[20:23]
ds_read_b64_tr_b16 v[202:203],v242 offset:10240
v_mfma_f32_16x16x32_bf16 v[20:23],v[216:219],a[232:235],v[20:23]
buffer_load_dwordx4 v252,s[28:31],s95 offen offset:1024 lds
v_mfma_f32_16x16x32_bf16 v[20:23],v[220:223],a[236:239],v[20:23]
ds_read_b64_tr_b16 v[204:205],v242 offset:12288
ds_read_b64_tr_b16 v[206:207],v242 offset:14336
v_mfma_f32_16x16x32_bf16 v[24:27],v[208:211],a[0:3],v[24:27]
buffer_store_dword v229,off,s[88:91],s79 sc1
v_mfma_f32_16x16x32_bf16 v[24:27],v[212:215],a[4:7],v[24:27]
v_mfma_f32_16x16x32_bf16 v[24:27],v[216:219],a[8:11],v[24:27]
buffer_load_dwordx4 v252,s[32:35],s95 offen offset:2048 lds
v_mfma_f32_16x16x32_bf16 v[24:27],v[220:223],a[12:15],v[24:27]
v_mfma_f32_16x16x32_bf16 v[28:31],v[208:211],a[64:67],v[28:31]
v_mfma_f32_16x16x32_bf16 v[28:31],v[212:215],a[68:71],v[28:31]
v_mfma_f32_16x16x32_bf16 v[28:31],v[216:219],a[72:75],v[28:31]
buffer_load_dwordx4 v252,s[36:39],s95 offen offset:3072 lds
v_mfma_f32_16x16x32_bf16 v[28:31],v[220:223],a[76:79],v[28:31]
s_waitcnt lgkmcnt(0)
v_mfma_f32_16x16x32_bf16 v[16:19],v[192:195],v[144:147],v[16:19]
s_add_u32 m0,s92,0x4000
ds_read_b64_tr_b16 v[208:209],v243
s_mov_b32 s95,0x54200
v_mfma_f32_16x16x32_bf16 v[16:19],v[196:199],v[148:151],v[16:19]
ds_read_b64_tr_b16 v[210:211],v243 offset:2048
v_mfma_f32_16x16x32_bf16 v[16:19],v[200:203],v[152:155],v[16:19]
buffer_load_dwordx4 v252,s[24:27],s95 offen lds
v_mfma_f32_16x16x32_bf16 v[16:19],v[204:207],v[156:159],v[16:19]
ds_read_b64_tr_b16 v[212:213],v243 offset:4096
ds_read_b64_tr_b16 v[214:215],v243 offset:6144
v_mfma_f32_16x16x32_bf16 v[20:23],v[192:195],v[176:179],v[20:23]
ds_read_b64_tr_b16 v[216:217],v243 offset:8192
v_mfma_f32_16x16x32_bf16 v[20:23],v[196:199],v[180:183],v[20:23]
ds_read_b64_tr_b16 v[218:219],v243 offset:10240
v_mfma_f32_16x16x32_bf16 v[20:23],v[200:203],v[184:187],v[20:23]
buffer_load_dwordx4 v252,s[28:31],s95 offen offset:1024 lds
v_mfma_f32_16x16x32_bf16 v[20:23],v[204:207],v[188:191],v[20:23]
ds_read_b64_tr_b16 v[220:221],v243 offset:12288
ds_read_b64_tr_b16 v[222:223],v243 offset:14336
v_mfma_f32_16x16x32_bf16 v[24:27],v[192:195],a[144:147],v[24:27]
v_mfma_f32_16x16x32_bf16 v[24:27],v[196:199],a[148:151],v[24:27]
v_mfma_f32_16x16x32_bf16 v[24:27],v[200:203],a[152:155],v[24:27]
buffer_load_dwordx4 v252,s[32:35],s95 offen offset:2048 lds
v_mfma_f32_16x16x32_bf16 v[24:27],v[204:207],a[156:159],v[24:27]
v_mfma_f32_16x16x32_bf16 v[28:31],v[192:195],a[176:179],v[28:31]
v_mfma_f32_16x16x32_bf16 v[28:31],v[196:199],a[180:183],v[28:31]
v_mfma_f32_16x16x32_bf16 v[28:31],v[200:203],a[184:187],v[28:31]
buffer_load_dwordx4 v252,s[36:39],s95 offen offset:3072 lds
v_mfma_f32_16x16x32_bf16 v[28:31],v[204:207],a[188:191],v[28:31]
s_waitcnt vmcnt(9) lgkmcnt(0)
v_mfma_f32_16x16x32_bf16 v[16:19],v[208:211],a[208:211],v[16:19]
s_barrier
ds_read_b64_tr_b16 v[192:193],v242 offset:32768
s_add_u32 m0,s92,0x8000
v_mfma_f32_16x16x32_bf16 v[16:19],v[212:215],a[212:215],v[16:19]
s_mov_b32 s95,0x280
ds_read_b64_tr_b16 v[194:195],v242 offset:34816
v_mfma_f32_16x16x32_bf16 v[16:19],v[216:219],a[216:219],v[16:19]
buffer_load_dwordx4 v252,s[24:27],s95 offen lds
v_mfma_f32_16x16x32_bf16 v[16:19],v[220:223],a[220:223],v[16:19]
ds_read_b64_tr_b16 v[196:197],v242 offset:36864
ds_read_b64_tr_b16 v[198:199],v242 offset:38912
v_mfma_f32_16x16x32_bf16 v[20:23],v[208:211],a[240:243],v[20:23]
ds_read_b64_tr_b16 v[200:201],v242 offset:40960
v_mfma_f32_16x16x32_bf16 v[20:23],v[212:215],a[244:247],v[20:23]
ds_read_b64_tr_b16 v[202:203],v242 offset:43008
v_mfma_f32_16x16x32_bf16 v[20:23],v[216:219],a[248:251],v[20:23]
buffer_load_dwordx4 v252,s[28:31],s95 offen offset:1024 lds
v_mfma_f32_16x16x32_bf16 v[20:23],v[220:223],a[252:255],v[20:23]
ds_read_b64_tr_b16 v[204:205],v242 offset:45056
ds_read_b64_tr_b16 v[206:207],v242 offset:47104
v_mfma_f32_16x16x32_bf16 v[24:27],v[208:211],a[16:19],v[24:27]
v_cvt_pk_bf16_f32 v56,v16,v17
v_cvt_pk_bf16_f32 v57,v18,v19
v_mfma_f32_16x16x32_bf16 v[24:27],v[212:215],a[20:23],v[24:27]
v_mfma_f32_16x16x32_bf16 v[24:27],v[216:219],a[24:27],v[24:27]
buffer_load_dwordx4 v252,s[32:35],s95 offen offset:2048 lds
v_mfma_f32_16x16x32_bf16 v[24:27],v[220:223],a[28:31],v[24:27]
v_cvt_pk_bf16_f32 v58,v20,v21
v_cvt_pk_bf16_f32 v59,v22,v23
v_mfma_f32_16x16x32_bf16 v[28:31],v[208:211],a[80:83],v[28:31]
buffer_store_dwordx4 v[56:59],v255,s[88:91],s99 offen sc1
v_mfma_f32_16x16x32_bf16 v[28:31],v[212:215],a[84:87],v[28:31]
s_add_u32 s99,s93,0x3000
v_mfma_f32_16x16x32_bf16 v[28:31],v[216:219],a[88:91],v[28:31]
buffer_load_dwordx4 v252,s[36:39],s95 offen offset:3072 lds
v_mfma_f32_16x16x32_bf16 v[28:31],v[220:223],a[92:95],v[28:31]
s_waitcnt lgkmcnt(0)
v_cvt_pk_bf16_f32 v60,v24,v25
v_cvt_pk_bf16_f32 v61,v26,v27
v_mfma_f32_16x16x32_bf16 v[0:3],v[192:195],v[128:131],0
s_add_u32 m0,s92,0xc000
ds_read_b64_tr_b16 v[208:209],v243 offset:32768
s_mov_b32 s95,0x38280
v_mfma_f32_16x16x32_bf16 v[0:3],v[196:199],v[132:135],v[0:3]
ds_read_b64_tr_b16 v[210:211],v243 offset:34816
v_cvt_pk_bf16_f32 v62,v28,v29
v_mfma_f32_16x16x32_bf16 v[0:3],v[200:203],v[136:139],v[0:3]
buffer_load_dwordx4 v252,s[24:27],s95 offen lds
v_mfma_f32_16x16x32_bf16 v[0:3],v[204:207],v[140:143],v[0:3]
ds_read_b64_tr_b16 v[212:213],v243 offset:36864
ds_read_b64_tr_b16 v[214:215],v243 offset:38912
v_mfma_f32_16x16x32_bf16 v[4:7],v[192:195],v[160:163],0
ds_read_b64_tr_b16 v[216:217],v243 offset:40960
v_cvt_pk_bf16_f32 v63,v30,v31
v_mfma_f32_16x16x32_bf16 v[4:7],v[196:199],v[164:167],v[4:7]
ds_read_b64_tr_b16 v[218:219],v243 offset:43008
v_mfma_f32_16x16x32_bf16 v[4:7],v[200:203],v[168:171],v[4:7]
buffer_load_dwordx4 v252,s[28:31],s95 offen offset:1024 lds
v_mfma_f32_16x16x32_bf16 v[4:7],v[204:207],v[172:175],v[4:7]
ds_read_b64_tr_b16 v[220:221],v243 offset:45056
ds_read_b64_tr_b16 v[222:223],v243 offset:47104
v_mfma_f32_16x16x32_bf16 v[8:11],v[192:195],a[128:131],0
buffer_store_dwordx4 v[60:63],v255,s[88:91],s99 offen sc1
v_mfma_f32_16x16x32_bf16 v[8:11],v[196:199],a[132:135],v[8:11]
s_add_u32 s99,s96,0x4000
v_mfma_f32_16x16x32_bf16 v[8:11],v[200:203],a[136:139],v[8:11]
buffer_load_dwordx4 v252,s[32:35],s95 offen offset:2048 lds
v_mfma_f32_16x16x32_bf16 v[8:11],v[204:207],a[140:143],v[8:11]
v_mfma_f32_16x16x32_bf16 v[12:15],v[192:195],a[160:163],0
v_mfma_f32_16x16x32_bf16 v[12:15],v[196:199],a[164:167],v[12:15]
v_mfma_f32_16x16x32_bf16 v[12:15],v[200:203],a[168:171],v[12:15]
buffer_load_dwordx4 v252,s[36:39],s95 offen offset:3072 lds
v_mfma_f32_16x16x32_bf16 v[12:15],v[204:207],a[172:175],v[12:15]
s_waitcnt vmcnt(10) lgkmcnt(0)
v_mfma_f32_16x16x32_bf16 v[0:3],v[208:211],a[192:195],v[0:3]
s_barrier
ds_read_b64_tr_b16 v[192:193],v240
s_add_u32 m0,s92,0x20000
v_mfma_f32_16x16x32_bf16 v[0:3],v[212:215],a[196:199],v[0:3]
s_mov_b32 s95,0x1c280
ds_read_b64_tr_b16 v[194:195],v240 offset:2048
v_mov_b32 v229,4
v_mfma_f32_16x16x32_bf16 v[0:3],v[216:219],a[200:203],v[0:3]
buffer_load_dwordx4 v252,s[24:27],s95 offen lds
v_mfma_f32_16x16x32_bf16 v[0:3],v[220:223],a[204:207],v[0:3]
ds_read_b64_tr_b16 v[196:197],v240 offset:4096
ds_read_b64_tr_b16 v[198:199],v240 offset:6144
v_mfma_f32_16x16x32_bf16 v[4:7],v[208:211],a[224:227],v[4:7]
ds_read_b64_tr_b16 v[200:201],v240 offset:8192
v_mfma_f32_16x16x32_bf16 v[4:7],v[212:215],a[228:231],v[4:7]
ds_read_b64_tr_b16 v[202:203],v240 offset:10240
v_mfma_f32_16x16x32_bf16 v[4:7],v[216:219],a[232:235],v[4:7]
buffer_load_dwordx4 v252,s[28:31],s95 offen offset:1024 lds
v_mfma_f32_16x16x32_bf16 v[4:7],v[220:223],a[236:239],v[4:7]
ds_read_b64_tr_b16 v[204:205],v240 offset:12288
ds_read_b64_tr_b16 v[206:207],v240 offset:14336
v_mfma_f32_16x16x32_bf16 v[8:11],v[208:211],a[0:3],v[8:11]
buffer_store_dword v229,off,s[88:91],s79 sc1
v_mfma_f32_16x16x32_bf16 v[8:11],v[212:215],a[4:7],v[8:11]
v_mfma_f32_16x16x32_bf16 v[8:11],v[216:219],a[8:11],v[8:11]
buffer_load_dwordx4 v252,s[32:35],s95 offen offset:2048 lds
v_mfma_f32_16x16x32_bf16 v[8:11],v[220:223],a[12:15],v[8:11]
v_mfma_f32_16x16x32_bf16 v[12:15],v[208:211],a[64:67],v[12:15]
v_mfma_f32_16x16x32_bf16 v[12:15],v[212:215],a[68:71],v[12:15]
v_mfma_f32_16x16x32_bf16 v[12:15],v[216:219],a[72:75],v[12:15]
buffer_load_dwordx4 v252,s[36:39],s95 offen offset:3072 lds
v_mfma_f32_16x16x32_bf16 v[12:15],v[220:223],a[76:79],v[12:15]
s_waitcnt lgkmcnt(0)
v_mfma_f32_16x16x32_bf16 v[0:3],v[192:195],v[144:147],v[0:3]
s_add_u32 m0,s92,0x24000
ds_read_b64_tr_b16 v[208:209],v241
s_mov_b32 s95,0x54280
v_mfma_f32_16x16x32_bf16 v[0:3],v[196:199],v[148:151],v[0:3]
ds_read_b64_tr_b16 v[210:211],v241 offset:2048
v_mfma_f32_16x16x32_bf16 v[0:3],v[200:203],v[152:155],v[0:3]
buffer_load_dwordx4 v252,s[24:27],s95 offen lds
v_mfma_f32_16x16x32_bf16 v[0:3],v[204:207],v[156:159],v[0:3]
ds_read_b64_tr_b16 v[212:213],v241 offset:4096
ds_read_b64_tr_b16 v[214:215],v241 offset:6144
v_mfma_f32_16x16x32_bf16 v[4:7],v[192:195],v[176:179],v[4:7]
ds_read_b64_tr_b16 v[216:217],v241 offset:8192
v_mfma_f32_16x16x32_bf16 v[4:7],v[196:199],v[180:183],v[4:7]
ds_read_b64_tr_b16 v[218:219],v241 offset:10240
v_mfma_f32_16x16x32_bf16 v[4:7],v[200:203],v[184:187],v[4:7]
buffer_load_dwordx4 v252,s[28:31],s95 offen offset:1024 lds
v_mfma_f32_16x16x32_bf16 v[4:7],v[204:207],v[188:191],v[4:7]
ds_read_b64_tr_b16 v[220:221],v241 offset:12288
ds_read_b64_tr_b16 v[222:223],v241 offset:14336
v_mfma_f32_16x16x32_bf16 v[8:11],v[192:195],a[144:147],v[8:11]
v_mfma_f32_16x16x32_bf16 v[8:11],v[196:199],a[148:151],v[8:11]
v_mfma_f32_16x16x32_bf16 v[8:11],v[200:203],a[152:155],v[8:11]
buffer_load_dwordx4 v252,s[32:35],s95 offen offset:2048 lds
v_mfma_f32_16x16x32_bf16 v[8:11],v[204:207],a[156:159],v[8:11]
v_mfma_f32_16x16x32_bf16 v[12:15],v[192:195],a[176:179],v[12:15]
v_mfma_f32_16x16x32_bf16 v[12:15],v[196:199],a[180:183],v[12:15]
v_mfma_f32_16x16x32_bf16 v[12:15],v[200:203],a[184:187],v[12:15]
buffer_load_dwordx4 v252,s[36:39],s95 offen offset:3072 lds
v_mfma_f32_16x16x32_bf16 v[12:15],v[204:207],a[188:191],v[12:15]
s_waitcnt vmcnt(9) lgkmcnt(0)
v_mfma_f32_16x16x32_bf16 v[0:3],v[208:211],a[208:211],v[0:3]
s_barrier
ds_read_b64_tr_b16 v[192:193],v240 offset:32768
s_add_u32 m0,s92,0x10000
v_mfma_f32_16x16x32_bf16 v[0:3],v[212:215],a[212:215],v[0:3]
s_mov_b32 s95,0x300
ds_read_b64_tr_b16 v[194:195],v240 offset:34816
v_mfma_f32_16x16x32_bf16 v[0:3],v[216:219],a[216:219],v[0:3]
buffer_load_dwordx4 v252,s[24:27],s95 offen lds
)"
R"(v_mfma_f32_16x16x32_bf16 v[0:3],v[220:223],a[220:223],v[0:3]
ds_read_b64_tr_b16 v[196:197],v240 offset:36864
ds_read_b64_tr_b16 v[198:199],v240 offset:38912
v_mfma_f32_16x16x32_bf16 v[4:7],v[208:211],a[240:243],v[4:7]
ds_read_b64_tr_b16 v[200:201],v240 offset:40960
v_mfma_f32_16x16x32_bf16 v[4:7],v[212:215],a[244:247],v[4:7]
buffer_load_dword v224,v226,s[88:91],s80 offen sc1
v_mfma_f32_16x16x32_bf16 v[4:7],v[216:219],a[248:251],v[4:7]
buffer_load_dwordx4 v252,s[28:31],s95 offen offset:1024 lds
v_mfma_f32_16x16x32_bf16 v[4:7],v[220:223],a[252:255],v[4:7]
ds_read_b64_tr_b16 v[202:203],v240 offset:43008
ds_read_b64_tr_b16 v[204:205],v240 offset:45056
v_mfma_f32_16x16x32_bf16 v[8:11],v[208:211],a[16:19],v[8:11]
ds_read_b64_tr_b16 v[206:207],v240 offset:47104
v_cvt_pk_bf16_f32 v64,v0,v1
v_mfma_f32_16x16x32_bf16 v[8:11],v[212:215],a[20:23],v[8:11]
v_cvt_pk_bf16_f32 v65,v2,v3
v_mfma_f32_16x16x32_bf16 v[8:11],v[216:219],a[24:27],v[8:11]
buffer_load_dwordx4 v252,s[32:35],s95 offen offset:2048 lds
v_mfma_f32_16x16x32_bf16 v[8:11],v[220:223],a[28:31],v[8:11]
v_cvt_pk_bf16_f32 v66,v4,v5
v_cvt_pk_bf16_f32 v67,v6,v7
v_mfma_f32_16x16x32_bf16 v[12:15],v[208:211],a[80:83],v[12:15]
buffer_store_dwordx4 v[64:67],v255,s[88:91],s99 offen sc1
v_mfma_f32_16x16x32_bf16 v[12:15],v[212:215],a[84:87],v[12:15]
s_add_u32 s99,s93,0x4000
v_mfma_f32_16x16x32_bf16 v[12:15],v[216:219],a[88:91],v[12:15]
buffer_load_dwordx4 v252,s[36:39],s95 offen offset:3072 lds
v_mfma_f32_16x16x32_bf16 v[12:15],v[220:223],a[92:95],v[12:15]
s_waitcnt lgkmcnt(0)
v_cvt_pk_bf16_f32 v68,v8,v9
v_cvt_pk_bf16_f32 v69,v10,v11
v_mfma_f32_16x16x32_bf16 v[16:19],v[192:195],v[128:131],0
s_add_u32 m0,s92,0x14000
ds_read_b64_tr_b16 v[208:209],v241 offset:32768
s_mov_b32 s95,0x38300
v_mfma_f32_16x16x32_bf16 v[16:19],v[196:199],v[132:135],v[16:19]
ds_read_b64_tr_b16 v[210:211],v241 offset:34816
v_cvt_pk_bf16_f32 v70,v12,v13
v_mfma_f32_16x16x32_bf16 v[16:19],v[200:203],v[136:139],v[16:19]
buffer_load_dwordx4 v252,s[24:27],s95 offen lds
v_mfma_f32_16x16x32_bf16 v[16:19],v[204:207],v[140:143],v[16:19]
ds_read_b64_tr_b16 v[212:213],v241 offset:36864
ds_read_b64_tr_b16 v[214:215],v241 offset:38912
v_mfma_f32_16x16x32_bf16 v[20:23],v[192:195],v[160:163],0
ds_read_b64_tr_b16 v[216:217],v241 offset:40960
v_cvt_pk_bf16_f32 v71,v14,v15
v_mfma_f32_16x16x32_bf16 v[20:23],v[196:199],v[164:167],v[20:23]
ds_read_b64_tr_b16 v[218:219],v241 offset:43008
v_mfma_f32_16x16x32_bf16 v[20:23],v[200:203],v[168:171],v[20:23]
buffer_load_dwordx4 v252,s[28:31],s95 offen offset:1024 lds
v_mfma_f32_16x16x32_bf16 v[20:23],v[204:207],v[172:175],v[20:23]
ds_read_b64_tr_b16 v[220:221],v241 offset:45056
ds_read_b64_tr_b16 v[222:223],v241 offset:47104
v_mfma_f32_16x16x32_bf16 v[24:27],v[192:195],a[128:131],0
buffer_store_dwordx4 v[68:71],v255,s[88:91],s99 offen sc1
v_mfma_f32_16x16x32_bf16 v[24:27],v[196:199],a[132:135],v[24:27]
s_add_u32 s99,s96,0x5000
v_mfma_f32_16x16x32_bf16 v[24:27],v[200:203],a[136:139],v[24:27]
buffer_load_dwordx4 v252,s[32:35],s95 offen offset:2048 lds
v_mfma_f32_16x16x32_bf16 v[24:27],v[204:207],a[140:143],v[24:27]
v_mfma_f32_16x16x32_bf16 v[28:31],v[192:195],a[160:163],0
v_mfma_f32_16x16x32_bf16 v[28:31],v[196:199],a[164:167],v[28:31]
v_mfma_f32_16x16x32_bf16 v[28:31],v[200:203],a[168:171],v[28:31]
buffer_load_dwordx4 v252,s[36:39],s95 offen offset:3072 lds
v_mfma_f32_16x16x32_bf16 v[28:31],v[204:207],a[172:175],v[28:31]
s_waitcnt vmcnt(11) lgkmcnt(0)
v_mfma_f32_16x16x32_bf16 v[16:19],v[208:211],a[192:195],v[16:19]
s_barrier
ds_read_b64_tr_b16 v[192:193],v246
s_add_u32 m0,s92,0x18000
v_mfma_f32_16x16x32_bf16 v[16:19],v[212:215],a[196:199],v[16:19]
s_mov_b32 s95,0x1c300
ds_read_b64_tr_b16 v[194:195],v246 offset:2048
v_mov_b32 v229,5
v_mfma_f32_16x16x32_bf16 v[16:19],v[216:219],a[200:203],v[16:19]
buffer_load_dwordx4 v252,s[24:27],s95 offen lds
v_mfma_f32_16x16x32_bf16 v[16:19],v[220:223],a[204:207],v[16:19]
ds_read_b64_tr_b16 v[196:197],v246 offset:4096
ds_read_b64_tr_b16 v[198:199],v246 offset:6144
v_mfma_f32_16x16x32_bf16 v[20:23],v[208:211],a[224:227],v[20:23]
ds_read_b64_tr_b16 v[200:201],v246 offset:8192
v_mfma_f32_16x16x32_bf16 v[20:23],v[212:215],a[228:231],v[20:23]
ds_read_b64_tr_b16 v[202:203],v246 offset:10240
v_mfma_f32_16x16x32_bf16 v[20:23],v[216:219],a[232:235],v[20:23]
buffer_load_dwordx4 v252,s[28:31],s95 offen offset:1024 lds
v_mfma_f32_16x16x32_bf16 v[20:23],v[220:223],a[236:239],v[20:23]
ds_read_b64_tr_b16 v[204:205],v246 offset:12288
ds_read_b64_tr_b16 v[206:207],v246 offset:14336
v_mfma_f32_16x16x32_bf16 v[24:27],v[208:211],a[0:3],v[24:27]
buffer_store_dword v229,off,s[88:91],s79 sc1
v_mfma_f32_16x16x32_bf16 v[24:27],v[212:215],a[4:7],v[24:27]
v_mfma_f32_16x16x32_bf16 v[24:27],v[216:219],a[8:11],v[24:27]
buffer_load_dwordx4 v252,s[32:35],s95 offen offset:2048 lds
v_mfma_f32_16x16x32_bf16 v[24:27],v[220:223],a[12:15],v[24:27]
v_mfma_f32_16x16x32_bf16 v[28:31],v[208:211],a[64:67],v[28:31]
v_mfma_f32_16x16x32_bf16 v[28:31],v[212:215],a[68:71],v[28:31]
v_mfma_f32_16x16x32_bf16 v[28:31],v[216:219],a[72:75],v[28:31]
buffer_load_dwordx4 v252,s[36:39],s95 offen offset:3072 lds
v_mfma_f32_16x16x32_bf16 v[28:31],v[220:223],a[76:79],v[28:31]
s_waitcnt lgkmcnt(0)
v_mfma_f32_16x16x32_bf16 v[16:19],v[192:195],v[144:147],v[16:19]
s_add_u32 m0,s92,0x1c000
ds_read_b64_tr_b16 v[208:209],v247
s_mov_b32 s95,0x54300
v_mfma_f32_16x16x32_bf16 v[16:19],v[196:199],v[148:151],v[16:19]
ds_read_b64_tr_b16 v[210:211],v247 offset:2048
v_mfma_f32_16x16x32_bf16 v[16:19],v[200:203],v[152:155],v[16:19]
buffer_load_dwordx4 v252,s[24:27],s95 offen lds
v_mfma_f32_16x16x32_bf16 v[16:19],v[204:207],v[156:159],v[16:19]
ds_read_b64_tr_b16 v[212:213],v247 offset:4096
ds_read_b64_tr_b16 v[214:215],v247 offset:6144
v_mfma_f32_16x16x32_bf16 v[20:23],v[192:195],v[176:179],v[20:23]
ds_read_b64_tr_b16 v[216:217],v247 offset:8192
v_mfma_f32_16x16x32_bf16 v[20:23],v[196:199],v[180:183],v[20:23]
ds_read_b64_tr_b16 v[218:219],v247 offset:10240
v_mfma_f32_16x16x32_bf16 v[20:23],v[200:203],v[184:187],v[20:23]
buffer_load_dwordx4 v252,s[28:31],s95 offen offset:1024 lds
v_mfma_f32_16x16x32_bf16 v[20:23],v[204:207],v[188:191],v[20:23]
ds_read_b64_tr_b16 v[220:221],v247 offset:12288
ds_read_b64_tr_b16 v[222:223],v247 offset:14336
v_mfma_f32_16x16x32_bf16 v[24:27],v[192:195],a[144:147],v[24:27]
v_mfma_f32_16x16x32_bf16 v[24:27],v[196:199],a[148:151],v[24:27]
v_mfma_f32_16x16x32_bf16 v[24:27],v[200:203],a[152:155],v[24:27]
buffer_load_dwordx4 v252,s[32:35],s95 offen offset:2048 lds
v_mfma_f32_16x16x32_bf16 v[24:27],v[204:207],a[156:159],v[24:27]
v_mfma_f32_16x16x32_bf16 v[28:31],v[192:195],a[176:179],v[28:31]
v_mfma_f32_16x16x32_bf16 v[28:31],v[196:199],a[180:183],v[28:31]
v_mfma_f32_16x16x32_bf16 v[28:31],v[200:203],a[184:187],v[28:31]
buffer_load_dwordx4 v252,s[36:39],s95 offen offset:3072 lds
v_mfma_f32_16x16x32_bf16 v[28:31],v[204:207],a[188:191],v[28:31]
s_waitcnt vmcnt(18)
s_waitcnt vmcnt(9) lgkmcnt(0)
v_mfma_f32_16x16x32_bf16 v[16:19],v[208:211],a[208:211],v[16:19]
s_barrier
ds_read_b64_tr_b16 v[192:193],v242
v_mfma_f32_16x16x32_bf16 v[16:19],v[212:215],a[212:215],v[16:19]
ds_read_b64_tr_b16 v[194:195],v242 offset:2048
v_mfma_f32_16x16x32_bf16 v[16:19],v[216:219],a[216:219],v[16:19]
s_add_u32 s60,s83,0
ds_read_b64_tr_b16 v[196:197],v242 offset:4096
v_cmp_gt_u32 vcc,s52,v224
v_mfma_f32_16x16x32_bf16 v[16:19],v[220:223],a[220:223],v[16:19]
s_and_b32 s97,vcc_lo,0xff
s_cselect_b32 s58,0,s42
s_add_u32 s61,s60,4096
ds_read_b64_tr_b16 v[198:199],v242 offset:6144
v_mfma_f32_16x16x32_bf16 v[20:23],v[208:211],a[240:243],v[20:23]
buffer_load_dwordx4 v[96:99],v255,s[56:59],s60 offen sc1
v_mfma_f32_16x16x32_bf16 v[20:23],v[212:215],a[244:247],v[20:23]
ds_read_b64_tr_b16 v[200:201],v242 offset:8192
ds_read_b64_tr_b16 v[202:203],v242 offset:10240
v_mfma_f32_16x16x32_bf16 v[20:23],v[216:219],a[248:251],v[20:23]
ds_read_b64_tr_b16 v[204:205],v242 offset:12288
v_cvt_pk_bf16_f32 v72,v16,v17
v_mfma_f32_16x16x32_bf16 v[20:23],v[220:223],a[252:255],v[20:23]
buffer_load_dwordx4 v[100:103],v255,s[56:59],s61 offen sc1
v_mfma_f32_16x16x32_bf16 v[24:27],v[208:211],a[16:19],v[24:27]
ds_read_b64_tr_b16 v[206:207],v242 offset:14336
v_cvt_pk_bf16_f32 v73,v18,v19
v_mfma_f32_16x16x32_bf16 v[24:27],v[212:215],a[20:23],v[24:27]
v_mfma_f32_16x16x32_bf16 v[24:27],v[216:219],a[24:27],v[24:27]
s_add_u32 s60,s83,114688
s_add_u32 s61,s60,4096
v_cvt_pk_bf16_f32 v74,v20,v21
v_mfma_f32_16x16x32_bf16 v[24:27],v[220:223],a[28:31],v[24:27]
buffer_load_dwordx4 v[104:107],v255,s[56:59],s60 offen sc1
v_mfma_f32_16x16x32_bf16 v[28:31],v[208:211],a[80:83],v[28:31]
v_cvt_pk_bf16_f32 v75,v22,v23
v_mfma_f32_16x16x32_bf16 v[28:31],v[212:215],a[84:87],v[28:31]
buffer_store_dwordx4 v[72:75],v255,s[88:91],s99 offen sc1
v_mfma_f32_16x16x32_bf16 v[28:31],v[216:219],a[88:91],v[28:31]
s_add_u32 s99,s93,0x5000
buffer_load_dwordx4 v[108:111],v255,s[56:59],s61 offen sc1
v_mfma_f32_16x16x32_bf16 v[28:31],v[220:223],a[92:95],v[28:31]
s_waitcnt lgkmcnt(0)
v_cvt_pk_bf16_f32 v76,v24,v25
v_cvt_pk_bf16_f32 v77,v26,v27
v_mfma_f32_16x16x32_bf16 v[0:3],v[192:195],v[128:131],0
ds_read_b64_tr_b16 v[208:209],v243
v_mfma_f32_16x16x32_bf16 v[0:3],v[196:199],v[132:135],v[0:3]
ds_read_b64_tr_b16 v[210:211],v243 offset:2048
v_mfma_f32_16x16x32_bf16 v[0:3],v[200:203],v[136:139],v[0:3]
s_add_u32 s60,s83,229376
ds_read_b64_tr_b16 v[212:213],v243 offset:4096
s_add_u32 s61,s60,4096
v_mfma_f32_16x16x32_bf16 v[0:3],v[204:207],v[140:143],v[0:3]
buffer_load_dwordx4 v[112:115],v255,s[56:59],s60 offen sc1
v_mfma_f32_16x16x32_bf16 v[4:7],v[192:195],v[160:163],0
ds_read_b64_tr_b16 v[214:215],v243 offset:6144
ds_read_b64_tr_b16 v[216:217],v243 offset:8192
v_mfma_f32_16x16x32_bf16 v[4:7],v[196:199],v[164:167],v[4:7]
ds_read_b64_tr_b16 v[218:219],v243 offset:10240
v_cvt_pk_bf16_f32 v78,v28,v29
v_mfma_f32_16x16x32_bf16 v[4:7],v[200:203],v[168:171],v[4:7]
ds_read_b64_tr_b16 v[220:221],v243 offset:12288
v_cvt_pk_bf16_f32 v79,v30,v31
v_mfma_f32_16x16x32_bf16 v[4:7],v[204:207],v[172:175],v[4:7]
buffer_load_dwordx4 v[116:119],v255,s[56:59],s61 offen sc1
v_mfma_f32_16x16x32_bf16 v[8:11],v[192:195],a[128:131],0
)"
R"(ds_read_b64_tr_b16 v[222:223],v243 offset:14336
v_mfma_f32_16x16x32_bf16 v[8:11],v[196:199],a[132:135],v[8:11]
buffer_store_dwordx4 v[76:79],v255,s[88:91],s99 offen sc1
v_mfma_f32_16x16x32_bf16 v[8:11],v[200:203],a[136:139],v[8:11]
s_add_u32 s60,s83,344064
s_add_u32 s61,s60,4096
v_mfma_f32_16x16x32_bf16 v[8:11],v[204:207],a[140:143],v[8:11]
s_add_u32 s99,s96,0x6000
buffer_load_dwordx4 v[120:123],v255,s[56:59],s60 offen sc1
v_mfma_f32_16x16x32_bf16 v[12:15],v[192:195],a[160:163],0
v_mfma_f32_16x16x32_bf16 v[12:15],v[196:199],a[164:167],v[12:15]
v_mfma_f32_16x16x32_bf16 v[12:15],v[200:203],a[168:171],v[12:15]
buffer_load_dwordx4 v[124:127],v255,s[56:59],s61 offen sc1
v_mfma_f32_16x16x32_bf16 v[12:15],v[204:207],a[172:175],v[12:15]
s_waitcnt vmcnt(10) lgkmcnt(0)
v_mfma_f32_16x16x32_bf16 v[0:3],v[208:211],a[192:195],v[0:3]
s_barrier
ds_read_b64_tr_b16 v[192:193],v242 offset:32768
v_mov_b32 v229,6
v_mfma_f32_16x16x32_bf16 v[0:3],v[212:215],a[196:199],v[0:3]
ds_read_b64_tr_b16 v[194:195],v242 offset:34816
v_mfma_f32_16x16x32_bf16 v[0:3],v[216:219],a[200:203],v[0:3]
ds_read_b64_tr_b16 v[196:197],v242 offset:36864
v_mfma_f32_16x16x32_bf16 v[0:3],v[220:223],a[204:207],v[0:3]
ds_read_b64_tr_b16 v[198:199],v242 offset:38912
v_mfma_f32_16x16x32_bf16 v[4:7],v[208:211],a[224:227],v[4:7]
ds_read_b64_tr_b16 v[200:201],v242 offset:40960
v_mfma_f32_16x16x32_bf16 v[4:7],v[212:215],a[228:231],v[4:7]
ds_read_b64_tr_b16 v[202:203],v242 offset:43008
v_mfma_f32_16x16x32_bf16 v[4:7],v[216:219],a[232:235],v[4:7]
ds_read_b64_tr_b16 v[204:205],v242 offset:45056
v_mfma_f32_16x16x32_bf16 v[4:7],v[220:223],a[236:239],v[4:7]
ds_read_b64_tr_b16 v[206:207],v242 offset:47104
v_mfma_f32_16x16x32_bf16 v[8:11],v[208:211],a[0:3],v[8:11]
buffer_store_dword v229,off,s[88:91],s79 sc1
v_mfma_f32_16x16x32_bf16 v[8:11],v[212:215],a[4:7],v[8:11]
v_mfma_f32_16x16x32_bf16 v[8:11],v[216:219],a[8:11],v[8:11]
v_mfma_f32_16x16x32_bf16 v[8:11],v[220:223],a[12:15],v[8:11]
v_mfma_f32_16x16x32_bf16 v[12:15],v[208:211],a[64:67],v[12:15]
v_mfma_f32_16x16x32_bf16 v[12:15],v[212:215],a[68:71],v[12:15]
v_mfma_f32_16x16x32_bf16 v[12:15],v[216:219],a[72:75],v[12:15]
v_mfma_f32_16x16x32_bf16 v[12:15],v[220:223],a[76:79],v[12:15]
s_waitcnt lgkmcnt(0)
v_mfma_f32_16x16x32_bf16 v[0:3],v[192:195],v[144:147],v[0:3]
ds_read_b64_tr_b16 v[208:209],v243 offset:32768
v_mfma_f32_16x16x32_bf16 v[0:3],v[196:199],v[148:151],v[0:3]
ds_read_b64_tr_b16 v[210:211],v243 offset:34816
v_mfma_f32_16x16x32_bf16 v[0:3],v[200:203],v[152:155],v[0:3]
ds_read_b64_tr_b16 v[212:213],v243 offset:36864
v_mfma_f32_16x16x32_bf16 v[0:3],v[204:207],v[156:159],v[0:3]
ds_read_b64_tr_b16 v[214:215],v243 offset:38912
v_mfma_f32_16x16x32_bf16 v[4:7],v[192:195],v[176:179],v[4:7]
ds_read_b64_tr_b16 v[216:217],v243 offset:40960
v_mfma_f32_16x16x32_bf16 v[4:7],v[196:199],v[180:183],v[4:7]
ds_read_b64_tr_b16 v[218:219],v243 offset:43008
v_mfma_f32_16x16x32_bf16 v[4:7],v[200:203],v[184:187],v[4:7]
ds_read_b64_tr_b16 v[220:221],v243 offset:45056
v_mfma_f32_16x16x32_bf16 v[4:7],v[204:207],v[188:191],v[4:7]
ds_read_b64_tr_b16 v[222:223],v243 offset:47104
v_mfma_f32_16x16x32_bf16 v[8:11],v[192:195],a[144:147],v[8:11]
v_mfma_f32_16x16x32_bf16 v[8:11],v[196:199],a[148:151],v[8:11]
v_mfma_f32_16x16x32_bf16 v[8:11],v[200:203],a[152:155],v[8:11]
v_mfma_f32_16x16x32_bf16 v[8:11],v[204:207],a[156:159],v[8:11]
v_mfma_f32_16x16x32_bf16 v[12:15],v[192:195],a[176:179],v[12:15]
v_mfma_f32_16x16x32_bf16 v[12:15],v[196:199],a[180:183],v[12:15]
v_mfma_f32_16x16x32_bf16 v[12:15],v[200:203],a[184:187],v[12:15]
v_mfma_f32_16x16x32_bf16 v[12:15],v[204:207],a[188:191],v[12:15]
s_waitcnt lgkmcnt(0)
v_mfma_f32_16x16x32_bf16 v[0:3],v[208:211],a[208:211],v[0:3]
v_mfma_f32_16x16x32_bf16 v[0:3],v[212:215],a[212:215],v[0:3]
v_mfma_f32_16x16x32_bf16 v[0:3],v[216:219],a[216:219],v[0:3]
v_mfma_f32_16x16x32_bf16 v[0:3],v[220:223],a[220:223],v[0:3]
v_mfma_f32_16x16x32_bf16 v[4:7],v[208:211],a[240:243],v[4:7]
v_mfma_f32_16x16x32_bf16 v[4:7],v[212:215],a[244:247],v[4:7]
v_mfma_f32_16x16x32_bf16 v[4:7],v[216:219],a[248:251],v[4:7]
v_mfma_f32_16x16x32_bf16 v[4:7],v[220:223],a[252:255],v[4:7]
v_mfma_f32_16x16x32_bf16 v[8:11],v[208:211],a[16:19],v[8:11]
v_mfma_f32_16x16x32_bf16 v[8:11],v[212:215],a[20:23],v[8:11]
v_mfma_f32_16x16x32_bf16 v[8:11],v[216:219],a[24:27],v[8:11]
v_mfma_f32_16x16x32_bf16 v[8:11],v[220:223],a[28:31],v[8:11]
v_cvt_pk_bf16_f32 v80,v0,v1
v_cvt_pk_bf16_f32 v81,v2,v3
v_mfma_f32_16x16x32_bf16 v[12:15],v[208:211],a[80:83],v[12:15]
v_mfma_f32_16x16x32_bf16 v[12:15],v[212:215],a[84:87],v[12:15]
v_cvt_pk_bf16_f32 v82,v4,v5
v_cvt_pk_bf16_f32 v83,v6,v7
v_mfma_f32_16x16x32_bf16 v[12:15],v[216:219],a[88:91],v[12:15]
buffer_store_dwordx4 v[80:83],v255,s[88:91],s99 offen sc1
v_mfma_f32_16x16x32_bf16 v[12:15],v[220:223],a[92:95],v[12:15]
s_add_u32 s99,s93,0x6000
v_cvt_pk_bf16_f32 v84,v8,v9
v_cvt_pk_bf16_f32 v85,v10,v11
s_nop 4
v_cvt_pk_bf16_f32 v86,v12,v13
v_cvt_pk_bf16_f32 v87,v14,v15
buffer_store_dwordx4 v[84:87],v255,s[88:91],s99 offen sc1
v_mov_b32 v192,0
v_mov_b32 v193,0
v_mov_b32 v194,0
v_mov_b32 v195,0
v_mov_b32 v196,0
v_mov_b32 v197,0
v_mov_b32 v198,0
v_mov_b32 v199,0
v_mov_b32 v200,0
v_mov_b32 v201,0
v_mov_b32 v202,0
v_mov_b32 v203,0
v_mov_b32 v204,0
v_mov_b32 v205,0
v_mov_b32 v206,0
v_mov_b32 v207,0
v_mbcnt_lo_u32_b32 v253,-1,0
v_mbcnt_hi_u32_b32 v253,-1,v253
v_and_b32 v230,15,v253
v_lshlrev_b32 v228,6,v230
v_bfe_u32 v230,v253,1,2
v_lshrrev_b32 v225,5,v253
v_xor_b32 v230,v230,v225
v_lshl_add_u32 v228,v230,4,v228
v_bfe_u32 v230,v253,4,1
v_lshl_add_u32 v228,v230,3,v228
s_add_u32 s101,s92,131072
v_add_u32 v228,s101,v228
v_xor_b32 v230,32,v228
v_add_u32 v225,s101,v255
s_mov_b32 s98,0x00003f80
s_mov_b32 s100,0x3f800000
s_mov_b64 s[24:25],s[88:89]
s_mov_b32 s74,1000000
s_lshr_b32 m0,%[h],1
s_waitcnt vmcnt(3)
v_mov_b32 v208,0
v_mov_b32 v209,0
v_mov_b32 v210,0
v_mov_b32 v211,0
v_mov_b32 v212,0
v_mov_b32 v213,0
v_mov_b32 v214,0
v_mov_b32 v215,0
v_mov_b32 v216,0
v_mov_b32 v217,0
v_mov_b32 v218,0
v_mov_b32 v219,0
v_mov_b32 v220,0
v_mov_b32 v221,0
v_mov_b32 v222,0
v_mov_b32 v223,0
v_dot2_f32_bf16 v208,v96,s98,v208
v_dot2_f32_bf16 v209,v96,s100,v209
v_dot2_f32_bf16 v210,v97,s98,v210
v_dot2_f32_bf16 v211,v97,s100,v211
v_dot2_f32_bf16 v216,v98,s98,v216
v_dot2_f32_bf16 v217,v98,s100,v217
v_dot2_f32_bf16 v218,v99,s98,v218
v_dot2_f32_bf16 v219,v99,s100,v219
v_dot2_f32_bf16 v212,v100,s98,v212
v_dot2_f32_bf16 v213,v100,s100,v213
v_dot2_f32_bf16 v214,v101,s98,v214
v_dot2_f32_bf16 v215,v101,s100,v215
v_dot2_f32_bf16 v220,v102,s98,v220
v_dot2_f32_bf16 v221,v102,s100,v221
v_dot2_f32_bf16 v222,v103,s98,v222
v_dot2_f32_bf16 v223,v103,s100,v223
v_dot2_f32_bf16 v208,v104,s98,v208
v_dot2_f32_bf16 v209,v104,s100,v209
v_dot2_f32_bf16 v210,v105,s98,v210
v_dot2_f32_bf16 v211,v105,s100,v211
v_dot2_f32_bf16 v216,v106,s98,v216
v_dot2_f32_bf16 v217,v106,s100,v217
v_dot2_f32_bf16 v218,v107,s98,v218
v_dot2_f32_bf16 v219,v107,s100,v219
v_dot2_f32_bf16 v212,v108,s98,v212
v_dot2_f32_bf16 v213,v108,s100,v213
v_dot2_f32_bf16 v214,v109,s98,v214
v_dot2_f32_bf16 v215,v109,s100,v215
v_dot2_f32_bf16 v220,v110,s98,v220
v_dot2_f32_bf16 v221,v110,s100,v221
v_dot2_f32_bf16 v222,v111,s98,v222
v_dot2_f32_bf16 v223,v111,s100,v223
v_dot2_f32_bf16 v208,v112,s98,v208
v_dot2_f32_bf16 v209,v112,s100,v209
buffer_load_dword v224,v226,s[88:91],s80 offen sc1
v_dot2_f32_bf16 v210,v113,s98,v210
v_dot2_f32_bf16 v211,v113,s100,v211
v_dot2_f32_bf16 v216,v114,s98,v216
v_dot2_f32_bf16 v217,v114,s100,v217
v_dot2_f32_bf16 v218,v115,s98,v218
v_dot2_f32_bf16 v219,v115,s100,v219
v_dot2_f32_bf16 v212,v116,s98,v212
v_dot2_f32_bf16 v213,v116,s100,v213
v_dot2_f32_bf16 v214,v117,s98,v214
v_dot2_f32_bf16 v215,v117,s100,v215
v_dot2_f32_bf16 v220,v118,s98,v220
v_dot2_f32_bf16 v221,v118,s100,v221
v_dot2_f32_bf16 v222,v119,s98,v222
v_dot2_f32_bf16 v223,v119,s100,v223
v_dot2_f32_bf16 v208,v120,s98,v208
v_dot2_f32_bf16 v209,v120,s100,v209
v_dot2_f32_bf16 v210,v121,s98,v210
)"
R"(v_dot2_f32_bf16 v211,v121,s100,v211
v_dot2_f32_bf16 v216,v122,s98,v216
v_dot2_f32_bf16 v217,v122,s100,v217
v_dot2_f32_bf16 v218,v123,s98,v218
v_dot2_f32_bf16 v219,v123,s100,v219
v_dot2_f32_bf16 v212,v124,s98,v212
v_dot2_f32_bf16 v213,v124,s100,v213
v_dot2_f32_bf16 v214,v125,s98,v214
v_dot2_f32_bf16 v215,v125,s100,v215
v_dot2_f32_bf16 v220,v126,s98,v220
v_dot2_f32_bf16 v221,v126,s100,v221
v_dot2_f32_bf16 v222,v127,s98,v222
v_dot2_f32_bf16 v223,v127,s100,v223
v_cvt_pk_bf16_f32 v208,0,v208
v_cvt_pk_bf16_f32 v209,0,v209
v_cvt_pk_bf16_f32 v210,0,v210
v_cvt_pk_bf16_f32 v211,0,v211
v_cvt_pk_bf16_f32 v212,0,v212
v_cvt_pk_bf16_f32 v213,0,v213
v_cvt_pk_bf16_f32 v214,0,v214
v_cvt_pk_bf16_f32 v215,0,v215
v_cvt_pk_bf16_f32 v216,0,v216
v_cvt_pk_bf16_f32 v217,0,v217
v_cvt_pk_bf16_f32 v218,0,v218
v_cvt_pk_bf16_f32 v219,0,v219
v_cvt_pk_bf16_f32 v220,0,v220
v_cvt_pk_bf16_f32 v221,0,v221
v_cvt_pk_bf16_f32 v222,0,v222
v_cvt_pk_bf16_f32 v223,0,v223
v_mul_f32 v144,0xbfb8aa3b,v208
v_mul_f32 v145,0xbfb8aa3b,v209
v_mul_f32 v146,0xbfb8aa3b,v210
v_mul_f32 v147,0xbfb8aa3b,v211
s_waitcnt vmcnt(1)
s_barrier
v_mov_b32 v229,8
buffer_store_dword v229,off,s[88:91],s79 sc1
s_waitcnt vmcnt(1)
v_writelane_b32 v224,8,m0
v_cmp_le_u32 vcc,s81,v224
s_nop 1
s_andn2_b32 s85,vcc_lo,s84
s_and_b32 s85,s85,255
s_or_b32 s84,s84,s85
s_mov_b32 s99,s55
s_add_u32 s101,s55,1024
s_bitcmp1_b32 s85,0
s_cselect_b32 s26,s42,0
buffer_load_dwordx4 v[0:3],v255,s[24:27],s99 offen sc1
buffer_load_dwordx4 v[64:67],v255,s[24:27],s101 offen sc1
s_add_u32 s99,s99,1835008
s_add_u32 s101,s101,1835008
s_bitcmp1_b32 s85,1
s_cselect_b32 s26,s42,0
buffer_load_dwordx4 v[4:7],v255,s[24:27],s99 offen sc1
buffer_load_dwordx4 v[68:71],v255,s[24:27],s101 offen sc1
s_add_u32 s99,s99,1835008
s_add_u32 s101,s101,1835008
s_bitcmp1_b32 s85,2
s_cselect_b32 s26,s42,0
buffer_load_dwordx4 v[8:11],v255,s[24:27],s99 offen sc1
buffer_load_dwordx4 v[72:75],v255,s[24:27],s101 offen sc1
s_add_u32 s99,s99,1835008
s_add_u32 s101,s101,1835008
s_bitcmp1_b32 s85,3
s_cselect_b32 s26,s42,0
buffer_load_dwordx4 v[12:15],v255,s[24:27],s99 offen sc1
buffer_load_dwordx4 v[76:79],v255,s[24:27],s101 offen sc1
s_add_u32 s99,s99,1835008
s_add_u32 s101,s101,1835008
s_bitcmp1_b32 s85,4
s_cselect_b32 s26,s42,0
buffer_load_dwordx4 v[16:19],v255,s[24:27],s99 offen sc1
buffer_load_dwordx4 v[80:83],v255,s[24:27],s101 offen sc1
s_add_u32 s99,s99,1835008
s_add_u32 s101,s101,1835008
s_bitcmp1_b32 s85,5
s_cselect_b32 s26,s42,0
buffer_load_dwordx4 v[20:23],v255,s[24:27],s99 offen sc1
buffer_load_dwordx4 v[84:87],v255,s[24:27],s101 offen sc1
s_add_u32 s99,s99,1835008
s_add_u32 s101,s101,1835008
s_bitcmp1_b32 s85,6
s_cselect_b32 s26,s42,0
buffer_load_dwordx4 v[24:27],v255,s[24:27],s99 offen sc1
buffer_load_dwordx4 v[88:91],v255,s[24:27],s101 offen sc1
s_add_u32 s99,s99,1835008
s_add_u32 s101,s101,1835008
s_bitcmp1_b32 s85,7
s_cselect_b32 s26,s42,0
buffer_load_dwordx4 v[28:31],v255,s[24:27],s99 offen sc1
buffer_load_dwordx4 v[92:95],v255,s[24:27],s101 offen sc1
s_mov_b64 exec,-1
v_mul_f32 v148,0xbfb8aa3b,v212
v_mul_f32 v149,0xbfb8aa3b,v213
v_mul_f32 v150,0xbfb8aa3b,v214
v_mul_f32 v151,0xbfb8aa3b,v215
v_mul_f32 v152,0xbfb8aa3b,v216
v_mul_f32 v153,0xbfb8aa3b,v217
v_mul_f32 v154,0xbfb8aa3b,v218
v_mul_f32 v155,0xbfb8aa3b,v219
v_mul_f32 v156,0xbfb8aa3b,v220
v_mul_f32 v157,0xbfb8aa3b,v221
v_mul_f32 v158,0xbfb8aa3b,v222
v_mul_f32 v159,0xbfb8aa3b,v223
v_exp_f32 v144,v144
v_exp_f32 v145,v145
v_exp_f32 v146,v146
v_exp_f32 v147,v147
v_exp_f32 v148,v148
v_exp_f32 v149,v149
v_exp_f32 v150,v150
v_exp_f32 v151,v151
v_exp_f32 v152,v152
v_exp_f32 v153,v153
v_exp_f32 v154,v154
v_exp_f32 v155,v155
v_exp_f32 v156,v156
v_exp_f32 v157,v157
v_exp_f32 v158,v158
v_exp_f32 v159,v159
v_add_f32 v144,1.0,v144
v_add_f32 v145,1.0,v145
v_add_f32 v146,1.0,v146
v_add_f32 v147,1.0,v147
v_add_f32 v148,1.0,v148
v_add_f32 v149,1.0,v149
v_add_f32 v150,1.0,v150
v_add_f32 v151,1.0,v151
v_add_f32 v152,1.0,v152
v_add_f32 v153,1.0,v153
v_add_f32 v154,1.0,v154
v_add_f32 v155,1.0,v155
v_add_f32 v156,1.0,v156
v_add_f32 v157,1.0,v157
v_add_f32 v158,1.0,v158
v_add_f32 v159,1.0,v159
v_rcp_f32 v144,v144
v_rcp_f32 v145,v145
v_rcp_f32 v146,v146
v_rcp_f32 v147,v147
v_rcp_f32 v148,v148
v_rcp_f32 v149,v149
v_rcp_f32 v150,v150
v_rcp_f32 v151,v151
v_rcp_f32 v152,v152
v_rcp_f32 v153,v153
v_rcp_f32 v154,v154
v_rcp_f32 v155,v155
v_rcp_f32 v156,v156
v_rcp_f32 v157,v157
v_rcp_f32 v158,v158
v_rcp_f32 v159,v159
v_mul_f32 v208,v208,v144
v_mul_f32 v209,v209,v145
v_mul_f32 v210,v210,v146
v_mul_f32 v211,v211,v147
v_mul_f32 v212,v212,v148
v_mul_f32 v213,v213,v149
v_mul_f32 v214,v214,v150
v_mul_f32 v215,v215,v151
v_mul_f32 v216,v216,v152
v_mul_f32 v217,v217,v153
v_mul_f32 v218,v218,v154
v_mul_f32 v219,v219,v155
v_mul_f32 v220,v220,v156
v_mul_f32 v221,v221,v157
v_mul_f32 v222,v222,v158
v_mul_f32 v223,v223,v159
v_cvt_pk_bf16_f32 v208,0,v208
v_cvt_pk_bf16_f32 v209,0,v209
v_cvt_pk_bf16_f32 v210,0,v210
v_cvt_pk_bf16_f32 v211,0,v211
v_cvt_pk_bf16_f32 v212,0,v212
v_cvt_pk_bf16_f32 v213,0,v213
v_cvt_pk_bf16_f32 v214,0,v214
v_cvt_pk_bf16_f32 v215,0,v215
v_cvt_pk_bf16_f32 v216,0,v216
v_cvt_pk_bf16_f32 v217,0,v217
v_cvt_pk_bf16_f32 v218,0,v218
v_cvt_pk_bf16_f32 v219,0,v219
v_cvt_pk_bf16_f32 v220,0,v220
v_cvt_pk_bf16_f32 v221,0,v221
v_cvt_pk_bf16_f32 v222,0,v222
v_cvt_pk_bf16_f32 v223,0,v223
v_lshlrev_b32 v128,16,v232
v_and_b32 v129,0xffff0000,v232
v_lshlrev_b32 v130,16,v233
v_and_b32 v131,0xffff0000,v233
v_lshlrev_b32 v132,16,v234
v_and_b32 v133,0xffff0000,v234
v_lshlrev_b32 v134,16,v235
v_and_b32 v135,0xffff0000,v235
v_lshlrev_b32 v136,16,v236
v_and_b32 v137,0xffff0000,v236
v_lshlrev_b32 v138,16,v237
v_and_b32 v139,0xffff0000,v237
v_lshlrev_b32 v140,16,v238
v_and_b32 v141,0xffff0000,v238
v_lshlrev_b32 v142,16,v239
)"
R"(v_and_b32 v143,0xffff0000,v239
s_cmp_lg_u32 s97,0
s_cbranch_scc1 Lg4b_%=
Lg2b_%=:
s_cmp_lg_u32 s84,255
s_cbranch_scc1 Lf0b_%=
s_cmp_lg_u32 s97,0
s_cbranch_scc1 Lf3b_%=
Lf1b_%=:
s_waitcnt vmcnt(14)
v_dot2_f32_bf16 v192,v0,s98,v192
v_dot2_f32_bf16 v193,v0,s100,v193
v_dot2_f32_bf16 v194,v1,s98,v194
v_dot2_f32_bf16 v195,v1,s100,v195
v_dot2_f32_bf16 v200,v2,s98,v200
v_dot2_f32_bf16 v201,v2,s100,v201
v_dot2_f32_bf16 v202,v3,s98,v202
v_dot2_f32_bf16 v203,v3,s100,v203
v_dot2_f32_bf16 v196,v64,s98,v196
v_dot2_f32_bf16 v197,v64,s100,v197
v_dot2_f32_bf16 v198,v65,s98,v198
v_dot2_f32_bf16 v199,v65,s100,v199
v_dot2_f32_bf16 v204,v66,s98,v204
v_dot2_f32_bf16 v205,v66,s100,v205
v_dot2_f32_bf16 v206,v67,s98,v206
v_dot2_f32_bf16 v207,v67,s100,v207
s_waitcnt vmcnt(12)
v_dot2_f32_bf16 v192,v4,s98,v192
v_dot2_f32_bf16 v193,v4,s100,v193
v_dot2_f32_bf16 v194,v5,s98,v194
v_dot2_f32_bf16 v195,v5,s100,v195
v_dot2_f32_bf16 v200,v6,s98,v200
v_dot2_f32_bf16 v201,v6,s100,v201
v_dot2_f32_bf16 v202,v7,s98,v202
v_dot2_f32_bf16 v203,v7,s100,v203
v_dot2_f32_bf16 v196,v68,s98,v196
v_dot2_f32_bf16 v197,v68,s100,v197
v_dot2_f32_bf16 v198,v69,s98,v198
v_dot2_f32_bf16 v199,v69,s100,v199
v_dot2_f32_bf16 v204,v70,s98,v204
v_dot2_f32_bf16 v205,v70,s100,v205
v_dot2_f32_bf16 v206,v71,s98,v206
v_dot2_f32_bf16 v207,v71,s100,v207
s_waitcnt vmcnt(10)
v_dot2_f32_bf16 v192,v8,s98,v192
v_dot2_f32_bf16 v193,v8,s100,v193
v_dot2_f32_bf16 v194,v9,s98,v194
v_dot2_f32_bf16 v195,v9,s100,v195
v_dot2_f32_bf16 v200,v10,s98,v200
v_dot2_f32_bf16 v201,v10,s100,v201
v_dot2_f32_bf16 v202,v11,s98,v202
v_dot2_f32_bf16 v203,v11,s100,v203
v_dot2_f32_bf16 v196,v72,s98,v196
v_dot2_f32_bf16 v197,v72,s100,v197
v_dot2_f32_bf16 v198,v73,s98,v198
v_dot2_f32_bf16 v199,v73,s100,v199
v_dot2_f32_bf16 v204,v74,s98,v204
v_dot2_f32_bf16 v205,v74,s100,v205
v_dot2_f32_bf16 v206,v75,s98,v206
v_dot2_f32_bf16 v207,v75,s100,v207
s_waitcnt vmcnt(8)
v_dot2_f32_bf16 v192,v12,s98,v192
v_dot2_f32_bf16 v193,v12,s100,v193
v_dot2_f32_bf16 v194,v13,s98,v194
v_dot2_f32_bf16 v195,v13,s100,v195
v_dot2_f32_bf16 v200,v14,s98,v200
v_dot2_f32_bf16 v201,v14,s100,v201
v_dot2_f32_bf16 v202,v15,s98,v202
v_dot2_f32_bf16 v203,v15,s100,v203
v_dot2_f32_bf16 v196,v76,s98,v196
v_dot2_f32_bf16 v197,v76,s100,v197
v_dot2_f32_bf16 v198,v77,s98,v198
v_dot2_f32_bf16 v199,v77,s100,v199
v_dot2_f32_bf16 v204,v78,s98,v204
v_dot2_f32_bf16 v205,v78,s100,v205
v_dot2_f32_bf16 v206,v79,s98,v206
v_dot2_f32_bf16 v207,v79,s100,v207
s_waitcnt vmcnt(6)
v_dot2_f32_bf16 v192,v16,s98,v192
v_dot2_f32_bf16 v193,v16,s100,v193
v_dot2_f32_bf16 v194,v17,s98,v194
v_dot2_f32_bf16 v195,v17,s100,v195
v_dot2_f32_bf16 v200,v18,s98,v200
v_dot2_f32_bf16 v201,v18,s100,v201
v_dot2_f32_bf16 v202,v19,s98,v202
v_dot2_f32_bf16 v203,v19,s100,v203
v_dot2_f32_bf16 v196,v80,s98,v196
v_dot2_f32_bf16 v197,v80,s100,v197
v_dot2_f32_bf16 v198,v81,s98,v198
v_dot2_f32_bf16 v199,v81,s100,v199
v_dot2_f32_bf16 v204,v82,s98,v204
v_dot2_f32_bf16 v205,v82,s100,v205
v_dot2_f32_bf16 v206,v83,s98,v206
v_dot2_f32_bf16 v207,v83,s100,v207
s_waitcnt vmcnt(4)
v_dot2_f32_bf16 v192,v20,s98,v192
v_dot2_f32_bf16 v193,v20,s100,v193
v_dot2_f32_bf16 v194,v21,s98,v194
v_dot2_f32_bf16 v195,v21,s100,v195
v_dot2_f32_bf16 v200,v22,s98,v200
v_dot2_f32_bf16 v201,v22,s100,v201
v_dot2_f32_bf16 v202,v23,s98,v202
v_dot2_f32_bf16 v203,v23,s100,v203
v_dot2_f32_bf16 v196,v84,s98,v196
v_dot2_f32_bf16 v197,v84,s100,v197
v_dot2_f32_bf16 v198,v85,s98,v198
v_dot2_f32_bf16 v199,v85,s100,v199
v_dot2_f32_bf16 v204,v86,s98,v204
v_dot2_f32_bf16 v205,v86,s100,v205
v_dot2_f32_bf16 v206,v87,s98,v206
v_dot2_f32_bf16 v207,v87,s100,v207
s_waitcnt vmcnt(2)
v_dot2_f32_bf16 v192,v24,s98,v192
v_dot2_f32_bf16 v193,v24,s100,v193
v_dot2_f32_bf16 v194,v25,s98,v194
v_dot2_f32_bf16 v195,v25,s100,v195
v_dot2_f32_bf16 v200,v26,s98,v200
v_dot2_f32_bf16 v201,v26,s100,v201
v_dot2_f32_bf16 v202,v27,s98,v202
v_dot2_f32_bf16 v203,v27,s100,v203
v_dot2_f32_bf16 v196,v88,s98,v196
v_dot2_f32_bf16 v197,v88,s100,v197
v_dot2_f32_bf16 v198,v89,s98,v198
v_dot2_f32_bf16 v199,v89,s100,v199
v_dot2_f32_bf16 v204,v90,s98,v204
v_dot2_f32_bf16 v205,v90,s100,v205
v_dot2_f32_bf16 v206,v91,s98,v206
v_dot2_f32_bf16 v207,v91,s100,v207
s_waitcnt vmcnt(0)
v_dot2_f32_bf16 v192,v28,s98,v192
v_dot2_f32_bf16 v193,v28,s100,v193
v_dot2_f32_bf16 v194,v29,s98,v194
v_dot2_f32_bf16 v195,v29,s100,v195
v_dot2_f32_bf16 v200,v30,s98,v200
v_dot2_f32_bf16 v201,v30,s100,v201
v_dot2_f32_bf16 v202,v31,s98,v202
v_dot2_f32_bf16 v203,v31,s100,v203
v_dot2_f32_bf16 v196,v92,s98,v196
v_dot2_f32_bf16 v197,v92,s100,v197
v_dot2_f32_bf16 v198,v93,s98,v198
v_dot2_f32_bf16 v199,v93,s100,v199
v_dot2_f32_bf16 v204,v94,s98,v204
v_dot2_f32_bf16 v205,v94,s100,v205
v_dot2_f32_bf16 v206,v95,s98,v206
v_dot2_f32_bf16 v207,v95,s100,v207
v_cvt_pk_bf16_f32 v192,0,v192
v_cvt_pk_bf16_f32 v193,0,v193
v_cvt_pk_bf16_f32 v194,0,v194
v_cvt_pk_bf16_f32 v195,0,v195
v_cvt_pk_bf16_f32 v196,0,v196
v_cvt_pk_bf16_f32 v197,0,v197
v_cvt_pk_bf16_f32 v198,0,v198
v_cvt_pk_bf16_f32 v199,0,v199
v_cvt_pk_bf16_f32 v200,0,v200
v_cvt_pk_bf16_f32 v201,0,v201
v_cvt_pk_bf16_f32 v202,0,v202
v_cvt_pk_bf16_f32 v203,0,v203
v_cvt_pk_bf16_f32 v204,0,v204
v_cvt_pk_bf16_f32 v205,0,v205
v_cvt_pk_bf16_f32 v206,0,v206
v_cvt_pk_bf16_f32 v207,0,v207
v_mul_f32 v192,v192,v208
v_mul_f32 v193,v193,v209
v_mul_f32 v194,v194,v210
v_mul_f32 v195,v195,v211
v_mul_f32 v196,v196,v212
v_mul_f32 v197,v197,v213
v_mul_f32 v198,v198,v214
v_mul_f32 v199,v199,v215
v_mul_f32 v200,v200,v216
v_mul_f32 v201,v201,v217
v_mul_f32 v202,v202,v218
v_mul_f32 v203,v203,v219
v_mul_f32 v204,v204,v220
v_mul_f32 v205,v205,v221
v_mul_f32 v206,v206,v222
v_mul_f32 v207,v207,v223
v_cvt_pk_bf16_f32 v192,v192,v193
v_cvt_pk_bf16_f32 v193,v194,v195
v_cvt_pk_bf16_f32 v194,v196,v197
v_cvt_pk_bf16_f32 v195,v198,v199
v_cvt_pk_bf16_f32 v196,v200,v201
v_cvt_pk_bf16_f32 v197,v202,v203
v_cvt_pk_bf16_f32 v198,v204,v205
v_cvt_pk_bf16_f32 v199,v206,v207
ds_write_b64 v228,v[192:193]
ds_write_b64 v230,v[194:195]
ds_write_b64 v228,v[196:197] offset:1024
ds_write_b64 v230,v[198:199] offset:1024
s_waitcnt lgkmcnt(0)
ds_read_b128 v[160:163],v225
ds_read_b128 v[164:167],v225 offset:1024
s_waitcnt lgkmcnt(0)
v_lshlrev_b32 v144,16,v160
v_and_b32 v145,0xffff0000,v160
v_lshlrev_b32 v146,16,v161
v_and_b32 v147,0xffff0000,v161
v_lshlrev_b32 v148,16,v162
v_and_b32 v149,0xffff0000,v162
v_lshlrev_b32 v150,16,v163
)"
R"(v_and_b32 v151,0xffff0000,v163
v_lshlrev_b32 v152,16,v164
v_and_b32 v153,0xffff0000,v164
v_lshlrev_b32 v154,16,v165
v_and_b32 v155,0xffff0000,v165
v_lshlrev_b32 v156,16,v166
v_and_b32 v157,0xffff0000,v166
v_lshlrev_b32 v158,16,v167
v_and_b32 v159,0xffff0000,v167
v_add_f32 v128,v128,v144
v_add_f32 v129,v129,v145
v_add_f32 v130,v130,v146
v_add_f32 v131,v131,v147
v_add_f32 v132,v132,v148
v_add_f32 v133,v133,v149
v_add_f32 v134,v134,v150
v_add_f32 v135,v135,v151
v_add_f32 v136,v136,v152
v_add_f32 v137,v137,v153
v_add_f32 v138,v138,v154
v_add_f32 v139,v139,v155
v_add_f32 v140,v140,v156
v_add_f32 v141,v141,v157
v_add_f32 v142,v142,v158
v_add_f32 v143,v143,v159
v_cvt_pk_bf16_f32 v128,v128,v129
v_cvt_pk_bf16_f32 v129,v130,v131
v_cvt_pk_bf16_f32 v130,v132,v133
v_cvt_pk_bf16_f32 v131,v134,v135
v_cvt_pk_bf16_f32 v132,v136,v137
v_cvt_pk_bf16_f32 v133,v138,v139
v_cvt_pk_bf16_f32 v134,v140,v141
v_cvt_pk_bf16_f32 v135,v142,v143
s_add_u32 s101,s82,14336
buffer_store_dwordx4 v[128:131],v227,s[48:51],s82 offen sc1
buffer_store_dwordx4 v[132:135],v227,s[48:51],s101 offen sc1
s_branch Lendb_%=
Lstub0b_%=:
s_getpc_b64 s[76:77]
s_add_u32 s76,s76,12
s_addc_u32 s77,s77,0
s_branch Lrare0b_%=
s_branch Lback0b_%=
Lstub1b_%=:
s_getpc_b64 s[76:77]
s_add_u32 s76,s76,12
s_addc_u32 s77,s77,0
s_branch Lrare1b_%=
s_branch Lback1b_%=
Lf1lb_%=:
s_lshr_b32 s97,%[tb],2
s_lshl_b32 s97,s97,10
s_add_u32 s97,s97,0x2b00000
v_mbcnt_lo_u32_b32 v254,-1,0
v_mbcnt_hi_u32_b32 v254,-1,v254
v_and_b32 v254,7,v254
v_lshlrev_b32 v254,7,v254
buffer_load_dword v254,v254,s[88:91],s97 offen sc1
s_branch Lloopb_%=
Lstub2b_%=:
s_getpc_b64 s[76:77]
s_add_u32 s76,s76,12
s_addc_u32 s77,s77,0
s_branch Lrare0b_%=
s_branch Lback2b_%=
Lroleb_%=:
s_mov_b32 s74,1000000
Lrwbb_%=:
s_sleep 8
v_mbcnt_lo_u32_b32 v255,-1,0
v_mbcnt_hi_u32_b32 v255,-1,v255
v_and_b32 v255,7,v255
v_lshlrev_b32 v255,7,v255
buffer_load_dword v255,v255,s[88:91],s97 offen offset:4 sc1
s_waitcnt vmcnt(0)
v_cmp_ne_u32 s[84:85],0,v255
s_nop 0
s_lshr_b32 s85,s84,1
s_or_b32 s84,s84,s85
s_and_b32 s84,s84,85
s_cmp_lg_u32 s84,85
s_cbranch_scc0 Lok3b_%=
s_sub_u32 s74,s74,1
s_cmp_lg_u32 s74,0
s_cbranch_scc1 Lrwbb_%=
s_branch Lok3b_%=
Lspin1b_%=:
s_mov_b32 s74,1000000
Lspin1bb_%=:
s_sleep 4
v_mbcnt_lo_u32_b32 v254,-1,0
v_mbcnt_hi_u32_b32 v254,-1,v254
v_and_b32 v254,7,v254
v_lshlrev_b32 v254,7,v254
buffer_load_dword v254,v254,s[88:91],s97 offen sc1
s_waitcnt vmcnt(0)
v_cmp_eq_u32 vcc,v254,v255
s_nop 1
s_cbranch_vccz Lok1b_%=
s_sub_u32 s74,s74,1
s_cmp_lg_u32 s74,0
s_cbranch_scc1 Lspin1bb_%=
s_branch Lok1b_%=
Lstub3b_%=:
s_getpc_b64 s[76:77]
s_add_u32 s76,s76,12
s_addc_u32 s77,s77,0
s_branch Lrare1b_%=
s_branch Lback3b_%=
Lg1b_%=:
buffer_load_dword v224,v226,s[88:91],s80 offen sc1
s_waitcnt vmcnt(0)
Lg4b_%=:
v_cmp_gt_u32 vcc,s52,v224
s_nop 0
s_and_b32 s97,vcc_lo,0xff
s_cbranch_scc0 Lg3b_%=
v_cmp_le_u32 vcc,s81,v224
s_nop 1
s_andn2_b32 s85,vcc_lo,s84
s_and_b32 s85,s85,255
s_or_b32 s84,s84,s85
s_mov_b32 s99,s55
s_add_u32 s101,s55,1024
s_bitcmp1_b32 s85,0
s_cselect_b64 exec,-1,0
buffer_load_dwordx4 v[0:3],v255,s[40:43],s99 offen sc1
buffer_load_dwordx4 v[64:67],v255,s[40:43],s101 offen sc1
s_add_u32 s99,s99,1835008
s_add_u32 s101,s101,1835008
s_bitcmp1_b32 s85,1
s_cselect_b64 exec,-1,0
buffer_load_dwordx4 v[4:7],v255,s[40:43],s99 offen sc1
buffer_load_dwordx4 v[68:71],v255,s[40:43],s101 offen sc1
s_add_u32 s99,s99,1835008
s_add_u32 s101,s101,1835008
s_bitcmp1_b32 s85,2
s_cselect_b64 exec,-1,0
buffer_load_dwordx4 v[8:11],v255,s[40:43],s99 offen sc1
buffer_load_dwordx4 v[72:75],v255,s[40:43],s101 offen sc1
s_add_u32 s99,s99,1835008
s_add_u32 s101,s101,1835008
s_bitcmp1_b32 s85,3
s_cselect_b64 exec,-1,0
buffer_load_dwordx4 v[12:15],v255,s[40:43],s99 offen sc1
buffer_load_dwordx4 v[76:79],v255,s[40:43],s101 offen sc1
s_add_u32 s99,s99,1835008
s_add_u32 s101,s101,1835008
s_bitcmp1_b32 s85,4
s_cselect_b64 exec,-1,0
buffer_load_dwordx4 v[16:19],v255,s[40:43],s99 offen sc1
buffer_load_dwordx4 v[80:83],v255,s[40:43],s101 offen sc1
s_add_u32 s99,s99,1835008
s_add_u32 s101,s101,1835008
s_bitcmp1_b32 s85,5
s_cselect_b64 exec,-1,0
buffer_load_dwordx4 v[20:23],v255,s[40:43],s99 offen sc1
buffer_load_dwordx4 v[84:87],v255,s[40:43],s101 offen sc1
s_add_u32 s99,s99,1835008
s_add_u32 s101,s101,1835008
s_bitcmp1_b32 s85,6
s_cselect_b64 exec,-1,0
buffer_load_dwordx4 v[24:27],v255,s[40:43],s99 offen sc1
buffer_load_dwordx4 v[88:91],v255,s[40:43],s101 offen sc1
s_add_u32 s99,s99,1835008
s_add_u32 s101,s101,1835008
s_bitcmp1_b32 s85,7
s_cselect_b64 exec,-1,0
buffer_load_dwordx4 v[28:31],v255,s[40:43],s99 offen sc1
buffer_load_dwordx4 v[92:95],v255,s[40:43],s101 offen sc1
s_mov_b64 exec,-1
s_sub_u32 s74,s74,1
s_cmp_lg_u32 s74,0
s_cbranch_scc1 Lg1b_%=
Lg3b_%=:
s_add_u32 s60,s83,0
s_add_u32 s61,s60,4096
buffer_load_dwordx4 v[96:99],v255,s[40:43],s60 offen sc1
buffer_load_dwordx4 v[100:103],v255,s[40:43],s61 offen sc1
s_add_u32 s60,s83,114688
s_add_u32 s61,s60,4096
buffer_load_dwordx4 v[104:107],v255,s[40:43],s60 offen sc1
buffer_load_dwordx4 v[108:111],v255,s[40:43],s61 offen sc1
s_add_u32 s60,s83,229376
s_add_u32 s61,s60,4096
buffer_load_dwordx4 v[112:115],v255,s[40:43],s60 offen sc1
buffer_load_dwordx4 v[116:119],v255,s[40:43],s61 offen sc1
s_add_u32 s60,s83,344064
s_add_u32 s61,s60,4096
buffer_load_dwordx4 v[120:123],v255,s[40:43],s60 offen sc1
buffer_load_dwordx4 v[124:127],v255,s[40:43],s61 offen sc1
s_waitcnt vmcnt(0)
v_cmp_le_u32 vcc,s81,v224
s_nop 1
s_andn2_b32 s85,vcc_lo,s84
s_and_b32 s85,s85,255
s_or_b32 s84,s84,s85
s_mov_b32 s99,s55
s_add_u32 s101,s55,1024
s_bitcmp1_b32 s85,0
)"
R"(s_cselect_b64 exec,-1,0
buffer_load_dwordx4 v[0:3],v255,s[40:43],s99 offen sc1
buffer_load_dwordx4 v[64:67],v255,s[40:43],s101 offen sc1
s_add_u32 s99,s99,1835008
s_add_u32 s101,s101,1835008
s_bitcmp1_b32 s85,1
s_cselect_b64 exec,-1,0
buffer_load_dwordx4 v[4:7],v255,s[40:43],s99 offen sc1
buffer_load_dwordx4 v[68:71],v255,s[40:43],s101 offen sc1
s_add_u32 s99,s99,1835008
s_add_u32 s101,s101,1835008
s_bitcmp1_b32 s85,2
s_cselect_b64 exec,-1,0
buffer_load_dwordx4 v[8:11],v255,s[40:43],s99 offen sc1
buffer_load_dwordx4 v[72:75],v255,s[40:43],s101 offen sc1
s_add_u32 s99,s99,1835008
s_add_u32 s101,s101,1835008
s_bitcmp1_b32 s85,3
s_cselect_b64 exec,-1,0
buffer_load_dwordx4 v[12:15],v255,s[40:43],s99 offen sc1
buffer_load_dwordx4 v[76:79],v255,s[40:43],s101 offen sc1
s_add_u32 s99,s99,1835008
s_add_u32 s101,s101,1835008
s_bitcmp1_b32 s85,4
s_cselect_b64 exec,-1,0
buffer_load_dwordx4 v[16:19],v255,s[40:43],s99 offen sc1
buffer_load_dwordx4 v[80:83],v255,s[40:43],s101 offen sc1
s_add_u32 s99,s99,1835008
s_add_u32 s101,s101,1835008
s_bitcmp1_b32 s85,5
s_cselect_b64 exec,-1,0
buffer_load_dwordx4 v[20:23],v255,s[40:43],s99 offen sc1
buffer_load_dwordx4 v[84:87],v255,s[40:43],s101 offen sc1
s_add_u32 s99,s99,1835008
s_add_u32 s101,s101,1835008
s_bitcmp1_b32 s85,6
s_cselect_b64 exec,-1,0
buffer_load_dwordx4 v[24:27],v255,s[40:43],s99 offen sc1
buffer_load_dwordx4 v[88:91],v255,s[40:43],s101 offen sc1
s_add_u32 s99,s99,1835008
s_add_u32 s101,s101,1835008
s_bitcmp1_b32 s85,7
s_cselect_b64 exec,-1,0
buffer_load_dwordx4 v[28:31],v255,s[40:43],s99 offen sc1
buffer_load_dwordx4 v[92:95],v255,s[40:43],s101 offen sc1
s_mov_b64 exec,-1
v_mov_b32 v208,0
v_mov_b32 v209,0
v_mov_b32 v210,0
v_mov_b32 v211,0
v_mov_b32 v212,0
v_mov_b32 v213,0
v_mov_b32 v214,0
v_mov_b32 v215,0
v_mov_b32 v216,0
v_mov_b32 v217,0
v_mov_b32 v218,0
v_mov_b32 v219,0
v_mov_b32 v220,0
v_mov_b32 v221,0
v_mov_b32 v222,0
v_mov_b32 v223,0
v_dot2_f32_bf16 v208,v96,s98,v208
v_dot2_f32_bf16 v209,v96,s100,v209
v_dot2_f32_bf16 v210,v97,s98,v210
v_dot2_f32_bf16 v211,v97,s100,v211
v_dot2_f32_bf16 v216,v98,s98,v216
v_dot2_f32_bf16 v217,v98,s100,v217
v_dot2_f32_bf16 v218,v99,s98,v218
v_dot2_f32_bf16 v219,v99,s100,v219
v_dot2_f32_bf16 v212,v100,s98,v212
v_dot2_f32_bf16 v213,v100,s100,v213
v_dot2_f32_bf16 v214,v101,s98,v214
v_dot2_f32_bf16 v215,v101,s100,v215
v_dot2_f32_bf16 v220,v102,s98,v220
v_dot2_f32_bf16 v221,v102,s100,v221
v_dot2_f32_bf16 v222,v103,s98,v222
v_dot2_f32_bf16 v223,v103,s100,v223
v_dot2_f32_bf16 v208,v104,s98,v208
v_dot2_f32_bf16 v209,v104,s100,v209
v_dot2_f32_bf16 v210,v105,s98,v210
v_dot2_f32_bf16 v211,v105,s100,v211
v_dot2_f32_bf16 v216,v106,s98,v216
v_dot2_f32_bf16 v217,v106,s100,v217
v_dot2_f32_bf16 v218,v107,s98,v218
v_dot2_f32_bf16 v219,v107,s100,v219
v_dot2_f32_bf16 v212,v108,s98,v212
v_dot2_f32_bf16 v213,v108,s100,v213
v_dot2_f32_bf16 v214,v109,s98,v214
v_dot2_f32_bf16 v215,v109,s100,v215
v_dot2_f32_bf16 v220,v110,s98,v220
v_dot2_f32_bf16 v221,v110,s100,v221
v_dot2_f32_bf16 v222,v111,s98,v222
v_dot2_f32_bf16 v223,v111,s100,v223
v_dot2_f32_bf16 v208,v112,s98,v208
v_dot2_f32_bf16 v209,v112,s100,v209
v_dot2_f32_bf16 v210,v113,s98,v210
v_dot2_f32_bf16 v211,v113,s100,v211
v_dot2_f32_bf16 v216,v114,s98,v216
v_dot2_f32_bf16 v217,v114,s100,v217
v_dot2_f32_bf16 v218,v115,s98,v218
v_dot2_f32_bf16 v219,v115,s100,v219
v_dot2_f32_bf16 v212,v116,s98,v212
v_dot2_f32_bf16 v213,v116,s100,v213
v_dot2_f32_bf16 v214,v117,s98,v214
v_dot2_f32_bf16 v215,v117,s100,v215
v_dot2_f32_bf16 v220,v118,s98,v220
v_dot2_f32_bf16 v221,v118,s100,v221
v_dot2_f32_bf16 v222,v119,s98,v222
v_dot2_f32_bf16 v223,v119,s100,v223
v_dot2_f32_bf16 v208,v120,s98,v208
v_dot2_f32_bf16 v209,v120,s100,v209
v_dot2_f32_bf16 v210,v121,s98,v210
v_dot2_f32_bf16 v211,v121,s100,v211
v_dot2_f32_bf16 v216,v122,s98,v216
v_dot2_f32_bf16 v217,v122,s100,v217
v_dot2_f32_bf16 v218,v123,s98,v218
v_dot2_f32_bf16 v219,v123,s100,v219
v_dot2_f32_bf16 v212,v124,s98,v212
v_dot2_f32_bf16 v213,v124,s100,v213
v_dot2_f32_bf16 v214,v125,s98,v214
v_dot2_f32_bf16 v215,v125,s100,v215
v_dot2_f32_bf16 v220,v126,s98,v220
v_dot2_f32_bf16 v221,v126,s100,v221
v_dot2_f32_bf16 v222,v127,s98,v222
v_dot2_f32_bf16 v223,v127,s100,v223
v_cvt_pk_bf16_f32 v208,0,v208
v_cvt_pk_bf16_f32 v209,0,v209
v_cvt_pk_bf16_f32 v210,0,v210
v_cvt_pk_bf16_f32 v211,0,v211
v_cvt_pk_bf16_f32 v212,0,v212
v_cvt_pk_bf16_f32 v213,0,v213
v_cvt_pk_bf16_f32 v214,0,v214
v_cvt_pk_bf16_f32 v215,0,v215
v_cvt_pk_bf16_f32 v216,0,v216
v_cvt_pk_bf16_f32 v217,0,v217
v_cvt_pk_bf16_f32 v218,0,v218
v_cvt_pk_bf16_f32 v219,0,v219
v_cvt_pk_bf16_f32 v220,0,v220
v_cvt_pk_bf16_f32 v221,0,v221
v_cvt_pk_bf16_f32 v222,0,v222
v_cvt_pk_bf16_f32 v223,0,v223
v_mul_f32 v144,0xbfb8aa3b,v208
v_mul_f32 v145,0xbfb8aa3b,v209
v_mul_f32 v146,0xbfb8aa3b,v210
v_mul_f32 v147,0xbfb8aa3b,v211
v_mul_f32 v148,0xbfb8aa3b,v212
v_mul_f32 v149,0xbfb8aa3b,v213
v_mul_f32 v150,0xbfb8aa3b,v214
v_mul_f32 v151,0xbfb8aa3b,v215
v_mul_f32 v152,0xbfb8aa3b,v216
v_mul_f32 v153,0xbfb8aa3b,v217
v_mul_f32 v154,0xbfb8aa3b,v218
v_mul_f32 v155,0xbfb8aa3b,v219
v_mul_f32 v156,0xbfb8aa3b,v220
v_mul_f32 v157,0xbfb8aa3b,v221
v_mul_f32 v158,0xbfb8aa3b,v222
v_mul_f32 v159,0xbfb8aa3b,v223
v_exp_f32 v144,v144
v_exp_f32 v145,v145
v_exp_f32 v146,v146
v_exp_f32 v147,v147
v_exp_f32 v148,v148
v_exp_f32 v149,v149
v_exp_f32 v150,v150
v_exp_f32 v151,v151
v_exp_f32 v152,v152
v_exp_f32 v153,v153
v_exp_f32 v154,v154
v_exp_f32 v155,v155
v_exp_f32 v156,v156
v_exp_f32 v157,v157
v_exp_f32 v158,v158
v_exp_f32 v159,v159
v_add_f32 v144,1.0,v144
v_add_f32 v145,1.0,v145
v_add_f32 v146,1.0,v146
v_add_f32 v147,1.0,v147
v_add_f32 v148,1.0,v148
v_add_f32 v149,1.0,v149
v_add_f32 v150,1.0,v150
v_add_f32 v151,1.0,v151
v_add_f32 v152,1.0,v152
v_add_f32 v153,1.0,v153
v_add_f32 v154,1.0,v154
v_add_f32 v155,1.0,v155
v_add_f32 v156,1.0,v156
v_add_f32 v157,1.0,v157
v_add_f32 v158,1.0,v158
v_add_f32 v159,1.0,v159
v_rcp_f32 v144,v144
v_rcp_f32 v145,v145
v_rcp_f32 v146,v146
v_rcp_f32 v147,v147
v_rcp_f32 v148,v148
v_rcp_f32 v149,v149
v_rcp_f32 v150,v150
v_rcp_f32 v151,v151
v_rcp_f32 v152,v152
v_rcp_f32 v153,v153
)"
R"(v_rcp_f32 v154,v154
v_rcp_f32 v155,v155
v_rcp_f32 v156,v156
v_rcp_f32 v157,v157
v_rcp_f32 v158,v158
v_rcp_f32 v159,v159
v_mul_f32 v208,v208,v144
v_mul_f32 v209,v209,v145
v_mul_f32 v210,v210,v146
v_mul_f32 v211,v211,v147
v_mul_f32 v212,v212,v148
v_mul_f32 v213,v213,v149
v_mul_f32 v214,v214,v150
v_mul_f32 v215,v215,v151
v_mul_f32 v216,v216,v152
v_mul_f32 v217,v217,v153
v_mul_f32 v218,v218,v154
v_mul_f32 v219,v219,v155
v_mul_f32 v220,v220,v156
v_mul_f32 v221,v221,v157
v_mul_f32 v222,v222,v158
v_mul_f32 v223,v223,v159
v_cvt_pk_bf16_f32 v208,0,v208
v_cvt_pk_bf16_f32 v209,0,v209
v_cvt_pk_bf16_f32 v210,0,v210
v_cvt_pk_bf16_f32 v211,0,v211
v_cvt_pk_bf16_f32 v212,0,v212
v_cvt_pk_bf16_f32 v213,0,v213
v_cvt_pk_bf16_f32 v214,0,v214
v_cvt_pk_bf16_f32 v215,0,v215
v_cvt_pk_bf16_f32 v216,0,v216
v_cvt_pk_bf16_f32 v217,0,v217
v_cvt_pk_bf16_f32 v218,0,v218
v_cvt_pk_bf16_f32 v219,0,v219
v_cvt_pk_bf16_f32 v220,0,v220
v_cvt_pk_bf16_f32 v221,0,v221
v_cvt_pk_bf16_f32 v222,0,v222
v_cvt_pk_bf16_f32 v223,0,v223
s_mov_b32 s97,1
s_branch Lg2b_%=
Lf0b_%=:
buffer_load_dword v224,v226,s[88:91],s80 offen sc1
s_waitcnt vmcnt(0)
v_cmp_le_u32 vcc,s81,v224
s_nop 1
s_andn2_b32 s85,vcc_lo,s84
s_and_b32 s85,s85,255
s_or_b32 s84,s84,s85
s_mov_b32 s99,s55
s_add_u32 s101,s55,1024
s_bitcmp1_b32 s85,0
s_cselect_b64 exec,-1,0
buffer_load_dwordx4 v[0:3],v255,s[40:43],s99 offen sc1
buffer_load_dwordx4 v[64:67],v255,s[40:43],s101 offen sc1
s_add_u32 s99,s99,1835008
s_add_u32 s101,s101,1835008
s_bitcmp1_b32 s85,1
s_cselect_b64 exec,-1,0
buffer_load_dwordx4 v[4:7],v255,s[40:43],s99 offen sc1
buffer_load_dwordx4 v[68:71],v255,s[40:43],s101 offen sc1
s_add_u32 s99,s99,1835008
s_add_u32 s101,s101,1835008
s_bitcmp1_b32 s85,2
s_cselect_b64 exec,-1,0
buffer_load_dwordx4 v[8:11],v255,s[40:43],s99 offen sc1
buffer_load_dwordx4 v[72:75],v255,s[40:43],s101 offen sc1
s_add_u32 s99,s99,1835008
s_add_u32 s101,s101,1835008
s_bitcmp1_b32 s85,3
s_cselect_b64 exec,-1,0
buffer_load_dwordx4 v[12:15],v255,s[40:43],s99 offen sc1
buffer_load_dwordx4 v[76:79],v255,s[40:43],s101 offen sc1
s_add_u32 s99,s99,1835008
s_add_u32 s101,s101,1835008
s_bitcmp1_b32 s85,4
s_cselect_b64 exec,-1,0
buffer_load_dwordx4 v[16:19],v255,s[40:43],s99 offen sc1
buffer_load_dwordx4 v[80:83],v255,s[40:43],s101 offen sc1
s_add_u32 s99,s99,1835008
s_add_u32 s101,s101,1835008
s_bitcmp1_b32 s85,5
s_cselect_b64 exec,-1,0
buffer_load_dwordx4 v[20:23],v255,s[40:43],s99 offen sc1
buffer_load_dwordx4 v[84:87],v255,s[40:43],s101 offen sc1
s_add_u32 s99,s99,1835008
s_add_u32 s101,s101,1835008
s_bitcmp1_b32 s85,6
s_cselect_b64 exec,-1,0
buffer_load_dwordx4 v[24:27],v255,s[40:43],s99 offen sc1
buffer_load_dwordx4 v[88:91],v255,s[40:43],s101 offen sc1
s_add_u32 s99,s99,1835008
s_add_u32 s101,s101,1835008
s_bitcmp1_b32 s85,7
s_cselect_b64 exec,-1,0
buffer_load_dwordx4 v[28:31],v255,s[40:43],s99 offen sc1
buffer_load_dwordx4 v[92:95],v255,s[40:43],s101 offen sc1
s_mov_b64 exec,-1
s_sub_u32 s74,s74,1
s_cmp_lt_i32 s74,1
s_cbranch_scc1 Lf3b_%=
s_cmp_lg_u32 s84,255
s_cbranch_scc1 Lf0b_%=
Lf3b_%=:
s_waitcnt vmcnt(0)
s_branch Lf1b_%=
Lrare0b_%=:
s_nop 7
v_max3_f32 v160,v128,v129,v130
v_max3_f32 v161,v136,v137,v138
v_max3_f32 v162,v144,v145,v146
v_max3_f32 v163,v152,v153,v154
v_max3_f32 v160,v131,v132,v160
v_max3_f32 v161,v139,v140,v161
v_max3_f32 v162,v147,v148,v162
v_max3_f32 v163,v155,v156,v163
v_max3_f32 v160,v133,v134,v160
v_max3_f32 v161,v141,v142,v161
v_max3_f32 v162,v149,v150,v162
v_max3_f32 v163,v157,v158,v163
v_max_f32 v160,v135,v160
v_max_f32 v161,v143,v161
v_max_f32 v162,v151,v162
v_max_f32 v163,v159,v163
v_mov_b32 v164,v160
v_mov_b32 v165,v161
v_mov_b32 v166,v162
v_mov_b32 v167,v163
v_permlane16_swap_b32 v160,v164
v_permlane16_swap_b32 v161,v165
v_permlane16_swap_b32 v162,v166
v_permlane16_swap_b32 v163,v167
v_max_f32 v160,v160,v164
v_max_f32 v161,v161,v165
v_max_f32 v162,v162,v166
v_max_f32 v163,v163,v167
v_mov_b32 v164,v160
v_mov_b32 v165,v161
v_mov_b32 v166,v162
v_mov_b32 v167,v163
v_permlane32_swap_b32 v160,v164
v_permlane32_swap_b32 v161,v165
v_permlane32_swap_b32 v162,v166
v_permlane32_swap_b32 v163,v167
v_max_f32 v160,v160,v164
v_max_f32 v161,v161,v165
v_max_f32 v162,v162,v166
v_max_f32 v163,v163,v167
v_add_f32 v160,0x42c00000,v160
v_max_f32 v160,0,v160
v_sub_f32 v244,v244,v160
v_exp_f32 v164,-v160
v_sub_f32 v128,v128,v160
v_sub_f32 v129,v129,v160
v_sub_f32 v130,v130,v160
v_sub_f32 v131,v131,v160
v_sub_f32 v132,v132,v160
v_sub_f32 v133,v133,v160
v_sub_f32 v134,v134,v160
v_sub_f32 v135,v135,v160
v_mul_f32 v224,v224,v164
v_mul_f32 v225,v225,v164
v_mul_f32 v226,v226,v164
v_mul_f32 v227,v227,v164
v_accvgpr_read_b32 v168,a0
v_accvgpr_read_b32 v169,a1
v_accvgpr_read_b32 v170,a2
v_accvgpr_read_b32 v171,a3
v_accvgpr_read_b32 v172,a4
v_accvgpr_read_b32 v173,a5
v_accvgpr_read_b32 v174,a6
v_accvgpr_read_b32 v175,a7
v_mul_f32 v168,v168,v164
v_mul_f32 v169,v169,v164
v_mul_f32 v170,v170,v164
v_mul_f32 v171,v171,v164
v_mul_f32 v172,v172,v164
v_mul_f32 v173,v173,v164
v_mul_f32 v174,v174,v164
v_mul_f32 v175,v175,v164
v_accvgpr_write_b32 a0,v168
v_accvgpr_write_b32 a1,v169
v_accvgpr_write_b32 a2,v170
v_accvgpr_write_b32 a3,v171
v_accvgpr_write_b32 a4,v172
v_accvgpr_write_b32 a5,v173
v_accvgpr_write_b32 a6,v174
v_accvgpr_write_b32 a7,v175
v_accvgpr_read_b32 v168,a8
v_accvgpr_read_b32 v169,a9
v_accvgpr_read_b32 v170,a10
v_accvgpr_read_b32 v171,a11
v_accvgpr_read_b32 v172,a12
v_accvgpr_read_b32 v173,a13
v_accvgpr_read_b32 v174,a14
v_accvgpr_read_b32 v175,a15
v_mul_f32 v168,v168,v164
v_mul_f32 v169,v169,v164
v_mul_f32 v170,v170,v164
v_mul_f32 v171,v171,v164
v_mul_f32 v172,v172,v164
)"
R"(v_mul_f32 v173,v173,v164
v_mul_f32 v174,v174,v164
v_mul_f32 v175,v175,v164
v_accvgpr_write_b32 a8,v168
v_accvgpr_write_b32 a9,v169
v_accvgpr_write_b32 a10,v170
v_accvgpr_write_b32 a11,v171
v_accvgpr_write_b32 a12,v172
v_accvgpr_write_b32 a13,v173
v_accvgpr_write_b32 a14,v174
v_accvgpr_write_b32 a15,v175
v_accvgpr_read_b32 v168,a16
v_accvgpr_read_b32 v169,a17
v_accvgpr_read_b32 v170,a18
v_accvgpr_read_b32 v171,a19
v_accvgpr_read_b32 v172,a20
v_accvgpr_read_b32 v173,a21
v_accvgpr_read_b32 v174,a22
v_accvgpr_read_b32 v175,a23
v_mul_f32 v168,v168,v164
v_mul_f32 v169,v169,v164
v_mul_f32 v170,v170,v164
v_mul_f32 v171,v171,v164
v_mul_f32 v172,v172,v164
v_mul_f32 v173,v173,v164
v_mul_f32 v174,v174,v164
v_mul_f32 v175,v175,v164
v_accvgpr_write_b32 a16,v168
v_accvgpr_write_b32 a17,v169
v_accvgpr_write_b32 a18,v170
v_accvgpr_write_b32 a19,v171
v_accvgpr_write_b32 a20,v172
v_accvgpr_write_b32 a21,v173
v_accvgpr_write_b32 a22,v174
v_accvgpr_write_b32 a23,v175
v_accvgpr_read_b32 v168,a24
v_accvgpr_read_b32 v169,a25
v_accvgpr_read_b32 v170,a26
v_accvgpr_read_b32 v171,a27
v_accvgpr_read_b32 v172,a28
v_accvgpr_read_b32 v173,a29
v_accvgpr_read_b32 v174,a30
v_accvgpr_read_b32 v175,a31
v_mul_f32 v168,v168,v164
v_mul_f32 v169,v169,v164
v_mul_f32 v170,v170,v164
v_mul_f32 v171,v171,v164
v_mul_f32 v172,v172,v164
v_mul_f32 v173,v173,v164
v_mul_f32 v174,v174,v164
v_mul_f32 v175,v175,v164
v_accvgpr_write_b32 a24,v168
v_accvgpr_write_b32 a25,v169
v_accvgpr_write_b32 a26,v170
v_accvgpr_write_b32 a27,v171
v_accvgpr_write_b32 a28,v172
v_accvgpr_write_b32 a29,v173
v_accvgpr_write_b32 a30,v174
v_accvgpr_write_b32 a31,v175
v_accvgpr_read_b32 v168,a32
v_accvgpr_read_b32 v169,a33
v_accvgpr_read_b32 v170,a34
v_accvgpr_read_b32 v171,a35
v_accvgpr_read_b32 v172,a36
v_accvgpr_read_b32 v173,a37
v_accvgpr_read_b32 v174,a38
v_accvgpr_read_b32 v175,a39
v_mul_f32 v168,v168,v164
v_mul_f32 v169,v169,v164
v_mul_f32 v170,v170,v164
v_mul_f32 v171,v171,v164
v_mul_f32 v172,v172,v164
v_mul_f32 v173,v173,v164
v_mul_f32 v174,v174,v164
v_mul_f32 v175,v175,v164
v_accvgpr_write_b32 a32,v168
v_accvgpr_write_b32 a33,v169
v_accvgpr_write_b32 a34,v170
v_accvgpr_write_b32 a35,v171
v_accvgpr_write_b32 a36,v172
v_accvgpr_write_b32 a37,v173
v_accvgpr_write_b32 a38,v174
v_accvgpr_write_b32 a39,v175
v_accvgpr_read_b32 v168,a40
v_accvgpr_read_b32 v169,a41
v_accvgpr_read_b32 v170,a42
v_accvgpr_read_b32 v171,a43
v_accvgpr_read_b32 v172,a44
v_accvgpr_read_b32 v173,a45
v_accvgpr_read_b32 v174,a46
v_accvgpr_read_b32 v175,a47
v_mul_f32 v168,v168,v164
v_mul_f32 v169,v169,v164
v_mul_f32 v170,v170,v164
v_mul_f32 v171,v171,v164
v_mul_f32 v172,v172,v164
v_mul_f32 v173,v173,v164
v_mul_f32 v174,v174,v164
v_mul_f32 v175,v175,v164
v_accvgpr_write_b32 a40,v168
v_accvgpr_write_b32 a41,v169
v_accvgpr_write_b32 a42,v170
v_accvgpr_write_b32 a43,v171
v_accvgpr_write_b32 a44,v172
v_accvgpr_write_b32 a45,v173
v_accvgpr_write_b32 a46,v174
v_accvgpr_write_b32 a47,v175
v_accvgpr_read_b32 v168,a48
v_accvgpr_read_b32 v169,a49
v_accvgpr_read_b32 v170,a50
v_accvgpr_read_b32 v171,a51
v_accvgpr_read_b32 v172,a52
v_accvgpr_read_b32 v173,a53
v_accvgpr_read_b32 v174,a54
v_accvgpr_read_b32 v175,a55
v_mul_f32 v168,v168,v164
v_mul_f32 v169,v169,v164
v_mul_f32 v170,v170,v164
v_mul_f32 v171,v171,v164
v_mul_f32 v172,v172,v164
v_mul_f32 v173,v173,v164
v_mul_f32 v174,v174,v164
v_mul_f32 v175,v175,v164
v_accvgpr_write_b32 a48,v168
v_accvgpr_write_b32 a49,v169
v_accvgpr_write_b32 a50,v170
v_accvgpr_write_b32 a51,v171
v_accvgpr_write_b32 a52,v172
v_accvgpr_write_b32 a53,v173
v_accvgpr_write_b32 a54,v174
v_accvgpr_write_b32 a55,v175
v_accvgpr_read_b32 v168,a56
v_accvgpr_read_b32 v169,a57
v_accvgpr_read_b32 v170,a58
v_accvgpr_read_b32 v171,a59
v_accvgpr_read_b32 v172,a60
v_accvgpr_read_b32 v173,a61
v_accvgpr_read_b32 v174,a62
v_accvgpr_read_b32 v175,a63
v_mul_f32 v168,v168,v164
v_mul_f32 v169,v169,v164
v_mul_f32 v170,v170,v164
v_mul_f32 v171,v171,v164
v_mul_f32 v172,v172,v164
v_mul_f32 v173,v173,v164
v_mul_f32 v174,v174,v164
v_mul_f32 v175,v175,v164
v_accvgpr_write_b32 a56,v168
v_accvgpr_write_b32 a57,v169
v_accvgpr_write_b32 a58,v170
v_accvgpr_write_b32 a59,v171
v_accvgpr_write_b32 a60,v172
v_accvgpr_write_b32 a61,v173
v_accvgpr_write_b32 a62,v174
v_accvgpr_write_b32 a63,v175
v_add_f32 v161,0x42c00000,v161
v_max_f32 v161,0,v161
v_sub_f32 v245,v245,v161
v_exp_f32 v165,-v161
v_sub_f32 v136,v136,v161
v_sub_f32 v137,v137,v161
v_sub_f32 v138,v138,v161
v_sub_f32 v139,v139,v161
v_sub_f32 v140,v140,v161
v_sub_f32 v141,v141,v161
v_sub_f32 v142,v142,v161
v_sub_f32 v143,v143,v161
v_mul_f32 v228,v228,v165
v_mul_f32 v229,v229,v165
v_mul_f32 v230,v230,v165
v_mul_f32 v231,v231,v165
v_accvgpr_read_b32 v168,a64
v_accvgpr_read_b32 v169,a65
v_accvgpr_read_b32 v170,a66
v_accvgpr_read_b32 v171,a67
v_accvgpr_read_b32 v172,a68
v_accvgpr_read_b32 v173,a69
v_accvgpr_read_b32 v174,a70
v_accvgpr_read_b32 v175,a71
v_mul_f32 v168,v168,v165
v_mul_f32 v169,v169,v165
v_mul_f32 v170,v170,v165
v_mul_f32 v171,v171,v165
v_mul_f32 v172,v172,v165
v_mul_f32 v173,v173,v165
v_mul_f32 v174,v174,v165
v_mul_f32 v175,v175,v165
v_accvgpr_write_b32 a64,v168
v_accvgpr_write_b32 a65,v169
v_accvgpr_write_b32 a66,v170
v_accvgpr_write_b32 a67,v171
v_accvgpr_write_b32 a68,v172
v_accvgpr_write_b32 a69,v173
v_accvgpr_write_b32 a70,v174
v_accvgpr_write_b32 a71,v175
v_accvgpr_read_b32 v168,a72
v_accvgpr_read_b32 v169,a73
v_accvgpr_read_b32 v170,a74
v_accvgpr_read_b32 v171,a75
v_accvgpr_read_b32 v172,a76
)"
R"(v_accvgpr_read_b32 v173,a77
v_accvgpr_read_b32 v174,a78
v_accvgpr_read_b32 v175,a79
v_mul_f32 v168,v168,v165
v_mul_f32 v169,v169,v165
v_mul_f32 v170,v170,v165
v_mul_f32 v171,v171,v165
v_mul_f32 v172,v172,v165
v_mul_f32 v173,v173,v165
v_mul_f32 v174,v174,v165
v_mul_f32 v175,v175,v165
v_accvgpr_write_b32 a72,v168
v_accvgpr_write_b32 a73,v169
v_accvgpr_write_b32 a74,v170
v_accvgpr_write_b32 a75,v171
v_accvgpr_write_b32 a76,v172
v_accvgpr_write_b32 a77,v173
v_accvgpr_write_b32 a78,v174
v_accvgpr_write_b32 a79,v175
v_accvgpr_read_b32 v168,a80
v_accvgpr_read_b32 v169,a81
v_accvgpr_read_b32 v170,a82
v_accvgpr_read_b32 v171,a83
v_accvgpr_read_b32 v172,a84
v_accvgpr_read_b32 v173,a85
v_accvgpr_read_b32 v174,a86
v_accvgpr_read_b32 v175,a87
v_mul_f32 v168,v168,v165
v_mul_f32 v169,v169,v165
v_mul_f32 v170,v170,v165
v_mul_f32 v171,v171,v165
v_mul_f32 v172,v172,v165
v_mul_f32 v173,v173,v165
v_mul_f32 v174,v174,v165
v_mul_f32 v175,v175,v165
v_accvgpr_write_b32 a80,v168
v_accvgpr_write_b32 a81,v169
v_accvgpr_write_b32 a82,v170
v_accvgpr_write_b32 a83,v171
v_accvgpr_write_b32 a84,v172
v_accvgpr_write_b32 a85,v173
v_accvgpr_write_b32 a86,v174
v_accvgpr_write_b32 a87,v175
v_accvgpr_read_b32 v168,a88
v_accvgpr_read_b32 v169,a89
v_accvgpr_read_b32 v170,a90
v_accvgpr_read_b32 v171,a91
v_accvgpr_read_b32 v172,a92
v_accvgpr_read_b32 v173,a93
v_accvgpr_read_b32 v174,a94
v_accvgpr_read_b32 v175,a95
v_mul_f32 v168,v168,v165
v_mul_f32 v169,v169,v165
v_mul_f32 v170,v170,v165
v_mul_f32 v171,v171,v165
v_mul_f32 v172,v172,v165
v_mul_f32 v173,v173,v165
v_mul_f32 v174,v174,v165
v_mul_f32 v175,v175,v165
v_accvgpr_write_b32 a88,v168
v_accvgpr_write_b32 a89,v169
v_accvgpr_write_b32 a90,v170
v_accvgpr_write_b32 a91,v171
v_accvgpr_write_b32 a92,v172
v_accvgpr_write_b32 a93,v173
v_accvgpr_write_b32 a94,v174
v_accvgpr_write_b32 a95,v175
v_accvgpr_read_b32 v168,a96
v_accvgpr_read_b32 v169,a97
v_accvgpr_read_b32 v170,a98
v_accvgpr_read_b32 v171,a99
v_accvgpr_read_b32 v172,a100
v_accvgpr_read_b32 v173,a101
v_accvgpr_read_b32 v174,a102
v_accvgpr_read_b32 v175,a103
v_mul_f32 v168,v168,v165
v_mul_f32 v169,v169,v165
v_mul_f32 v170,v170,v165
v_mul_f32 v171,v171,v165
v_mul_f32 v172,v172,v165
v_mul_f32 v173,v173,v165
v_mul_f32 v174,v174,v165
v_mul_f32 v175,v175,v165
v_accvgpr_write_b32 a96,v168
v_accvgpr_write_b32 a97,v169
v_accvgpr_write_b32 a98,v170
v_accvgpr_write_b32 a99,v171
v_accvgpr_write_b32 a100,v172
v_accvgpr_write_b32 a101,v173
v_accvgpr_write_b32 a102,v174
v_accvgpr_write_b32 a103,v175
v_accvgpr_read_b32 v168,a104
v_accvgpr_read_b32 v169,a105
v_accvgpr_read_b32 v170,a106
v_accvgpr_read_b32 v171,a107
v_accvgpr_read_b32 v172,a108
v_accvgpr_read_b32 v173,a109
v_accvgpr_read_b32 v174,a110
v_accvgpr_read_b32 v175,a111
v_mul_f32 v168,v168,v165
v_mul_f32 v169,v169,v165
v_mul_f32 v170,v170,v165
v_mul_f32 v171,v171,v165
v_mul_f32 v172,v172,v165
v_mul_f32 v173,v173,v165
v_mul_f32 v174,v174,v165
v_mul_f32 v175,v175,v165
v_accvgpr_write_b32 a104,v168
v_accvgpr_write_b32 a105,v169
v_accvgpr_write_b32 a106,v170
v_accvgpr_write_b32 a107,v171
v_accvgpr_write_b32 a108,v172
v_accvgpr_write_b32 a109,v173
v_accvgpr_write_b32 a110,v174
v_accvgpr_write_b32 a111,v175
v_accvgpr_read_b32 v168,a112
v_accvgpr_read_b32 v169,a113
v_accvgpr_read_b32 v170,a114
v_accvgpr_read_b32 v171,a115
v_accvgpr_read_b32 v172,a116
v_accvgpr_read_b32 v173,a117
v_accvgpr_read_b32 v174,a118
v_accvgpr_read_b32 v175,a119
v_mul_f32 v168,v168,v165
v_mul_f32 v169,v169,v165
v_mul_f32 v170,v170,v165
v_mul_f32 v171,v171,v165
v_mul_f32 v172,v172,v165
v_mul_f32 v173,v173,v165
v_mul_f32 v174,v174,v165
v_mul_f32 v175,v175,v165
v_accvgpr_write_b32 a112,v168
v_accvgpr_write_b32 a113,v169
v_accvgpr_write_b32 a114,v170
v_accvgpr_write_b32 a115,v171
v_accvgpr_write_b32 a116,v172
v_accvgpr_write_b32 a117,v173
v_accvgpr_write_b32 a118,v174
v_accvgpr_write_b32 a119,v175
v_accvgpr_read_b32 v168,a120
v_accvgpr_read_b32 v169,a121
v_accvgpr_read_b32 v170,a122
v_accvgpr_read_b32 v171,a123
v_accvgpr_read_b32 v172,a124
v_accvgpr_read_b32 v173,a125
v_accvgpr_read_b32 v174,a126
v_accvgpr_read_b32 v175,a127
v_mul_f32 v168,v168,v165
v_mul_f32 v169,v169,v165
v_mul_f32 v170,v170,v165
v_mul_f32 v171,v171,v165
v_mul_f32 v172,v172,v165
v_mul_f32 v173,v173,v165
v_mul_f32 v174,v174,v165
v_mul_f32 v175,v175,v165
v_accvgpr_write_b32 a120,v168
v_accvgpr_write_b32 a121,v169
v_accvgpr_write_b32 a122,v170
v_accvgpr_write_b32 a123,v171
v_accvgpr_write_b32 a124,v172
v_accvgpr_write_b32 a125,v173
v_accvgpr_write_b32 a126,v174
v_accvgpr_write_b32 a127,v175
v_add_f32 v162,0x42c00000,v162
v_max_f32 v162,0,v162
v_sub_f32 v246,v246,v162
v_exp_f32 v166,-v162
v_sub_f32 v144,v144,v162
v_sub_f32 v145,v145,v162
v_sub_f32 v146,v146,v162
v_sub_f32 v147,v147,v162
v_sub_f32 v148,v148,v162
v_sub_f32 v149,v149,v162
v_sub_f32 v150,v150,v162
v_sub_f32 v151,v151,v162
v_mul_f32 v232,v232,v166
v_mul_f32 v233,v233,v166
v_mul_f32 v234,v234,v166
v_mul_f32 v235,v235,v166
v_accvgpr_read_b32 v168,a128
v_accvgpr_read_b32 v169,a129
v_accvgpr_read_b32 v170,a130
v_accvgpr_read_b32 v171,a131
v_accvgpr_read_b32 v172,a132
v_accvgpr_read_b32 v173,a133
v_accvgpr_read_b32 v174,a134
v_accvgpr_read_b32 v175,a135
v_mul_f32 v168,v168,v166
v_mul_f32 v169,v169,v166
v_mul_f32 v170,v170,v166
v_mul_f32 v171,v171,v166
v_mul_f32 v172,v172,v166
v_mul_f32 v173,v173,v166
v_mul_f32 v174,v174,v166
v_mul_f32 v175,v175,v166
v_accvgpr_write_b32 a128,v168
v_accvgpr_write_b32 a129,v169
v_accvgpr_write_b32 a130,v170
v_accvgpr_write_b32 a131,v171
v_accvgpr_write_b32 a132,v172
)"
R"(v_accvgpr_write_b32 a133,v173
v_accvgpr_write_b32 a134,v174
v_accvgpr_write_b32 a135,v175
v_accvgpr_read_b32 v168,a136
v_accvgpr_read_b32 v169,a137
v_accvgpr_read_b32 v170,a138
v_accvgpr_read_b32 v171,a139
v_accvgpr_read_b32 v172,a140
v_accvgpr_read_b32 v173,a141
v_accvgpr_read_b32 v174,a142
v_accvgpr_read_b32 v175,a143
v_mul_f32 v168,v168,v166
v_mul_f32 v169,v169,v166
v_mul_f32 v170,v170,v166
v_mul_f32 v171,v171,v166
v_mul_f32 v172,v172,v166
v_mul_f32 v173,v173,v166
v_mul_f32 v174,v174,v166
v_mul_f32 v175,v175,v166
v_accvgpr_write_b32 a136,v168
v_accvgpr_write_b32 a137,v169
v_accvgpr_write_b32 a138,v170
v_accvgpr_write_b32 a139,v171
v_accvgpr_write_b32 a140,v172
v_accvgpr_write_b32 a141,v173
v_accvgpr_write_b32 a142,v174
v_accvgpr_write_b32 a143,v175
v_accvgpr_read_b32 v168,a144
v_accvgpr_read_b32 v169,a145
v_accvgpr_read_b32 v170,a146
v_accvgpr_read_b32 v171,a147
v_accvgpr_read_b32 v172,a148
v_accvgpr_read_b32 v173,a149
v_accvgpr_read_b32 v174,a150
v_accvgpr_read_b32 v175,a151
v_mul_f32 v168,v168,v166
v_mul_f32 v169,v169,v166
v_mul_f32 v170,v170,v166
v_mul_f32 v171,v171,v166
v_mul_f32 v172,v172,v166
v_mul_f32 v173,v173,v166
v_mul_f32 v174,v174,v166
v_mul_f32 v175,v175,v166
v_accvgpr_write_b32 a144,v168
v_accvgpr_write_b32 a145,v169
v_accvgpr_write_b32 a146,v170
v_accvgpr_write_b32 a147,v171
v_accvgpr_write_b32 a148,v172
v_accvgpr_write_b32 a149,v173
v_accvgpr_write_b32 a150,v174
v_accvgpr_write_b32 a151,v175
v_accvgpr_read_b32 v168,a152
v_accvgpr_read_b32 v169,a153
v_accvgpr_read_b32 v170,a154
v_accvgpr_read_b32 v171,a155
v_accvgpr_read_b32 v172,a156
v_accvgpr_read_b32 v173,a157
v_accvgpr_read_b32 v174,a158
v_accvgpr_read_b32 v175,a159
v_mul_f32 v168,v168,v166
v_mul_f32 v169,v169,v166
v_mul_f32 v170,v170,v166
v_mul_f32 v171,v171,v166
v_mul_f32 v172,v172,v166
v_mul_f32 v173,v173,v166
v_mul_f32 v174,v174,v166
v_mul_f32 v175,v175,v166
v_accvgpr_write_b32 a152,v168
v_accvgpr_write_b32 a153,v169
v_accvgpr_write_b32 a154,v170
v_accvgpr_write_b32 a155,v171
v_accvgpr_write_b32 a156,v172
v_accvgpr_write_b32 a157,v173
v_accvgpr_write_b32 a158,v174
v_accvgpr_write_b32 a159,v175
v_accvgpr_read_b32 v168,a160
v_accvgpr_read_b32 v169,a161
v_accvgpr_read_b32 v170,a162
v_accvgpr_read_b32 v171,a163
v_accvgpr_read_b32 v172,a164
v_accvgpr_read_b32 v173,a165
v_accvgpr_read_b32 v174,a166
v_accvgpr_read_b32 v175,a167
v_mul_f32 v168,v168,v166
v_mul_f32 v169,v169,v166
v_mul_f32 v170,v170,v166
v_mul_f32 v171,v171,v166
v_mul_f32 v172,v172,v166
v_mul_f32 v173,v173,v166
v_mul_f32 v174,v174,v166
v_mul_f32 v175,v175,v166
v_accvgpr_write_b32 a160,v168
v_accvgpr_write_b32 a161,v169
v_accvgpr_write_b32 a162,v170
v_accvgpr_write_b32 a163,v171
v_accvgpr_write_b32 a164,v172
v_accvgpr_write_b32 a165,v173
v_accvgpr_write_b32 a166,v174
v_accvgpr_write_b32 a167,v175
v_accvgpr_read_b32 v168,a168
v_accvgpr_read_b32 v169,a169
v_accvgpr_read_b32 v170,a170
v_accvgpr_read_b32 v171,a171
v_accvgpr_read_b32 v172,a172
v_accvgpr_read_b32 v173,a173
v_accvgpr_read_b32 v174,a174
v_accvgpr_read_b32 v175,a175
v_mul_f32 v168,v168,v166
v_mul_f32 v169,v169,v166
v_mul_f32 v170,v170,v166
v_mul_f32 v171,v171,v166
v_mul_f32 v172,v172,v166
v_mul_f32 v173,v173,v166
v_mul_f32 v174,v174,v166
v_mul_f32 v175,v175,v166
v_accvgpr_write_b32 a168,v168
v_accvgpr_write_b32 a169,v169
v_accvgpr_write_b32 a170,v170
v_accvgpr_write_b32 a171,v171
v_accvgpr_write_b32 a172,v172
v_accvgpr_write_b32 a173,v173
v_accvgpr_write_b32 a174,v174
v_accvgpr_write_b32 a175,v175
v_accvgpr_read_b32 v168,a176
v_accvgpr_read_b32 v169,a177
v_accvgpr_read_b32 v170,a178
v_accvgpr_read_b32 v171,a179
v_accvgpr_read_b32 v172,a180
v_accvgpr_read_b32 v173,a181
v_accvgpr_read_b32 v174,a182
v_accvgpr_read_b32 v175,a183
v_mul_f32 v168,v168,v166
v_mul_f32 v169,v169,v166
v_mul_f32 v170,v170,v166
v_mul_f32 v171,v171,v166
v_mul_f32 v172,v172,v166
v_mul_f32 v173,v173,v166
v_mul_f32 v174,v174,v166
v_mul_f32 v175,v175,v166
v_accvgpr_write_b32 a176,v168
v_accvgpr_write_b32 a177,v169
v_accvgpr_write_b32 a178,v170
v_accvgpr_write_b32 a179,v171
v_accvgpr_write_b32 a180,v172
v_accvgpr_write_b32 a181,v173
v_accvgpr_write_b32 a182,v174
v_accvgpr_write_b32 a183,v175
v_accvgpr_read_b32 v168,a184
v_accvgpr_read_b32 v169,a185
v_accvgpr_read_b32 v170,a186
v_accvgpr_read_b32 v171,a187
v_accvgpr_read_b32 v172,a188
v_accvgpr_read_b32 v173,a189
v_accvgpr_read_b32 v174,a190
v_accvgpr_read_b32 v175,a191
v_mul_f32 v168,v168,v166
v_mul_f32 v169,v169,v166
v_mul_f32 v170,v170,v166
v_mul_f32 v171,v171,v166
v_mul_f32 v172,v172,v166
v_mul_f32 v173,v173,v166
v_mul_f32 v174,v174,v166
v_mul_f32 v175,v175,v166
v_accvgpr_write_b32 a184,v168
v_accvgpr_write_b32 a185,v169
v_accvgpr_write_b32 a186,v170
v_accvgpr_write_b32 a187,v171
v_accvgpr_write_b32 a188,v172
v_accvgpr_write_b32 a189,v173
v_accvgpr_write_b32 a190,v174
v_accvgpr_write_b32 a191,v175
v_add_f32 v163,0x42c00000,v163
v_max_f32 v163,0,v163
v_sub_f32 v247,v247,v163
v_exp_f32 v167,-v163
v_sub_f32 v152,v152,v163
v_sub_f32 v153,v153,v163
v_sub_f32 v154,v154,v163
v_sub_f32 v155,v155,v163
v_sub_f32 v156,v156,v163
v_sub_f32 v157,v157,v163
v_sub_f32 v158,v158,v163
v_sub_f32 v159,v159,v163
v_mul_f32 v236,v236,v167
v_mul_f32 v237,v237,v167
v_mul_f32 v238,v238,v167
v_mul_f32 v239,v239,v167
v_accvgpr_read_b32 v168,a192
v_accvgpr_read_b32 v169,a193
v_accvgpr_read_b32 v170,a194
v_accvgpr_read_b32 v171,a195
v_accvgpr_read_b32 v172,a196
v_accvgpr_read_b32 v173,a197
v_accvgpr_read_b32 v174,a198
v_accvgpr_read_b32 v175,a199
v_mul_f32 v168,v168,v167
v_mul_f32 v169,v169,v167
v_mul_f32 v170,v170,v167
v_mul_f32 v171,v171,v167
v_mul_f32 v172,v172,v167
)"
R"(v_mul_f32 v173,v173,v167
v_mul_f32 v174,v174,v167
v_mul_f32 v175,v175,v167
v_accvgpr_write_b32 a192,v168
v_accvgpr_write_b32 a193,v169
v_accvgpr_write_b32 a194,v170
v_accvgpr_write_b32 a195,v171
v_accvgpr_write_b32 a196,v172
v_accvgpr_write_b32 a197,v173
v_accvgpr_write_b32 a198,v174
v_accvgpr_write_b32 a199,v175
v_accvgpr_read_b32 v168,a200
v_accvgpr_read_b32 v169,a201
v_accvgpr_read_b32 v170,a202
v_accvgpr_read_b32 v171,a203
v_accvgpr_read_b32 v172,a204
v_accvgpr_read_b32 v173,a205
v_accvgpr_read_b32 v174,a206
v_accvgpr_read_b32 v175,a207
v_mul_f32 v168,v168,v167
v_mul_f32 v169,v169,v167
v_mul_f32 v170,v170,v167
v_mul_f32 v171,v171,v167
v_mul_f32 v172,v172,v167
v_mul_f32 v173,v173,v167
v_mul_f32 v174,v174,v167
v_mul_f32 v175,v175,v167
v_accvgpr_write_b32 a200,v168
v_accvgpr_write_b32 a201,v169
v_accvgpr_write_b32 a202,v170
v_accvgpr_write_b32 a203,v171
v_accvgpr_write_b32 a204,v172
v_accvgpr_write_b32 a205,v173
v_accvgpr_write_b32 a206,v174
v_accvgpr_write_b32 a207,v175
v_accvgpr_read_b32 v168,a208
v_accvgpr_read_b32 v169,a209
v_accvgpr_read_b32 v170,a210
v_accvgpr_read_b32 v171,a211
v_accvgpr_read_b32 v172,a212
v_accvgpr_read_b32 v173,a213
v_accvgpr_read_b32 v174,a214
v_accvgpr_read_b32 v175,a215
v_mul_f32 v168,v168,v167
v_mul_f32 v169,v169,v167
v_mul_f32 v170,v170,v167
v_mul_f32 v171,v171,v167
v_mul_f32 v172,v172,v167
v_mul_f32 v173,v173,v167
v_mul_f32 v174,v174,v167
v_mul_f32 v175,v175,v167
v_accvgpr_write_b32 a208,v168
v_accvgpr_write_b32 a209,v169
v_accvgpr_write_b32 a210,v170
v_accvgpr_write_b32 a211,v171
v_accvgpr_write_b32 a212,v172
v_accvgpr_write_b32 a213,v173
v_accvgpr_write_b32 a214,v174
v_accvgpr_write_b32 a215,v175
v_accvgpr_read_b32 v168,a216
v_accvgpr_read_b32 v169,a217
v_accvgpr_read_b32 v170,a218
v_accvgpr_read_b32 v171,a219
v_accvgpr_read_b32 v172,a220
v_accvgpr_read_b32 v173,a221
v_accvgpr_read_b32 v174,a222
v_accvgpr_read_b32 v175,a223
v_mul_f32 v168,v168,v167
v_mul_f32 v169,v169,v167
v_mul_f32 v170,v170,v167
v_mul_f32 v171,v171,v167
v_mul_f32 v172,v172,v167
v_mul_f32 v173,v173,v167
v_mul_f32 v174,v174,v167
v_mul_f32 v175,v175,v167
v_accvgpr_write_b32 a216,v168
v_accvgpr_write_b32 a217,v169
v_accvgpr_write_b32 a218,v170
v_accvgpr_write_b32 a219,v171
v_accvgpr_write_b32 a220,v172
v_accvgpr_write_b32 a221,v173
v_accvgpr_write_b32 a222,v174
v_accvgpr_write_b32 a223,v175
v_accvgpr_read_b32 v168,a224
v_accvgpr_read_b32 v169,a225
v_accvgpr_read_b32 v170,a226
v_accvgpr_read_b32 v171,a227
v_accvgpr_read_b32 v172,a228
v_accvgpr_read_b32 v173,a229
v_accvgpr_read_b32 v174,a230
v_accvgpr_read_b32 v175,a231
v_mul_f32 v168,v168,v167
v_mul_f32 v169,v169,v167
v_mul_f32 v170,v170,v167
v_mul_f32 v171,v171,v167
v_mul_f32 v172,v172,v167
v_mul_f32 v173,v173,v167
v_mul_f32 v174,v174,v167
v_mul_f32 v175,v175,v167
v_accvgpr_write_b32 a224,v168
v_accvgpr_write_b32 a225,v169
v_accvgpr_write_b32 a226,v170
v_accvgpr_write_b32 a227,v171
v_accvgpr_write_b32 a228,v172
v_accvgpr_write_b32 a229,v173
v_accvgpr_write_b32 a230,v174
v_accvgpr_write_b32 a231,v175
v_accvgpr_read_b32 v168,a232
v_accvgpr_read_b32 v169,a233
v_accvgpr_read_b32 v170,a234
v_accvgpr_read_b32 v171,a235
v_accvgpr_read_b32 v172,a236
v_accvgpr_read_b32 v173,a237
v_accvgpr_read_b32 v174,a238
v_accvgpr_read_b32 v175,a239
v_mul_f32 v168,v168,v167
v_mul_f32 v169,v169,v167
v_mul_f32 v170,v170,v167
v_mul_f32 v171,v171,v167
v_mul_f32 v172,v172,v167
v_mul_f32 v173,v173,v167
v_mul_f32 v174,v174,v167
v_mul_f32 v175,v175,v167
v_accvgpr_write_b32 a232,v168
v_accvgpr_write_b32 a233,v169
v_accvgpr_write_b32 a234,v170
v_accvgpr_write_b32 a235,v171
v_accvgpr_write_b32 a236,v172
v_accvgpr_write_b32 a237,v173
v_accvgpr_write_b32 a238,v174
v_accvgpr_write_b32 a239,v175
v_accvgpr_read_b32 v168,a240
v_accvgpr_read_b32 v169,a241
v_accvgpr_read_b32 v170,a242
v_accvgpr_read_b32 v171,a243
v_accvgpr_read_b32 v172,a244
v_accvgpr_read_b32 v173,a245
v_accvgpr_read_b32 v174,a246
v_accvgpr_read_b32 v175,a247
v_mul_f32 v168,v168,v167
v_mul_f32 v169,v169,v167
v_mul_f32 v170,v170,v167
v_mul_f32 v171,v171,v167
v_mul_f32 v172,v172,v167
v_mul_f32 v173,v173,v167
v_mul_f32 v174,v174,v167
v_mul_f32 v175,v175,v167
v_accvgpr_write_b32 a240,v168
v_accvgpr_write_b32 a241,v169
v_accvgpr_write_b32 a242,v170
v_accvgpr_write_b32 a243,v171
v_accvgpr_write_b32 a244,v172
v_accvgpr_write_b32 a245,v173
v_accvgpr_write_b32 a246,v174
v_accvgpr_write_b32 a247,v175
v_accvgpr_read_b32 v168,a248
v_accvgpr_read_b32 v169,a249
v_accvgpr_read_b32 v170,a250
v_accvgpr_read_b32 v171,a251
v_accvgpr_read_b32 v172,a252
v_accvgpr_read_b32 v173,a253
v_accvgpr_read_b32 v174,a254
v_accvgpr_read_b32 v175,a255
v_mul_f32 v168,v168,v167
v_mul_f32 v169,v169,v167
v_mul_f32 v170,v170,v167
v_mul_f32 v171,v171,v167
v_mul_f32 v172,v172,v167
v_mul_f32 v173,v173,v167
v_mul_f32 v174,v174,v167
v_mul_f32 v175,v175,v167
v_accvgpr_write_b32 a248,v168
v_accvgpr_write_b32 a249,v169
v_accvgpr_write_b32 a250,v170
v_accvgpr_write_b32 a251,v171
v_accvgpr_write_b32 a252,v172
v_accvgpr_write_b32 a253,v173
v_accvgpr_write_b32 a254,v174
v_accvgpr_write_b32 a255,v175
s_nop 3
s_setpc_b64 s[76:77]
Lrare1b_%=:
s_nop 7
v_max3_f32 v128,v160,v161,v162
v_max3_f32 v129,v168,v169,v170
v_max3_f32 v130,v176,v177,v178
v_max3_f32 v131,v184,v185,v186
v_max3_f32 v128,v163,v164,v128
v_max3_f32 v129,v171,v172,v129
v_max3_f32 v130,v179,v180,v130
v_max3_f32 v131,v187,v188,v131
v_max3_f32 v128,v165,v166,v128
v_max3_f32 v129,v173,v174,v129
v_max3_f32 v130,v181,v182,v130
v_max3_f32 v131,v189,v190,v131
v_max_f32 v128,v167,v128
v_max_f32 v129,v175,v129
v_max_f32 v130,v183,v130
v_max_f32 v131,v191,v131
v_mov_b32 v132,v128
)"
R"(v_mov_b32 v133,v129
v_mov_b32 v134,v130
v_mov_b32 v135,v131
v_permlane16_swap_b32 v128,v132
v_permlane16_swap_b32 v129,v133
v_permlane16_swap_b32 v130,v134
v_permlane16_swap_b32 v131,v135
v_max_f32 v128,v128,v132
v_max_f32 v129,v129,v133
v_max_f32 v130,v130,v134
v_max_f32 v131,v131,v135
v_mov_b32 v132,v128
v_mov_b32 v133,v129
v_mov_b32 v134,v130
v_mov_b32 v135,v131
v_permlane32_swap_b32 v128,v132
v_permlane32_swap_b32 v129,v133
v_permlane32_swap_b32 v130,v134
v_permlane32_swap_b32 v131,v135
v_max_f32 v128,v128,v132
v_max_f32 v129,v129,v133
v_max_f32 v130,v130,v134
v_max_f32 v131,v131,v135
v_add_f32 v128,0x42c00000,v128
v_max_f32 v128,0,v128
v_sub_f32 v244,v244,v128
v_exp_f32 v132,-v128
v_sub_f32 v160,v160,v128
v_sub_f32 v161,v161,v128
v_sub_f32 v162,v162,v128
v_sub_f32 v163,v163,v128
v_sub_f32 v164,v164,v128
v_sub_f32 v165,v165,v128
v_sub_f32 v166,v166,v128
v_sub_f32 v167,v167,v128
v_mul_f32 v224,v224,v132
v_mul_f32 v225,v225,v132
v_mul_f32 v226,v226,v132
v_mul_f32 v227,v227,v132
v_accvgpr_read_b32 v136,a0
v_accvgpr_read_b32 v137,a1
v_accvgpr_read_b32 v138,a2
v_accvgpr_read_b32 v139,a3
v_accvgpr_read_b32 v140,a4
v_accvgpr_read_b32 v141,a5
v_accvgpr_read_b32 v142,a6
v_accvgpr_read_b32 v143,a7
v_mul_f32 v136,v136,v132
v_mul_f32 v137,v137,v132
v_mul_f32 v138,v138,v132
v_mul_f32 v139,v139,v132
v_mul_f32 v140,v140,v132
v_mul_f32 v141,v141,v132
v_mul_f32 v142,v142,v132
v_mul_f32 v143,v143,v132
v_accvgpr_write_b32 a0,v136
v_accvgpr_write_b32 a1,v137
v_accvgpr_write_b32 a2,v138
v_accvgpr_write_b32 a3,v139
v_accvgpr_write_b32 a4,v140
v_accvgpr_write_b32 a5,v141
v_accvgpr_write_b32 a6,v142
v_accvgpr_write_b32 a7,v143
v_accvgpr_read_b32 v136,a8
v_accvgpr_read_b32 v137,a9
v_accvgpr_read_b32 v138,a10
v_accvgpr_read_b32 v139,a11
v_accvgpr_read_b32 v140,a12
v_accvgpr_read_b32 v141,a13
v_accvgpr_read_b32 v142,a14
v_accvgpr_read_b32 v143,a15
v_mul_f32 v136,v136,v132
v_mul_f32 v137,v137,v132
v_mul_f32 v138,v138,v132
v_mul_f32 v139,v139,v132
v_mul_f32 v140,v140,v132
v_mul_f32 v141,v141,v132
v_mul_f32 v142,v142,v132
v_mul_f32 v143,v143,v132
v_accvgpr_write_b32 a8,v136
v_accvgpr_write_b32 a9,v137
v_accvgpr_write_b32 a10,v138
v_accvgpr_write_b32 a11,v139
v_accvgpr_write_b32 a12,v140
v_accvgpr_write_b32 a13,v141
v_accvgpr_write_b32 a14,v142
v_accvgpr_write_b32 a15,v143
v_accvgpr_read_b32 v136,a16
v_accvgpr_read_b32 v137,a17
v_accvgpr_read_b32 v138,a18
v_accvgpr_read_b32 v139,a19
v_accvgpr_read_b32 v140,a20
v_accvgpr_read_b32 v141,a21
v_accvgpr_read_b32 v142,a22
v_accvgpr_read_b32 v143,a23
v_mul_f32 v136,v136,v132
v_mul_f32 v137,v137,v132
v_mul_f32 v138,v138,v132
v_mul_f32 v139,v139,v132
v_mul_f32 v140,v140,v132
v_mul_f32 v141,v141,v132
v_mul_f32 v142,v142,v132
v_mul_f32 v143,v143,v132
v_accvgpr_write_b32 a16,v136
v_accvgpr_write_b32 a17,v137
v_accvgpr_write_b32 a18,v138
v_accvgpr_write_b32 a19,v139
v_accvgpr_write_b32 a20,v140
v_accvgpr_write_b32 a21,v141
v_accvgpr_write_b32 a22,v142
v_accvgpr_write_b32 a23,v143
v_accvgpr_read_b32 v136,a24
v_accvgpr_read_b32 v137,a25
v_accvgpr_read_b32 v138,a26
v_accvgpr_read_b32 v139,a27
v_accvgpr_read_b32 v140,a28
v_accvgpr_read_b32 v141,a29
v_accvgpr_read_b32 v142,a30
v_accvgpr_read_b32 v143,a31
v_mul_f32 v136,v136,v132
v_mul_f32 v137,v137,v132
v_mul_f32 v138,v138,v132
v_mul_f32 v139,v139,v132
v_mul_f32 v140,v140,v132
v_mul_f32 v141,v141,v132
v_mul_f32 v142,v142,v132
v_mul_f32 v143,v143,v132
v_accvgpr_write_b32 a24,v136
v_accvgpr_write_b32 a25,v137
v_accvgpr_write_b32 a26,v138
v_accvgpr_write_b32 a27,v139
v_accvgpr_write_b32 a28,v140
v_accvgpr_write_b32 a29,v141
v_accvgpr_write_b32 a30,v142
v_accvgpr_write_b32 a31,v143
v_accvgpr_read_b32 v136,a32
v_accvgpr_read_b32 v137,a33
v_accvgpr_read_b32 v138,a34
v_accvgpr_read_b32 v139,a35
v_accvgpr_read_b32 v140,a36
v_accvgpr_read_b32 v141,a37
v_accvgpr_read_b32 v142,a38
v_accvgpr_read_b32 v143,a39
v_mul_f32 v136,v136,v132
v_mul_f32 v137,v137,v132
v_mul_f32 v138,v138,v132
v_mul_f32 v139,v139,v132
v_mul_f32 v140,v140,v132
v_mul_f32 v141,v141,v132
v_mul_f32 v142,v142,v132
v_mul_f32 v143,v143,v132
v_accvgpr_write_b32 a32,v136
v_accvgpr_write_b32 a33,v137
v_accvgpr_write_b32 a34,v138
v_accvgpr_write_b32 a35,v139
v_accvgpr_write_b32 a36,v140
v_accvgpr_write_b32 a37,v141
v_accvgpr_write_b32 a38,v142
v_accvgpr_write_b32 a39,v143
v_accvgpr_read_b32 v136,a40
v_accvgpr_read_b32 v137,a41
v_accvgpr_read_b32 v138,a42
v_accvgpr_read_b32 v139,a43
v_accvgpr_read_b32 v140,a44
v_accvgpr_read_b32 v141,a45
v_accvgpr_read_b32 v142,a46
v_accvgpr_read_b32 v143,a47
v_mul_f32 v136,v136,v132
v_mul_f32 v137,v137,v132
v_mul_f32 v138,v138,v132
v_mul_f32 v139,v139,v132
v_mul_f32 v140,v140,v132
v_mul_f32 v141,v141,v132
v_mul_f32 v142,v142,v132
v_mul_f32 v143,v143,v132
v_accvgpr_write_b32 a40,v136
v_accvgpr_write_b32 a41,v137
v_accvgpr_write_b32 a42,v138
v_accvgpr_write_b32 a43,v139
v_accvgpr_write_b32 a44,v140
v_accvgpr_write_b32 a45,v141
v_accvgpr_write_b32 a46,v142
v_accvgpr_write_b32 a47,v143
v_accvgpr_read_b32 v136,a48
v_accvgpr_read_b32 v137,a49
v_accvgpr_read_b32 v138,a50
v_accvgpr_read_b32 v139,a51
v_accvgpr_read_b32 v140,a52
v_accvgpr_read_b32 v141,a53
v_accvgpr_read_b32 v142,a54
v_accvgpr_read_b32 v143,a55
v_mul_f32 v136,v136,v132
v_mul_f32 v137,v137,v132
v_mul_f32 v138,v138,v132
v_mul_f32 v139,v139,v132
v_mul_f32 v140,v140,v132
v_mul_f32 v141,v141,v132
v_mul_f32 v142,v142,v132
v_mul_f32 v143,v143,v132
v_accvgpr_write_b32 a48,v136
)"
R"(v_accvgpr_write_b32 a49,v137
v_accvgpr_write_b32 a50,v138
v_accvgpr_write_b32 a51,v139
v_accvgpr_write_b32 a52,v140
v_accvgpr_write_b32 a53,v141
v_accvgpr_write_b32 a54,v142
v_accvgpr_write_b32 a55,v143
v_accvgpr_read_b32 v136,a56
v_accvgpr_read_b32 v137,a57
v_accvgpr_read_b32 v138,a58
v_accvgpr_read_b32 v139,a59
v_accvgpr_read_b32 v140,a60
v_accvgpr_read_b32 v141,a61
v_accvgpr_read_b32 v142,a62
v_accvgpr_read_b32 v143,a63
v_mul_f32 v136,v136,v132
v_mul_f32 v137,v137,v132
v_mul_f32 v138,v138,v132
v_mul_f32 v139,v139,v132
v_mul_f32 v140,v140,v132
v_mul_f32 v141,v141,v132
v_mul_f32 v142,v142,v132
v_mul_f32 v143,v143,v132
v_accvgpr_write_b32 a56,v136
v_accvgpr_write_b32 a57,v137
v_accvgpr_write_b32 a58,v138
v_accvgpr_write_b32 a59,v139
v_accvgpr_write_b32 a60,v140
v_accvgpr_write_b32 a61,v141
v_accvgpr_write_b32 a62,v142
v_accvgpr_write_b32 a63,v143
v_add_f32 v129,0x42c00000,v129
v_max_f32 v129,0,v129
v_sub_f32 v245,v245,v129
v_exp_f32 v133,-v129
v_sub_f32 v168,v168,v129
v_sub_f32 v169,v169,v129
v_sub_f32 v170,v170,v129
v_sub_f32 v171,v171,v129
v_sub_f32 v172,v172,v129
v_sub_f32 v173,v173,v129
v_sub_f32 v174,v174,v129
v_sub_f32 v175,v175,v129
v_mul_f32 v228,v228,v133
v_mul_f32 v229,v229,v133
v_mul_f32 v230,v230,v133
v_mul_f32 v231,v231,v133
v_accvgpr_read_b32 v136,a64
v_accvgpr_read_b32 v137,a65
v_accvgpr_read_b32 v138,a66
v_accvgpr_read_b32 v139,a67
v_accvgpr_read_b32 v140,a68
v_accvgpr_read_b32 v141,a69
v_accvgpr_read_b32 v142,a70
v_accvgpr_read_b32 v143,a71
v_mul_f32 v136,v136,v133
v_mul_f32 v137,v137,v133
v_mul_f32 v138,v138,v133
v_mul_f32 v139,v139,v133
v_mul_f32 v140,v140,v133
v_mul_f32 v141,v141,v133
v_mul_f32 v142,v142,v133
v_mul_f32 v143,v143,v133
v_accvgpr_write_b32 a64,v136
v_accvgpr_write_b32 a65,v137
v_accvgpr_write_b32 a66,v138
v_accvgpr_write_b32 a67,v139
v_accvgpr_write_b32 a68,v140
v_accvgpr_write_b32 a69,v141
v_accvgpr_write_b32 a70,v142
v_accvgpr_write_b32 a71,v143
v_accvgpr_read_b32 v136,a72
v_accvgpr_read_b32 v137,a73
v_accvgpr_read_b32 v138,a74
v_accvgpr_read_b32 v139,a75
v_accvgpr_read_b32 v140,a76
v_accvgpr_read_b32 v141,a77
v_accvgpr_read_b32 v142,a78
v_accvgpr_read_b32 v143,a79
v_mul_f32 v136,v136,v133
v_mul_f32 v137,v137,v133
v_mul_f32 v138,v138,v133
v_mul_f32 v139,v139,v133
v_mul_f32 v140,v140,v133
v_mul_f32 v141,v141,v133
v_mul_f32 v142,v142,v133
v_mul_f32 v143,v143,v133
v_accvgpr_write_b32 a72,v136
v_accvgpr_write_b32 a73,v137
v_accvgpr_write_b32 a74,v138
v_accvgpr_write_b32 a75,v139
v_accvgpr_write_b32 a76,v140
v_accvgpr_write_b32 a77,v141
v_accvgpr_write_b32 a78,v142
v_accvgpr_write_b32 a79,v143
v_accvgpr_read_b32 v136,a80
v_accvgpr_read_b32 v137,a81
v_accvgpr_read_b32 v138,a82
v_accvgpr_read_b32 v139,a83
v_accvgpr_read_b32 v140,a84
v_accvgpr_read_b32 v141,a85
v_accvgpr_read_b32 v142,a86
v_accvgpr_read_b32 v143,a87
v_mul_f32 v136,v136,v133
v_mul_f32 v137,v137,v133
v_mul_f32 v138,v138,v133
v_mul_f32 v139,v139,v133
v_mul_f32 v140,v140,v133
v_mul_f32 v141,v141,v133
v_mul_f32 v142,v142,v133
v_mul_f32 v143,v143,v133
v_accvgpr_write_b32 a80,v136
v_accvgpr_write_b32 a81,v137
v_accvgpr_write_b32 a82,v138
v_accvgpr_write_b32 a83,v139
v_accvgpr_write_b32 a84,v140
v_accvgpr_write_b32 a85,v141
v_accvgpr_write_b32 a86,v142
v_accvgpr_write_b32 a87,v143
v_accvgpr_read_b32 v136,a88
v_accvgpr_read_b32 v137,a89
v_accvgpr_read_b32 v138,a90
v_accvgpr_read_b32 v139,a91
v_accvgpr_read_b32 v140,a92
v_accvgpr_read_b32 v141,a93
v_accvgpr_read_b32 v142,a94
v_accvgpr_read_b32 v143,a95
v_mul_f32 v136,v136,v133
v_mul_f32 v137,v137,v133
v_mul_f32 v138,v138,v133
v_mul_f32 v139,v139,v133
v_mul_f32 v140,v140,v133
v_mul_f32 v141,v141,v133
v_mul_f32 v142,v142,v133
v_mul_f32 v143,v143,v133
v_accvgpr_write_b32 a88,v136
v_accvgpr_write_b32 a89,v137
v_accvgpr_write_b32 a90,v138
v_accvgpr_write_b32 a91,v139
v_accvgpr_write_b32 a92,v140
v_accvgpr_write_b32 a93,v141
v_accvgpr_write_b32 a94,v142
v_accvgpr_write_b32 a95,v143
v_accvgpr_read_b32 v136,a96
v_accvgpr_read_b32 v137,a97
v_accvgpr_read_b32 v138,a98
v_accvgpr_read_b32 v139,a99
v_accvgpr_read_b32 v140,a100
v_accvgpr_read_b32 v141,a101
v_accvgpr_read_b32 v142,a102
v_accvgpr_read_b32 v143,a103
v_mul_f32 v136,v136,v133
v_mul_f32 v137,v137,v133
v_mul_f32 v138,v138,v133
v_mul_f32 v139,v139,v133
v_mul_f32 v140,v140,v133
v_mul_f32 v141,v141,v133
v_mul_f32 v142,v142,v133
v_mul_f32 v143,v143,v133
v_accvgpr_write_b32 a96,v136
v_accvgpr_write_b32 a97,v137
v_accvgpr_write_b32 a98,v138
v_accvgpr_write_b32 a99,v139
v_accvgpr_write_b32 a100,v140
v_accvgpr_write_b32 a101,v141
v_accvgpr_write_b32 a102,v142
v_accvgpr_write_b32 a103,v143
v_accvgpr_read_b32 v136,a104
v_accvgpr_read_b32 v137,a105
v_accvgpr_read_b32 v138,a106
v_accvgpr_read_b32 v139,a107
v_accvgpr_read_b32 v140,a108
v_accvgpr_read_b32 v141,a109
v_accvgpr_read_b32 v142,a110
v_accvgpr_read_b32 v143,a111
v_mul_f32 v136,v136,v133
v_mul_f32 v137,v137,v133
v_mul_f32 v138,v138,v133
v_mul_f32 v139,v139,v133
v_mul_f32 v140,v140,v133
v_mul_f32 v141,v141,v133
v_mul_f32 v142,v142,v133
v_mul_f32 v143,v143,v133
v_accvgpr_write_b32 a104,v136
v_accvgpr_write_b32 a105,v137
v_accvgpr_write_b32 a106,v138
v_accvgpr_write_b32 a107,v139
v_accvgpr_write_b32 a108,v140
v_accvgpr_write_b32 a109,v141
v_accvgpr_write_b32 a110,v142
v_accvgpr_write_b32 a111,v143
v_accvgpr_read_b32 v136,a112
v_accvgpr_read_b32 v137,a113
v_accvgpr_read_b32 v138,a114
v_accvgpr_read_b32 v139,a115
v_accvgpr_read_b32 v140,a116
v_accvgpr_read_b32 v141,a117
v_accvgpr_read_b32 v142,a118
v_accvgpr_read_b32 v143,a119
v_mul_f32 v136,v136,v133
)"
R"(v_mul_f32 v137,v137,v133
v_mul_f32 v138,v138,v133
v_mul_f32 v139,v139,v133
v_mul_f32 v140,v140,v133
v_mul_f32 v141,v141,v133
v_mul_f32 v142,v142,v133
v_mul_f32 v143,v143,v133
v_accvgpr_write_b32 a112,v136
v_accvgpr_write_b32 a113,v137
v_accvgpr_write_b32 a114,v138
v_accvgpr_write_b32 a115,v139
v_accvgpr_write_b32 a116,v140
v_accvgpr_write_b32 a117,v141
v_accvgpr_write_b32 a118,v142
v_accvgpr_write_b32 a119,v143
v_accvgpr_read_b32 v136,a120
v_accvgpr_read_b32 v137,a121
v_accvgpr_read_b32 v138,a122
v_accvgpr_read_b32 v139,a123
v_accvgpr_read_b32 v140,a124
v_accvgpr_read_b32 v141,a125
v_accvgpr_read_b32 v142,a126
v_accvgpr_read_b32 v143,a127
v_mul_f32 v136,v136,v133
v_mul_f32 v137,v137,v133
v_mul_f32 v138,v138,v133
v_mul_f32 v139,v139,v133
v_mul_f32 v140,v140,v133
v_mul_f32 v141,v141,v133
v_mul_f32 v142,v142,v133
v_mul_f32 v143,v143,v133
v_accvgpr_write_b32 a120,v136
v_accvgpr_write_b32 a121,v137
v_accvgpr_write_b32 a122,v138
v_accvgpr_write_b32 a123,v139
v_accvgpr_write_b32 a124,v140
v_accvgpr_write_b32 a125,v141
v_accvgpr_write_b32 a126,v142
v_accvgpr_write_b32 a127,v143
v_add_f32 v130,0x42c00000,v130
v_max_f32 v130,0,v130
v_sub_f32 v246,v246,v130
v_exp_f32 v134,-v130
v_sub_f32 v176,v176,v130
v_sub_f32 v177,v177,v130
v_sub_f32 v178,v178,v130
v_sub_f32 v179,v179,v130
v_sub_f32 v180,v180,v130
v_sub_f32 v181,v181,v130
v_sub_f32 v182,v182,v130
v_sub_f32 v183,v183,v130
v_mul_f32 v232,v232,v134
v_mul_f32 v233,v233,v134
v_mul_f32 v234,v234,v134
v_mul_f32 v235,v235,v134
v_accvgpr_read_b32 v136,a128
v_accvgpr_read_b32 v137,a129
v_accvgpr_read_b32 v138,a130
v_accvgpr_read_b32 v139,a131
v_accvgpr_read_b32 v140,a132
v_accvgpr_read_b32 v141,a133
v_accvgpr_read_b32 v142,a134
v_accvgpr_read_b32 v143,a135
v_mul_f32 v136,v136,v134
v_mul_f32 v137,v137,v134
v_mul_f32 v138,v138,v134
v_mul_f32 v139,v139,v134
v_mul_f32 v140,v140,v134
v_mul_f32 v141,v141,v134
v_mul_f32 v142,v142,v134
v_mul_f32 v143,v143,v134
v_accvgpr_write_b32 a128,v136
v_accvgpr_write_b32 a129,v137
v_accvgpr_write_b32 a130,v138
v_accvgpr_write_b32 a131,v139
v_accvgpr_write_b32 a132,v140
v_accvgpr_write_b32 a133,v141
v_accvgpr_write_b32 a134,v142
v_accvgpr_write_b32 a135,v143
v_accvgpr_read_b32 v136,a136
v_accvgpr_read_b32 v137,a137
v_accvgpr_read_b32 v138,a138
v_accvgpr_read_b32 v139,a139
v_accvgpr_read_b32 v140,a140
v_accvgpr_read_b32 v141,a141
v_accvgpr_read_b32 v142,a142
v_accvgpr_read_b32 v143,a143
v_mul_f32 v136,v136,v134
v_mul_f32 v137,v137,v134
v_mul_f32 v138,v138,v134
v_mul_f32 v139,v139,v134
v_mul_f32 v140,v140,v134
v_mul_f32 v141,v141,v134
v_mul_f32 v142,v142,v134
v_mul_f32 v143,v143,v134
v_accvgpr_write_b32 a136,v136
v_accvgpr_write_b32 a137,v137
v_accvgpr_write_b32 a138,v138
v_accvgpr_write_b32 a139,v139
v_accvgpr_write_b32 a140,v140
v_accvgpr_write_b32 a141,v141
v_accvgpr_write_b32 a142,v142
v_accvgpr_write_b32 a143,v143
v_accvgpr_read_b32 v136,a144
v_accvgpr_read_b32 v137,a145
v_accvgpr_read_b32 v138,a146
v_accvgpr_read_b32 v139,a147
v_accvgpr_read_b32 v140,a148
v_accvgpr_read_b32 v141,a149
v_accvgpr_read_b32 v142,a150
v_accvgpr_read_b32 v143,a151
v_mul_f32 v136,v136,v134
v_mul_f32 v137,v137,v134
v_mul_f32 v138,v138,v134
v_mul_f32 v139,v139,v134
v_mul_f32 v140,v140,v134
v_mul_f32 v141,v141,v134
v_mul_f32 v142,v142,v134
v_mul_f32 v143,v143,v134
v_accvgpr_write_b32 a144,v136
v_accvgpr_write_b32 a145,v137
v_accvgpr_write_b32 a146,v138
v_accvgpr_write_b32 a147,v139
v_accvgpr_write_b32 a148,v140
v_accvgpr_write_b32 a149,v141
v_accvgpr_write_b32 a150,v142
v_accvgpr_write_b32 a151,v143
v_accvgpr_read_b32 v136,a152
v_accvgpr_read_b32 v137,a153
v_accvgpr_read_b32 v138,a154
v_accvgpr_read_b32 v139,a155
v_accvgpr_read_b32 v140,a156
v_accvgpr_read_b32 v141,a157
v_accvgpr_read_b32 v142,a158
v_accvgpr_read_b32 v143,a159
v_mul_f32 v136,v136,v134
v_mul_f32 v137,v137,v134
v_mul_f32 v138,v138,v134
v_mul_f32 v139,v139,v134
v_mul_f32 v140,v140,v134
v_mul_f32 v141,v141,v134
v_mul_f32 v142,v142,v134
v_mul_f32 v143,v143,v134
v_accvgpr_write_b32 a152,v136
v_accvgpr_write_b32 a153,v137
v_accvgpr_write_b32 a154,v138
v_accvgpr_write_b32 a155,v139
v_accvgpr_write_b32 a156,v140
v_accvgpr_write_b32 a157,v141
v_accvgpr_write_b32 a158,v142
v_accvgpr_write_b32 a159,v143
v_accvgpr_read_b32 v136,a160
v_accvgpr_read_b32 v137,a161
v_accvgpr_read_b32 v138,a162
v_accvgpr_read_b32 v139,a163
v_accvgpr_read_b32 v140,a164
v_accvgpr_read_b32 v141,a165
v_accvgpr_read_b32 v142,a166
v_accvgpr_read_b32 v143,a167
v_mul_f32 v136,v136,v134
v_mul_f32 v137,v137,v134
v_mul_f32 v138,v138,v134
v_mul_f32 v139,v139,v134
v_mul_f32 v140,v140,v134
v_mul_f32 v141,v141,v134
v_mul_f32 v142,v142,v134
v_mul_f32 v143,v143,v134
v_accvgpr_write_b32 a160,v136
v_accvgpr_write_b32 a161,v137
v_accvgpr_write_b32 a162,v138
v_accvgpr_write_b32 a163,v139
v_accvgpr_write_b32 a164,v140
v_accvgpr_write_b32 a165,v141
v_accvgpr_write_b32 a166,v142
v_accvgpr_write_b32 a167,v143
v_accvgpr_read_b32 v136,a168
v_accvgpr_read_b32 v137,a169
v_accvgpr_read_b32 v138,a170
v_accvgpr_read_b32 v139,a171
v_accvgpr_read_b32 v140,a172
v_accvgpr_read_b32 v141,a173
v_accvgpr_read_b32 v142,a174
v_accvgpr_read_b32 v143,a175
v_mul_f32 v136,v136,v134
v_mul_f32 v137,v137,v134
v_mul_f32 v138,v138,v134
v_mul_f32 v139,v139,v134
v_mul_f32 v140,v140,v134
v_mul_f32 v141,v141,v134
v_mul_f32 v142,v142,v134
v_mul_f32 v143,v143,v134
v_accvgpr_write_b32 a168,v136
v_accvgpr_write_b32 a169,v137
v_accvgpr_write_b32 a170,v138
v_accvgpr_write_b32 a171,v139
v_accvgpr_write_b32 a172,v140
v_accvgpr_write_b32 a173,v141
v_accvgpr_write_b32 a174,v142
v_accvgpr_write_b32 a175,v143
v_accvgpr_read_b32 v136,a176
)"
R"(v_accvgpr_read_b32 v137,a177
v_accvgpr_read_b32 v138,a178
v_accvgpr_read_b32 v139,a179
v_accvgpr_read_b32 v140,a180
v_accvgpr_read_b32 v141,a181
v_accvgpr_read_b32 v142,a182
v_accvgpr_read_b32 v143,a183
v_mul_f32 v136,v136,v134
v_mul_f32 v137,v137,v134
v_mul_f32 v138,v138,v134
v_mul_f32 v139,v139,v134
v_mul_f32 v140,v140,v134
v_mul_f32 v141,v141,v134
v_mul_f32 v142,v142,v134
v_mul_f32 v143,v143,v134
v_accvgpr_write_b32 a176,v136
v_accvgpr_write_b32 a177,v137
v_accvgpr_write_b32 a178,v138
v_accvgpr_write_b32 a179,v139
v_accvgpr_write_b32 a180,v140
v_accvgpr_write_b32 a181,v141
v_accvgpr_write_b32 a182,v142
v_accvgpr_write_b32 a183,v143
v_accvgpr_read_b32 v136,a184
v_accvgpr_read_b32 v137,a185
v_accvgpr_read_b32 v138,a186
v_accvgpr_read_b32 v139,a187
v_accvgpr_read_b32 v140,a188
v_accvgpr_read_b32 v141,a189
v_accvgpr_read_b32 v142,a190
v_accvgpr_read_b32 v143,a191
v_mul_f32 v136,v136,v134
v_mul_f32 v137,v137,v134
v_mul_f32 v138,v138,v134
v_mul_f32 v139,v139,v134
v_mul_f32 v140,v140,v134
v_mul_f32 v141,v141,v134
v_mul_f32 v142,v142,v134
v_mul_f32 v143,v143,v134
v_accvgpr_write_b32 a184,v136
v_accvgpr_write_b32 a185,v137
v_accvgpr_write_b32 a186,v138
v_accvgpr_write_b32 a187,v139
v_accvgpr_write_b32 a188,v140
v_accvgpr_write_b32 a189,v141
v_accvgpr_write_b32 a190,v142
v_accvgpr_write_b32 a191,v143
v_add_f32 v131,0x42c00000,v131
v_max_f32 v131,0,v131
v_sub_f32 v247,v247,v131
v_exp_f32 v135,-v131
v_sub_f32 v184,v184,v131
v_sub_f32 v185,v185,v131
v_sub_f32 v186,v186,v131
v_sub_f32 v187,v187,v131
v_sub_f32 v188,v188,v131
v_sub_f32 v189,v189,v131
v_sub_f32 v190,v190,v131
v_sub_f32 v191,v191,v131
v_mul_f32 v236,v236,v135
v_mul_f32 v237,v237,v135
v_mul_f32 v238,v238,v135
v_mul_f32 v239,v239,v135
v_accvgpr_read_b32 v136,a192
v_accvgpr_read_b32 v137,a193
v_accvgpr_read_b32 v138,a194
v_accvgpr_read_b32 v139,a195
v_accvgpr_read_b32 v140,a196
v_accvgpr_read_b32 v141,a197
v_accvgpr_read_b32 v142,a198
v_accvgpr_read_b32 v143,a199
v_mul_f32 v136,v136,v135
v_mul_f32 v137,v137,v135
v_mul_f32 v138,v138,v135
v_mul_f32 v139,v139,v135
v_mul_f32 v140,v140,v135
v_mul_f32 v141,v141,v135
v_mul_f32 v142,v142,v135
v_mul_f32 v143,v143,v135
v_accvgpr_write_b32 a192,v136
v_accvgpr_write_b32 a193,v137
v_accvgpr_write_b32 a194,v138
v_accvgpr_write_b32 a195,v139
v_accvgpr_write_b32 a196,v140
v_accvgpr_write_b32 a197,v141
v_accvgpr_write_b32 a198,v142
v_accvgpr_write_b32 a199,v143
v_accvgpr_read_b32 v136,a200
v_accvgpr_read_b32 v137,a201
v_accvgpr_read_b32 v138,a202
v_accvgpr_read_b32 v139,a203
v_accvgpr_read_b32 v140,a204
v_accvgpr_read_b32 v141,a205
v_accvgpr_read_b32 v142,a206
v_accvgpr_read_b32 v143,a207
v_mul_f32 v136,v136,v135
v_mul_f32 v137,v137,v135
v_mul_f32 v138,v138,v135
v_mul_f32 v139,v139,v135
v_mul_f32 v140,v140,v135
v_mul_f32 v141,v141,v135
v_mul_f32 v142,v142,v135
v_mul_f32 v143,v143,v135
v_accvgpr_write_b32 a200,v136
v_accvgpr_write_b32 a201,v137
v_accvgpr_write_b32 a202,v138
v_accvgpr_write_b32 a203,v139
v_accvgpr_write_b32 a204,v140
v_accvgpr_write_b32 a205,v141
v_accvgpr_write_b32 a206,v142
v_accvgpr_write_b32 a207,v143
v_accvgpr_read_b32 v136,a208
v_accvgpr_read_b32 v137,a209
v_accvgpr_read_b32 v138,a210
v_accvgpr_read_b32 v139,a211
v_accvgpr_read_b32 v140,a212
v_accvgpr_read_b32 v141,a213
v_accvgpr_read_b32 v142,a214
v_accvgpr_read_b32 v143,a215
v_mul_f32 v136,v136,v135
v_mul_f32 v137,v137,v135
v_mul_f32 v138,v138,v135
v_mul_f32 v139,v139,v135
v_mul_f32 v140,v140,v135
v_mul_f32 v141,v141,v135
v_mul_f32 v142,v142,v135
v_mul_f32 v143,v143,v135
v_accvgpr_write_b32 a208,v136
v_accvgpr_write_b32 a209,v137
v_accvgpr_write_b32 a210,v138
v_accvgpr_write_b32 a211,v139
v_accvgpr_write_b32 a212,v140
v_accvgpr_write_b32 a213,v141
v_accvgpr_write_b32 a214,v142
v_accvgpr_write_b32 a215,v143
v_accvgpr_read_b32 v136,a216
v_accvgpr_read_b32 v137,a217
v_accvgpr_read_b32 v138,a218
v_accvgpr_read_b32 v139,a219
v_accvgpr_read_b32 v140,a220
v_accvgpr_read_b32 v141,a221
v_accvgpr_read_b32 v142,a222
v_accvgpr_read_b32 v143,a223
v_mul_f32 v136,v136,v135
v_mul_f32 v137,v137,v135
v_mul_f32 v138,v138,v135
v_mul_f32 v139,v139,v135
v_mul_f32 v140,v140,v135
v_mul_f32 v141,v141,v135
v_mul_f32 v142,v142,v135
v_mul_f32 v143,v143,v135
v_accvgpr_write_b32 a216,v136
v_accvgpr_write_b32 a217,v137
v_accvgpr_write_b32 a218,v138
v_accvgpr_write_b32 a219,v139
v_accvgpr_write_b32 a220,v140
v_accvgpr_write_b32 a221,v141
v_accvgpr_write_b32 a222,v142
v_accvgpr_write_b32 a223,v143
v_accvgpr_read_b32 v136,a224
v_accvgpr_read_b32 v137,a225
v_accvgpr_read_b32 v138,a226
v_accvgpr_read_b32 v139,a227
v_accvgpr_read_b32 v140,a228
v_accvgpr_read_b32 v141,a229
v_accvgpr_read_b32 v142,a230
v_accvgpr_read_b32 v143,a231
v_mul_f32 v136,v136,v135
v_mul_f32 v137,v137,v135
v_mul_f32 v138,v138,v135
v_mul_f32 v139,v139,v135
v_mul_f32 v140,v140,v135
v_mul_f32 v141,v141,v135
v_mul_f32 v142,v142,v135
v_mul_f32 v143,v143,v135
v_accvgpr_write_b32 a224,v136
v_accvgpr_write_b32 a225,v137
v_accvgpr_write_b32 a226,v138
v_accvgpr_write_b32 a227,v139
v_accvgpr_write_b32 a228,v140
v_accvgpr_write_b32 a229,v141
v_accvgpr_write_b32 a230,v142
v_accvgpr_write_b32 a231,v143
v_accvgpr_read_b32 v136,a232
v_accvgpr_read_b32 v137,a233
v_accvgpr_read_b32 v138,a234
v_accvgpr_read_b32 v139,a235
v_accvgpr_read_b32 v140,a236
v_accvgpr_read_b32 v141,a237
v_accvgpr_read_b32 v142,a238
v_accvgpr_read_b32 v143,a239
v_mul_f32 v136,v136,v135
v_mul_f32 v137,v137,v135
v_mul_f32 v138,v138,v135
v_mul_f32 v139,v139,v135
v_mul_f32 v140,v140,v135
v_mul_f32 v141,v141,v135
v_mul_f32 v142,v142,v135
v_mul_f32 v143,v143,v135
v_accvgpr_write_b32 a232,v136
)"
R"(v_accvgpr_write_b32 a233,v137
v_accvgpr_write_b32 a234,v138
v_accvgpr_write_b32 a235,v139
v_accvgpr_write_b32 a236,v140
v_accvgpr_write_b32 a237,v141
v_accvgpr_write_b32 a238,v142
v_accvgpr_write_b32 a239,v143
v_accvgpr_read_b32 v136,a240
v_accvgpr_read_b32 v137,a241
v_accvgpr_read_b32 v138,a242
v_accvgpr_read_b32 v139,a243
v_accvgpr_read_b32 v140,a244
v_accvgpr_read_b32 v141,a245
v_accvgpr_read_b32 v142,a246
v_accvgpr_read_b32 v143,a247
v_mul_f32 v136,v136,v135
v_mul_f32 v137,v137,v135
v_mul_f32 v138,v138,v135
v_mul_f32 v139,v139,v135
v_mul_f32 v140,v140,v135
v_mul_f32 v141,v141,v135
v_mul_f32 v142,v142,v135
v_mul_f32 v143,v143,v135
v_accvgpr_write_b32 a240,v136
v_accvgpr_write_b32 a241,v137
v_accvgpr_write_b32 a242,v138
v_accvgpr_write_b32 a243,v139
v_accvgpr_write_b32 a244,v140
v_accvgpr_write_b32 a245,v141
v_accvgpr_write_b32 a246,v142
v_accvgpr_write_b32 a247,v143
v_accvgpr_read_b32 v136,a248
v_accvgpr_read_b32 v137,a249
v_accvgpr_read_b32 v138,a250
v_accvgpr_read_b32 v139,a251
v_accvgpr_read_b32 v140,a252
v_accvgpr_read_b32 v141,a253
v_accvgpr_read_b32 v142,a254
v_accvgpr_read_b32 v143,a255
v_mul_f32 v136,v136,v135
v_mul_f32 v137,v137,v135
v_mul_f32 v138,v138,v135
v_mul_f32 v139,v139,v135
v_mul_f32 v140,v140,v135
v_mul_f32 v141,v141,v135
v_mul_f32 v142,v142,v135
v_mul_f32 v143,v143,v135
v_accvgpr_write_b32 a248,v136
v_accvgpr_write_b32 a249,v137
v_accvgpr_write_b32 a250,v138
v_accvgpr_write_b32 a251,v139
v_accvgpr_write_b32 a252,v140
v_accvgpr_write_b32 a253,v141
v_accvgpr_write_b32 a254,v142
v_accvgpr_write_b32 a255,v143
s_nop 3
s_setpc_b64 s[76:77]
Lendb_%=:
)"
  OPERANDS);
}

extern "C" void kernel_launch(void* const* d_in, const int* in_sizes, int n_in, void* d_out, int out_size, void* d_ws, size_t ws_size,
                              hipStream_t stream) {
  k1_attn<<<256, 256, 0, stream>>>((const char*)d_in[1], (const char*)d_in[2], (const char*)d_in[3], (const char*)d_in[4],
                                   (const char*)d_in[6], (const char*)d_in[7], (const char*)d_in[8], (const char*)d_in[0], (char*)d_ws, (char*)d_out);
}
```
